```python
import torch

def _softmax(x, axis=-1):
    return torch.softmax(x, dim=axis)

def _gelu(x):
    return torch.nn.functional.gelu(x, approximate='tanh')

def _silu(x):
    return torch.nn.functional.silu(x)

def _sigmoid(x):
    return torch.sigmoid(x)

def _layernorm(x, axis=-1, eps=1e-05):
    m = x.mean(dim=axis, keepdim=True)
    v = x.var(dim=axis, keepdim=True, unbiased=False)
    return (x - m) / torch.sqrt(v + eps)

def _scatter_add(tgt, idx, src, axis=0):
    return tgt.clone().index_add_(axis, idx, src)

def _segment_sum(src, seg, n):
    out = torch.zeros((n,) + src.shape[1:], dtype=src.dtype)
    return out.index_add_(0, seg, src)

def _pad(x, pw):
    flat = tuple((v for pair in reversed(pw) for v in pair))
    return torch.nn.functional.pad(x, flat)
kernel_name = 'parallel_sum__linear_kernel_hedgehog__mixer_gmlp__prompt'

def setup_inputs():

    _w = lambda a: (lambda t: t.to(torch.bfloat16) if t.is_floating_point() else t)(torch.as_tensor(a))

    def _A_setup():
        B, S, D, H = (1, 2048, 512, 32)
        Dh = D // H
        return {'x': _w((lambda *s: torch.randn(*s, dtype=torch.bfloat16) * 0.05)(B, S, D)), 'wqkv': _w((lambda *s: torch.randn(*s, dtype=torch.bfloat16) * 0.8)(D, 3 * D)), 'w_phi': _w((lambda *s: torch.randn(*s, dtype=torch.bfloat16) * 0.8)(H, Dh, Dh) * Dh ** (-0.5)), 'wo': _w((lambda *s: torch.randn(*s, dtype=torch.bfloat16) * 0.1)(D, D))}
    _A_in = _A_setup()

    def _B_setup():
        B, S, D, F = (1, 2048, 512, 1376)
        Fh = F // 2
        return {'x': _w((lambda *s: torch.randn(*s, dtype=torch.bfloat16) * 0.05)(B, S, D)), 'w_in': _w((lambda *s: torch.randn(*s, dtype=torch.bfloat16) * 0.4)(D, 2 * Fh)), 'w_sp': _w(0.01 * (lambda *s: torch.randn(*s, dtype=torch.bfloat16) * 0.4)(S, S)), 'b_sp': _w(1.0 + 0.01 * (lambda *s: torch.randn(*s, dtype=torch.bfloat16) * 0.05)(S)), 'w_out': _w((lambda *s: torch.randn(*s, dtype=torch.bfloat16) * 0.05)(Fh, D))}
    _B_in = _B_setup()
    return {'A_x': _A_in['x'], 'A_wqkv': _A_in['wqkv'], 'A_w_phi': _A_in['w_phi'], 'A_wo': _A_in['wo'], 'B_x': _B_in['x'], 'B_w_in': _B_in['w_in'], 'B_w_sp': _B_in['w_sp'], 'B_b_sp': _B_in['b_sp'], 'B_w_out': _B_in['w_out']}

def reference(A_x, A_wqkv, A_w_phi, A_wo, B_x, B_w_in, B_w_sp, B_b_sp, B_w_out):

    def _A_ref(x, wqkv, w_phi, wo):
        B, S, D = x.shape
        H = 32
        Dh = D // H
        qkv = (lambda x, *d: x.permute(*d))((x @ wqkv).reshape(B, S, 3, H, Dh), 2, 0, 3, 1, 4)
        q, k, v = (qkv[0], qkv[1], qkv[2])
        pq = torch.einsum('bhsd,hdf->bhsf', q, w_phi)
        pk = torch.einsum('bhsd,hdf->bhsf', k, w_phi)
        phi_q = torch.concatenate([_softmax(pq, axis=-1), _softmax(-pq, axis=-1)], axis=-1)
        phi_k = torch.concatenate([_softmax(pk, axis=-1), _softmax(-pk, axis=-1)], axis=-1)
        kv = torch.einsum('bhsf,bhsd->bhfd', phi_k, v)
        z = torch.einsum('bhsf->bhf', phi_k)
        num = torch.einsum('bhqf,bhfd->bhqd', phi_q, kv)
        den = torch.einsum('bhqf,bhf->bhq', phi_q, z)[..., None] + 1e-06
        o = num / den
        return (lambda x, *d: x.permute(*d))(o, 0, 2, 1, 3).reshape(B, S, D) @ wo

    def _B_ref(x, w_in, w_sp, b_sp, w_out):
        B, S, D = x.shape
        F = w_in.shape[-1]
        Fh = F // 2
        z = _gelu(torch.einsum('bsd,df->bsf', x, w_in))
        z1 = z[..., :Fh].clone()
        z2 = _layernorm(z[..., Fh:], axis=-1)
        g = torch.einsum('ts,bsf->btf', w_sp, z2) + b_sp[None, :, None]
        return torch.einsum('bsf,fd->bsd', z1 * g, w_out)
    x = A_x
    xn = _layernorm(x, axis=-1)
    return x + _A_ref(xn, A_wqkv, A_w_phi, A_wo) + _B_ref(xn, B_w_in, B_w_sp, B_b_sp, B_w_out)
```

```cpp
#include <hip/hip_runtime.h>
#include <hip/hip_bf16.h>
#define MIMIC 0
typedef unsigned short u16;
__device__ __forceinline__ float bf2f(u16 v){ return __uint_as_float(((unsigned)v)<<16); }
__device__ __forceinline__ u16 f2bf(float f){ unsigned u=__float_as_uint(f); u += 0x7FFF + ((u>>16)&1); return (u16)(u>>16); }
__device__ __forceinline__ float R(float f){
  return f;
}
__global__ void k_ln(const float* in, int ld, int n, float* out, int ldo){
  int s=blockIdx.x; __shared__ float red[256];
  float a=0; for(int i=threadIdx.x;i<n;i+=256) a+=in[(size_t)s*ld+i];
  red[threadIdx.x]=a; __syncthreads();
  for(int o=128;o>0;o>>=1){ if(threadIdx.x<o) red[threadIdx.x]+=red[threadIdx.x+o]; __syncthreads(); }
  float m=red[0]/n; __syncthreads();
  a=0; for(int i=threadIdx.x;i<n;i+=256){ float d=in[(size_t)s*ld+i]-m; a+=d*d; }
  red[threadIdx.x]=a; __syncthreads();
  for(int o=128;o>0;o>>=1){ if(threadIdx.x<o) red[threadIdx.x]+=red[threadIdx.x+o]; __syncthreads(); }
  float v=red[0]/n;
  float mb=R(m), sb=R(sqrtf(R(R(v)+1e-5f)));
  for(int i=threadIdx.x;i<n;i+=256) out[(size_t)s*ldo+i]=R(R(in[(size_t)s*ld+i]-mb)/sb);
}
__global__ void k_cvt(const u16* in, float* out, int n){ int i=blockIdx.x*256+threadIdx.x; if(i<n) out[i]=bf2f(in[i]); }
__global__ void k_gemm(const float* A,int lda,const float* B,int ldb,float* C,int ldc,int M,int N,int K){
  __shared__ float As[16][17], Bs[16][17];
  int tx=threadIdx.x&15, ty=threadIdx.x>>4; int row=blockIdx.y*16+ty, col=blockIdx.x*16+tx;
  float acc=0;
  for(int k0=0;k0<K;k0+=16){
    As[ty][tx]=A[(size_t)row*lda+k0+tx]; Bs[ty][tx]=B[(size_t)(k0+ty)*ldb+col]; __syncthreads();
    for(int k=0;k<16;++k) acc+=As[ty][k]*Bs[k][tx];
    __syncthreads();
  }
  C[(size_t)row*ldc+col]=R(acc);
}
__global__ void k_phi(const float* qkv,const float* wphi,float* phiq,float* phik){
  int idx=blockIdx.x*256+threadIdx.x;
  int s=idx>>6, which=(idx>>5)&1, h=idx&31;
  const float* q=qkv+(size_t)s*1536+which*512+h*16;
  float p[16]; float mx=-1e30f, mn=1e30f;
  for(int f=0;f<16;++f){ float a=0; for(int d=0;d<16;++d) a+=q[d]*wphi[h*256+d*16+f]; p[f]=R(a); mx=fmaxf(mx,p[f]); mn=fminf(mn,p[f]); }
  float sp=0,sn=0; for(int f=0;f<16;++f){ sp+=expf(p[f]-mx); sn+=expf(mn-p[f]); }
  float* o=(which?phik:phiq)+((size_t)s*32+h)*32;
  for(int f=0;f<16;++f){ o[f]=R(expf(p[f]-mx)/sp); o[16+f]=R(expf(mn-p[f])/sn); }
}
__global__ void k_kv(const float* phik,const float* qkv,float* kv){
  int h=blockIdx.x; int t=threadIdx.x; if(t>=32*17) return; int f=t/17,d=t%17;
  float a=0; for(int s=0;s<2048;++s){ float p=phik[((size_t)s*32+h)*32+f]; a+= d<16? p*qkv[(size_t)s*1536+1024+h*16+d] : p; }
  kv[(h*32+f)*17+d]=R(a);
}
__global__ void k_o(const float* phiq,const float* kv,float* o){
  int idx=blockIdx.x*256+threadIdx.x; int s=idx>>9, h=(idx>>4)&31, d=idx&15;
  float num=0,den=0; for(int f=0;f<32;++f){ float p=phiq[((size_t)s*32+h)*32+f]; num+=p*kv[(h*32+f)*17+d]; den+=p*kv[(h*32+f)*17+16]; }
  o[idx]=R(R(num)/R(R(den)+1e-6f));
}
__global__ void k_gelu(float* z,int n){ int i=blockIdx.x*256+threadIdx.x; if(i<n){ float x=z[i]; float t=tanhf(0.7978845608028654f*(x+0.044715f*x*x*x)); z[i]=R(0.5f*x*(1.f+t)); } }
__global__ void k_h(const float* z,const float* g,const float* b,float* h){ int i=blockIdx.x*256+threadIdx.x; int s=i/688,f=i%688; h[i]=R(z[(size_t)s*1376+f]*R(g[i]+b[s])); }
__global__ void k_out(const float* x,const float* a,const float* b,u16* out){ int i=blockIdx.x*256+threadIdx.x; out[i]=f2bf(R(x[i]+a[i])+b[i]); }
extern "C" void kernel_launch(void* const* d_in, const int* in_sizes, int n_in,
                              void* d_out, int out_size, void* d_ws, size_t ws_size,
                              hipStream_t st) {
  float* w=(float*)d_ws; size_t off=0;
  auto al=[&](size_t n){ float* p=w+off; off+=n; return p; };
  float* in[9]; for(int i=0;i<9;++i){ in[i]=al(in_sizes[i]); k_cvt<<<(in_sizes[i]+255)/256,256,0,st>>>((const u16*)d_in[i],in[i],in_sizes[i]); }
  float *xn=al(2048*512),*qkv=al(2048*1536),*phiq=al(2048*1024),*phik=al(2048*1024),*kv=al(32*32*17+32),*o=al(2048*512),*ao=al(2048*512),
        *z=al(2048*1376),*z2=al(2048*688),*g=al(2048*688),*h=al(2048*688),*bo=al(2048*512);
  k_ln<<<2048,256,0,st>>>(in[0],512,512,xn,512);
  k_gemm<<<dim3(1536/16,128),256,0,st>>>(xn,512,in[1],1536,qkv,1536,2048,1536,512);
  k_phi<<<2048*64/256,256,0,st>>>(qkv,in[2],phiq,phik);
  k_kv<<<32,576,0,st>>>(phik,qkv,kv);
  k_o<<<2048*512/256,256,0,st>>>(phiq,kv,o);
  k_gemm<<<dim3(512/16,128),256,0,st>>>(o,512,in[3],512,ao,512,2048,512,512);
  k_gemm<<<dim3(1376/16,128),256,0,st>>>(xn,512,in[5],1376,z,1376,2048,1376,512);
  k_gelu<<<2048*1376/256,256,0,st>>>(z,2048*1376);
  k_ln<<<2048,256,0,st>>>(z+688,1376,688,z2,688);
  k_gemm<<<dim3(688/16,128),256,0,st>>>(in[6],2048,z2,688,g,688,2048,688,2048);
  k_h<<<2048*688/256,256,0,st>>>(z,g,in[7],h);
  k_gemm<<<dim3(512/16,128),256,0,st>>>(h,688,in[8],512,bo,512,2048,512,688);
  k_out<<<2048*512/256,256,0,st>>>(in[0],ao,bo,(u16*)d_out);
}
```

```cpp
#include <hip/hip_runtime.h>
#include <hip/hip_bf16.h>

typedef unsigned int u32;
typedef unsigned long long u64;
typedef short s16x4 __attribute__((ext_vector_type(4)));
typedef __bf16 bf16x2 __attribute__((ext_vector_type(2)));
typedef __bf16 bf16x8 __attribute__((ext_vector_type(8)));
typedef float f32x4 __attribute__((ext_vector_type(4)));
typedef float f32x2 __attribute__((ext_vector_type(2)));
typedef u32 u32x4 __attribute__((ext_vector_type(4)));
typedef u32 u32x2 __attribute__((ext_vector_type(2)));
typedef __amdgpu_buffer_rsrc_t rsrc_t;
#define LDSP __attribute__((address_space(3)))
#define DI __device__ __forceinline__

#define STAMPS 0

constexpr u32 WS_EP = 0;
constexpr u32 WS_F2 = 4096;
constexpr u32 WS_F3 = WS_F2 + 8192;
constexpr u32 WS_F4 = WS_F3 + 8192;
constexpr u32 WS_F5 = WS_F4 + 2048;
constexpr u32 WS_F6 = WS_F5 + 2048;
constexpr u32 WS_F7 = WS_F6 + 8192;
constexpr u32 WS_SG = WS_F7 + 65536;
constexpr u32 WS_ZB = WS_SG + 524288;
constexpr u32 WS_Z1 = WS_ZB + 43 * 65536;
constexpr u32 WS_KP = WS_Z1 + 128 * 43 * 512;
constexpr u32 WS_KF = WS_KP + 128 * 8704;
constexpr u32 WS_KZ = WS_KF + 32768;
constexpr u32 WS_OF = WS_KZ + 2048;
constexpr u32 WS_HF = WS_OF + 128 * 16384;
constexpr u32 WS_ST = WS_HF + 128 * 22 * 1024;
constexpr u32 WS_END = WS_ST + 256 * 8 * 32 * 8;

constexpr u32 L_MISC = 131072;
constexpr u32 L_KVS = L_MISC;
constexpr u32 L_KZS = L_MISC + 24576;
constexpr u32 L_WPHI = L_MISC + 26112;
constexpr u32 L_BNC = L_MISC + 28160;
constexpr u32 L_ZST = L_MISC;
constexpr u32 L_WOUT = 0;
constexpr u32 L_RED = 90112;
constexpr u32 L_WO = 90112;
constexpr u32 L_FIN = 0;

constexpr u64 Z2S = 0x26211B16110B0500ull;
constexpr u64 Z1S = 0x26211C16110C0600ull;
DI int grp_start(u64 tab, int c) { return c >= 8 ? 43 : (int)((tab >> (8 * c)) & 0xFF); }

#define WAITV(n) asm volatile("s_waitcnt vmcnt(%0)" ::"n"(n) : "memory")
#define SCHED() __builtin_amdgcn_sched_barrier(0)
#define BARRIER() do { SCHED(); asm volatile("s_waitcnt lgkmcnt(0)\n\ts_barrier" ::: "memory"); SCHED(); } while (0)
#define WAITL0() asm volatile("s_waitcnt lgkmcnt(0)" ::: "memory")

DI void dma16(u32 lds, u32 voff, rsrc_t srd, u32 soff) {
  asm volatile("s_mov_b32 m0, %0\n\ts_nop 0\n\tbuffer_load_dwordx4 %1, %2, %3 offen lds" ::"s"(lds), "v"(voff), "s"(srd), "s"(soff) : "memory");
}
DI u32x4 ld16_sc1(u32 voff, rsrc_t srd, u32 soff) {
  u32x4 r;
  asm volatile("buffer_load_dwordx4 %0, %1, %2, %3 offen sc1" : "=v"(r) : "v"(voff), "s"(srd), "s"(soff) : "memory");
  return r;
}
DI u32x4 ld16(u32 voff, rsrc_t srd, u32 soff) {
  u32x4 r;
  asm volatile("buffer_load_dwordx4 %0, %1, %2, %3 offen" : "=v"(r) : "v"(voff), "s"(srd), "s"(soff) : "memory");
  return r;
}
DI u32x2 ld8_sc1(u32 voff, rsrc_t srd, u32 soff) {
  u32x2 r;
  asm volatile("buffer_load_dwordx2 %0, %1, %2, %3 offen sc1" : "=v"(r) : "v"(voff), "s"(srd), "s"(soff) : "memory");
  return r;
}
DI u32 ld2(u32 voff, rsrc_t srd, u32 soff) {
  u32 r;
  asm volatile("buffer_load_ushort %0, %1, %2, %3 offen" : "=v"(r) : "v"(voff), "s"(srd), "s"(soff) : "memory");
  return r;
}
DI void st16_sc1(u32x4 v, u32 voff, rsrc_t srd, u32 soff) { __builtin_amdgcn_raw_buffer_store_b128(v, srd, voff, soff, 16); }
DI void st8_sc1(u32x2 v, u32 voff, rsrc_t srd, u32 soff) { __builtin_amdgcn_raw_buffer_store_b64(v, srd, voff, soff, 16); }
#define PIN(x) asm volatile("" : "+v"(x))

DI u32x4 lds16(u32 a) { return *(const LDSP u32x4*)(size_t)a; }
DI u32x2 lds8(u32 a) { return *(const LDSP u32x2*)(size_t)a; }
DI f32x4 lds16f(u32 a) { return *(const LDSP f32x4*)(size_t)a; }
DI void sts16f(u32 a, f32x4 v) { *(LDSP f32x4*)(size_t)a = v; }
DI void sts4f(u32 a, float v) { *(LDSP float*)(size_t)a = v; }
DI u32x2 ldstr(u32 a) { return __builtin_bit_cast(u32x2, __builtin_amdgcn_ds_read_tr16_b64_v4i16((LDSP s16x4*)(size_t)a)); }

DI f32x4 mfma32(u32x4 a, u32x4 b, f32x4 c) {
  return __builtin_amdgcn_mfma_f32_16x16x32_bf16(__builtin_bit_cast(bf16x8, a), __builtin_bit_cast(bf16x8, b), c, 0, 0, 0);
}
DI f32x4 mfma16(u32x2 a, u32x2 b, f32x4 c) {
  return __builtin_amdgcn_mfma_f32_16x16x16bf16_1k(__builtin_bit_cast(s16x4, a), __builtin_bit_cast(s16x4, b), c, 0, 0, 0);
}
DI u32 pk2(float a, float b) { bf16x2 v = {(__bf16)a, (__bf16)b}; return __builtin_bit_cast(u32, v); }
DI float blo(u32 u) { return __uint_as_float(u << 16); }
DI float bhi(u32 u) { return __uint_as_float(u & 0xffff0000u); }
DI float rbf(float a) { return blo(pk2(a, a)); }
DI float dot2(u32 a, u32 b, float c) {
  return __builtin_amdgcn_fdot2_f32_bf16(__builtin_bit_cast(bf16x2, a), __builtin_bit_cast(bf16x2, b), c, false);
}
DI f32x2 fma2(f32x2 a, f32x2 b, f32x2 c) { return __builtin_elementwise_fma(a, b, c); }
DI f32x2 bc2(float a) { return (f32x2){a, a}; }
DI f32x2 lo2(f32x4 v) { return (f32x2){v[0], v[1]}; }
DI f32x2 hi2(f32x4 v) { return (f32x2){v[2], v[3]}; }
DI u32 pk2v(f32x2 v) { return pk2(v[0], v[1]); }
DI f32x2 gelu2(f32x2 x) {
  f32x2 u = fma2(x * x, bc2(-0.10294324f), bc2(-2.3022082f)) * x;
  f32x2 e = {__builtin_amdgcn_exp2f(u[0]), __builtin_amdgcn_exp2f(u[1])};
  e = e + bc2(1.0f);
  f32x2 r = {__builtin_amdgcn_rcpf(e[0]), __builtin_amdgcn_rcpf(e[1])};
  return x * r;
}
DI int opaque_v(int v) { asm volatile("" : "+v"(v)); return v; }
template <bool MX> DI void red2g(float& a, float& b) {
  auto s = __builtin_amdgcn_permlane32_swap(__float_as_uint(a), __float_as_uint(b), false, false);
  float c = MX ? fmaxf(__uint_as_float(s[0]), __uint_as_float(s[1])) : __uint_as_float(s[0]) + __uint_as_float(s[1]);
  auto s2 = __builtin_amdgcn_permlane16_swap(__float_as_uint(c), __float_as_uint(c), false, false);
  float d = MX ? fmaxf(__uint_as_float(s2[0]), __uint_as_float(s2[1])) : __uint_as_float(s2[0]) + __uint_as_float(s2[1]);
  auto s3 = __builtin_amdgcn_permlane32_swap(__float_as_uint(d), __float_as_uint(d), false, false);
  a = __uint_as_float(s3[0]);
  b = __uint_as_float(s3[1]);
}
template <int CTRL> DI float dpp(float v) {
  return __uint_as_float(__builtin_amdgcn_update_dpp(0, __float_as_uint(v), CTRL, 0xF, 0xF, true));
}
DI u32x4 hedgehog(f32x4 p) {
  p[0] = rbf(p[0]); p[1] = rbf(p[1]); p[2] = rbf(p[2]); p[3] = rbf(p[3]);
  float mx = fmaxf(fmaxf(p[0], p[1]), fmaxf(p[2], p[3]));
  float nm = -fminf(fminf(p[0], p[1]), fminf(p[2], p[3]));
  red2g<true>(mx, nm);
  const float L2E = 1.44269504f;
  float a = -mx * L2E, b = -nm * L2E;
  f32x4 ep, en;
#pragma unroll
  for (int j = 0; j < 4; ++j) {
    ep[j] = __builtin_amdgcn_exp2f(p[j] * L2E + a);
    en[j] = __builtin_amdgcn_exp2f(-p[j] * L2E + b);
  }
  float sp = (ep[0] + ep[1]) + (ep[2] + ep[3]), sn = (en[0] + en[1]) + (en[2] + en[3]);
  red2g<false>(sp, sn);
  sp = __builtin_amdgcn_rcpf(sp);
  sn = __builtin_amdgcn_rcpf(sn);
  u32x4 r;
  r[0] = pk2(ep[0] * sp, ep[1] * sp); r[1] = pk2(ep[2] * sp, ep[3] * sp);
  r[2] = pk2(en[0] * sn, en[1] * sn); r[3] = pk2(en[2] * sn, en[3] * sn);
  return r;
}
DI void ln_fin(float s, float q, float invn, float& r, float& mr) {
  float m = s * invn;
  float v = fmaxf(q * invn - m * m, 0.0f);
  float sd = rbf(__builtin_amdgcn_sqrtf(rbf(rbf(v) + 1e-5f)));
  r = __builtin_amdgcn_rcpf(sd);
  mr = rbf(m) * r;
}

constexpr int SPIN_CAP = 1 << 17;
DI void poll_flags(u32 voff, bool act, rsrc_t srd, u32 soff, u32 tlo, u32 thi) {
#pragma nounroll
  for (int it = 0; it < SPIN_CAP; ++it) {
    u32x2 v;
    asm volatile("buffer_load_dwordx2 %0, %1, %2, %3 offen sc1\n\ts_waitcnt vmcnt(0)" : "=&v"(v) : "v"(voff), "s"(srd), "s"(soff) : "memory");
    bool ok = !act || (v[0] == tlo && v[1] == thi);
    if (__all(ok)) break;
    __builtin_amdgcn_s_sleep(2);
  }
}
DI void set_flag(u32 off, rsrc_t srd, u32 tlo, u32 thi, int lane) {
  if (lane == 0) { u32x2 v = {tlo, thi}; st8_sc1(v, 0, srd, off); }
}

#define STAMP(i)

struct Ctx {
  int lane, w, rw, cw, i16, g, P, c, p, c2, rot;
  u32 tlo, thi;
  rsrc_t sx, sw, sa, sws;
  u32 vx0, vx1, vw0, vw1, vA0, vA1;
  u32 wstride64;
};

DI void issue_S(const Ctx& C, int n) {
  const u32 slot = (n & 3) * 32768;
  const u32 ks = (n + C.rot) & 7;
  dma16(slot + C.w * 2048, C.vx0, C.sx, ks * 128);
  dma16(slot + C.w * 2048 + 1024, C.vx1, C.sx, ks * 128);
  dma16(slot + 16384 + C.w * 2048, C.vw0, C.sw, ks * C.wstride64);
  dma16(slot + 16384 + C.w * 2048 + 1024, C.vw1, C.sw, ks * C.wstride64);
}
template <bool Z> DI void issue_T(const Ctx& C, int j, u32 v0, u32 v1) {
  const u32 slot = (j & 3) * 32768;
#pragma unroll
  for (int u = 0; u < 2; ++u) {
    const u32 ks = (2 * j + u + C.rot) & 7;
    dma16(slot + u * 16384 + C.w * 2048, v0, C.sw, ks * C.wstride64);
    if (Z) dma16(slot + u * 16384 + C.w * 2048 + 1024, v1, C.sw, ks * C.wstride64);
  }
}
DI void issue_A(const Ctx& C, int a) {
  const u32 base = (a & 3) * 32768 + C.w * 4096;
  const u32 so = C.w * 512 + (a >> 1) * 128 + (a & 1) * 32 * 4096;
  dma16(base, C.vA0, C.sa, so);
  dma16(base + 1024, C.vA1, C.sa, so);
  dma16(base + 2048, C.vA0, C.sa, so + 16 * 4096);
  dma16(base + 3072, C.vA1, C.sa, so + 16 * 4096);
}

__global__ __launch_bounds__(512, 2) void fused_kernel(const char* __restrict__ x, const char* __restrict__ wqkv, const char* __restrict__ wphi,
                                                        const char* __restrict__ wo, const char* __restrict__ win, const char* __restrict__ wsp,
                                                        const char* __restrict__ bsp, const char* __restrict__ wout, char* __restrict__ out, char* ws) {
  __shared__ __attribute__((aligned(1024))) char smem[163840];
  Ctx C;
  const int tid = threadIdx.x, lane = tid & 63;
  const int w = __builtin_amdgcn_readfirstlane(tid >> 6);
  const int rw = w & 3, cw = w >> 2, i16 = lane & 15, g = lane >> 4;
  const int b = blockIdx.x, xcd = b & 7, r = b >> 3;
  const int Pl = r >> 3, t = (r >> 2) & 1, cl = r & 3;
  const int P = (xcd >> 1) * 4 + Pl, c = (xcd & 1) * 4 + cl;
  const int p = 2 * P + (cl & 1);
  const int c2 = (xcd & 1) * 4 + (t ? 1 + (cl >> 1) : 3 * (cl >> 1));
  C.lane = lane; C.w = w; C.rw = rw; C.cw = cw; C.i16 = i16; C.g = g; C.P = P; C.c = c; C.p = p; C.c2 = c2;
  C.rot = (2 * Pl + 2 * cl + t) & 7;
  asm volatile("" ::"v"((u32)(size_t)(LDSP char*)smem));
  STAMP(0);

  C.sx = __builtin_amdgcn_make_buffer_rsrc((void*)(x + (size_t)P * 131072), 0, 131072, 0x00020000);
  C.sw = __builtin_amdgcn_make_buffer_rsrc((void*)(t ? wqkv : win), 0, t ? 512 * 3072 : 512 * 2752, 0x00020000);
  C.sa = __builtin_amdgcn_make_buffer_rsrc((void*)(wsp + (size_t)p * 262144), 0, 262144, 0x00020000);
  C.sws = __builtin_amdgcn_make_buffer_rsrc((void*)ws, 0, WS_END, 0x00020000);
  const u32 wstride = t ? 3072 : 2752;
  C.wstride64 = wstride * 64;
  C.vx0 = (16 * w + (lane >> 3)) * 1024 + (((lane & 7) ^ g) * 16);
  C.vx1 = (16 * w + 8 + (lane >> 3)) * 1024 + (((lane & 7) ^ (4 + g)) * 16);
  const int wq = (lane >> 5) * 4 + ((lane & 3) ^ ((w & 1) << 1));
  const u32 wrow = (8 * w + ((lane >> 2) & 7)) * wstride;
  const int z2s = grp_start(Z2S, c), z2n = grp_start(Z2S, c + 1) - z2s;
  const int z1s = grp_start(Z1S, c), z1n = grp_start(Z1S, c + 1) - z1s;
  u32 vt0, vt1;
  if (t == 0) {
    const int q5 = min(wq, 5);
    C.vw0 = wrow + 1376 + 32 * z2s + 16 * q5;
    C.vw1 = wrow + 1376 + 32 * z2s + 16 * min(6 + q5, 2 * z2n - 1);
    vt0 = wrow + 32 * z1s + 16 * q5;
    vt1 = wrow + 32 * z1s + 16 * min(6 + q5, 2 * z1n - 1);
  } else {
    C.vw0 = wrow + (wq < 4 ? 1024 : 2048 - 64) + 128 * c + 16 * wq;
    C.vw1 = C.vw0 + 64;
    vt0 = wrow + 128 * c + 16 * wq;
    vt1 = vt0;
  }
  issue_S(C, 0); issue_S(C, 1); issue_S(C, 2); issue_S(C, 3);
  C.vA0 = (lane >> 3) * 4096 + (((lane & 7) ^ g) * 16);
  C.vA1 = (8 + (lane >> 3)) * 4096 + (((lane & 7) ^ (4 + g)) * 16);
  {
    u64 ep;
    asm volatile("s_load_dwordx2 %0, %1, 0x0\n\ts_waitcnt lgkmcnt(0)" : "=s"(ep) : "s"(ws) : "memory");
    ep += 1;
    C.tlo = (u32)ep;
    C.thi = (u32)(ep >> 32);
  }
  const u32 tlo = C.tlo, thi = C.thi;
  u32 ONES;
  asm volatile("s_mov_b32 %0, 0x3f803f80" : "=s"(ONES));
  const rsrc_t sws = C.sws;

  const u32 xrd = (32 * rw + i16) * 128 + ((g ^ (i16 >> 1)) * 16);
  const u32 trb = g * 2048 + ((lane >> 2) & 3) * 64 + ((((g & 1) << 1) | ((lane >> 1) & 1)) * 16) + (lane & 1) * 8;
  const u32 bnc = L_BNC + w * 512;

  u32x4 af[4][8];
#define XF(ks, rt) af[(rt) + 2 * ((ks) >> 3)][(ks) & 7]

  if (t == 0) {
    f32x4 acc[2][3];
    float sx[2] = {0, 0}, sq[2] = {0, 0}, cs[4] = {0, 0, 0, 0};
#pragma unroll
    for (int a = 0; a < 2; ++a)
#pragma unroll
      for (int j = 0; j < 3; ++j) acc[a][j] = (f32x4){0, 0, 0, 0};
    const u32 tr0 = trb + cw * 1024, tr1 = tr0 ^ 32;
#pragma unroll
    for (int n = 0; n < 8; ++n) {
      if (n == 0) WAITV(12); else WAITV(8);
      BARRIER();
      if (n >= 1 && n <= 4) issue_S(C, n + 3);
      if (n >= 5) issue_T<true>(C, n - 5, vt0, vt1);
      const u32 slot = (n & 3) * 32768;
#pragma unroll
      for (int kk = 0; kk < 2; ++kk) {
        const int ks = 2 * n + kk;
#pragma unroll
        for (int rt = 0; rt < 2; ++rt) XF(ks, rt) = lds16(slot + rt * 2048 + (xrd ^ (kk * 64)));
#pragma unroll
        for (int j = 0; j < 3; ++j) {
          const u32 a = slot + 16384 + kk * 8192 + (j >> 1) * 512 + ((j & 1) ? tr1 : tr0);
          u32x2 lo = ldstr(a), hi = ldstr(a + 256);
          u32x4 wf = {lo[0], lo[1], hi[0], hi[1]};
          acc[0][j] = mfma32(XF(ks, 0), wf, acc[0][j]);
          acc[1][j] = mfma32(XF(ks, 1), wf, acc[1][j]);
#pragma unroll
          for (int e = 0; e < 4; ++e) cs[j] = dot2(wf[e], ONES, cs[j]);
        }
#pragma unroll
        for (int rt = 0; rt < 2; ++rt)
#pragma unroll
          for (int e = 0; e < 4; ++e) {
            sx[rt] = dot2(XF(ks, rt)[e], ONES, sx[rt]);
            sq[rt] = dot2(XF(ks, rt)[e], XF(ks, rt)[e], sq[rt]);
          }
      }
    }
    BARRIER();
    issue_T<true>(C, 3, vt0, vt1);
    STAMP(1);
    float xr[2], xmr[2];
    red2g<false>(sx[0], sq[0]); red2g<false>(sx[1], sq[1]);
    red2g<false>(cs[0], cs[1]); red2g<false>(cs[2], cs[3]);
    ln_fin(sx[0], sq[0], 1.0f / 512, xr[0], xmr[0]);
    ln_fin(sx[1], sq[1], 1.0f / 512, xr[1], xmr[1]);
    sts4f(bnc + i16 * 4, xr[0]); sts4f(bnc + 64 + i16 * 4, xr[1]);
    sts4f(bnc + 128 + i16 * 4, xmr[0]); sts4f(bnc + 192 + i16 * 4, xmr[1]);
    u32x4 zp[3];
    float ps[8], pq[8];
    const float v2 = (3 * cw + 2 < z2n) ? 1.0f : 0.0f;
#pragma unroll
    for (int rt = 0; rt < 2; ++rt) {
      const f32x4 rr = lds16f(bnc + rt * 64 + g * 16), mm = lds16f(bnc + 128 + rt * 64 + g * 16);
      f32x2 s0, s1, q0, q1;
#pragma unroll
      for (int j = 0; j < 3; ++j) {
        f32x2 z0 = gelu2(fma2(lo2(acc[rt][j]), lo2(rr), lo2(mm) * bc2(-cs[j])));
        f32x2 z1 = gelu2(fma2(hi2(acc[rt][j]), hi2(rr), hi2(mm) * bc2(-cs[j])));
        zp[j][2 * rt] = pk2v(z0); zp[j][2 * rt + 1] = pk2v(z1);
        if (j == 2) { z0 *= bc2(v2); z1 *= bc2(v2); }
        if (j == 0) { s0 = z0; s1 = z1; q0 = z0 * z0; q1 = z1 * z1; }
        else { s0 += z0; s1 += z1; q0 = fma2(z0, z0, q0); q1 = fma2(z1, z1, q1); }
      }
      ps[4 * rt] = s0[0]; ps[4 * rt + 1] = s0[1]; ps[4 * rt + 2] = s1[0]; ps[4 * rt + 3] = s1[1];
      pq[4 * rt] = q0[0]; pq[4 * rt + 1] = q0[1]; pq[4 * rt + 2] = q1[0]; pq[4 * rt + 3] = q1[1];
      SCHED();
    }
    PIN(zp[0]); PIN(zp[1]); PIN(zp[2]);
    {
      const bool b3 = lane & 8, b2 = lane & 4, b1 = lane & 2;
#pragma unroll
      for (int e = 0; e < 4; ++e) {
        ps[e] = (b3 ? ps[e + 4] : ps[e]) + dpp<0x128>(b3 ? ps[e] : ps[e + 4]);
        pq[e] = (b3 ? pq[e + 4] : pq[e]) + dpp<0x128>(b3 ? pq[e] : pq[e + 4]);
      }
#pragma unroll
      for (int e = 0; e < 2; ++e) {
        ps[e] = (b2 ? ps[e + 2] : ps[e]) + dpp<0x141>(b2 ? ps[e] : ps[e + 2]);
        pq[e] = (b2 ? pq[e + 2] : pq[e]) + dpp<0x141>(b2 ? pq[e] : pq[e + 2]);
      }
      ps[0] = (b1 ? ps[1] : ps[0]) + dpp<0x4E>(b1 ? ps[0] : ps[1]);
      pq[0] = (b1 ? pq[1] : pq[0]) + dpp<0x4E>(b1 ? pq[0] : pq[1]);
      ps[0] += dpp<0xB1>(ps[0]);
      pq[0] += dpp<0xB1>(pq[0]);
      const int row = 32 * rw + 16 * (i16 >> 3) + 4 * g + ((i16 >> 1) & 3);
      u32x4 gr = {__float_as_uint(ps[0]), tlo, __float_as_uint(pq[0]), tlo};
      if ((lane & 1) == 0) st16_sc1(gr, row * 16, sws, WS_SG + ((P * 8 + c) * 2 + cw) * 2048);
    }
    STAMP(2);
#pragma unroll
    for (int a = 0; a < 2; ++a)
#pragma unroll
      for (int j = 0; j < 3; ++j) acc[a][j] = (f32x4){0, 0, 0, 0};
    cs[0] = cs[1] = cs[2] = cs[3] = 0;
#pragma unroll
    for (int n = 0; n < 4; ++n) {
      if (n == 0) WAITV(12); else if (n == 1) WAITV(8); else if (n == 2) WAITV(4); else WAITV(0);
      BARRIER();
      const u32 slot = n * 32768;
#pragma unroll
      for (int kq = 0; kq < 4; ++kq) {
        const int ks = 4 * n + kq;
#pragma unroll
        for (int j = 0; j < 3; ++j) {
          const u32 a = slot + kq * 8192 + (j >> 1) * 512 + ((j & 1) ? tr1 : tr0);
          u32x2 lo = ldstr(a), hi = ldstr(a + 256);
          u32x4 wf = {lo[0], lo[1], hi[0], hi[1]};
          acc[0][j] = mfma32(wf, XF(ks, 0), acc[0][j]);
          acc[1][j] = mfma32(wf, XF(ks, 1), acc[1][j]);
#pragma unroll
          for (int e = 0; e < 4; ++e) cs[j] = dot2(wf[e], ONES, cs[j]);
        }
      }
#pragma unroll
      for (int j = 0; j < 3; ++j) { PIN(cs[j]); PIN(acc[0][j]); PIN(acc[1][j]); }
    }
    STAMP(3);
    {
      const u32 vo = (2 * g) * 4096 + (32 * rw + 16 * cw + i16) * 16;
      const u32 so = WS_SG + P * 32768;
      u32x4 q0, q1, q2, q3;
#pragma nounroll
      for (int it = 0; it < SPIN_CAP; ++it) {
        asm volatile(
            "buffer_load_dwordx4 %0, %4, %5, %6 offen sc1\n\t"
            "buffer_load_dwordx4 %1, %4, %5, %6 offen offset:2048 sc1\n\t"
            "buffer_load_dwordx4 %2, %4, %5, %7 offen sc1\n\t"
            "buffer_load_dwordx4 %3, %4, %5, %7 offen offset:2048 sc1\n\t"
            "s_waitcnt vmcnt(0)"
            : "=&v"(q0), "=&v"(q1), "=&v"(q2), "=&v"(q3)
            : "v"(vo), "s"(sws), "s"(so), "s"(so + 4096)
            : "memory");
        bool ok = q0[1] == tlo && q0[3] == tlo && q1[1] == tlo && q1[3] == tlo && q2[1] == tlo && q2[3] == tlo && q3[1] == tlo && q3[3] == tlo;
        if (__all(ok)) break;
        __builtin_amdgcn_s_sleep(2);
      }
      float s = (__uint_as_float(q0[0]) + __uint_as_float(q1[0])) + (__uint_as_float(q2[0]) + __uint_as_float(q3[0]));
      float q = (__uint_as_float(q0[2]) + __uint_as_float(q1[2])) + (__uint_as_float(q2[2]) + __uint_as_float(q3[2]));
      red2g<false>(s, q);
      float r2, mr2;
      ln_fin(s, q, 1.0f / 688, r2, mr2);
      sts4f(L_ZST + (32 * rw + 16 * cw + i16) * 4, r2);
      sts4f(L_ZST + 512 + (32 * rw + 16 * cw + i16) * 4, mr2);
    }
    BARRIER();
    STAMP(4);
#pragma unroll
    for (int rt = 0; rt < 2; ++rt) {
      const f32x4 rr = lds16f(L_ZST + (32 * rw + 16 * rt + 4 * g) * 4), mm = lds16f(L_ZST + 512 + (32 * rw + 16 * rt + 4 * g) * 4);
#pragma unroll
      for (int j = 0; j < 3; ++j) {
        const u32 u0 = zp[j][2 * rt], u1 = zp[j][2 * rt + 1];
        zp[j][2 * rt] = pk2(blo(u0) * rr[0] - mm[0], bhi(u0) * rr[1] - mm[1]);
        zp[j][2 * rt + 1] = pk2(blo(u1) * rr[2] - mm[2], bhi(u1) * rr[3] - mm[3]);
      }
    }
    PIN(zp[0]); PIN(zp[1]); PIN(zp[2]);
#pragma unroll
    for (int j = 0; j < 3; ++j)
      if (3 * cw + j < z2n) st16_sc1(zp[j], lane * 16, sws, WS_ZB + ((z2s + 3 * cw + j) * 64 + 4 * P + rw) * 1024);
    red2g<false>(cs[0], cs[1]); red2g<false>(cs[2], cs[3]);
#pragma unroll
    for (int j = 0; j < 3; ++j) sts4f(bnc + 256 + j * 64 + i16 * 4, cs[j]);
    u32x2 z1p[2][3];
#pragma unroll
    for (int j = 0; j < 3; ++j) {
      const f32x4 cc = lds16f(bnc + 256 + j * 64 + g * 16);
#pragma unroll
      for (int rt = 0; rt < 2; ++rt) {
        const f32x2 z0 = gelu2(fma2(lo2(acc[rt][j]), bc2(xr[rt]), lo2(cc) * bc2(-xmr[rt])));
        const f32x2 z1 = gelu2(fma2(hi2(acc[rt][j]), bc2(xr[rt]), hi2(cc) * bc2(-xmr[rt])));
        z1p[rt][j] = (u32x2){pk2v(z0), pk2v(z1)};
        PIN(z1p[rt][j]);
      }
      if (j == 0) {
        WAITV(0);
        set_flag(WS_F2 + ((c * 16 + P) * 8 + w) * 8, sws, tlo, thi, lane);
        STAMP(5);
      }
    }
#pragma unroll
    for (int j = 0; j < 3; ++j)
#pragma unroll
      for (int rt = 0; rt < 2; ++rt)
        if (3 * cw + j < z1n) st8_sc1(z1p[rt][j], lane * 8, sws, WS_Z1 + ((8 * P + 2 * rw + rt) * 43 + z1s + 3 * cw + j) * 512);
    WAITV(0);
    set_flag(WS_F3 + (((P * 4 + rw) * 8 + c) * 2 + cw) * 8, sws, tlo, thi, lane);
    issue_A(C, 0); issue_A(C, 1); issue_A(C, 2); issue_A(C, 3);
    STAMP(6);
  } else {
    if (w < 2) {
      rsrc_t sp = __builtin_amdgcn_make_buffer_rsrc((void*)wphi, 0, 16384, 0x00020000);
      dma16(L_WPHI + w * 1024, lane * 16, sp, c * 2048 + w * 1024);
    }
    f32x4 acc[2][4];
    float sx[2] = {0, 0}, sq[2] = {0, 0}, cs[4] = {0, 0, 0, 0};
#pragma unroll
    for (int a = 0; a < 2; ++a)
#pragma unroll
      for (int j = 0; j < 4; ++j) acc[a][j] = (f32x4){0, 0, 0, 0};
    const u32 tr0 = trb + cw * 1024, tr1 = tr0 ^ 32;
#pragma unroll
    for (int n = 0; n < 8; ++n) {
      if (n == 0) WAITV(12); else if (n <= 5) WAITV(8); else if (n == 6) WAITV(6); else WAITV(4);
      BARRIER();
      if (n >= 1 && n <= 4) issue_S(C, n + 3);
      if (n >= 5) issue_T<false>(C, n - 5, vt0, vt1);
      const u32 slot = (n & 3) * 32768;
#pragma unroll
      for (int kk = 0; kk < 2; ++kk) {
        const int ks = 2 * n + kk;
#pragma unroll
        for (int rt = 0; rt < 2; ++rt) XF(ks, rt) = lds16(slot + rt * 2048 + (xrd ^ (kk * 64)));
#pragma unroll
        for (int j = 0; j < 4; ++j) {
          const u32 a = slot + 16384 + kk * 8192 + (j >> 1) * 512 + ((j & 1) ? tr1 : tr0);
          u32x2 lo = ldstr(a), hi = ldstr(a + 256);
          u32x4 wf = {lo[0], lo[1], hi[0], hi[1]};
          if (j < 2) {
            acc[0][j] = mfma32(wf, XF(ks, 0), acc[0][j]);
            acc[1][j] = mfma32(wf, XF(ks, 1), acc[1][j]);
          } else {
            acc[0][j] = mfma32(XF(ks, 0), wf, acc[0][j]);
            acc[1][j] = mfma32(XF(ks, 1), wf, acc[1][j]);
          }
#pragma unroll
          for (int e = 0; e < 4; ++e) cs[j] = dot2(wf[e], ONES, cs[j]);
        }
#pragma unroll
        for (int rt = 0; rt < 2; ++rt)
#pragma unroll
          for (int e = 0; e < 4; ++e) {
            sx[rt] = dot2(XF(ks, rt)[e], ONES, sx[rt]);
            sq[rt] = dot2(XF(ks, rt)[e], XF(ks, rt)[e], sq[rt]);
          }
      }
    }
    BARRIER();
    issue_T<false>(C, 3, vt0, vt1);
    STAMP(1);
    float xr[2], xmr[2];
    red2g<false>(sx[0], sq[0]); red2g<false>(sx[1], sq[1]);
    red2g<false>(cs[0], cs[1]); red2g<false>(cs[2], cs[3]);
    ln_fin(sx[0], sq[0], 1.0f / 512, xr[0], xmr[0]);
    ln_fin(sx[1], sq[1], 1.0f / 512, xr[1], xmr[1]);
    sts4f(bnc + i16 * 4, xr[0]); sts4f(bnc + 64 + i16 * 4, xr[1]);
    sts4f(bnc + 128 + i16 * 4, xmr[0]); sts4f(bnc + 192 + i16 * 4, xmr[1]);
    sts4f(bnc + 256 + i16 * 4, cs[0]); sts4f(bnc + 320 + i16 * 4, cs[1]);
    const u32 wpa = L_WPHI + cw * 1024 + (4 * g + ((lane >> 2) & 3)) * 32 + (lane & 3) * 8;
    const u32x2 wp0 = ldstr(wpa), wp1 = ldstr(wpa + 512);
    u32x2 ident;
    {
      const int dlt = i16 - 4 * g;
      ident[0] = dlt == 0 ? 0x00003F80u : (dlt == 1 ? 0x3F800000u : 0u);
      ident[1] = dlt == 2 ? 0x00003F80u : (dlt == 3 ? 0x3F800000u : 0u);
    }
    const f32x4 zero4 = {0, 0, 0, 0};
    const u32x4 ones4 = {ONES, ONES, ONES, ONES};
    f32x4 kvp[2][2], kzp[2][2];
#pragma unroll
    for (int h = 0; h < 2; ++h) {
      const f32x4 cc = lds16f(bnc + 256 + h * 64 + g * 16);
      u32x4 php, phn, vf;
#pragma unroll
      for (int rt = 0; rt < 2; ++rt) {
        u32x2 kp = {pk2(acc[rt][h][0] * xr[rt] - xmr[rt] * cc[0], acc[rt][h][1] * xr[rt] - xmr[rt] * cc[1]),
                    pk2(acc[rt][h][2] * xr[rt] - xmr[rt] * cc[2], acc[rt][h][3] * xr[rt] - xmr[rt] * cc[3])};
        const u32x4 ph = hedgehog(mfma16(h ? wp1 : wp0, kp, zero4));
        const f32x4 tp = mfma16((u32x2){ph[0], ph[1]}, ident, zero4);
        const f32x4 tn = mfma16((u32x2){ph[2], ph[3]}, ident, zero4);
        php[2 * rt] = pk2(tp[0], tp[1]); php[2 * rt + 1] = pk2(tp[2], tp[3]);
        phn[2 * rt] = pk2(tn[0], tn[1]); phn[2 * rt + 1] = pk2(tn[2], tn[3]);
        const f32x4 rr = lds16f(bnc + rt * 64 + g * 16), mm = lds16f(bnc + 128 + rt * 64 + g * 16);
        const f32x4 av = acc[rt][2 + h];
        vf[2 * rt] = pk2(av[0] * rr[0] - mm[0] * cs[2 + h], av[1] * rr[1] - mm[1] * cs[2 + h]);
        vf[2 * rt + 1] = pk2(av[2] * rr[2] - mm[2] * cs[2 + h], av[3] * rr[3] - mm[3] * cs[2 + h]);
      }
      kvp[h][0] = mfma32(php, vf, zero4);
      kvp[h][1] = mfma32(phn, vf, zero4);
      kzp[h][0] = mfma32(php, ones4, zero4);
      kzp[h][1] = mfma32(phn, ones4, zero4);
    }
    if (rw != 0) {
#pragma unroll
      for (int h = 0; h < 2; ++h)
#pragma unroll
        for (int s = 0; s < 2; ++s) {
          sts16f(L_KVS + (((rw - 1) * 2 + cw) * 2 + h) * 2048 + s * 1024 + lane * 16, kvp[h][s]);
          if (i16 == 0) sts16f(L_KZS + (((rw - 1) * 2 + cw) * 2 + h) * 128 + g * 32 + s * 16, kzp[h][s]);
        }
    }
    STAMP(2);
    f32x4 aq[2][2];
    aq[0][0] = aq[0][1] = aq[1][0] = aq[1][1] = zero4;
    cs[0] = cs[1] = 0;
    const u32 tq0 = trb + cw * 512, tq1 = tq0 ^ 32;
#pragma unroll
    for (int n = 0; n < 4; ++n) {
      if (n == 0) WAITV(6); else if (n == 1) WAITV(4); else if (n == 2) WAITV(6); else WAITV(8);
      BARRIER();
      if (n == 0 && rw == 0) {
#pragma unroll
        for (int h = 0; h < 2; ++h)
#pragma unroll
          for (int s = 0; s < 2; ++s) {
#pragma unroll
            for (int o = 0; o < 3; ++o) {
              kvp[h][s] += lds16f(L_KVS + ((o * 2 + cw) * 2 + h) * 2048 + s * 1024 + lane * 16);
              kzp[h][s] += lds16f(L_KZS + ((o * 2 + cw) * 2 + h) * 128 + g * 32 + s * 16);
            }
            const u32 so = WS_KP + (c * 16 + P) * 8704;
            st16_sc1(__builtin_bit_cast(u32x4, kvp[h][s]), lane * 16, sws, so + (2 * cw + h) * 2048 + s * 1024);
            if (i16 == 0) st16_sc1(__builtin_bit_cast(u32x4, kzp[h][s]), g * 32 + s * 16, sws, so + 8192 + (2 * cw + h) * 128);
          }
        WAITV(0);
        set_flag(WS_F4 + ((c * 2 + cw) * 16 + P) * 8, sws, tlo, thi, lane);
      }
      if (n >= 1) issue_A(C, n - 1);
      const u32 slot = n * 32768;
#pragma unroll
      for (int kq = 0; kq < 4; ++kq) {
        const int ks = 4 * n + kq;
#pragma unroll
        for (int j = 0; j < 2; ++j) {
          const u32 a = slot + kq * 8192 + (j ? tq1 : tq0);
          u32x2 lo = ldstr(a), hi = ldstr(a + 256);
          u32x4 wf = {lo[0], lo[1], hi[0], hi[1]};
          aq[0][j] = mfma32(wf, XF(ks, 0), aq[0][j]);
          aq[1][j] = mfma32(wf, XF(ks, 1), aq[1][j]);
#pragma unroll
          for (int e = 0; e < 4; ++e) cs[j] = dot2(wf[e], ONES, cs[j]);
        }
      }
#pragma unroll
      for (int j = 0; j < 2; ++j) { PIN(cs[j]); PIN(aq[0][j]); PIN(aq[1][j]); }
    }
    BARRIER();
    issue_A(C, 3);
    STAMP(3);
    red2g<false>(cs[0], cs[1]);
    sts4f(bnc + 256 + i16 * 4, cs[0]); sts4f(bnc + 320 + i16 * 4, cs[1]);
    u32x4 phq[2][2];
#pragma unroll
    for (int h = 0; h < 2; ++h) {
      const f32x4 cc = lds16f(bnc + 256 + h * 64 + g * 16);
#pragma unroll
      for (int rt = 0; rt < 2; ++rt) {
        u32x2 qp = {pk2(aq[rt][h][0] * xr[rt] - xmr[rt] * cc[0], aq[rt][h][1] * xr[rt] - xmr[rt] * cc[1]),
                    pk2(aq[rt][h][2] * xr[rt] - xmr[rt] * cc[2], aq[rt][h][3] * xr[rt] - xmr[rt] * cc[3])};
        phq[rt][h] = hedgehog(mfma16(h ? wp1 : wp0, qp, zero4));
        PIN(phq[rt][h]);
      }
    }
    STAMP(4);
    if (w == 7) {
      const int hd = P >> 2, gs = P & 3;
      poll_flags(i16 * 8, true, sws, WS_F4 + (c * 2 + (hd >> 1)) * 128, tlo, thi);
      const u32 vkv = (4 * g) * 8704 + hd * 2048 + (gs * 16 + i16) * 16;
      const u32 vkz = (4 * g) * 8704 + 8192 + hd * 128 + gs * 32;
      u32x4 a[4][2], z[4][2];
#pragma unroll
      for (int o = 0; o < 4; ++o) {
        const u32 so = WS_KP + c * 16 * 8704 + o * 8704;
        a[o][0] = ld16_sc1(vkv, sws, so); a[o][1] = ld16_sc1(vkv, sws, so + 1024);
        z[o][0] = ld16_sc1(vkz, sws, so); z[o][1] = ld16_sc1(vkz, sws, so + 16);
      }
      asm volatile("s_waitcnt vmcnt(0)"
                   : "+v"(a[0][0]), "+v"(a[0][1]), "+v"(a[1][0]), "+v"(a[1][1]), "+v"(a[2][0]), "+v"(a[2][1]), "+v"(a[3][0]), "+v"(a[3][1]),
                     "+v"(z[0][0]), "+v"(z[0][1]), "+v"(z[1][0]), "+v"(z[1][1]), "+v"(z[2][0]), "+v"(z[2][1]), "+v"(z[3][0]), "+v"(z[3][1])
                   :
                   : "memory");
      float sa[8], sz[8];
#pragma unroll
      for (int s = 0; s < 2; ++s)
#pragma unroll
        for (int e = 0; e < 4; ++e) {
          sa[4 * s + e] = (__uint_as_float(a[0][s][e]) + __uint_as_float(a[1][s][e])) + (__uint_as_float(a[2][s][e]) + __uint_as_float(a[3][s][e]));
          sz[4 * s + e] = (__uint_as_float(z[0][s][e]) + __uint_as_float(z[1][s][e])) + (__uint_as_float(z[2][s][e]) + __uint_as_float(z[3][s][e]));
          red2g<false>(sa[4 * s + e], sz[4 * s + e]);
        }
      u32x4 fa = {pk2(sa[0], sa[1]), pk2(sa[2], sa[3]), pk2(sa[4], sa[5]), pk2(sa[6], sa[7])};
      u32x4 fz = {pk2(sz[0], sz[1]), pk2(sz[2], sz[3]), pk2(sz[4], sz[5]), pk2(sz[6], sz[7])};
      if (g == 0) st16_sc1(fa, i16 * 16, sws, WS_KF + (4 * c + hd) * 1024 + gs * 256);
      if (lane == 0) st16_sc1(fz, 0, sws, WS_KZ + (4 * c + hd) * 64 + gs * 16);
      WAITV(0);
      set_flag(WS_F5 + (c * 16 + P) * 8, sws, tlo, thi, lane);
    }
    STAMP(5);
    {
      poll_flags((lane & 7) * 8, true, sws, WS_F5 + (c * 16 + 8 * cw) * 8, tlo, thi);
      u32x4 kf0 = ld16_sc1(lane * 16, sws, WS_KF + (4 * c + 2 * cw) * 1024);
      u32x4 kf1 = ld16_sc1(lane * 16, sws, WS_KF + (4 * c + 2 * cw + 1) * 1024);
      u32x4 kz0 = ld16_sc1(g * 16, sws, WS_KZ + (4 * c + 2 * cw) * 64);
      u32x4 kz1 = ld16_sc1(g * 16, sws, WS_KZ + (4 * c + 2 * cw + 1) * 64);
      asm volatile("s_waitcnt vmcnt(0)" : "+v"(kf0), "+v"(kf1), "+v"(kz0), "+v"(kz1)::"memory");
#pragma unroll
      for (int rt = 0; rt < 2; ++rt) {
        u32x4 of;
#pragma unroll
        for (int h = 0; h < 2; ++h) {
          const f32x4 num = mfma32(h ? kf1 : kf0, phq[rt][h], zero4);
          const f32x4 den = mfma32(h ? kz1 : kz0, phq[rt][h], zero4);
          const float inv = __builtin_amdgcn_rcpf(rbf(rbf(den[0]) + 1e-6f));
          of[2 * h] = pk2(rbf(num[0]) * inv, rbf(num[1]) * inv);
          of[2 * h + 1] = pk2(rbf(num[2]) * inv, rbf(num[3]) * inv);
        }
        st16_sc1(of, lane * 16, sws, WS_OF + ((8 * P + 2 * rw + rt) * 16 + 2 * c + cw) * 1024);
      }
      WAITV(0);
      set_flag(WS_F6 + ((P * 8 + w) * 8 + c) * 8, sws, tlo, thi, lane);
    }
    STAMP(6);
  }

  {
    const u32 ard = w * 4096 + i16 * 128 + (((g >> 1) ^ (i16 >> 1)) * 16) + (g & 1) * 8;
#pragma unroll
    for (int a = 0; a < 8; ++a) {
      if (a <= 4) WAITV(12); else if (a == 5) WAITV(8); else if (a == 6) WAITV(4); else WAITV(0);
      const u32 base = (a & 3) * 32768;
#pragma unroll
      for (int rr = 0; rr < 2; ++rr)
#pragma unroll
        for (int kk = 0; kk < 2; ++kk) {
          const u32x2 lo = lds8(base + rr * 2048 + (ard ^ (64 * kk))), hi = lds8(base + rr * 2048 + (ard ^ (64 * kk) ^ 32));
          af[2 * (a & 1) + rr][2 * (a >> 1) + kk] = (u32x4){lo[0], lo[1], hi[0], hi[1]};
        }
      if (a < 4) { WAITL0(); issue_A(C, a + 4); }
    }
  }
  BARRIER();
  STAMP(7);
  const int rt3 = w & 3, hi3 = w >> 2;
  const u32 vw3 = ((lane >> 2) & 7) * 1024 + c2 * 128 + ((lane >> 5) * 4 + ((lane & 3) ^ (((lane >> 4) & 1) << 1))) * 16;
  {
    rsrc_t so_ = __builtin_amdgcn_make_buffer_rsrc((void*)wout, 0, 688 * 1024, 0x00020000);
#pragma unroll
    for (int e = 0; e < 11; ++e) dma16(L_WOUT + (w * 11 + e) * 1024, vw3, so_, (w * 11 + e) * 8192);
  }
  const int gs = grp_start(Z2S, c2), gn = grp_start(Z2S, c2 + 1) - gs;
  poll_flags(i16 * 8, true, sws, WS_F2 + (c2 * 16 + 2 * w) * 64, tlo, thi);
  STAMP(8);
  u32x4 bfr[2][8];
  const u32 zb0 = WS_ZB + (gs * 64 + 8 * w) * 1024;
#pragma unroll
  for (int u = 0; u < 2; ++u)
#pragma unroll
    for (int i = 0; i < 8; ++i) bfr[u][i] = ld16_sc1(lane * 16, sws, zb0 + u * 65536 + i * 1024);
  poll_flags(i16 * 8, true, sws, WS_F3 + ((p >> 1) * 4 + 2 * (p & 1) + (rt3 >> 1)) * 128, tlo, thi);
  u32x2 z1r[3];
  u32 bs;
  {
    rsrc_t sb = __builtin_amdgcn_make_buffer_rsrc((void*)bsp, 0, 4096, 0x00020000);
    bs = ld2(i16 * 2, sb, (64 * p + 16 * rt3) * 2);
#pragma unroll
    for (int u = 0; u < 3; ++u) z1r[u] = ld8_sc1(lane * 8, sws, WS_Z1 + ((4 * p + rt3) * 43 + min(gs + hi3 + 2 * u, 42)) * 512);
    asm volatile("s_waitcnt vmcnt(0)" : "+v"(bs), "+v"(z1r[0]), "+v"(z1r[1]), "+v"(z1r[2])::"memory");
  }
  const float bsf = blo(bs);
  STAMP(9);
#pragma unroll
  for (int j = 0; j < 6; ++j) {
    if (j < 5 || gn == 6) {
      const int u = j & 1;
      if (j >= 2) {
        if (j < 4) WAITV(8); else WAITV(0);
      }
#pragma unroll
      for (int i = 0; i < 8; ++i) PIN(bfr[u][i]);
      f32x4 acc[4];
#pragma unroll
      for (int rt = 0; rt < 4; ++rt) acc[rt] = (f32x4){0, 0, 0, 0};
#pragma unroll
      for (int i = 0; i < 8; ++i)
#pragma unroll
        for (int rt = 0; rt < 4; ++rt) acc[rt] = mfma32(bfr[u][i], af[rt][i], acc[rt]);
      if (j < 4) {
        const u32 so = (j + 2 < gn) ? zb0 + (j + 2) * 65536 : 0x7FFF0000u;
#pragma unroll
        for (int i = 0; i < 8; ++i) bfr[u][i] = ld16_sc1(lane * 16, sws, so + i * 1024);
      }
#pragma unroll
      for (int rt = 0; rt < 4; ++rt) sts16f(L_RED + u * 32768 + w * 4096 + rt * 1024 + lane * 16, acc[rt]);
      BARRIER();
      if (hi3 == u) {
        f32x4 s = lds16f(L_RED + u * 32768 + rt3 * 1024 + lane * 16);
#pragma unroll
        for (int o = 1; o < 8; ++o) s += lds16f(L_RED + u * 32768 + o * 4096 + rt3 * 1024 + lane * 16);
        const u32x2 zz = z1r[j >> 1];
        u32x2 hv = {pk2(blo(zz[0]) * rbf(rbf(s[0]) + bsf), bhi(zz[0]) * rbf(rbf(s[1]) + bsf)),
                    pk2(blo(zz[1]) * rbf(rbf(s[2]) + bsf), bhi(zz[1]) * rbf(rbf(s[3]) + bsf))};
        const int n = gs + j;
        st8_sc1(hv, lane * 16, sws, WS_HF + ((4 * p + rt3) * 22 + (n >> 1)) * 1024 + (n & 1) * 8);
        WAITV(0);
        set_flag(WS_F7 + ((p * 4 + rt3) * 64 + n) * 8, sws, tlo, thi, lane);
      }
    }
  }
  STAMP(10);
  BARRIER();
  {
    rsrc_t so_ = __builtin_amdgcn_make_buffer_rsrc((void*)wo, 0, 512 * 1024, 0x00020000);
#pragma unroll
    for (int e = 0; e < 8; ++e) dma16(L_WO + (w * 8 + e) * 1024, vw3, so_, (w * 8 + e) * 8192);
  }
  const u32 b3 = (g >> 1) * 1024 + (4 * (g & 1) + ((lane >> 2) & 3)) * 64 + ((((g & 1) << 1) | ((lane >> 1) & 1)) * 16) + (lane & 1) * 8;
  f32x4 a3[4];
#pragma unroll
  for (int ct = 0; ct < 4; ++ct) a3[ct] = (f32x4){0, 0, 0, 0};
  poll_flags((lane & 7) * 8, true, sws, WS_F6 + (((p >> 1) * 8 + hi3 * 4 + 2 * (p & 1) + (rt3 >> 1)) * 8) * 8, tlo, thi);
  u32x4 of[8];
#pragma unroll
  for (int e = 0; e < 8; ++e) of[e] = ld16_sc1(lane * 16, sws, WS_OF + ((4 * p + rt3) * 16 + 2 * e + hi3) * 1024);
  rsrc_t sxo = __builtin_amdgcn_make_buffer_rsrc((void*)(x + (size_t)p * 65536), 0, 65536, 0x00020000);
  u32x4 xres = ld16((tid >> 3) * 1024 + (tid & 7) * 16, sxo, c2 * 128);
  asm volatile("s_waitcnt vmcnt(0)"
               : "+v"(of[0]), "+v"(of[1]), "+v"(of[2]), "+v"(of[3]), "+v"(of[4]), "+v"(of[5]), "+v"(of[6]), "+v"(of[7]), "+v"(xres)::"memory");
  BARRIER();
  STAMP(11);
#pragma unroll
  for (int e = 0; e < 8; ++e) {
    const u32 a = L_WO + (2 * e) * 4096 + hi3 * 4096;
#pragma unroll
    for (int ct = 0; ct < 4; ++ct) {
      const u32 aa = a + (ct >> 1) * 512 + (b3 ^ ((ct & 1) * 32));
      u32x2 lo = ldstr(aa), hi = ldstr(aa + 2048);
      a3[ct] = mfma32((u32x4){lo[0], lo[1], hi[0], hi[1]}, of[e], a3[ct]);
    }
  }
  STAMP(12);
  poll_flags(lane * 8, lane < 43 && ((lane >> 1) & 1) == hi3, sws, WS_F7 + (p * 4 + rt3) * 512, tlo, thi);
  STAMP(13);
  u32x4 hf[11];
#pragma unroll
  for (int e = 0; e < 11; ++e) hf[e] = ld16_sc1(lane * 16, sws, WS_HF + ((4 * p + rt3) * 22 + 2 * e + hi3) * 1024);
#pragma unroll
  for (int e = 0; e < 11; ++e) {
    asm volatile("s_waitcnt vmcnt(%1)" : "+v"(hf[e]) : "n"(10 - e) : "memory");
    if (e == 10 && hi3 == 1) { hf[e][2] = 0; hf[e][3] = 0; }
    const u32 a = L_WOUT + (2 * e) * 4096 + hi3 * 4096;
#pragma unroll
    for (int ct = 0; ct < 4; ++ct) {
      const u32 aa = a + (ct >> 1) * 512 + (b3 ^ ((ct & 1) * 32));
      u32x2 lo = ldstr(aa), hi = ldstr(aa + 2048);
      a3[ct] = mfma32((u32x4){lo[0], lo[1], hi[0], hi[1]}, hf[e], a3[ct]);
    }
  }
  BARRIER();
  STAMP(14);
#pragma unroll
  for (int ct = 0; ct < 4; ++ct) sts16f(L_FIN + hi3 * 17408 + (16 * rt3 + i16) * 272 + (16 * ct + 4 * g) * 4, a3[ct]);
  BARRIER();
  {
    const u32 a = L_FIN + (tid >> 3) * 272 + (tid & 7) * 32;
    const f32x4 s0 = lds16f(a) + lds16f(a + 17408), s1 = lds16f(a + 16) + lds16f(a + 17408 + 16);
    u32x4 o = {pk2(blo(xres[0]) + s0[0], bhi(xres[0]) + s0[1]), pk2(blo(xres[1]) + s0[2], bhi(xres[1]) + s0[3]),
               pk2(blo(xres[2]) + s1[0], bhi(xres[2]) + s1[1]), pk2(blo(xres[3]) + s1[2], bhi(xres[3]) + s1[3])};
    *(u32x4*)(out + (size_t)(64 * p + (tid >> 3)) * 1024 + c2 * 128 + (tid & 7) * 16) = o;
  }
  if (b == 0 && tid == 0) *(u64*)(ws + WS_EP) = ((u64)thi << 32) | tlo;
  STAMP(15);
}

extern "C" void kernel_launch(void* const* d_in, const int* in_sizes, int n_in, void* d_out, int out_size, void* d_ws, size_t ws_size,
                              hipStream_t stream) {
  fused_kernel<<<256, 512, 0, stream>>>((const char*)d_in[0], (const char*)d_in[1], (const char*)d_in[2], (const char*)d_in[3],
                                        (const char*)d_in[5], (const char*)d_in[6], (const char*)d_in[7], (const char*)d_in[8], (char*)d_out,
                                        (char*)d_ws);
}
```

```cpp
#include <hip/hip_runtime.h>
#include <hip/hip_bf16.h>

typedef unsigned int u32;
typedef unsigned long long u64;
typedef short s16x4 __attribute__((ext_vector_type(4)));
typedef __bf16 bf16x2 __attribute__((ext_vector_type(2)));
typedef __bf16 bf16x8 __attribute__((ext_vector_type(8)));
typedef float f32x16 __attribute__((ext_vector_type(16)));
typedef float f32x4 __attribute__((ext_vector_type(4)));
typedef float f32x2 __attribute__((ext_vector_type(2)));
typedef u32 u32x4 __attribute__((ext_vector_type(4)));
typedef u32 u32x2 __attribute__((ext_vector_type(2)));
typedef __amdgpu_buffer_rsrc_t rsrc_t;
#define LDSP __attribute__((address_space(3)))
#define DI __device__ __forceinline__

#define STAMPS 0

constexpr u32 WS_EP = 0;
constexpr u32 WS_F2 = 4096;
constexpr u32 WS_F3 = WS_F2 + 8192;
constexpr u32 WS_F6 = WS_F3 + 8192;
constexpr u32 WS_F7 = WS_F6 + 8192;
constexpr u32 WS_XG = WS_F7 + 32768;
constexpr u32 WS_SG = WS_XG + 32768;
constexpr u32 WS_ZB = WS_SG + 524288;
constexpr u32 WS_Z1 = WS_ZB + 43 * 65536;
constexpr u32 KP_SZ = 17408;
constexpr u32 WS_KP = WS_Z1 + 64 * 43 * 1024;
constexpr u32 WS_KF = WS_KP + 128 * KP_SZ;
constexpr u32 WS_KZ = WS_KF + 65536;
constexpr u32 WS_OF = WS_KZ + 4096;
constexpr u32 WS_HF = WS_OF + 128 * 16384;
constexpr u32 WS_HL = WS_HF + 128 * 16 * 1024;
constexpr u32 WS_ST = WS_HL + 128 * 16 * 1024;
constexpr u32 WS_END = WS_ST + 256 * 8 * 32 * 8;

constexpr u32 LZ_W = 65536;
constexpr u32 LZ_U = 139264;
constexpr u32 LQ_M = 122880;
constexpr u32 LQ_PHI = 81920;
constexpr u32 LQ_V = 65536;
constexpr u32 L_TAB = 155648;
constexpr u32 L_XS = L_TAB;
constexpr u32 L_CS = L_TAB + 1024;
constexpr u32 L_ZST = L_TAB + 3072;
constexpr u32 L_WPHI = L_TAB + 3072;
constexpr u32 L_KFS = L_TAB + 1024;
constexpr u32 L_JUNK = L_TAB + 7168;
constexpr u32 L_RED = 0;
constexpr u32 L_WOUT = 65536;
constexpr u32 L_WO = 0;

constexpr u64 Z2S = 0x26211B16110B0500ull;
constexpr u64 Z1S = 0x26211C16110C0600ull;
DI int grp_start(u64 tab, int c) { return c >= 8 ? 43 : (int)((tab >> (8 * c)) & 0xFF); }

#define SCHED() __builtin_amdgcn_sched_barrier(0)
#define WAITV(n) do { asm volatile("s_waitcnt vmcnt(%0)" ::"n"(n) : "memory"); SCHED(); } while (0)
#define BARRIER() do { SCHED(); asm volatile("s_waitcnt lgkmcnt(0)\n\ts_barrier" ::: "memory"); SCHED(); } while (0)
#define BAR_IF(g_, v, n) do { SCHED(); asm volatile("s_cmp_lg_u32 %0, %1\n\ts_cbranch_scc1 1f\n\ts_waitcnt vmcnt(%2) lgkmcnt(0)\n\ts_barrier\n1:" ::"s"(g_), "n"(v), "n"(n) : "scc", "memory"); SCHED(); } while (0)
#define WAITV_IF(g_, v, n) do { asm volatile("s_cmp_lg_u32 %0, %1\n\ts_cbranch_scc1 1f\n\ts_waitcnt vmcnt(%2)\n1:" ::"s"(g_), "n"(v), "n"(n) : "scc", "memory"); SCHED(); } while (0)
#define WAITL0() asm volatile("s_waitcnt lgkmcnt(0)" ::: "memory")
#define PIN(x) asm volatile("" : "+v"(x))

DI void dma16(u32 lds, u32 voff, rsrc_t srd, u32 soff) {
  asm volatile("s_mov_b32 m0, %0\n\ts_nop 0\n\tbuffer_load_dwordx4 %1, %2, %3 offen lds" ::"s"(lds), "v"(voff), "s"(srd), "s"(soff) : "memory");
}
DI void touch(u32 lds, u32 voff, rsrc_t srd, u32 soff) {
  asm volatile("s_mov_b32 m0, %0\n\ts_nop 0\n\tbuffer_load_dword %1, %2, %3 offen lds" ::"s"(lds), "v"(voff), "s"(srd), "s"(soff) : "memory");
}
DI u32x4 ld16_sc1(u32 voff, rsrc_t srd, u32 soff) {
  u32x4 r;
  asm volatile("buffer_load_dwordx4 %0, %1, %2, %3 offen sc1" : "=v"(r) : "v"(voff), "s"(srd), "s"(soff) : "memory");
  return r;
}
template <int OFF> DI u32x4 ld16_sc1o(u32 voff, rsrc_t srd, u32 soff) {
  u32x4 r;
  asm volatile("buffer_load_dwordx4 %0, %1, %2, %3 offen offset:%4 sc1" : "=v"(r) : "v"(voff), "s"(srd), "s"(soff), "n"(OFF) : "memory");
  return r;
}
#define BPOL " sc1"
DI u32x4 ld16_b(u32 voff, rsrc_t srd, u32 soff) {
  u32x4 r;
  asm volatile("buffer_load_dwordx4 %0, %1, %2, %3 offen" BPOL : "=v"(r) : "v"(voff), "s"(srd), "s"(soff) : "memory");
  return r;
}
template <int OFF> DI u32x4 ld16_bo(u32 voff, rsrc_t srd, u32 soff) {
  u32x4 r;
  asm volatile("buffer_load_dwordx4 %0, %1, %2, %3 offen offset:%4" BPOL : "=v"(r) : "v"(voff), "s"(srd), "s"(soff), "n"(OFF) : "memory");
  return r;
}
DI u32x4 ld16(u32 voff, rsrc_t srd, u32 soff) {
  u32x4 r;
  asm volatile("buffer_load_dwordx4 %0, %1, %2, %3 offen" : "=v"(r) : "v"(voff), "s"(srd), "s"(soff) : "memory");
  return r;
}
DI u32x2 ld8_sc1(u32 voff, rsrc_t srd, u32 soff) {
  u32x2 r;
  asm volatile("buffer_load_dwordx2 %0, %1, %2, %3 offen sc1" : "=v"(r) : "v"(voff), "s"(srd), "s"(soff) : "memory");
  return r;
}
DI u32 ld2(u32 voff, rsrc_t srd, u32 soff) {
  u32 r;
  asm volatile("buffer_load_ushort %0, %1, %2, %3 offen" : "=v"(r) : "v"(voff), "s"(srd), "s"(soff) : "memory");
  return r;
}
DI void st16_sc1(u32x4 v, u32 voff, rsrc_t srd, u32 soff) {
  asm volatile("buffer_store_dwordx4 %0, %1, %2, %3 offen sc1\n\ts_nop 1" ::"v"(v), "v"(voff), "s"(srd), "s"(soff) : "memory");
}
DI void st8_sc1(u32x2 v, u32 voff, rsrc_t srd, u32 soff) {
  asm volatile("buffer_store_dwordx2 %0, %1, %2, %3 offen sc1" ::"v"(v), "v"(voff), "s"(srd), "s"(soff) : "memory");
}

DI u32x4 lds16(u32 a) { return *(const LDSP u32x4*)(size_t)a; }
DI u32x2 lds8(u32 a) { return *(const volatile LDSP u32x2*)(size_t)a; }
DI float lds4f(u32 a) { return *(const LDSP float*)(size_t)a; }
DI f32x4 lds16f(u32 a) { return *(const LDSP f32x4*)(size_t)a; }
DI void sts16f(u32 a, f32x4 v) { *(LDSP f32x4*)(size_t)a = v; }
DI void sts16(u32 a, u32x4 v) { *(LDSP u32x4*)(size_t)a = v; }
DI void sts8(u32 a, u32x2 v) { *(volatile LDSP u32x2*)(size_t)a = v; }
DI void sts4f(u32 a, float v) { *(LDSP float*)(size_t)a = v; }
DI u32x2 ldstr(u32 a) {
  return __builtin_bit_cast(u32x2, __builtin_amdgcn_ds_read_tr16_b64_v4i16((LDSP s16x4*)(size_t)a)); }
DI u32x4 cat(u32x2 lo, u32x2 hi) { return (u32x4){lo[0], lo[1], hi[0], hi[1]}; }
DI u32x4 ldfrag(u32 a, u32 d) { u32x2 lo = ldstr(a), hi = ldstr(a + d); return cat(lo, hi); }

DI f32x4 mfma32(u32x4 a, u32x4 b, f32x4 c) {
  return __builtin_amdgcn_mfma_f32_16x16x32_bf16(__builtin_bit_cast(bf16x8, a), __builtin_bit_cast(bf16x8, b), c, 0, 0, 0);
}
DI f32x16 mfmaL(u32x4 a, u32x4 b, f32x16 c) {
  return __builtin_amdgcn_mfma_f32_32x32x16_bf16(__builtin_bit_cast(bf16x8, a), __builtin_bit_cast(bf16x8, b), c, 0, 0, 0);
}
DI u32 pk2(float a, float b) { bf16x2 v = {(__bf16)a, (__bf16)b}; return __builtin_bit_cast(u32, v); }
DI float blo(u32 u) { return __uint_as_float(u << 16); }
DI float bhi(u32 u) { return __uint_as_float(u & 0xffff0000u); }
DI float rbf(float a) { return blo(pk2(a, a)); }
DI float dot2(u32 a, u32 b, float c) {
  return __builtin_amdgcn_fdot2_f32_bf16(__builtin_bit_cast(bf16x2, a), __builtin_bit_cast(bf16x2, b), c, false);
}
DI f32x2 fma2(f32x2 a, f32x2 b, f32x2 c) { return __builtin_elementwise_fma(a, b, c); }
DI f32x2 bc2(float a) { return (f32x2){a, a}; }
DI f32x2 lo2(f32x4 v) { return (f32x2){v[0], v[1]}; }
DI f32x2 hi2(f32x4 v) { return (f32x2){v[2], v[3]}; }
DI u32 pk2v(f32x2 v) { return pk2(v[0], v[1]); }
DI f32x2 gelu2(f32x2 x) {
  f32x2 u = fma2(x * x, bc2(-0.10294324f), bc2(-2.3022082f)) * x;
  f32x2 e = {__builtin_amdgcn_exp2f(u[0]), __builtin_amdgcn_exp2f(u[1])};
  e = e + bc2(1.0f);
  f32x2 r = {__builtin_amdgcn_rcpf(e[0]), __builtin_amdgcn_rcpf(e[1])};
  return x * r;
}
DI void red2g(float& a, float& b) {
  auto s = __builtin_amdgcn_permlane32_swap(__float_as_uint(a), __float_as_uint(b), false, false);
  float c = __uint_as_float(s[0]) + __uint_as_float(s[1]);
  auto s2 = __builtin_amdgcn_permlane16_swap(__float_as_uint(c), __float_as_uint(c), false, false);
  float d = __uint_as_float(s2[0]) + __uint_as_float(s2[1]);
  auto s3 = __builtin_amdgcn_permlane32_swap(__float_as_uint(d), __float_as_uint(d), false, false);
  a = __uint_as_float(s3[0]);
  b = __uint_as_float(s3[1]);
}
template <int CTRL> DI float dpp(float v) {
  return __uint_as_float(__builtin_amdgcn_update_dpp(0, __float_as_uint(v), CTRL, 0xF, 0xF, true));
}
DI void hedgehog16(const f32x16& p, float s, u32x4 (&o)[4]) {
  float mx = fmaxf(p[0], p[1]), mn = fminf(p[0], p[1]);
#pragma unroll
  for (int i = 2; i < 16; i += 2) { mx = fmaxf(fmaxf(mx, p[i]), p[i + 1]); mn = fminf(fminf(mn, p[i]), p[i + 1]); }
  const float c = s * 1.44269504f;
  const float a = -mx * c, b = mn * c;
  f32x2 ep[8], en[8];
#pragma unroll
  for (int i = 0; i < 8; ++i) {
    const f32x2 v = {p[2 * i], p[2 * i + 1]};
    const f32x2 u = fma2(v, bc2(c), bc2(a)), w = fma2(v, bc2(-c), bc2(b));
    ep[i] = (f32x2){__builtin_amdgcn_exp2f(u[0]), __builtin_amdgcn_exp2f(u[1])};
    en[i] = (f32x2){__builtin_amdgcn_exp2f(w[0]), __builtin_amdgcn_exp2f(w[1])};
  }
  const f32x2 sp = ((ep[0] + ep[1]) + (ep[2] + ep[3])) + ((ep[4] + ep[5]) + (ep[6] + ep[7]));
  const f32x2 sn = ((en[0] + en[1]) + (en[2] + en[3])) + ((en[4] + en[5]) + (en[6] + en[7]));
  const float ip = __builtin_amdgcn_rcpf(sp[0] + sp[1]), in = __builtin_amdgcn_rcpf(sn[0] + sn[1]);
#pragma unroll
  for (int i = 0; i < 8; ++i) {
    o[i >> 2][i & 3] = pk2v(ep[i] * bc2(ip));
    o[2 + (i >> 2)][i & 3] = pk2v(en[i] * bc2(in));
  }
}
DI void ln_fin(float s, float q, float invn, float& r, float& m) {
  m = s * invn;
  float v = fmaxf(q * invn - m * m, 0.0f);
  float sd = rbf(__builtin_amdgcn_sqrtf(rbf(rbf(v) + 1e-5f)));
  r = __builtin_amdgcn_rcpf(sd);
  m = rbf(m);
}

constexpr int SPIN_CAP = 1 << 17;
DI void poll_flags(u32 voff, bool act, rsrc_t srd, u32 soff, u32 tlo, u32 thi) {
#pragma nounroll
  for (int it = 0; it < SPIN_CAP; ++it) {
    u32x2 v;
    asm volatile("buffer_load_dwordx2 %0, %1, %2, %3 offen sc1\n\ts_waitcnt vmcnt(0)" : "=&v"(v) : "v"(voff), "s"(srd), "s"(soff) : "memory");
    bool ok = !act || (v[0] == tlo && v[1] == thi);
    if (__all(ok)) break;
    __builtin_amdgcn_s_sleep(1);
  }
}
DI bool tag_ok(u32x4 q, u32 t) { return q[1] == t && q[3] == t; }
DI void poll_gran(u32x4& q, u32 voff, rsrc_t srd, u32 soff, u32 tlo) {
#pragma nounroll
  for (int it = 0; it < SPIN_CAP; ++it) {
    if (__all(tag_ok(q, tlo))) break;
    __builtin_amdgcn_s_sleep(1);
    asm volatile("buffer_load_dwordx4 %0, %1, %2, %3 offen sc1\n\ts_waitcnt vmcnt(0)" : "=&v"(q) : "v"(voff), "s"(srd), "s"(soff) : "memory");
  }
}
DI u32 opq(u32 v) { asm volatile("" : "+v"(v)); return v; }

#define STAMP(i)
#define STAMPC(i)

#define GQ_N 3
#define PREFETCH 0
#define VM(x) do { x; ++vq; } while (0)
#define WAITQ(mark) WAITV(vq - (mark))

__global__ __launch_bounds__(512, 2) void fused_kernel(const char* __restrict__ x, const char* __restrict__ wqkv, const char* __restrict__ wphi,
                                                        const char* __restrict__ wo, const char* __restrict__ win, const char* __restrict__ wsp,
                                                        const char* __restrict__ bsp, const char* __restrict__ wout, char* __restrict__ out, char* ws) {
  __shared__ __attribute__((aligned(1024))) char smem[163840];
  const int tid = threadIdx.x, lane = tid & 63;
  const int w = __builtin_amdgcn_readfirstlane(tid >> 6);
  const int rw = w & 3, cw = w >> 2, i16 = lane & 15, g = lane >> 4;
  const int b = blockIdx.x, xcd = b & 7, r = b >> 3;
  const int Pl = r >> 3, t = (r >> 2) & 1, cl = r & 3;
  const int P = (xcd >> 1) * 4 + Pl, c = (xcd & 1) * 4 + cl;
  const int p = 2 * P + (cl & 1);
  const int c2 = (xcd & 1) * 4 + (t ? 1 + (cl >> 1) : 3 * (cl >> 1));
  const int rot = 2 * ((Pl + cl) & 3);
  asm volatile("" ::"v"((u32)(size_t)(LDSP char*)smem));
  STAMP(0);
  STAMPC(30);

  const rsrc_t sx = __builtin_amdgcn_make_buffer_rsrc((void*)(x + (size_t)P * 131072), 0, 131072, 0x00020000);
  const u32 l16 = opq(lane * 16);
  const u32 vx0 = (16 * w + (lane >> 3)) * 1024 + (((lane & 7) ^ g) * 16);
  const u32 vx1 = (vx0 + 8192) ^ 64;
#define DMA_X(dst, n, e) dma16((dst) + w * 2048 + (e) * 1024, (e) ? vx1 : vx0, sx, (((n) + rot) & 7) * 128)
  DMA_X(0, 0, 0);
  DMA_X(0, 0, 1);
  const rsrc_t sa = __builtin_amdgcn_make_buffer_rsrc((void*)(wsp + (size_t)p * 262144), 0, 262144, 0x00020000);
  const rsrc_t sws = __builtin_amdgcn_make_buffer_rsrc((void*)ws, 0, WS_END, 0x00020000);
  const u32 vA0 = (lane >> 3) * 4096 + (((lane & 7) ^ g) * 16);
  const u32 vA1 = (vA0 + 32768) ^ 64;
#define DMA_U(u, base, e) dma16((base) + w * 2048 + (e) * 1024, (e) ? vA1 : vA0, sa, w * 512 + (((u) >> 1) & 3) * 128 + (((u) >> 3) * 32 + ((u) & 1) * 16) * 4096)
  const f32x4 zero4 = {0, 0, 0, 0};
  u32x4 ones4 = {0x3F803F80u, 0x3F803F80u, 0x3F803F80u, 0x3F803F80u};
  u32 tlo, thi;
  u32x4 af[4][8];
#define XF(ks, rt) af[(rt) + 2 * ((ks) >> 3)][(ks) & 7]
  u32x4 bfr[2][8];
  const u32 ard = w * 2048 + i16 * 128 + (((g >> 1) ^ (i16 >> 1)) * 16) + (g & 1) * 8;
#define READ_U(u, base)                                                                                             \
  {                                                                                                                 \
    const u32 ab_ = opq(ard + (base));                                                                              \
    _Pragma("unroll") for (int kk = 0; kk < 2; ++kk)                                                               \
      XF(((u) & ~1) + kk, (u) & 1) = cat(lds8(ab_ ^ (64 * kk)), lds8(ab_ ^ (64 * kk + 32)));                        \
  }
#define LOAD_EPOCH()                                                                                                \
  {                                                                                                                 \
    if (PREFETCH) VM(touch(L_JUNK, l16 * 8, sa, ((t * 2 + (cl >> 1)) * 8 + w) * 8192));                             \
    u64 ep;                                   \
    asm volatile("s_load_dwordx2 %0, %1, 0x0\n\ts_waitcnt lgkmcnt(0)" : "=s"(ep) : "s"(ws) : "memory");              \
    ep += 1;                                                                                                        \
    tlo = (u32)ep;                                                                                                  \
    thi = (u32)(ep >> 32);                                                                                          \
    PIN(ones4);                                                                                                     \
  }
#define EARLY_STATS(mark)                                                                                           \
  {                                                                                                                 \
    WAITQ(mark); PIN(xrow);                                                                                         \
    float s_ = 0, q_ = 0;                                                                                           \
    _Pragma("unroll") for (int e = 0; e < 4; ++e) { s_ = dot2(xrow[e], ones4[0], s_); q_ = dot2(xrow[e], xrow[e], q_); } \
    s_ += dpp<0x128>(s_); q_ += dpp<0x128>(q_);                                                                     \
    s_ += dpp<0x124>(s_); q_ += dpp<0x124>(q_);                                                                     \
    s_ += dpp<0x4E>(s_); q_ += dpp<0x4E>(q_);                                                                       \
    s_ += dpp<0xB1>(s_); q_ += dpp<0xB1>(q_);                                                                       \
    red2g(s_, q_);                                                                                                  \
    float r_, m_;                                                                                                   \
    ln_fin(s_, q_, 1.0f / 512, r_, m_);                                                                             \
    VM(st16_sc1((u32x4){__float_as_uint(r_), tlo, __float_as_uint(m_), tlo}, l16 << 22, sws, WS_XG + (P * 128 + (t * 8 + c) * 8 + w) * 16)); \
  }
#define GATHER_XS()                                                                                                 \
  WAITQ(mxg); PIN(xg);                                                                                              \
  poll_gran(xg, i16 * 16, sws, WS_XG + (P * 128 + 16 * w) * 16, tlo);                                               \
  sts4f(L_XS + (16 * w + i16) * 4, __uint_as_float(xg[0]));                                                         \
  sts4f(L_XS + 512 + (16 * w + i16) * 4, __uint_as_float(xg[2]));
#define RELANE() const int lane = opq(l16) >> 4, i16 = lane & 15, g = lane >> 4, tid = w * 64 + lane; (void)tid; (void)g; (void)i16
#define SET_FLAG(off) st8_sc1((u32x2){tlo, thi}, l16 << 22, sws, (off))
  const int gs = grp_start(Z2S, c2), gn = grp_start(Z2S, c2 + 1) - gs;
  const int trot = (p & 7) >= 5 ? (p & 7) - 5 : (p & 7);
#define TILE(j, n_) (((j) + trot) >= (n_) ? (j) + trot - (n_) : (j) + trot)
  const u32 zb0 = WS_ZB + (gs * 64 + 8 * w) * 1024;
#define DMA_WOUT(e)                                                                                                 \
  {                                                                                                                 \
    const rsrc_t so_ = __builtin_amdgcn_make_buffer_rsrc((void*)wout, 0, 688 * 1024, 0x00020000);                   \
    const int ws_ = grp_start(Z2S, w), wn_ = grp_start(Z2S, w + 1) - ws_;                                           \
    if ((e) < 6) dma16(L_WOUT + ((e) < 4 ? 2 * w + ((e) >> 1) : 16 + w) * 2048 + ((e) & 1) * 1024, vwo, so_, ((ws_ + TILE((e) >> 1, wn_)) * 2 + ((e) & 1)) * 8192); \
    else if ((e) == 6) dma16(L_WOUT + 24 * 2048 + w * 1024, vwo, so_, ((((w >> 1) == 0 ? 5 : ((w >> 1) == 1 ? 11 : ((w >> 1) == 2 ? 27 : 64))) + TILE(3, 6)) * 2 + (w & 1)) * 8192); \
    else dma16(L_WOUT + (28 + 2 * w) * 2048 + ((e) - 7) * 1024, vwo, so_, ((ws_ + TILE(wn_ - 1 - (((e) - 7) >> 1), wn_)) * 2 + (((e) - 7) & 1)) * 8192); \
  }
#define VWO() const u32 vwo = ((lane >> 2) & 7) * 1024 + c2 * 128 + (lane >> 5) * 64 + (((lane & 3) ^ (((lane >> 4) & 1) << 1)) * 16)
#define LOAD_B(u, so_)                                                                                              \
  bfr[u][0] = ld16_bo<0>(l16, sws, so_); bfr[u][1] = ld16_bo<1024>(l16, sws, so_);                                  \
  bfr[u][2] = ld16_bo<2048>(l16, sws, so_); bfr[u][3] = ld16_bo<3072>(l16, sws, so_);                               \
  bfr[u][4] = ld16_bo<0>(l16, sws, (so_) + 4096); bfr[u][5] = ld16_bo<1024>(l16, sws, (so_) + 4096);                \
  bfr[u][6] = ld16_bo<2048>(l16, sws, (so_) + 4096); bfr[u][7] = ld16_bo<3072>(l16, sws, (so_) + 4096);

  if (t == 0) {
    int vq = 2;
    const rsrc_t sw = __builtin_amdgcn_make_buffer_rsrc((void*)win, 0, 512 * 2752, 0x00020000);
    const int z2s = grp_start(Z2S, c), z2n = grp_start(Z2S, c + 1) - z2s;
    const int z1s = grp_start(Z1S, c), z1n = grp_start(Z1S, c + 1) - z1s;
    u32 vw[3];
    int wch[3];
#pragma unroll
    for (int e = 0; e < 3; ++e) {
      const int sg = 2 * e + (lane >> 5), hi = sg >= 3;
      wch[e] = (sg - 3 * hi) * 4 + ((lane & 3) ^ (hi << 1));
      vw[e] = wch[e] < 2 * z2n ? (16 * w + 8 * hi + ((lane >> 2) & 7)) * 2752 + 1376 + 32 * z2s + 16 * wch[e] : 0x7FFF0000u;
    }
#define DMA_W(bk, e) dma16(LZ_W + ((bk) % 3) * 24576 + w * 3072 + (e) * 1024, vw[e], sw, ((2 * (bk) + rot) & 7) * (64 * 2752))
#define UBASE(u) ((u) % 5 == 0 ? LZ_U : ((u) % 5 - 1) * 16384)
#define ISS_X(n) do { VM(DMA_X(((n) & 3) * 16384, n, 0)); VM(DMA_X(((n) & 3) * 16384, n, 1)); mx[n] = vq; } while (0)
#define ISS_U(u) do { VM(DMA_U(u, UBASE(u), 0)); VM(DMA_U(u, UBASE(u), 1)); mu[u] = vq; } while (0)
    int mx[8], mw[8], mu[16];
    mx[0] = vq;
    VM(DMA_W(0, 0)); VM(DMA_W(0, 1)); VM(DMA_W(0, 2)); mw[0] = vq;
    u32x4 xrow;
    VM(xrow = ld16(l16, sx, (c * 8 + w) * 1024));
    const int mxr = vq;
    ISS_X(1);
    ISS_X(2);
    VM(DMA_W(1, 0)); VM(DMA_W(1, 1)); VM(DMA_W(1, 2)); mw[1] = vq;
    LOAD_EPOCH()
    const u32 xa0 = opq((32 * rw + i16) * 128 + ((g ^ (i16 >> 1)) * 16)), xa1 = xa0 ^ 64;
    u32 ta[3];
    int tl[3];
    {
      const u32 trb = LZ_W + g * 1536 + ((lane >> 2) & 3) * 64 + ((((g & 1) << 1) | ((lane >> 1) & 1)) * 16) + (lane & 1) * 8;
#pragma unroll
      for (int j = 0; j < 3; ++j) {
        tl[j] = 3 * cw + (j + rw) % 3;
        ta[j] = (trb + (tl[j] >> 1) * 512) ^ ((tl[j] & 1) * 32);
      }
    }
#define NEXT_BLOCK(bk) _Pragma("unroll") for (int j = 0; j < 3; ++j) ta[j] = opq(ta[j] + ((bk) == 0 ? 0 : ((bk) % 3 == 0 ? -49152 : 24576)))
    f32x4 acc[2][3], csa = zero4;
#pragma unroll
    for (int j = 0; j < 3; ++j) acc[0][j] = acc[1][j] = zero4;
    u32x4 wf[2][3];
    u32x4 xg;
    int mxg = 0;
    WAITQ(mw[0]);
    BARRIER();
    STAMP(16);
#pragma unroll
    for (int n = 0; n < 8; ++n) {
      if ((n & 1) == 0) { NEXT_BLOCK(n >> 1); }
#pragma unroll
      for (int kk = 0; kk < 2; ++kk) {
        XF(2 * n + kk, 0) = lds16((kk ? xa1 : xa0) + (n & 3) * 16384);
        XF(2 * n + kk, 1) = lds16((kk ? xa1 : xa0) + (n & 3) * 16384 + 2048);
#pragma unroll
        for (int j = 0; j < 3; ++j) wf[kk][j] = ldfrag(ta[j] + ((2 * n + kk) & 3) * 6144, 256);
        SCHED();
        if (kk == 0) {
          if (n <= 4) ISS_X(n + 3);
          if (n == 5) { ISS_U(0); VM(xg = ld16_sc1(i16 * 16, sws, WS_XG + (P * 128 + 16 * w) * 16)); mxg = vq; }
          if (n == 6) { VM(DMA_W(5, 0)); VM(DMA_W(5, 1)); VM(DMA_W(5, 2)); mw[5] = vq; }
        } else {
          if (n == 4) {
#pragma unroll
            for (int e = 0; e < 3; ++e)
              vw[e] = wch[e] < 2 * z1n ? (16 * w + 8 * (2 * e + (lane >> 5) >= 3) + ((lane >> 2) & 7)) * 2752 + 32 * z1s + 16 * wch[e] : 0x7FFF0000u;
          }
          if (n == 0 || n == 2 || n == 4) { VM(DMA_W(n / 2 + 2, 0)); VM(DMA_W(n / 2 + 2, 1)); }
          if (n == 1 || n == 3 || n == 5) { VM(DMA_W(n / 2 + 2, 2)); mw[n / 2 + 2] = vq; }
          if (n >= 5) ISS_U(n - 4);
        }
        SCHED();
      }
      if (n == 0) EARLY_STATS(mxr)
      if (n == 7) {
        csa = mfma32(ones4, wf[0][0], csa);
        csa = mfma32(ones4, wf[1][0], csa);
        GATHER_XS()
        sts4f(L_CS + tl[0] * 64 + i16 * 4, csa[0]);
      }
      const int mk = n == 7 ? mw[4] : ((n & 1) ? (mx[n + 1] > mw[(n + 1) >> 1] ? mx[n + 1] : mw[(n + 1) >> 1]) : mx[n + 1]);
      BAR_IF(cw, 1, vq - mk);
#pragma unroll
      for (int kk = 0; kk < 2; ++kk) {
#pragma unroll
        for (int m = 0; m < 6; ++m) acc[m & 1][m >> 1] = mfma32(XF(2 * n + kk, m & 1), wf[kk][m >> 1], acc[m & 1][m >> 1]);
        if (n < 7) csa = mfma32(ones4, wf[kk][0], csa);
      }
      BAR_IF(cw, 0, vq - mk);
      STAMP(17 + n);
    }
    STAMP(1);
    u32x4 zp[3];
    VM(DMA_W(6, 0)); VM(DMA_W(6, 1)); VM(DMA_W(6, 2)); mw[6] = vq;
    {
      f32x2 ps[4], pq[4];
      float cs[3];
#pragma unroll
      for (int j = 0; j < 3; ++j) cs[j] = -lds4f(L_CS + tl[j] * 64 + i16 * 4);
#pragma unroll
      for (int rt = 0; rt < 2; ++rt) {
        const f32x4 rr = lds16f(L_XS + (32 * rw + 16 * rt + 4 * g) * 4), mm = lds16f(L_XS + 512 + (32 * rw + 16 * rt + 4 * g) * 4);
#pragma unroll
        for (int j = 0; j < 3; ++j) {
          const f32x2 z0 = gelu2(fma2(lo2(mm), bc2(cs[j]), lo2(acc[rt][j])) * lo2(rr));
          const f32x2 z1 = gelu2(fma2(hi2(mm), bc2(cs[j]), hi2(acc[rt][j])) * hi2(rr));
          zp[j][2 * rt] = pk2v(z0); zp[j][2 * rt + 1] = pk2v(z1);
          if (j == 0) { ps[2 * rt] = z0; ps[2 * rt + 1] = z1; pq[2 * rt] = z0 * z0; pq[2 * rt + 1] = z1 * z1; }
          else { ps[2 * rt] += z0; ps[2 * rt + 1] += z1; pq[2 * rt] = fma2(z0, z0, pq[2 * rt]); pq[2 * rt + 1] = fma2(z1, z1, pq[2 * rt + 1]); }
        }
        if (rt == 0) ISS_U(4);
      }
      const bool b3 = lane & 8, b2 = lane & 4, b1 = lane & 2;
#pragma unroll
      for (int m = 0; m < 2; ++m)
#pragma unroll
        for (int e = 0; e < 2; ++e) {
          ps[m][e] = (b3 ? ps[m + 2][e] : ps[m][e]) + dpp<0x128>(b3 ? ps[m][e] : ps[m + 2][e]);
          pq[m][e] = (b3 ? pq[m + 2][e] : pq[m][e]) + dpp<0x128>(b3 ? pq[m][e] : pq[m + 2][e]);
        }
#pragma unroll
      for (int e = 0; e < 2; ++e) {
        ps[0][e] = (b2 ? ps[1][e] : ps[0][e]) + dpp<0x141>(b2 ? ps[0][e] : ps[1][e]);
        pq[0][e] = (b2 ? pq[1][e] : pq[0][e]) + dpp<0x141>(b2 ? pq[0][e] : pq[1][e]);
      }
      float s_ = (b1 ? ps[0][1] : ps[0][0]) + dpp<0x4E>(b1 ? ps[0][0] : ps[0][1]);
      float q_ = (b1 ? pq[0][1] : pq[0][0]) + dpp<0x4E>(b1 ? pq[0][0] : pq[0][1]);
      s_ += dpp<0xB1>(s_);
      q_ += dpp<0xB1>(q_);
      const int row = 32 * rw + 16 * (i16 >> 3) + 4 * g + ((i16 >> 1) & 3);
      VM(st16_sc1((u32x4){__float_as_uint(s_), tlo, __float_as_uint(q_), tlo}, row * 16 + ((lane & 1) << 30), sws, WS_SG + ((P * 8 + c) * 2 + cw) * 2048));
      PIN(zp[0]); PIN(zp[1]); PIN(zp[2]);
    }
    STAMP(2);
    {
    RELANE();
#pragma unroll
    for (int j = 0; j < 3; ++j) acc[0][j] = acc[1][j] = zero4;
    csa = zero4;
    u32x4 q0, q1, q2, q3;
    int mq = 0, mz = 0;
    const u32 gvo = (2 * g) * 4096 + (32 * rw + 16 * cw + i16) * 16;
    const u32 gso = WS_SG + P * 32768;
#pragma unroll
    for (int n = 0; n < 8; ++n) {
      if ((n & 1) == 0) { NEXT_BLOCK(4 + (n >> 1)); }
      if (n >= 1) { WAITQ(mu[2 * n - 1]); READ_U(2 * n - 2, UBASE(2 * n - 2)) READ_U(2 * n - 1, UBASE(2 * n - 1)) }
#pragma unroll
      for (int kk = 0; kk < 2; ++kk)
#pragma unroll
        for (int j = 0; j < 3; ++j) wf[kk][j] = ldfrag(ta[j] + ((2 * n + kk) & 3) * 6144, 256);
      SCHED();
      if (n == 2) { VM(DMA_W(7, 0)); VM(DMA_W(7, 1)); VM(DMA_W(7, 2)); mw[7] = vq; }
      if (n == GQ_N) {
        VM(q0 = ld16_sc1o<0>(gvo, sws, gso)); VM(q1 = ld16_sc1o<2048>(gvo, sws, gso));
        VM(q2 = ld16_sc1o<0>(gvo, sws, gso + 4096)); VM(q3 = ld16_sc1o<2048>(gvo, sws, gso + 4096));
        mq = vq;
      }
      if (n == GQ_N + 1) {
        WAITQ(mq); PIN(q0); PIN(q1); PIN(q2); PIN(q3);
#pragma nounroll
        for (int it = 0; it < SPIN_CAP; ++it) {
          if (__all(tag_ok(q0, tlo) && tag_ok(q1, tlo) && tag_ok(q2, tlo) && tag_ok(q3, tlo))) break;
          __builtin_amdgcn_s_sleep(1);
          asm volatile(
              "buffer_load_dwordx4 %0, %4, %5, %6 offen sc1\n\t"
              "buffer_load_dwordx4 %1, %4, %5, %6 offen offset:2048 sc1\n\t"
              "buffer_load_dwordx4 %2, %4, %5, %7 offen sc1\n\t"
              "buffer_load_dwordx4 %3, %4, %5, %7 offen offset:2048 sc1\n\t"
              "s_waitcnt vmcnt(0)"
              : "=&v"(q0), "=&v"(q1), "=&v"(q2), "=&v"(q3)
              : "v"(gvo), "s"(sws), "s"(gso), "s"(gso + 4096)
              : "memory");
        }
        float s = (__uint_as_float(q0[0]) + __uint_as_float(q1[0])) + (__uint_as_float(q2[0]) + __uint_as_float(q3[0]));
        float q = (__uint_as_float(q0[2]) + __uint_as_float(q1[2])) + (__uint_as_float(q2[2]) + __uint_as_float(q3[2]));
        red2g(s, q);
        float r2, m2;
        ln_fin(s, q, 1.0f / 688, r2, m2);
        sts4f(L_ZST + (32 * rw + 16 * cw + i16) * 4, r2);
        sts4f(L_ZST + 512 + (32 * rw + 16 * cw + i16) * 4, -m2 * r2);
        STAMP(3);
      }
      if (n == GQ_N + 2) {
#pragma unroll
        for (int rt = 0; rt < 2; ++rt) {
          const u32 ra = L_ZST + (32 * rw + 16 * rt + 4 * g) * 4;
          const f32x4 r4 = lds16f(ra), m4 = lds16f(ra + 512);
#pragma unroll
          for (int j = 0; j < 3; ++j) {
            const u32 u0 = zp[j][2 * rt], u1 = zp[j][2 * rt + 1];
            zp[j][2 * rt] = pk2v(fma2((f32x2){blo(u0), bhi(u0)}, lo2(r4), lo2(m4)));
            zp[j][2 * rt + 1] = pk2v(fma2((f32x2){blo(u1), bhi(u1)}, hi2(r4), hi2(m4)));
          }
        }
#pragma unroll
        for (int j = 0; j < 3; ++j)
          VM(st16_sc1(zp[j], l16, sws, (tl[j] < z2n) ? WS_ZB + ((z2s + tl[j]) * 64 + 4 * P + rw) * 1024 : 0x7FFF0000u));
        mz = vq;
        STAMP(4);
      }
      if (n >= 1 && n <= 6) { WAITL0(); ISS_U(2 * n + 3); if (n < 6) ISS_U(2 * n + 4); }
      if (n == 7) {
        csa = mfma32(ones4, wf[0][0], csa);
        csa = mfma32(ones4, wf[1][0], csa);
        sts4f(L_CS + 512 + tl[0] * 64 + i16 * 4, csa[0]);
      }
      const int nw = (n & 1) && n < 7 ? vq - mw[5 + (n >> 1)] : 63;
      BAR_IF(cw, 1, nw);
#pragma unroll
      for (int kk = 0; kk < 2; ++kk) {
#pragma unroll
        for (int m = 0; m < 6; ++m) acc[m & 1][m >> 1] = mfma32(wf[kk][m >> 1], XF(2 * n + kk, m & 1), acc[m & 1][m >> 1]);
        if (n < 7) csa = mfma32(ones4, wf[kk][0], csa);
      }
#pragma unroll
      for (int j = 0; j < 3; ++j) { PIN(acc[0][j]); PIN(acc[1][j]); }
      PIN(csa);
      BAR_IF(cw, 0, nw);
      if (n < 5) STAMP(25 + n);
    }
    WAITQ(mz);
    VM(SET_FLAG(WS_F2 + ((c * 16 + P) * 8 + w) * 8));
    STAMP(5);
    {
      float xr[2], xm[2];
#pragma unroll
      for (int rt = 0; rt < 2; ++rt) {
        xr[rt] = lds4f(L_XS + (32 * rw + 16 * rt + i16) * 4);
        xm[rt] = -lds4f(L_XS + 512 + (32 * rw + 16 * rt + i16) * 4);
      }
#pragma unroll
      for (int j = 0; j < 3; ++j) {
        const f32x4 cc = lds16f(L_CS + 512 + tl[j] * 64 + g * 16);
        u32x4 zz;
#pragma unroll
        for (int rt = 0; rt < 2; ++rt) {
          zz[2 * rt] = pk2v(fma2(lo2(cc), bc2(xm[rt]), lo2(acc[rt][j])) * bc2(xr[rt]));
          zz[2 * rt + 1] = pk2v(fma2(hi2(cc), bc2(xm[rt]), hi2(acc[rt][j])) * bc2(xr[rt]));
        }
        VM(st16_sc1(zz, l16, sws, (tl[j] < z1n) ? WS_Z1 + ((4 * P + rw) * 43 + z1s + tl[j]) * 1024 : 0x7FFF0000u));
      }
    }
    WAITQ(mu[15]);
    READ_U(14, UBASE(14)) READ_U(15, UBASE(15))
    {
      VWO();
#pragma unroll
      for (int e = 0; e < 7; ++e) DMA_WOUT(e)
    }
    STAMP(6);
    }
  } else {
    int vq = 2;
    const rsrc_t sw = __builtin_amdgcn_make_buffer_rsrc((void*)wqkv, 0, 512 * 3072, 0x00020000);
    const u32 vw = (8 * w + ((lane >> 2) & 7)) * 3072 + 128 * c + ((lane >> 5) * 4 + ((lane & 3) ^ ((w & 1) << 1))) * 16;
#define DMA_S(n, e)                                                                                                 \
  do {                                                                                                              \
    if ((e) < 2) DMA_X(((n) % 3) * 40960, n, (e) & 1);                                                              \
    else dma16(((n) % 3) * 40960 + 16384 + w * 3072 + ((e) - 2) * 1024, vw, sw, (((n) + rot) & 7) * (64 * 3072) + ((e) - 2) * 1024); \
  } while (0)
#define ISS_Q(u, base) do { VM(DMA_U(u, base, 0)); VM(DMA_U(u, base, 1)); mu[u] = vq; } while (0)
    int ms[8], mu[16];
    VM(DMA_S(0, 2)); VM(DMA_S(0, 3)); VM(DMA_S(0, 4)); ms[0] = vq;
    u32x4 xrow;
    VM(xrow = ld16(l16, sx, ((8 + c) * 8 + w) * 1024));
    const int mxr = vq;
#pragma unroll
    for (int e = 0; e < 5; ++e) { VM(DMA_S(1, e)); ms[1] = vq; }
    {
      rsrc_t sp = __builtin_amdgcn_make_buffer_rsrc((void*)wphi, 0, 16384, 0x00020000);
      VM(dma16(L_WPHI + (w & 1) * 1024, l16, sp, c * 2048 + (w & 1) * 1024));
    }
    LOAD_EPOCH()
    const int l32 = lane & 31, hh = lane >> 5;
    u32 xa = (32 * rw + l32) * 128 + ((hh ^ ((l32 >> 1) & 7)) * 16);
    u32 wa = 16384 + hh * 3072 + cw * 512 + ((lane >> 2) & 3) * 64 + (((((g & 1) << 1) | ((lane >> 1) & 1)) ^ (hh << 1)) * 16) + (lane & 1) * 8;
    u32 ca = 16384 + g * 3072 + min(w, 5) * 512 + ((lane >> 2) & 3) * 64 + ((((g & 1) << 1) | ((lane >> 1) & 1)) * 16) + (lane & 1) * 8;
    const f32x16 zero16 = {0, 0, 0, 0, 0, 0, 0, 0, 0, 0, 0, 0, 0, 0, 0, 0};
    f32x16 acc[3] = {zero16, zero16, zero16};
    f32x4 csa[2] = {zero4, zero4};
    u32x4 xg;
    int mxg = 0;
    WAITQ(ms[0]);
    BARRIER();
    STAMP(16);
#pragma unroll
    for (int n = 0; n < 8; ++n) {
      u32x4 xf[4], wf[4][3], cf[4];
      {
        const int d = n == 0 ? 0 : (n % 3 == 0 ? -81920 : 40960);
        xa = opq(xa + d); wa = opq(wa + d); ca = opq(ca + d);
      }
#pragma unroll
      for (int sq = 0; sq < 4; ++sq) {
        xf[sq] = lds16(xa ^ (32 * sq));
#pragma unroll
        for (int m = 0; m < 3; ++m) wf[sq][m] = ldfrag(wa + sq * 6144 + m * 1024, 256);
        cf[sq] = ldfrag((ca ^ (32 * (sq & 1))) + (sq >> 1) * 12288, 256);
        SCHED();
        if (n <= 5) { VM(DMA_S(n + 2, sq)); if (sq == 3) { VM(DMA_S(n + 2, 4)); ms[n + 2] = vq; } }
        if (n == 5 && sq == 3) { VM(xg = ld16_sc1(i16 * 16, sws, WS_XG + (P * 128 + 16 * w) * 16)); mxg = vq; }
        if (n == 4 && sq == 3) ISS_Q(0, LQ_M);
        if (n == 6) { if (sq == 0) ISS_Q(1, LQ_M + 16384); if (sq == 1) ISS_Q(2, LQ_PHI); if (sq == 2) ISS_Q(3, LQ_PHI + 16384); }
        if (n == 7) { if (sq == 0) ISS_Q(4, 0); if (sq == 1) ISS_Q(5, 16384); }
        SCHED();
      }
      if (n == 0) EARLY_STATS(mxr)
      if (n == 7) {
#pragma unroll
        for (int sq = 0; sq < 4; ++sq) csa[sq & 1] = mfma32(ones4, cf[sq], csa[sq & 1]);
        GATHER_XS()
        sts4f(L_CS + w * 128 + i16 * 4, csa[0][0]);
        sts4f(L_CS + w * 128 + 64 + i16 * 4, csa[1][0]);
        WAITQ(mu[3]);
        READ_U(2, LQ_PHI) READ_U(3, LQ_PHI + 16384)
      }
      const int nw = n < 7 ? vq - ms[n + 1] : 63;
      BAR_IF(cw, 1, nw);
#pragma unroll
      for (int sq = 0; sq < 4; ++sq) {
#pragma unroll
        for (int m = 0; m < 3; ++m) acc[m] = mfmaL(wf[sq][m], xf[sq], acc[m]);
        if (n < 7) csa[sq & 1] = mfma32(ones4, cf[sq], csa[sq & 1]);
      }
      BAR_IF(cw, 0, nw);
      STAMP(17 + n);
    }
    STAMP(1);
    READ_U(0, LQ_M) READ_U(1, LQ_M + 16384)
    ISS_Q(6, 32768);
    const float xr = lds4f(L_XS + (32 * rw + l32) * 4), xm = -lds4f(L_XS + 512 + (32 * rw + l32) * 4);
    u32x4 wp0, wp1;
    {
      const u32 a = L_WPHI + (2 * cw + (lane & 1)) * 512 + (4 * hh + ((lane >> 2) & 3)) * 32 + (2 * (g & 1) + ((lane >> 1) & 1)) * 8;
      const u32x4 f = ldfrag(a, 256);
      const bool own = (lane >> 2) & 1;
#pragma unroll
      for (int e = 0; e < 4; ++e) { wp0[e] = own ? 0u : f[e]; wp1[e] = own ? f[e] : 0u; }
    }
#define FEATURES(e_, dst)                                                                                           \
  {                                                                                                                 \
    u32x4 kp[2];                                                                                                    \
    _Pragma("unroll") for (int a = 0; a < 4; ++a) {                                                                \
      const f32x4 cc = lds16f(L_CS + (2 * (e_) + cw) * 128 + a * 32 + hh * 16);                                      \
      kp[a >> 1][2 * (a & 1)] = pk2v(fma2(lo2(cc), bc2(xm), (f32x2){acc[e_][4 * a], acc[e_][4 * a + 1]}));          \
      kp[a >> 1][2 * (a & 1) + 1] = pk2v(fma2(hi2(cc), bc2(xm), (f32x2){acc[e_][4 * a + 2], acc[e_][4 * a + 3]}));  \
    }                                                                                                               \
    f32x16 pl = mfmaL(wp0, kp[0], zero16);                                                                          \
    pl = mfmaL(wp1, kp[1], pl);                                                                                     \
    hedgehog16(pl, xr, dst);                                                                                        \
  }
    {
      const int s_ = 32 * rw + l32;
      const u32 key = (((s_ >> 1) & 3) ^ (((s_ >> 3) & 1) << 1)) * 16;
      ISS_Q(7, 49152);
      {
        const u32 va = LQ_V + (s_ >> 3) * 1024 + cw * 512 + (s_ & 7) * 64 + hh * 8;
#pragma unroll
        for (int a = 0; a < 4; ++a) {
          const f32x4 cc = lds16f(L_CS + (4 + cw) * 128 + a * 32 + hh * 16);
          sts8(va + ((a * 16) ^ key), (u32x2){pk2v(fma2(lo2(cc), bc2(xm), (f32x2){acc[2][4 * a], acc[2][4 * a + 1]}) * bc2(xr)),
                                              pk2v(fma2(hi2(cc), bc2(xm), (f32x2){acc[2][4 * a + 2], acc[2][4 * a + 3]}) * bc2(xr))});
        }
      }
      WAITL0();
      ISS_Q(8, LQ_M); ISS_Q(9, LQ_M + 16384);
      u32x4 ph[4];
      FEATURES(1, ph)
      const u32 pa = LQ_PHI + (s_ >> 3) * 2048 + (2 * cw + hh) * 512 + (s_ & 7) * 64;
#pragma unroll
      for (int a = 0; a < 4; ++a) sts16(pa + ((a * 16) ^ key), ph[a]);
    }
    WAITQ(mu[5]);
    READ_U(4, 0) READ_U(5, 16384)
    BARRIER();
    STAMP(2);
    {
      const u32 kx = (((lane >> 3) & 1) ^ ((g & 1) << 1)) * 16;
      const u32 pa = LQ_PHI + g * 2048 + (w >> 1) * 512 + ((lane >> 2) & 3) * 64 + (((2 * (w & 1) + ((lane >> 1) & 1)) * 16) ^ kx) + (lane & 1) * 8;
      const u32 va = LQ_V + g * 1024 + (w >> 2) * 512 + ((lane >> 2) & 3) * 64 + (((2 * ((w >> 1) & 1) + ((lane >> 1) & 1)) * 16) ^ kx) + (lane & 1) * 8;
      u32x4 pf[4], vf[4];
#pragma unroll
      for (int k = 0; k < 4; ++k) {
        pf[k] = cat(ldstr(pa + k * 8192), ldstr((pa ^ 32) + k * 8192 + 256));
        vf[k] = cat(ldstr(va + k * 4096), ldstr((va ^ 32) + k * 4096 + 256));
      }
      f32x4 kv = zero4, kz = zero4;
#pragma unroll
      for (int k = 0; k < 4; ++k) { kv = mfma32(pf[k], vf[k], kv); kz = mfma32(pf[k], ones4, kz); }
      const u32 so = WS_KP + (c * 16 + P) * KP_SZ;
      u32x4 g0 = {__float_as_uint(kv[0]), tlo, __float_as_uint(kv[1]), tlo}, g1 = {__float_as_uint(kv[2]), tlo, __float_as_uint(kv[3]), tlo};
      u32x4 g2 = {__float_as_uint((lane & 1) ? kz[2] : kz[0]), tlo, __float_as_uint((lane & 1) ? kz[3] : kz[1]), tlo};
      PIN(g0); PIN(g1); PIN(g2);
      asm volatile("s_nop 7" ::: "memory");
      VM(st16_sc1(g0, l16, sws, so + w * 2048));
      VM(st16_sc1(g1, l16, sws, so + w * 2048 + 1024));
      VM(st16_sc1(g2, g * 32 + (lane & 1) * 16 + ((i16 >> 1) << 26), sws, so + 16384 + w * 128));
    }
    STAMP(3);
    ISS_Q(10, 0); ISS_Q(11, 16384);
    WAITQ(mu[9]);
    READ_U(6, 32768) READ_U(7, 49152) READ_U(8, LQ_M) READ_U(9, LQ_M + 16384)
    WAITL0();
    ISS_Q(12, 32768); ISS_Q(13, 49152);
    u32x4 phq[4];
    FEATURES(0, phq)
    WAITQ(mu[11]);
    READ_U(10, 0) READ_U(11, 16384)
    BARRIER();
    ISS_Q(14, 0); ISS_Q(15, 16384);
#pragma unroll
    for (int a = 0; a < 4; ++a) sts16(LQ_M + w * 4096 + a * 1024 + l16, phq[a]);
    {
      VWO();
#pragma unroll
      for (int e = 0; e < 7; ++e) DMA_WOUT(e)
    }
    STAMP(4);
    if (w >= 5) {
      const int id = 2 * P + (w == 7 ? (lane & 1) : w - 5), wq = id >> 2, gq = id & 3;
      const u32 vi0 = (4 * g) * KP_SZ + (w == 7 ? 16384 + wq * 128 + gq * 32 : wq * 2048 + (gq * 16 + i16) * 16);
      const u32 vi1 = vi0 + (w == 7 ? 16 : 1024);
      const u32 vo = (w == 7 ? (WS_KZ - WS_KF) + c * 512 + id * 16 + ((i16 >> 1) << 25) : c * 8192 + id * 256 + i16 * 16) + (g << 28);
      float sa[4];
#pragma nounroll
      for (int it = 0; it < SPIN_CAP; ++it) {
        u32x4 a[4][2];
#pragma unroll
        for (int o = 0; o < 4; ++o) {
          a[o][0] = ld16_sc1(vi0, sws, WS_KP + (c * 16 + o) * KP_SZ);
          a[o][1] = ld16_sc1(vi1, sws, WS_KP + (c * 16 + o) * KP_SZ);
        }
        WAITV(0);
        bool ok = true;
#pragma unroll
        for (int o = 0; o < 4; ++o) { PIN(a[o][0]); PIN(a[o][1]); ok = ok && tag_ok(a[o][0], tlo) && tag_ok(a[o][1], tlo); }
#pragma unroll
        for (int e = 0; e < 4; ++e)
          sa[e] = (__uint_as_float(a[0][e >> 1][2 * (e & 1)]) + __uint_as_float(a[1][e >> 1][2 * (e & 1)])) +
                  (__uint_as_float(a[2][e >> 1][2 * (e & 1)]) + __uint_as_float(a[3][e >> 1][2 * (e & 1)]));
        if (__all(ok)) break;
        __builtin_amdgcn_s_sleep(1);
      }
      red2g(sa[0], sa[1]);
      red2g(sa[2], sa[3]);
      st16_sc1((u32x4){pk2(sa[0], sa[1]), tlo, pk2(sa[2], sa[3]), tlo}, vo, sws, WS_KF);
    }
    WAITV(7);
    READ_U(12, 32768) READ_U(13, 49152) READ_U(14, 0) READ_U(15, 16384)
  }

  tlo = __builtin_amdgcn_readfirstlane(tlo);
  thi = __builtin_amdgcn_readfirstlane(thi);
  int w2 = w;
  asm volatile("" : "+s"(w2), "+s"(tlo), "+s"(thi));
  {
  const int w = w2, rw = w & 3, cw = w >> 2;
  RELANE();
  STAMP(7);
  poll_flags(i16 * 8, true, sws, WS_F2 + (c2 * 16 + 2 * w) * 64, tlo, thi);
  if (t == 0) SET_FLAG(WS_F3 + (((P * 4 + rw) * 8 + c) * 2 + cw) * 8);
  STAMP(8);
  { const u32 so_ = zb0 + TILE(0, gn) * 65536; LOAD_B(0, so_) }
  BARRIER();
  { const u32 so_ = zb0 + TILE(1, gn) * 65536; LOAD_B(1, so_) }
  STAMP(9);
  const u32 redw = opq(L_RED + w * 4096 + lane * 16), redr = opq(L_RED + rw * 1024 + lane * 16);
  const u32 f7 = WS_F7 + (p * 4 + rw) * 256;
  const u32 f3s = WS_F3 + ((p >> 1) * 4 + 2 * (p & 1) + (rw >> 1)) * 128;
  const u32 s6 = WS_F6 + (((p >> 1) * 8 + cw * 4 + 2 * (p & 1) + (rw >> 1)) * 8) * 8;
  u32x4 kfg, kzg;
  u32x2 g6, z1r[6], hold;
  u32 bs;
#define REDUCE(jt)                                                                                                  \
  {                                                                                                                 \
    const u32 ra_ = redr + ((jt) & 1) * 32768;                                                                      \
    f32x4 sm = (lds16f(ra_) + lds16f(ra_ + 4096)) + (lds16f(ra_ + 8192) + lds16f(ra_ + 12288));                     \
    sm += (lds16f(ra_ + 16384) + lds16f(ra_ + 20480)) + (lds16f(ra_ + 24576) + lds16f(ra_ + 28672));               \
    const u32x2 zz = z1r[jt];                                                                                       \
    const f32x2 za = gelu2((f32x2){blo(zz[0]), bhi(zz[0])}), zb = gelu2((f32x2){blo(zz[1]), bhi(zz[1])});           \
    const f32x2 bb = bc2(blo(bs));                                                                                  \
    const u32 h0 = pk2v(za * (lo2(sm) + bb)), h1 = pk2v(zb * (hi2(sm) + bb));                                       \
    const u32 hb_ = WS_HF + (((4 * p + rw) * 2 + ((jt) >> 1)) * 8 + c2) * 1024;                                     \
    if ((jt) == 0 || ((jt) == 2 && gn == 6)) hold = (u32x2){h0, h1};                                                \
    else if ((jt) == 1 || ((jt) == 3 && gn == 6)) st16_sc1((u32x4){hold[0], hold[1], h0, h1}, l16, sws, hb_);       \
    else if ((jt) == 2) st16_sc1((u32x4){h0, h1, 0u, 0u}, l16, sws, hb_);                                           \
    else st16_sc1((u32x4){h0, tlo, h1, tlo}, l16, sws, WS_HL + (((4 * p + rw) * 8 + c2) * 2 + (gn - 1 - (jt))) * 1024); \
  }
#pragma unroll
  for (int j = 0; j < 6; ++j) {
    if (j < 5 || gn == 6) {
      const int u = j & 1;
      if (j < 5) WAITV(8); else WAITV(8);
#pragma unroll
      for (int i = 0; i < 8; ++i) PIN(bfr[u][i]);
      f32x4 acc[4];
#pragma unroll
      for (int rt = 0; rt < 4; ++rt) acc[rt] = zero4;
      __builtin_amdgcn_s_setprio(1);
#pragma unroll
      for (int hp = 0; hp < 2; ++hp)
#pragma unroll
        for (int i = 0; i < 8; ++i) {
          acc[2 * hp] = mfma32(bfr[u][i], af[2 * hp][i], acc[2 * hp]);
          acc[2 * hp + 1] = mfma32(bfr[u][i], af[2 * hp + 1][i], acc[2 * hp + 1]);
          SCHED();
          if (hp == 1 && (i == 1 || i == 3)) sts16f(redw + u * 32768 + (i >> 1) * 1024, acc[i >> 1]);
          SCHED();
        }
      __builtin_amdgcn_s_setprio(0);
      sts16f(redw + u * 32768 + 2048, acc[2]);
      sts16f(redw + u * 32768 + 3072, acc[3]);
      if (j == 1 && t != 0) {
        PIN(kfg); PIN(kzg);
        poll_gran(kfg, l16, sws, WS_KF + c * 8192 + w * 1024, tlo);
        poll_gran(kzg, g * 16, sws, WS_KZ + c * 512 + w * 64, tlo);
        sts8(L_KFS + (w >> 1) * 1024 + i16 * 64 + (w & 1) * 32 + g * 8, (u32x2){kfg[0], kfg[2]});
        sts8(L_KFS + 4096 + w * 32 + g * 8, (u32x2){kzg[0], kzg[2]});
        sts8(L_KFS + 4352 + (lane & 7) * 8, (u32x2){0u, 0u});
      }
      BAR_IF(cw, 1, 63);
      if (j == 0) {
        if (t != 0) {
          kfg = ld16_sc1(l16, sws, WS_KF + c * 8192 + w * 1024);
          kzg = ld16_sc1(g * 16, sws, WS_KZ + c * 512 + w * 64);
        }
        if (cw == 0) {
          poll_flags(i16 * 8, true, sws, f3s, tlo, thi);
          rsrc_t sb = __builtin_amdgcn_make_buffer_rsrc((void*)bsp, 0, 4096, 0x00020000);
          bs = ld2(i16 * 2, sb, (64 * p + 16 * rw) * 2);
#pragma unroll
          for (int v = 0; v < 6; ++v)
            z1r[v] = ld8_sc1(l16, sws, WS_Z1 + ((2 * p + (rw >> 1)) * 43 + gs + TILE(min(v, gn - 1), gn)) * 1024 + (rw & 1) * 8);
        }
      }
      if (j >= 1 && cw == 0) {
        if (j == 1) { PIN(bs); _Pragma("unroll") for (int v = 0; v < 6; ++v) PIN(z1r[v]); }
        if (j == 3) SET_FLAG(f7 + c2 * 8);
        if ((j == 4 && gn == 5) || j == 5) SET_FLAG(f7 + 64 + c2 * 8);
        REDUCE(j - 1)
      }
      if (j == 2 && t != 0) {
        const int l32 = lane & 31, hh = lane >> 5;
        const bool own = ((lane >> 2) & 1) == hh;
        const u32 ka = own ? L_KFS + (2 * cw + hh) * 1024 + ((l32 & 3) + 4 * (l32 >> 3)) * 64 : L_KFS + 4352;
        const u32 za = own ? L_KFS + 4096 + (2 * cw + hh) * 64 : L_KFS + 4352;
        const u32 qa = LQ_M + w * 4096 + l16;
        const f32x16 zero16 = {0, 0, 0, 0, 0, 0, 0, 0, 0, 0, 0, 0, 0, 0, 0, 0};
        f32x16 den = zero16, num = zero16;
#pragma unroll
        for (int a = 0; a < 4; ++a) {
          const u32x4 pq_ = lds16(qa + a * 1024);
          den = mfmaL(lds16(za + a * 16), pq_, den);
          num = mfmaL(lds16(ka + a * 16), pq_, num);
        }
        const float inv = __builtin_amdgcn_rcpf(den[0] + 1e-6f);
        const u32 vo = (l32 >> 4) * 16384 + hh * 512 + i16 * 16;
#pragma unroll
        for (int a = 0; a < 2; ++a) {
          const u32x4 of = {pk2(num[8 * a] * inv, num[8 * a + 1] * inv), pk2(num[8 * a + 2] * inv, num[8 * a + 3] * inv),
                            pk2(num[8 * a + 4] * inv, num[8 * a + 5] * inv), pk2(num[8 * a + 6] * inv, num[8 * a + 7] * inv)};
          st16_sc1(of, vo, sws, WS_OF + ((8 * P + 2 * rw) * 16 + 2 * c + cw) * 1024 + a * 256);
        }
        STAMP(5);
      }
      if (j == 3) {
        if (t != 0) SET_FLAG(WS_F6 + ((P * 8 + w) * 8 + c) * 8);
        VWO();
#pragma unroll
        for (int e = 7; e < 11; ++e) DMA_WOUT(e)
      }
      if (j < 4) {
        const u32 so_ = (j + 2 < gn) ? zb0 + TILE(j + 2, gn) * 65536 : 0x7FFF0000u;
        LOAD_B(u, so_)
      }
      if (j == 4) {
        poll_flags((lane & 7) * 8, true, sws, s6, tlo, thi);
#pragma unroll
        for (int e = 0; e < 8; ++e) bfr[0][e] = ld16_b(l16, sws, WS_OF + ((4 * p + rw) * 16 + 2 * e + cw) * 1024);
      }
      BAR_IF(cw, 0, 63);
    }
  }
  if (cw == 0) {
    if (gn == 6) REDUCE(5) else REDUCE(4)
  }
  {
    RELANE();
    STAMP(10);
    BARRIER();
    const u32 vw3 = ((lane >> 2) & 7) * 1024 + c2 * 128 + (lane >> 5) * 64;
    {
      const rsrc_t so_ = __builtin_amdgcn_make_buffer_rsrc((void*)wo, 0, 512 * 1024, 0x00020000);
      const u32 vwo0 = vw3 + (lane & 3) * 16, vwo1 = vwo0 ^ 32;
#pragma unroll
      for (int e = 0; e < 8; ++e) dma16(L_WO + (w * 8 + e) * 1024, (e & 1) ? vwo1 : vwo0, so_, (w * 8 + e) * 8192);
    }
    poll_flags(i16 * 8, (lane & 1) == cw, sws, f7, tlo, thi);
    u32x4 hf[8], hl[4][2];
#pragma unroll
    for (int e = 0; e < 8; ++e) hf[e] = ld16_b(l16, sws, WS_HF + ((4 * p + rw) * 16 + 2 * e + cw) * 1024);
    BARRIER();
    STAMP(11);
    const u32 qb = ((lane >> 2) & 3) * 64 + ((((g & 1) << 1) | ((lane >> 1) & 1)) * 16) + (lane & 1) * 8;
    f32x4 a3[4];
#pragma unroll
    for (int ct = 0; ct < 4; ++ct) a3[ct] = zero4;
    u32x4 wf[3][4];
#define RDW2(dst, lo0, hi0, offl, offh) _Pragma("unroll") for (int ct = 0; ct < 4; ++ct) dst[ct] = cat(ldstr(((lo0) ^ ((ct & 1) * 32)) + (offl) + (ct >> 1) * 512), ldstr(((hi0) ^ ((ct & 1) * 32)) + (offh) + (ct >> 1) * 512))
#define MM3(src, frag) _Pragma("unroll") for (int ct = 0; ct < 4; ++ct) a3[ct] = mfma32(src[ct], frag, a3[ct])
    const u32 lb = WS_HL + ((4 * p + rw) * 8 + cw) * 2048;
    const rsrc_t sxo = __builtin_amdgcn_make_buffer_rsrc((void*)(x + (size_t)p * 65536), 0, 65536, 0x00020000);
    {
      const u32 oa = opq(L_WO + qb + g * 1024 + cw * 4096);
      RDW2(wf[0], oa, oa, 0, 256);
      RDW2(wf[1], oa, oa, 8192, 8192 + 256);
#pragma unroll
      for (int e = 0; e < 8; ++e) {
        if (e + 2 < 8) RDW2(wf[(e + 2) % 3], oa, oa, (e + 2) * 8192, (e + 2) * 8192 + 256);
        PIN(bfr[0][e]);
        SCHED();
        MM3(wf[e % 3], bfr[0][e]);
        SCHED();
        hl[e >> 1][e & 1] = ld16_sc1(l16, sws, lb + (e >> 1) * 4096 + (e & 1) * 1024);
      }
    }
    u32x4 xres = ld16((tid >> 3) * 1024 + (tid & 7) * 16, sxo, c2 * 128);
    STAMP(12);
    const u32 w0 = opq(L_WOUT + qb + (g >> 1) * 1024 + (g & 1) * 256);
    {
      const u32 wa = w0 + cw * 4096, wb = w0 + cw * 2048;
#define RDE(dst, e) if ((e) < 4) { RDW2(dst, wa, wa, (e) * 8192, (e) * 8192 + 2048); } else { RDW2(dst, wb, w0, 32768 + ((e) - 4) * 4096, 49152 + ((e) - 4) * 2048); }
      RDE(wf[0], 0)
      RDE(wf[1], 1)
      WAITV(9);
#pragma unroll
      for (int e = 0; e < 8; ++e) {
        if (e + 2 < 8) { RDE(wf[(e + 2) % 3], e + 2) }
        PIN(hf[e]);
        SCHED();
        MM3(wf[e % 3], hf[e]);
        SCHED();
      }
    }
    STAMP(13);
    {
      const u32 wl_ = opq(w0 + cw * 4096 + 57344);
      u32x4 wg[4][4];
#pragma unroll
      for (int e = 0; e < 4; ++e) RDW2(wg[e], wl_, wl_, e * 8192, e * 8192 + 2048);
      WAITV(0);
      PIN(xres);
#pragma unroll
      for (int e = 0; e < 4; ++e) {
        PIN(hl[e][0]); PIN(hl[e][1]);
        poll_gran(hl[e][0], l16, sws, lb + e * 4096, tlo);
        poll_gran(hl[e][1], l16, sws, lb + e * 4096 + 1024, tlo);
        MM3(wg[e], ((u32x4){hl[e][0][0], hl[e][0][2], hl[e][1][0], hl[e][1][2]}));
      }
    }
    STAMP(14);
    BARRIER();
#pragma unroll
    for (int ct = 0; ct < 4; ++ct) sts16f(cw * 17408 + (16 * rw + i16) * 272 + (16 * ct + 4 * g) * 4, a3[ct]);
    BARRIER();
    {
      const u32 a = (tid >> 3) * 272 + (tid & 7) * 32;
      const f32x4 s0 = lds16f(a) + lds16f(a + 17408), s1 = lds16f(a + 16) + lds16f(a + 17408 + 16);
      u32x4 o = {pk2(blo(xres[0]) + s0[0], bhi(xres[0]) + s0[1]), pk2(blo(xres[1]) + s0[2], bhi(xres[1]) + s0[3]),
                 pk2(blo(xres[2]) + s1[0], bhi(xres[2]) + s1[1]), pk2(blo(xres[3]) + s1[2], bhi(xres[3]) + s1[3])};
      *(u32x4*)(out + (size_t)(64 * p + (tid >> 3)) * 1024 + c2 * 128 + (tid & 7) * 16) = o;
    }
  }
  }
  if (b == 0 && w == 0 && l16 == 0) *(u64*)(ws + WS_EP) = ((u64)thi << 32) | tlo;
  STAMP(15);
  STAMPC(31);
}

extern "C" void kernel_launch(void* const* d_in, const int* in_sizes, int n_in, void* d_out, int out_size, void* d_ws, size_t ws_size,
                              hipStream_t stream) {
  fused_kernel<<<256, 512, 0, stream>>>((const char*)d_in[0], (const char*)d_in[1], (const char*)d_in[2], (const char*)d_in[3],
                                        (const char*)d_in[5], (const char*)d_in[6], (const char*)d_in[7], (const char*)d_in[8], (char*)d_out,
                                        (char*)d_ws);
}
```

```cpp
#include <hip/hip_runtime.h>
#include <hip/hip_bf16.h>

typedef unsigned int u32;
typedef unsigned long long u64;
typedef short s16x4 __attribute__((ext_vector_type(4)));
typedef __bf16 bf16x2 __attribute__((ext_vector_type(2)));
typedef __bf16 bf16x8 __attribute__((ext_vector_type(8)));
typedef float f32x16 __attribute__((ext_vector_type(16)));
typedef float f32x4 __attribute__((ext_vector_type(4)));
typedef float f32x2 __attribute__((ext_vector_type(2)));
typedef u32 u32x4 __attribute__((ext_vector_type(4)));
typedef u32 u32x2 __attribute__((ext_vector_type(2)));
typedef __amdgpu_buffer_rsrc_t rsrc_t;
#define LDSP __attribute__((address_space(3)))
#define DI __device__ __forceinline__

#define STAMPS 0

constexpr u32 WS_EP = 0;
constexpr u32 WS_FA = 4096;
constexpr u32 WS_FB = WS_FA + 16384;
constexpr u32 WS_FC = WS_FB + 16384;
constexpr u32 WS_FD = WS_FC + 16384;
constexpr u32 WS_XG = WS_FD + 65536;
constexpr u32 WS_SG = WS_XG + 32768;
constexpr u32 WS_ZB = WS_SG + 524288;
constexpr u32 WS_Z1 = WS_ZB + 43 * 65536;
constexpr u32 KP_SZ = 17408;
constexpr u32 WS_KP = WS_Z1 + 64 * 43 * 1024;
constexpr u32 WS_KF = WS_KP + 128 * KP_SZ;
constexpr u32 WS_KZ = WS_KF + 65536;
constexpr u32 WS_OF = WS_KZ + 4096;
constexpr u32 WS_HF = WS_OF + 128 * 16384;
constexpr u32 WS_HL = WS_HF + 128 * 16 * 1024;
constexpr u32 WS_ST = WS_HL + 128 * 16 * 1024;
constexpr u32 WS_END = WS_ST + 256 * 8 * 32 * 8;

constexpr u32 LZ_W = 65536;
constexpr u32 LZ_U = 139264;
constexpr u32 LQ_M = 122880;
constexpr u32 LQ_A = 81920;
constexpr u32 LQ_PHI = 49152;
constexpr u32 LQ_V = 32768;
constexpr u32 L_TAB = 155648;
constexpr u32 L_XS = L_TAB;
constexpr u32 L_CS = L_TAB + 1024;
constexpr u32 L_ZST = L_TAB + 3072;
constexpr u32 L_WPHI = L_TAB + 3072;
constexpr u32 L_KFS = L_TAB + 2048;
constexpr u32 L_JUNK = L_TAB + 7168;
constexpr u32 L_WOUT = 0;
constexpr u32 L_RED = 57344;
constexpr u32 L_WO1 = 122880;

constexpr u64 Z2S = 0x26211B16110B0500ull;
constexpr u64 Z1S = 0x26211C16110C0600ull;
DI int grp_start(u64 tab, int c) { return c >= 8 ? 43 : (int)((tab >> (8 * c)) & 0xFF); }

#define SCHED() __builtin_amdgcn_sched_barrier(0)
#define TIC()
#define TOC(acc)
#define WAITV(n) do { TIC(); asm volatile("s_waitcnt vmcnt(%0)" ::"n"(n) : "memory"); TOC(acv); SCHED(); } while (0)
#define BARRIER() do { SCHED(); TIC(); asm volatile("s_waitcnt lgkmcnt(0)\n\ts_barrier" ::: "memory"); TOC(acb); SCHED(); } while (0)
#define WAITV_IF(g_, v, n) do { TIC(); asm volatile("s_cmp_lg_u32 %0, %1\n\ts_cbranch_scc1 1f\n\ts_waitcnt vmcnt(%2)\n1:" ::"s"(g_), "n"(v), "n"(n) : "scc", "memory"); TOC(acv); SCHED(); } while (0)
#define BAR_IF(g_, v, n) do { SCHED(); asm volatile("s_cmp_lg_u32 %0, %1\n\ts_cbranch_scc1 1f\n\ts_waitcnt vmcnt(%2) lgkmcnt(0)\n\ts_barrier\n1:" ::"s"(g_), "n"(v), "n"(n) : "scc", "memory"); SCHED(); } while (0)
#define WAITL0() asm volatile("s_waitcnt lgkmcnt(0)" ::: "memory")
#define PIN(x) asm volatile("" : "+v"(x))

DI void dma16(u32 lds, u32 voff, rsrc_t srd, u32 soff) {
  asm volatile("s_mov_b32 m0, %0\n\ts_nop 0\n\tbuffer_load_dwordx4 %1, %2, %3 offen lds" ::"s"(lds), "v"(voff), "s"(srd), "s"(soff) : "memory");
}
DI void touch(u32 lds, u32 voff, rsrc_t srd, u32 soff) {
  asm volatile("s_mov_b32 m0, %0\n\ts_nop 0\n\tbuffer_load_dword %1, %2, %3 offen lds" ::"s"(lds), "v"(voff), "s"(srd), "s"(soff) : "memory");
}
DI u32x4 ld16_sc1(u32 voff, rsrc_t srd, u32 soff) {
  u32x4 r;
  asm volatile("buffer_load_dwordx4 %0, %1, %2, %3 offen sc1" : "=v"(r) : "v"(voff), "s"(srd), "s"(soff) : "memory");
  return r;
}
template <int OFF> DI u32x4 ld16_sc1o(u32 voff, rsrc_t srd, u32 soff) {
  u32x4 r;
  asm volatile("buffer_load_dwordx4 %0, %1, %2, %3 offen offset:%4 sc1" : "=v"(r) : "v"(voff), "s"(srd), "s"(soff), "n"(OFF) : "memory");
  return r;
}
#define BPOL " sc1"
DI u32x4 ld16_b(u32 voff, rsrc_t srd, u32 soff) {
  u32x4 r;
  asm volatile("buffer_load_dwordx4 %0, %1, %2, %3 offen" BPOL : "=v"(r) : "v"(voff), "s"(srd), "s"(soff) : "memory");
  return r;
}
template <int OFF> DI u32x4 ld16_bo(u32 voff, rsrc_t srd, u32 soff) {
  u32x4 r;
  asm volatile("buffer_load_dwordx4 %0, %1, %2, %3 offen offset:%4" BPOL : "=v"(r) : "v"(voff), "s"(srd), "s"(soff), "n"(OFF) : "memory");
  return r;
}
DI void ld16_b_if6(u32x4& r, int gn, u32 voff, rsrc_t srd, u32 soff) {
  asm volatile("s_cmp_lg_u32 %4, 6\n\ts_cbranch_scc1 1f\n\tbuffer_load_dwordx4 %0, %1, %2, %3 offen" BPOL "\n1:" : "+v"(r) : "v"(voff), "s"(srd), "s"(soff), "s"(gn) : "scc", "memory");
}
DI u32x4 ld16(u32 voff, rsrc_t srd, u32 soff) {
  u32x4 r;
  asm volatile("buffer_load_dwordx4 %0, %1, %2, %3 offen" : "=v"(r) : "v"(voff), "s"(srd), "s"(soff) : "memory");
  return r;
}
DI u32x2 ld8_sc1(u32 voff, rsrc_t srd, u32 soff) {
  u32x2 r;
  asm volatile("buffer_load_dwordx2 %0, %1, %2, %3 offen sc1" : "=v"(r) : "v"(voff), "s"(srd), "s"(soff) : "memory");
  return r;
}
DI u32 ld2(u32 voff, rsrc_t srd, u32 soff) {
  u32 r;
  asm volatile("buffer_load_ushort %0, %1, %2, %3 offen" : "=v"(r) : "v"(voff), "s"(srd), "s"(soff) : "memory");
  return r;
}
DI void st16_sc1(u32x4 v, u32 voff, rsrc_t srd, u32 soff) {
  asm volatile("buffer_store_dwordx4 %0, %1, %2, %3 offen sc1\n\ts_nop 1" ::"v"(v), "v"(voff), "s"(srd), "s"(soff) : "memory");
}
DI void st8_sc1(u32x2 v, u32 voff, rsrc_t srd, u32 soff) {
  asm volatile("buffer_store_dwordx2 %0, %1, %2, %3 offen sc1" ::"v"(v), "v"(voff), "s"(srd), "s"(soff) : "memory");
}

DI u32x4 lds16(u32 a) { return *(const LDSP u32x4*)(size_t)a; }
DI u32x2 lds8(u32 a) { return *(const volatile LDSP u32x2*)(size_t)a; }
DI float lds4f(u32 a) { return *(const LDSP float*)(size_t)a; }
DI f32x4 lds16f(u32 a) { return *(const LDSP f32x4*)(size_t)a; }
DI void sts16f(u32 a, f32x4 v) { *(LDSP f32x4*)(size_t)a = v; }
DI void sts16(u32 a, u32x4 v) { *(LDSP u32x4*)(size_t)a = v; }
DI void sts8(u32 a, u32x2 v) { *(volatile LDSP u32x2*)(size_t)a = v; }
DI void sts4f(u32 a, float v) { *(LDSP float*)(size_t)a = v; }
DI u32x2 ldstr(u32 a) {
  return __builtin_bit_cast(u32x2, __builtin_amdgcn_ds_read_tr16_b64_v4i16((LDSP s16x4*)(size_t)a)); }
DI u32x4 cat(u32x2 lo, u32x2 hi) { return (u32x4){lo[0], lo[1], hi[0], hi[1]}; }
DI u32x4 ldfrag(u32 a, u32 d) { u32x2 lo = ldstr(a), hi = ldstr(a + d); return cat(lo, hi); }

DI f32x4 mfma32(u32x4 a, u32x4 b, f32x4 c) {
  return __builtin_amdgcn_mfma_f32_16x16x32_bf16(__builtin_bit_cast(bf16x8, a), __builtin_bit_cast(bf16x8, b), c, 0, 0, 0);
}
DI f32x16 mfmaL(u32x4 a, u32x4 b, f32x16 c) {
  return __builtin_amdgcn_mfma_f32_32x32x16_bf16(__builtin_bit_cast(bf16x8, a), __builtin_bit_cast(bf16x8, b), c, 0, 0, 0);
}
DI u32 pk2(float a, float b) { bf16x2 v = {(__bf16)a, (__bf16)b}; return __builtin_bit_cast(u32, v); }
DI float blo(u32 u) { return __uint_as_float(u << 16); }
DI float bhi(u32 u) { return __uint_as_float(u & 0xffff0000u); }
DI float rbf(float a) { return blo(pk2(a, a)); }
DI float dot2(u32 a, u32 b, float c) {
  return __builtin_amdgcn_fdot2_f32_bf16(__builtin_bit_cast(bf16x2, a), __builtin_bit_cast(bf16x2, b), c, false);
}
DI f32x2 fma2(f32x2 a, f32x2 b, f32x2 c) { return __builtin_elementwise_fma(a, b, c); }
DI f32x2 bc2(float a) { return (f32x2){a, a}; }
DI f32x2 lo2(f32x4 v) { return (f32x2){v[0], v[1]}; }
DI f32x2 hi2(f32x4 v) { return (f32x2){v[2], v[3]}; }
DI u32 pk2v(f32x2 v) { return pk2(v[0], v[1]); }
DI f32x2 gelu2(f32x2 x) {
  f32x2 u = fma2(x * x, bc2(-0.10294324f), bc2(-2.3022082f)) * x;
  f32x2 e = {__builtin_amdgcn_exp2f(u[0]), __builtin_amdgcn_exp2f(u[1])};
  e = e + bc2(1.0f);
  f32x2 r = {__builtin_amdgcn_rcpf(e[0]), __builtin_amdgcn_rcpf(e[1])};
  return x * r;
}
DI void red2g(float& a, float& b) {
  auto s = __builtin_amdgcn_permlane32_swap(__float_as_uint(a), __float_as_uint(b), false, false);
  float c = __uint_as_float(s[0]) + __uint_as_float(s[1]);
  auto s2 = __builtin_amdgcn_permlane16_swap(__float_as_uint(c), __float_as_uint(c), false, false);
  float d = __uint_as_float(s2[0]) + __uint_as_float(s2[1]);
  auto s3 = __builtin_amdgcn_permlane32_swap(__float_as_uint(d), __float_as_uint(d), false, false);
  a = __uint_as_float(s3[0]);
  b = __uint_as_float(s3[1]);
}
template <int CTRL> DI float dpp(float v) {
  return __uint_as_float(__builtin_amdgcn_update_dpp(0, __float_as_uint(v), CTRL, 0xF, 0xF, true));
}
DI void hedgehog16(const f32x16& p, float s, u32x4 (&o)[4]) {
  float mx = fmaxf(p[0], p[1]), mn = fminf(p[0], p[1]);
#pragma unroll
  for (int i = 2; i < 16; i += 2) { mx = fmaxf(fmaxf(mx, p[i]), p[i + 1]); mn = fminf(fminf(mn, p[i]), p[i + 1]); }
  const float c = s * 1.44269504f;
  const float a = -mx * c, b = mn * c;
  f32x2 ep[8], en[8];
#pragma unroll
  for (int i = 0; i < 8; ++i) {
    const f32x2 v = {p[2 * i], p[2 * i + 1]};
    const f32x2 u = fma2(v, bc2(c), bc2(a)), w = fma2(v, bc2(-c), bc2(b));
    ep[i] = (f32x2){__builtin_amdgcn_exp2f(u[0]), __builtin_amdgcn_exp2f(u[1])};
    en[i] = (f32x2){__builtin_amdgcn_exp2f(w[0]), __builtin_amdgcn_exp2f(w[1])};
  }
  const f32x2 sp = ((ep[0] + ep[1]) + (ep[2] + ep[3])) + ((ep[4] + ep[5]) + (ep[6] + ep[7]));
  const f32x2 sn = ((en[0] + en[1]) + (en[2] + en[3])) + ((en[4] + en[5]) + (en[6] + en[7]));
  const float ip = __builtin_amdgcn_rcpf(sp[0] + sp[1]), in = __builtin_amdgcn_rcpf(sn[0] + sn[1]);
#pragma unroll
  for (int i = 0; i < 8; ++i) {
    o[i >> 2][i & 3] = pk2v(ep[i] * bc2(ip));
    o[2 + (i >> 2)][i & 3] = pk2v(en[i] * bc2(in));
  }
}
DI void ln_fin(float s, float q, float invn, float& r, float& m) {
  m = s * invn;
  float v = fmaxf(q * invn - m * m, 0.0f);
  float sd = rbf(__builtin_amdgcn_sqrtf(rbf(rbf(v) + 1e-5f)));
  r = __builtin_amdgcn_rcpf(sd);
  m = rbf(m);
}

#define POLL_SLEEP 6
constexpr int SPIN_CAP = 1 << 17;
DI void poll_flags(u32 voff, bool act, rsrc_t srd, u32 soff, u32 tlo, u32 thi) {
#pragma nounroll
  for (int it = 0; it < SPIN_CAP; ++it) {
    u32x2 v;
    asm volatile("buffer_load_dwordx2 %0, %1, %2, %3 offen sc1\n\ts_waitcnt vmcnt(0)" : "=&v"(v) : "v"(voff), "s"(srd), "s"(soff) : "memory");
    bool ok = !act || (v[0] == tlo && v[1] == thi);
    if (__all(ok)) break;
    __builtin_amdgcn_s_sleep(POLL_SLEEP);
  }
}
DI bool tag_ok(u32x4 q, u32 t) { return q[1] == t && q[3] == t; }
DI void poll_gran(u32x4& q, u32 voff, rsrc_t srd, u32 soff, u32 tlo) {
#pragma nounroll
  for (int it = 0; it < SPIN_CAP; ++it) {
    if (__all(tag_ok(q, tlo))) break;
    __builtin_amdgcn_s_sleep(POLL_SLEEP);
    asm volatile("buffer_load_dwordx4 %0, %1, %2, %3 offen sc1\n\ts_waitcnt vmcnt(0)" : "=&v"(q) : "v"(voff), "s"(srd), "s"(soff) : "memory");
  }
}
DI u32 opq(u32 v) { asm volatile("" : "+v"(v)); return v; }

#define STAMP(i)
#define STAMPC(i)

#define GQ_N 3
#define PREFETCH 0
#define VM(x) do { x; ++vq; } while (0)
#define WAITQ(mark) WAITV(vq - (mark))

__global__ __launch_bounds__(512, 2) void fused_kernel(const char* __restrict__ x, const char* __restrict__ wqkv, const char* __restrict__ wphi,
                                                        const char* __restrict__ wo, const char* __restrict__ win, const char* __restrict__ wsp,
                                                        const char* __restrict__ bsp, const char* __restrict__ wout, char* __restrict__ out, char* ws) {
  __shared__ __attribute__((aligned(1024))) char smem[163840];
  const int tid = threadIdx.x, lane = tid & 63;
  const int w = __builtin_amdgcn_readfirstlane(tid >> 6);
  const int rw = w & 3, cw = w >> 2, i16 = lane & 15, g = lane >> 4;
  const int b = blockIdx.x, xcd = b & 7, r = b >> 3;
  const int Pl = r >> 3, t = (r >> 2) & 1, cl = r & 3;
  const int P = (xcd >> 1) * 4 + Pl, c = (xcd & 1) * 4 + cl;
  const int p = 2 * P + (cl & 1);
  const int c2 = (xcd & 1) * 4 + (t ? 1 + (cl >> 1) : 3 * (cl >> 1));
  const int rot = 2 * ((Pl + cl) & 3);
  u32 acv = 0, acb = 0; (void)acv; (void)acb;
  asm volatile("" ::"v"((u32)(size_t)(LDSP char*)smem));
  STAMP(0);
  STAMPC(30);

  u64 ep;
  asm volatile("s_load_dwordx2 %0, %1, 0x0" : "=s"(ep) : "s"(ws) : "memory");
  const rsrc_t sx = __builtin_amdgcn_make_buffer_rsrc((void*)(x + (size_t)P * 131072), 0, 131072, 0x00020000);
  const u32 l16 = opq(lane * 16);
  const u32 vx0 = (16 * w + (lane >> 3)) * 1024 + (((lane & 7) ^ g) * 16);
  const u32 vx1 = (vx0 + 8192) ^ 64;
#define DMA_X(dst, n, e) dma16((dst) + w * 2048 + (e) * 1024, (e) ? vx1 : vx0, sx, (((n) + rot) & 7) * 128)
  DMA_X(0, 0, 0);
  DMA_X(0, 0, 1);
  const rsrc_t sa = __builtin_amdgcn_make_buffer_rsrc((void*)(wsp + (size_t)p * 262144), 0, 262144, 0x00020000);
  const rsrc_t sws = __builtin_amdgcn_make_buffer_rsrc((void*)ws, 0, WS_END, 0x00020000);
  const u32 vA0 = (lane >> 3) * 4096 + (((lane & 7) ^ g) * 16);
  const u32 vA1 = (vA0 + 32768) ^ 64;
#define DMA_U(u, base, e) dma16((base) + w * 2048 + (e) * 1024, (e) ? vA1 : vA0, sa, w * 512 + (((u) >> 1) & 3) * 128 + (((u) >> 3) * 32 + ((u) & 1) * 16) * 4096)
  const f32x4 zero4 = {0, 0, 0, 0};
  u32x4 ones4 = {0x3F803F80u, 0x3F803F80u, 0x3F803F80u, 0x3F803F80u};
  u32 tlo, thi;
  u32x4 af[4][8];
  u32x4 bfr[2][8];
  u32x2 gf;
  const u32 ard = w * 2048 + i16 * 128 + (((g >> 1) ^ (i16 >> 1)) * 16) + (g & 1) * 8;
#define READ_U(u, base)                                                                                             \
  {                                                                                                                 \
    const u32 ab_ = opq(ard + (base));                                                                              \
    _Pragma("unroll") for (int kk = 0; kk < 2; ++kk)                                                               \
      af[2 * ((u) >> 3) + ((u) & 1)][((u) & 6) + kk] = cat(lds8(ab_ ^ (64 * kk)), lds8(ab_ ^ (64 * kk + 32)));      \
  }
#define USE_EPOCH() { asm volatile("" : "+s"(ep)); ep += 1; tlo = (u32)ep; thi = (u32)(ep >> 32); PIN(ones4); }
#define EARLY_STATS(mark)                                                                                           \
  {                                                                                                                 \
    WAITQ(mark); PIN(xrow);                                                                                         \
    float s_ = 0, q_ = 0;                                                                                           \
    _Pragma("unroll") for (int e = 0; e < 4; ++e) { s_ = dot2(xrow[e], ones4[0], s_); q_ = dot2(xrow[e], xrow[e], q_); } \
    s_ += dpp<0x128>(s_); q_ += dpp<0x128>(q_);                                                                     \
    s_ += dpp<0x124>(s_); q_ += dpp<0x124>(q_);                                                                     \
    s_ += dpp<0x4E>(s_); q_ += dpp<0x4E>(q_);                                                                       \
    s_ += dpp<0xB1>(s_); q_ += dpp<0xB1>(q_);                                                                       \
    red2g(s_, q_);                                                                                                  \
    float r_, m_;                                                                                                   \
    ln_fin(s_, q_, 1.0f / 512, r_, m_);                                                                             \
    VM(st16_sc1((u32x4){__float_as_uint(r_), tlo, __float_as_uint(m_), tlo}, l16 << 22, sws, WS_XG + (P * 128 + (t * 8 + c) * 8 + w) * 16)); \
  }
#define GATHER_XS()                                                                                                 \
  WAITQ(mxg); PIN(xg);                                                                                              \
  poll_gran(xg, i16 * 16, sws, WS_XG + (P * 128 + 16 * w) * 16, tlo);                                               \
  sts4f(L_XS + (16 * w + i16) * 4, __uint_as_float(xg[0]));                                                         \
  sts4f(L_XS + 512 + (16 * w + i16) * 4, __uint_as_float(xg[2]));
#define RELANE() const int lane = opq(l16) >> 4, i16 = lane & 15, g = lane >> 4, tid = w * 64 + lane; (void)tid; (void)g; (void)i16
#define SET_FLAG(off) st8_sc1((u32x2){tlo, thi}, l16 << 22, sws, (off))
  const int gs = grp_start(Z2S, c2), gn = grp_start(Z2S, c2 + 1) - gs;
  const int trot = (p & 7) >= 5 ? (p & 7) - 5 : (p & 7);
#define TILE(j, n_) (((j) + trot) >= (n_) ? (j) + trot - (n_) : (j) + trot)
  const u32 zb0 = WS_ZB + (gs * 64 + 8 * w) * 1024;
#define DMA_WOUT(e)                                                                                                 \
  {                                                                                                                 \
    const rsrc_t so_ = __builtin_amdgcn_make_buffer_rsrc((void*)wout, 0, 688 * 1024, 0x00020000);                   \
    const int ws_ = grp_start(Z2S, w), wn_ = grp_start(Z2S, w + 1) - ws_;                                           \
    if ((e) < 6) dma16(L_WOUT + ((e) < 4 ? 2 * w + ((e) >> 1) : 16 + w) * 2048 + ((e) & 1) * 1024, vwo, so_, ((ws_ + TILE((e) >> 1, wn_)) * 2 + ((e) & 1)) * 8192); \
    else if ((e) == 6) dma16(L_WOUT + 24 * 2048 + w * 1024, vwo, so_, ((((w >> 1) == 0 ? 5 : ((w >> 1) == 1 ? 11 : ((w >> 1) == 2 ? 27 : 64))) + TILE(3, 6)) * 2 + (w & 1)) * 8192); \
  }
#define DMA_WOUT_AT(dst, e)                                                                                         \
  {                                                                                                                 \
    const rsrc_t so_ = __builtin_amdgcn_make_buffer_rsrc((void*)wout, 0, 688 * 1024, 0x00020000);                   \
    const int ws_ = grp_start(Z2S, w), wn_ = grp_start(Z2S, w + 1) - ws_;                                           \
    dma16((dst) + ((e) - 7) * 1024, vwo, so_, ((ws_ + TILE(wn_ - 1 - (((e) - 7) >> 1), wn_)) * 2 + (((e) - 7) & 1)) * 8192); \
  }
#define VWO() const u32 vwo = ((lane >> 2) & 7) * 1024 + c2 * 128 + (lane >> 5) * 64 + (((lane & 3) ^ (((lane >> 4) & 1) << 1)) * 16)
#define LOAD_B(u, so_)                                                                                              \
  bfr[u][0] = ld16_bo<0>(l16, sws, so_); bfr[u][1] = ld16_bo<1024>(l16, sws, so_);                                  \
  bfr[u][2] = ld16_bo<2048>(l16, sws, so_); bfr[u][3] = ld16_bo<3072>(l16, sws, so_);                               \
  bfr[u][4] = ld16_bo<0>(l16, sws, (so_) + 4096); bfr[u][5] = ld16_bo<1024>(l16, sws, (so_) + 4096);                \
  bfr[u][6] = ld16_bo<2048>(l16, sws, (so_) + 4096); bfr[u][7] = ld16_bo<3072>(l16, sws, (so_) + 4096);

  if (t == 0) {
    int vq = 2;
    const rsrc_t sw = __builtin_amdgcn_make_buffer_rsrc((void*)win, 0, 512 * 2752, 0x00020000);
    const int z2s = grp_start(Z2S, c), z2n = grp_start(Z2S, c + 1) - z2s;
    const int z1s = grp_start(Z1S, c), z1n = grp_start(Z1S, c + 1) - z1s;
    u32 vw[3];
    int wch[3];
#pragma unroll
    for (int e = 0; e < 3; ++e) {
      const int sg = 2 * e + (lane >> 5), hi = sg >= 3;
      wch[e] = (sg - 3 * hi) * 4 + ((lane & 3) ^ (hi << 1));
      vw[e] = wch[e] < 2 * z2n ? (16 * w + 8 * hi + ((lane >> 2) & 7)) * 2752 + 1376 + 32 * z2s + 16 * wch[e] : 0x7FFF0000u;
    }
#define DMA_W(bk, e) dma16(LZ_W + ((bk) % 3) * 24576 + w * 3072 + (e) * 1024, vw[e], sw, ((2 * (bk) + rot) & 7) * (64 * 2752))
#define UBASE(u) ((u) % 5 == 0 ? LZ_U : ((u) % 5 - 1) * 16384)
#define ISS_X(n) do { VM(DMA_X(((n) & 3) * 16384, n, 0)); VM(DMA_X(((n) & 3) * 16384, n, 1)); mx[n] = vq; } while (0)
#define ISS_U(u) do { VM(DMA_U(u, UBASE(u), 0)); VM(DMA_U(u, UBASE(u), 1)); mu[u] = vq; } while (0)
#define ISS_W(bk, e0, e1) do { _Pragma("unroll") for (int e_ = e0; e_ < e1; ++e_) VM(DMA_W(bk, e_)); mw[bk] = vq; } while (0)
    int mx[8], mw[8], mu[16];
    mx[0] = vq;
    ISS_W(0, 0, 3);
    u32x4 xrow;
    VM(xrow = ld16(l16, sx, (c * 8 + w) * 1024));
    const int mxr = vq;
    ISS_X(1);
    const u32 xa0 = opq((32 * rw + i16) * 128 + ((g ^ (i16 >> 1)) * 16)), xa1 = xa0 ^ 64;
    u32 ta[3];
    int tl[3];
    {
      const u32 trb = LZ_W + g * 1536 + ((lane >> 2) & 3) * 64 + ((((g & 1) << 1) | ((lane >> 1) & 1)) * 16) + (lane & 1) * 8;
#pragma unroll
      for (int j = 0; j < 3; ++j) {
        tl[j] = 3 * cw + (j + rw) % 3;
        ta[j] = (trb + (tl[j] >> 1) * 512) ^ ((tl[j] & 1) * 32);
      }
    }
#define NEXT_BLOCK(bk) _Pragma("unroll") for (int j = 0; j < 3; ++j) ta[j] = opq(ta[j] + ((bk) == 0 ? 0 : ((bk) % 3 == 0 ? -49152 : 24576)))
    f32x4 acc[2][3], csa = zero4;
#pragma unroll
    for (int j = 0; j < 3; ++j) acc[0][j] = acc[1][j] = zero4;
    u32x4 xf[16][2], wf[2][3];
    u32x4 xg;
    int mxg = 0;
    WAITQ(mw[0]);
    BARRIER();
    USE_EPOCH()
    STAMP(7);
#define P1_MEM(n, h)                                                                                                \
  {                                                                                                                 \
    if ((n) == 0) { if ((h) == 0) ISS_X(2); if ((h) == 1) ISS_W(1, 0, 2); if ((h) == 2) ISS_W(1, 2, 3); if ((h) == 3) ISS_X(3); } \
    if ((n) == 1) { if ((h) == 0) ISS_W(2, 0, 2); if ((h) == 1) ISS_W(2, 2, 3); if ((h) == 2) VM(DMA_X(0, 4, 0)); if ((h) == 3) { VM(DMA_X(0, 4, 1)); mx[4] = vq; } } \
    if ((n) == 2 || (n) == 4) { if ((h) < 2) { VM(DMA_X((((n) + 3) & 3) * 16384, (n) + 3, (h) & 1)); mx[(n) + 3] = vq; } else ISS_W((n) / 2 + 2, (h) - 2, (h) - 1); } \
    if ((n) == 3) { if ((h) == 0) ISS_W(3, 2, 3); else if ((h) < 3) { VM(DMA_X(32768, 6, (h) - 1)); mx[6] = vq; } else ISS_U(0); } \
    if ((n) == 5) { if ((h) == 0) ISS_W(4, 2, 3); if ((h) == 1) { VM(xg = ld16_sc1(i16 * 16, sws, WS_XG + (P * 128 + 16 * w) * 16)); mxg = vq; } \
                    if ((h) >= 2) { VM(DMA_U(1, UBASE(1), (h) - 2)); mu[1] = vq; } }                                 \
    if ((n) == 6) { if ((h) < 3) ISS_W(5, h, (h) + 1); else ISS_U(2); }                                             \
    if ((n) == 7) { if ((h) < 2) { VM(DMA_U(3, UBASE(3), h)); mu[3] = vq; } }                                       \
  }
#pragma unroll
    for (int ks = 0; ks <= 16; ++ks) {
      const int n = ks >> 1, kk = ks & 1;
      if (ks > 0 && ks < 16 && kk == 0) {
        { const int mk = (n & 1) ? mx[n] : (mx[n] > mw[n >> 1] ? mx[n] : mw[n >> 1]); WAITQ(mk); }
        BARRIER();
      }
      if (ks < 16 && (ks & 3) == 0) { NEXT_BLOCK(ks >> 2); }
      if (ks == 8) {
#pragma unroll
        for (int e = 0; e < 3; ++e)
          vw[e] = wch[e] < 2 * z1n ? (16 * w + 8 * (2 * e + (lane >> 5) >= 3) + ((lane >> 2) & 7)) * 2752 + 32 * z1s + 16 * wch[e] : 0x7FFF0000u;
      }
      const int k1 = ks > 0 ? ks - 1 : 0, q1 = k1 & 1;
#pragma unroll
      for (int m = 0; m < 7; ++m) {
        if (ks > 0) {
          if (m < 6) acc[m & 1][m >> 1] = mfma32(xf[k1][m & 1], wf[q1][m >> 1], acc[m & 1][m >> 1]);
          else csa = mfma32(ones4, wf[q1][0], csa);
          SCHED();
        }
        if (ks < 16) {
          if (m == 0) { P1_MEM(n, 2 * kk) xf[ks & 15][0] = lds16((kk ? xa1 : xa0) + (n & 3) * 16384); xf[ks & 15][1] = lds16((kk ? xa1 : xa0) + (n & 3) * 16384 + 2048); }
          else if (m < 4) wf[kk][m - 1] = ldfrag(ta[m - 1] + (ks & 3) * 6144, 256);
          if (m == 4) P1_MEM(n, 2 * kk + 1)
        }
        SCHED();
      }
      if (ks == 2) EARLY_STATS(mxr)
    }
    GATHER_XS()
    sts4f(L_CS + tl[0] * 64 + i16 * 4, csa[0]);
    WAITQ(mu[1] > mw[4] ? mu[1] : mw[4]);
    READ_U(0, UBASE(0)) READ_U(1, UBASE(1))
    BARRIER();
    STAMP(1);
    u32x4 zp[3];
    ISS_W(6, 0, 3);
    {
      f32x2 ps[4], pq[4];
      float cs[3];
#pragma unroll
      for (int j = 0; j < 3; ++j) cs[j] = -lds4f(L_CS + tl[j] * 64 + i16 * 4);
#pragma unroll
      for (int rt = 0; rt < 2; ++rt) {
        const f32x4 rr = lds16f(L_XS + (32 * rw + 16 * rt + 4 * g) * 4), mm = lds16f(L_XS + 512 + (32 * rw + 16 * rt + 4 * g) * 4);
        f32x2 z0[3], z1[3];
#pragma unroll
        for (int j = 0; j < 3; ++j) {
          z0[j] = gelu2(fma2(lo2(mm), bc2(cs[j]), lo2(acc[rt][j])) * lo2(rr));
          z1[j] = gelu2(fma2(hi2(mm), bc2(cs[j]), hi2(acc[rt][j])) * hi2(rr));
          zp[j][2 * rt] = pk2v(z0[j]); zp[j][2 * rt + 1] = pk2v(z1[j]);
        }
        ps[2 * rt] = z0[0] + z0[1] + z0[2]; ps[2 * rt + 1] = z1[0] + z1[1] + z1[2];
        pq[2 * rt] = fma2(z0[2], z0[2], fma2(z0[1], z0[1], z0[0] * z0[0]));
        pq[2 * rt + 1] = fma2(z1[2], z1[2], fma2(z1[1], z1[1], z1[0] * z1[0]));
        SCHED();
        if (rt == 0) { ISS_U(4); ISS_U(5); }
        else ISS_U(6);
        SCHED();
      }
      const bool b3 = lane & 8, b2 = lane & 4, b1 = lane & 2;
#pragma unroll
      for (int m = 0; m < 2; ++m)
#pragma unroll
        for (int e = 0; e < 2; ++e) {
          ps[m][e] = (b3 ? ps[m + 2][e] : ps[m][e]) + dpp<0x128>(b3 ? ps[m][e] : ps[m + 2][e]);
          pq[m][e] = (b3 ? pq[m + 2][e] : pq[m][e]) + dpp<0x128>(b3 ? pq[m][e] : pq[m + 2][e]);
        }
#pragma unroll
      for (int e = 0; e < 2; ++e) {
        ps[0][e] = (b2 ? ps[1][e] : ps[0][e]) + dpp<0x141>(b2 ? ps[0][e] : ps[1][e]);
        pq[0][e] = (b2 ? pq[1][e] : pq[0][e]) + dpp<0x141>(b2 ? pq[0][e] : pq[1][e]);
      }
      float s_ = (b1 ? ps[0][1] : ps[0][0]) + dpp<0x4E>(b1 ? ps[0][0] : ps[0][1]);
      float q_ = (b1 ? pq[0][1] : pq[0][0]) + dpp<0x4E>(b1 ? pq[0][0] : pq[0][1]);
      s_ += dpp<0xB1>(s_);
      q_ += dpp<0xB1>(q_);
      const int row = 32 * rw + 16 * (i16 >> 3) + 4 * g + ((i16 >> 1) & 3);
      VM(st16_sc1((u32x4){__float_as_uint(s_), tlo, __float_as_uint(q_), tlo}, row * 16 + ((lane & 1) << 30), sws, WS_SG + ((P * 8 + c) * 2 + cw) * 2048));
      PIN(zp[0]); PIN(zp[1]); PIN(zp[2]);
    }
    STAMP(2);
    {
    RELANE();
#pragma unroll
    for (int j = 0; j < 3; ++j) acc[0][j] = acc[1][j] = zero4;
    csa = zero4;
    u32x4 q0, q1, q2, q3;
    int mq = 0, mz = 0;
    const u32 gvo = (2 * g) * 4096 + (32 * rw + 16 * cw + i16) * 16;
    const u32 gso = WS_SG + P * 32768;
#pragma unroll
    for (int ks = 0; ks <= 16; ++ks) {
      if (ks == 8) {
        WAITQ(mq); PIN(q0); PIN(q1); PIN(q2); PIN(q3);
#pragma nounroll
        for (int it = 0; it < SPIN_CAP; ++it) {
          if (__all(tag_ok(q0, tlo) && tag_ok(q1, tlo) && tag_ok(q2, tlo) && tag_ok(q3, tlo))) break;
          __builtin_amdgcn_s_sleep(POLL_SLEEP);
          asm volatile(
              "buffer_load_dwordx4 %0, %4, %5, %6 offen sc1\n\t"
              "buffer_load_dwordx4 %1, %4, %5, %6 offen offset:2048 sc1\n\t"
              "buffer_load_dwordx4 %2, %4, %5, %7 offen sc1\n\t"
              "buffer_load_dwordx4 %3, %4, %5, %7 offen offset:2048 sc1\n\t"
              "s_waitcnt vmcnt(0)"
              : "=&v"(q0), "=&v"(q1), "=&v"(q2), "=&v"(q3)
              : "v"(gvo), "s"(sws), "s"(gso), "s"(gso + 4096)
              : "memory");
        }
        float s = (__uint_as_float(q0[0]) + __uint_as_float(q1[0])) + (__uint_as_float(q2[0]) + __uint_as_float(q3[0]));
        float q = (__uint_as_float(q0[2]) + __uint_as_float(q1[2])) + (__uint_as_float(q2[2]) + __uint_as_float(q3[2]));
        red2g(s, q);
        float r2, m2;
        ln_fin(s, q, 1.0f / 688, r2, m2);
        sts4f(L_ZST + (32 * rw + 16 * cw + i16) * 4, r2);
        sts4f(L_ZST + 512 + (32 * rw + 16 * cw + i16) * 4, -m2 * r2);
        STAMP(3);
      }
      if (ks < 16 && (ks & 3) == 0) {
        if (ks > 0) { WAITQ(ks == 12 ? mz : mw[4 + (ks >> 2)]); BARRIER(); }
        if (ks == 12) { if (w == 0) SET_FLAG(WS_FA + (P * 8 + c) * 128); STAMP(5); }
        NEXT_BLOCK(4 + (ks >> 2));
      }
      if (ks == 8) {
#pragma unroll
        for (int rt = 0; rt < 2; ++rt) {
          const u32 ra = L_ZST + (32 * rw + 16 * rt + 4 * g) * 4;
          const f32x4 r4 = lds16f(ra), m4 = lds16f(ra + 512);
#pragma unroll
          for (int j = 0; j < 3; ++j) {
            const u32 u0 = zp[j][2 * rt], u1 = zp[j][2 * rt + 1];
            zp[j][2 * rt] = pk2v(fma2((f32x2){blo(u0), bhi(u0)}, lo2(r4), lo2(m4)));
            zp[j][2 * rt + 1] = pk2v(fma2((f32x2){blo(u1), bhi(u1)}, hi2(r4), hi2(m4)));
          }
        }
#pragma unroll
        for (int j = 0; j < 3; ++j)
          VM(st16_sc1(zp[j], l16, sws, (tl[j] < z2n) ? WS_ZB + ((z2s + tl[j]) * 64 + 4 * P + rw) * 1024 : 0x7FFF0000u));
        mz = vq;
        STAMP(4);
        SCHED();
      }
      if (ks >= 3 && ks <= 9) { WAITQ(mu[ks - 1]); READ_U(ks - 1, UBASE(ks - 1)) }
      if (ks >= 9 && ks <= 15) { WAITQ(mu[ks]); READ_U(ks, UBASE(ks)) }
      const int k1 = ks > 0 ? ks - 1 : 0, q1_ = k1 & 1;
#pragma unroll
      for (int m = 0; m < 7; ++m) {
        if (ks > 0) {
          if (m < 6) acc[m & 1][m >> 1] = mfma32(wf[q1_][m >> 1], xf[k1][m & 1], acc[m & 1][m >> 1]);
          else csa = mfma32(ones4, wf[q1_][0], csa);
          SCHED();
        }
        if (ks < 16 && m >= 1 && m < 4) wf[ks & 1][m - 1] = ldfrag(ta[m - 1] + (ks & 3) * 6144, 256);
        if (m == 0 || m == 4) {
          const int h = m == 4;
          if (ks >= 4 && ks <= 12) { VM(DMA_U(ks + 3, UBASE(ks + 3), h)); mu[ks + 3] = vq; }
        }
        if (m == 6) {
          if (ks >= 4 && ks <= 6) ISS_W(7, ks - 4, ks - 3);
        }
        if (m == 2) {
          if (ks == 5) { VM(q0 = ld16_sc1o<0>(gvo, sws, gso)); VM(q1 = ld16_sc1o<2048>(gvo, sws, gso)); }
          if (ks == 6) { VM(q2 = ld16_sc1o<0>(gvo, sws, gso + 4096)); VM(q3 = ld16_sc1o<2048>(gvo, sws, gso + 4096)); mq = vq; }
        }
        SCHED();
      }
      if ((ks & 3) == 3 || ks == 16) {
#pragma unroll
        for (int j = 0; j < 3; ++j) { PIN(acc[0][j]); PIN(acc[1][j]); }
        PIN(csa);
      }
    }
    sts4f(L_CS + 512 + tl[0] * 64 + i16 * 4, csa[0]);
    gf = ld8_sc1((lane & 1) * 1024, sws, WS_FA + (16 * w + c2) * 128);
    BARRIER();
    VWO();
    DMA_WOUT(0) DMA_WOUT(1)
    WAITV(2);
    PIN(gf);
    if (!__all(gf[0] == tlo && gf[1] == thi)) poll_flags((lane & 1) * 1024, true, sws, WS_FA + (16 * w + c2) * 128, tlo, thi);
    STAMP(6);
    {
      float xr[2], xm[2];
#pragma unroll
      for (int rt = 0; rt < 2; ++rt) {
        xr[rt] = lds4f(L_XS + (32 * rw + 16 * rt + i16) * 4);
        xm[rt] = -lds4f(L_XS + 512 + (32 * rw + 16 * rt + i16) * 4);
      }
      const u32 so0 = zb0 + TILE(0, gn) * 65536;
#pragma unroll
      for (int j = 0; j < 3; ++j) {
        const f32x4 cc = lds16f(L_CS + 512 + tl[j] * 64 + g * 16);
        bfr[0][3 * j] = ld16_b(l16, sws, so0 + 3 * j * 1024);
        bfr[0][3 * j + 1] = ld16_b(l16, sws, so0 + (3 * j + 1) * 1024);
        if (j < 2) bfr[0][3 * j + 2] = ld16_b(l16, sws, so0 + (3 * j + 2) * 1024);
        u32x4 zz;
#pragma unroll
        for (int rt = 0; rt < 2; ++rt) {
          zz[2 * rt] = pk2v(gelu2(fma2(lo2(cc), bc2(xm[rt]), lo2(acc[rt][j])) * bc2(xr[rt])));
          zz[2 * rt + 1] = pk2v(gelu2(fma2(hi2(cc), bc2(xm[rt]), hi2(acc[rt][j])) * bc2(xr[rt])));
        }
        st16_sc1(zz, l16, sws, (tl[j] < z1n) ? WS_Z1 + ((4 * P + rw) * 43 + z1s + tl[j]) * 1024 : 0x7FFF0000u);
        SCHED();
      }
    }
    }
  } else {
    int vq = 2;
    const rsrc_t sw = __builtin_amdgcn_make_buffer_rsrc((void*)wqkv, 0, 512 * 3072, 0x00020000);
    const u32 vw = (8 * w + ((lane >> 2) & 7)) * 3072 + 128 * c + ((lane >> 5) * 4 + ((lane & 3) ^ ((w & 1) << 1))) * 16;
#define DMA_S(n, e)                                                                                                 \
  do {                                                                                                              \
    if ((e) < 2) DMA_X(((n) % 3) * 40960, n, (e) & 1);                                                              \
    else dma16(((n) % 3) * 40960 + 16384 + w * 3072 + ((e) - 2) * 1024, vw, sw, (((n) + rot) & 7) * (64 * 3072) + ((e) - 2) * 1024); \
  } while (0)
#define ISS_Q(u, base) do { VM(DMA_U(u, base, 0)); VM(DMA_U(u, base, 1)); mu[u] = vq; } while (0)
    int ms[8], mu[16];
    VM(DMA_S(0, 2)); VM(DMA_S(0, 3)); VM(DMA_S(0, 4)); ms[0] = vq;
    u32x4 xrow;
    VM(xrow = ld16(l16, sx, ((8 + c) * 8 + w) * 1024));
    const int mxr = vq;
#pragma unroll
    for (int e = 0; e < 5; ++e) { VM(DMA_S(1, e)); ms[1] = vq; }
    const int l32 = lane & 31, hh = lane >> 5;
    u32 xa = (32 * rw + l32) * 128 + ((hh ^ ((l32 >> 1) & 7)) * 16);
    u32 wa = 16384 + hh * 3072 + cw * 512 + ((lane >> 2) & 3) * 64 + (((((g & 1) << 1) | ((lane >> 1) & 1)) ^ (hh << 1)) * 16) + (lane & 1) * 8;
    u32 ca = 16384 + g * 3072 + min(w, 5) * 512 + ((lane >> 2) & 3) * 64 + ((((g & 1) << 1) | ((lane >> 1) & 1)) * 16) + (lane & 1) * 8;
    const f32x16 zero16 = {0, 0, 0, 0, 0, 0, 0, 0, 0, 0, 0, 0, 0, 0, 0, 0};
    f32x16 acc[3] = {zero16, zero16, zero16};
    f32x4 csa[2] = {zero4, zero4};
    u32x4 xg;
    int mxg = 0;
    WAITQ(ms[0]);
    BARRIER();
    USE_EPOCH()
    STAMP(7);
#pragma unroll
    for (int n = 0; n < 8; ++n) {
      u32x4 xf[4], wf[4][3], cf[4];
      {
        const int d = n == 0 ? 0 : (n % 3 == 0 ? -81920 : 40960);
        xa = opq(xa + d); wa = opq(wa + d); ca = opq(ca + d);
      }
      if (n == 4) { WAITQ(mu[1]); READ_U(0, LQ_M) READ_U(1, LQ_M + 16384) }
      if (n == 7) { WAITQ(mu[3]); READ_U(2, LQ_M) READ_U(3, LQ_M + 16384) }
#pragma unroll
      for (int sq = 0; sq < 4; ++sq) {
        if (n <= 5) { VM(DMA_S(n + 2, sq)); if (sq == 3) { VM(DMA_S(n + 2, 4)); ms[n + 2] = vq; } }
        if (n == 6 && sq < 2) ISS_Q(4 + sq, LQ_A + sq * 16384);
        if (n == 7 && sq < 2) ISS_Q(6 + sq, sq * 16384);
        SCHED();
        xf[sq] = lds16(xa ^ (32 * sq));
#pragma unroll
        for (int m = 0; m < 3; ++m) wf[sq][m] = ldfrag(wa + sq * 6144 + m * 1024, 256);
        cf[sq] = ldfrag((ca ^ (32 * (sq & 1))) + (sq >> 1) * 12288, 256);
        SCHED();
      }
      if (n == 0) {
        rsrc_t sp = __builtin_amdgcn_make_buffer_rsrc((void*)wphi, 0, 16384, 0x00020000);
        VM(dma16(L_WPHI + (w & 1) * 1024, l16, sp, c * 2048 + (w & 1) * 1024));
        EARLY_STATS(mxr)
      }
      if (n == 1 || n == 2) ISS_Q(n - 1, LQ_M + (n - 1) * 16384);
      if (n == 4) { VM(xg = ld16_sc1(i16 * 16, sws, WS_XG + (P * 128 + 16 * w) * 16)); mxg = vq; }
      if (n == 5) ISS_Q(2, LQ_M);
      if (n == 6) ISS_Q(3, LQ_M + 16384);
      if (n == 7) {
#pragma unroll
        for (int sq = 0; sq < 4; ++sq) csa[sq & 1] = mfma32(ones4, cf[sq], csa[sq & 1]);
        GATHER_XS()
        sts4f(L_CS + w * 128 + i16 * 4, csa[0][0]);
        sts4f(L_CS + w * 128 + 64 + i16 * 4, csa[1][0]);
      }
      const int nw = n < 7 ? vq - ms[n + 1] : 63;
      BAR_IF(cw, 1, nw);
#pragma unroll
      for (int sq = 0; sq < 4; ++sq) {
#pragma unroll
        for (int m = 0; m < 3; ++m) acc[m] = mfmaL(wf[sq][m], xf[sq], acc[m]);
        if (n < 7) csa[sq & 1] = mfma32(ones4, cf[sq], csa[sq & 1]);
      }
      BAR_IF(cw, 0, nw);
    }
    STAMP(1);
    ISS_Q(8, LQ_M);
    READ_U(4, LQ_A) READ_U(5, LQ_A + 16384)
    const float xr = lds4f(L_XS + (32 * rw + l32) * 4), xm = -lds4f(L_XS + 512 + (32 * rw + l32) * 4);
    u32x4 wp0, wp1;
    {
      const u32 a = L_WPHI + (2 * cw + (lane & 1)) * 512 + (4 * hh + ((lane >> 2) & 3)) * 32 + (2 * (g & 1) + ((lane >> 1) & 1)) * 8;
      const u32x4 f = ldfrag(a, 256);
      const bool own = (lane >> 2) & 1;
#pragma unroll
      for (int e = 0; e < 4; ++e) { wp0[e] = own ? 0u : f[e]; wp1[e] = own ? f[e] : 0u; }
    }
#define FEATURES(e_, dst)                                                                                           \
  {                                                                                                                 \
    u32x4 kp[2];                                                                                                    \
    _Pragma("unroll") for (int a = 0; a < 4; ++a) {                                                                \
      const f32x4 cc = lds16f(L_CS + (2 * (e_) + cw) * 128 + a * 32 + hh * 16);                                      \
      kp[a >> 1][2 * (a & 1)] = pk2v(fma2(lo2(cc), bc2(xm), (f32x2){acc[e_][4 * a], acc[e_][4 * a + 1]}));          \
      kp[a >> 1][2 * (a & 1) + 1] = pk2v(fma2(hi2(cc), bc2(xm), (f32x2){acc[e_][4 * a + 2], acc[e_][4 * a + 3]}));  \
    }                                                                                                               \
    f32x16 pl = mfmaL(wp0, kp[0], zero16);                                                                          \
    pl = mfmaL(wp1, kp[1], pl);                                                                                     \
    hedgehog16(pl, xr, dst);                                                                                        \
  }
    {
      const int s_ = 32 * rw + l32;
      const u32 key = (((s_ >> 1) & 3) ^ (((s_ >> 3) & 1) << 1)) * 16;
      ISS_Q(9, LQ_M + 16384);
      {
        const u32 va = LQ_V + (s_ >> 3) * 1024 + cw * 512 + (s_ & 7) * 64 + hh * 8;
#pragma unroll
        for (int a = 0; a < 4; ++a) {
          const f32x4 cc = lds16f(L_CS + (4 + cw) * 128 + a * 32 + hh * 16);
          sts8(va + ((a * 16) ^ key), (u32x2){pk2v(fma2(lo2(cc), bc2(xm), (f32x2){acc[2][4 * a], acc[2][4 * a + 1]}) * bc2(xr)),
                                              pk2v(fma2(hi2(cc), bc2(xm), (f32x2){acc[2][4 * a + 2], acc[2][4 * a + 3]}) * bc2(xr))});
        }
      }
      WAITL0();
      ISS_Q(10, LQ_A); ISS_Q(11, LQ_A + 16384);
      u32x4 ph[4];
      FEATURES(1, ph)
      const u32 pa = LQ_PHI + (s_ >> 3) * 2048 + (2 * cw + hh) * 512 + (s_ & 7) * 64;
#pragma unroll
      for (int a = 0; a < 4; ++a) sts16(pa + ((a * 16) ^ key), ph[a]);
    }
    WAITQ(mu[9]);
    READ_U(6, 0) READ_U(7, 16384) READ_U(8, LQ_M) READ_U(9, LQ_M + 16384)
    BARRIER();
    STAMP(2);
    {
      const u32 kx = (((lane >> 3) & 1) ^ ((g & 1) << 1)) * 16;
      const u32 pa = LQ_PHI + g * 2048 + (w >> 1) * 512 + ((lane >> 2) & 3) * 64 + (((2 * (w & 1) + ((lane >> 1) & 1)) * 16) ^ kx) + (lane & 1) * 8;
      const u32 va = LQ_V + g * 1024 + (w >> 2) * 512 + ((lane >> 2) & 3) * 64 + (((2 * ((w >> 1) & 1) + ((lane >> 1) & 1)) * 16) ^ kx) + (lane & 1) * 8;
      u32x4 pf[4], vf[4];
#pragma unroll
      for (int k = 0; k < 4; ++k) {
        pf[k] = cat(ldstr(pa + k * 8192), ldstr((pa ^ 32) + k * 8192 + 256));
        vf[k] = cat(ldstr(va + k * 4096), ldstr((va ^ 32) + k * 4096 + 256));
      }
      f32x4 kv = zero4, kz = zero4;
#pragma unroll
      for (int k = 0; k < 4; ++k) { kv = mfma32(pf[k], vf[k], kv); kz = mfma32(pf[k], ones4, kz); }
      const u32 so = WS_KP + (c * 16 + P) * KP_SZ;
      u32x4 g0 = {__float_as_uint(kv[0]), tlo, __float_as_uint(kv[1]), tlo}, g1 = {__float_as_uint(kv[2]), tlo, __float_as_uint(kv[3]), tlo};
      u32x4 g2 = {__float_as_uint((lane & 1) ? kz[2] : kz[0]), tlo, __float_as_uint((lane & 1) ? kz[3] : kz[1]), tlo};
      PIN(g0); PIN(g1); PIN(g2);
      asm volatile("s_nop 7" ::: "memory");
      VM(st16_sc1(g0, l16, sws, so + w * 2048));
      VM(st16_sc1(g1, l16, sws, so + w * 2048 + 1024));
      VM(st16_sc1(g2, g * 32 + (lane & 1) * 16 + ((i16 >> 1) << 26), sws, so + 16384 + w * 128));
    }
    STAMP(3);
    ISS_Q(12, LQ_M); ISS_Q(13, LQ_M + 16384);
    WAITQ(mu[11]);
    READ_U(10, LQ_A) READ_U(11, LQ_A + 16384)
    BARRIER();
    ISS_Q(14, LQ_A); ISS_Q(15, LQ_A + 16384);
    u32x4 phq[4];
    FEATURES(0, phq)
    PIN(phq[0]); PIN(phq[1]); PIN(phq[2]); PIN(phq[3]);
    STAMP(4);
    if (w >= 5) {
      const int id = 2 * P + (w == 7 ? (lane & 1) : w - 5), wq = id >> 2, gq = id & 3;
      const u32 vi0 = (4 * g) * KP_SZ + (w == 7 ? 16384 + wq * 128 + gq * 32 : wq * 2048 + (gq * 16 + i16) * 16);
      const u32 vi1 = vi0 + (w == 7 ? 16 : 1024);
      const u32 vo = (w == 7 ? (WS_KZ - WS_KF) + c * 512 + id * 16 + ((i16 >> 1) << 25) : c * 8192 + id * 256 + i16 * 16) + (g << 28);
      float sa[4];
#pragma nounroll
      for (int it = 0; it < SPIN_CAP; ++it) {
        u32x4 a[4][2];
#pragma unroll
        for (int o = 0; o < 4; ++o) {
          a[o][0] = ld16_sc1(vi0, sws, WS_KP + (c * 16 + o) * KP_SZ);
          a[o][1] = ld16_sc1(vi1, sws, WS_KP + (c * 16 + o) * KP_SZ);
        }
        WAITV(0);
        bool ok = true;
#pragma unroll
        for (int o = 0; o < 4; ++o) { PIN(a[o][0]); PIN(a[o][1]); ok = ok && tag_ok(a[o][0], tlo) && tag_ok(a[o][1], tlo); }
#pragma unroll
        for (int e = 0; e < 4; ++e)
          sa[e] = (__uint_as_float(a[0][e >> 1][2 * (e & 1)]) + __uint_as_float(a[1][e >> 1][2 * (e & 1)])) +
                  (__uint_as_float(a[2][e >> 1][2 * (e & 1)]) + __uint_as_float(a[3][e >> 1][2 * (e & 1)]));
        if (__all(ok)) break;
        __builtin_amdgcn_s_sleep(POLL_SLEEP);
      }
      red2g(sa[0], sa[1]);
      red2g(sa[2], sa[3]);
      st16_sc1((u32x4){pk2(sa[0], sa[1]), tlo, pk2(sa[2], sa[3]), tlo}, vo, sws, WS_KF);
    }
    {
      VWO();
#pragma unroll
      for (int e = 0; e < 7; ++e) DMA_WOUT(e)
    }
    gf = ld8_sc1((lane & 1) * 1024, sws, WS_FA + (16 * w + c2) * 128);
    WAITV(8);
    READ_U(12, LQ_M) READ_U(13, LQ_M + 16384) READ_U(14, LQ_A) READ_U(15, LQ_A + 16384)
    BARRIER();
#pragma unroll
    for (int a = 0; a < 4; ++a) sts16(L_WO1 + w * 4096 + a * 1024 + l16, phq[a]);
    WAITV(0);
    PIN(gf);
    if (!__all(gf[0] == tlo && gf[1] == thi)) poll_flags((lane & 1) * 1024, true, sws, WS_FA + (16 * w + c2) * 128, tlo, thi);
    STAMP(6);
    { const u32 so_ = zb0 + TILE(0, gn) * 65536; LOAD_B(0, so_) }
  }

  tlo = __builtin_amdgcn_readfirstlane(tlo);
  thi = __builtin_amdgcn_readfirstlane(thi);
  int w2 = w, c22 = c2, p2 = p, cc2 = c;
  asm volatile("" : "+s"(w2), "+s"(tlo), "+s"(thi), "+s"(c22), "+s"(p2), "+s"(cc2));
  {
  const int w = w2, rw = w & 3, cw = w >> 2, c2 = c22, p = p2, c = cc2, P = p >> 1;
  RELANE();
  { const u32 so_ = zb0 + TILE(1, gn) * 65536; LOAD_B(1, so_) }
  STAMP(9);
  const u32 redw = opq(L_RED + w * 4096 + lane * 16), redr = opq(L_RED + rw * 1024 + lane * 16);
  const u32 fd = WS_FD + (p * 16 + c2) * 128;
  const u32 f3s = WS_FB + (P * 8 + (c2 > 0 ? c2 - 1 : 0)) * 128;
  const u32 vw3 = ((lane >> 2) & 7) * 1024 + c2 * 128 + (lane >> 5) * 64;
  const rsrc_t swo = __builtin_amdgcn_make_buffer_rsrc((void*)wo, 0, 512 * 1024, 0x00020000);
#define DMA_WO(dst, half, e) dma16((dst) + w * 4096 + (e) * 1024, (vw3 + (lane & 3) * 16) ^ (((e) & 1) * 32), swo, ((w + 8 * (half)) * 4 + (e)) * 8192)
  u32x4 kfg, kzg;
  u32x2 z1r[6], hold;
  u32 bs;
  f32x4 sm0;
#define EMIT_H(jt, sm_)                                                                                             \
  {                                                                                                                 \
    const f32x2 bb = bc2(blo(bs));                                                                                  \
    const u32 h0 = pk2v((f32x2){blo(z1r[jt][0]), bhi(z1r[jt][0])} * (lo2(sm_) + bb));                               \
    const u32 h1 = pk2v((f32x2){blo(z1r[jt][1]), bhi(z1r[jt][1])} * (hi2(sm_) + bb));                               \
    const u32 hb_ = WS_HF + (((4 * p + rw) * 2 + ((jt) >> 1)) * 8 + c2) * 1024;                                     \
    if ((jt) == 0 || ((jt) == 2 && gn == 6)) hold = (u32x2){h0, h1};                                                \
    else if ((jt) == 1 || ((jt) == 3 && gn == 6)) st16_sc1((u32x4){hold[0], hold[1], h0, h1}, l16, sws, hb_);       \
    else if ((jt) == 2) st16_sc1((u32x4){h0, h1, 0u, 0u}, l16, sws, hb_);                                           \
    else st16_sc1((u32x4){h0, tlo, h1, tlo}, l16, sws, WS_HL + (((4 * p + rw) * 8 + c2) * 2 + (gn - 1 - (jt))) * 1024); \
  }
#pragma unroll
  for (int j = 0; j < 6; ++j) {
    if (j < 5 || gn == 6) {
      const int u = j & 1;
      if (j == 0 || j == 2) WAITV(8);
      if (j == 1) WAITV(9);
      if (j == 3) { WAITV(11); WAITV_IF(cw, 1, 10); WAITV_IF(t, 0, 9); WAITV_IF(2 * cw - t, 2, 8); }
      if (j == 4) { WAITV(12); WAITV_IF(t, 0, 9); WAITV_IF(2 * gn + t, 11, 4); }
      if (j == 5) { WAITV(5); WAITV_IF(cw, 1, 4); }
      STAMP(17 + 2 * j);
#pragma unroll
      for (int i = 0; i < 8; ++i) PIN(bfr[u][i]);
      {
        f32x4 a_[4];
#pragma unroll
        for (int rt = 0; rt < 4; ++rt) {
          a_[rt] = zero4;
#pragma unroll
          for (int i = 0; i < 8; ++i) a_[rt] = mfma32(bfr[u][i], af[rt][i], a_[rt]);
          SCHED();
          if (rt > 0) sts16f(redw + u * 32768 + (rt - 1) * 1024, a_[rt - 1]);
          SCHED();
        }
        if (j == 1 && t != 0) {
          PIN(kfg); PIN(kzg);
          poll_gran(kfg, l16, sws, WS_KF + c * 8192 + w * 1024, tlo);
          poll_gran(kzg, g * 16, sws, WS_KZ + c * 512 + w * 64, tlo);
          sts8(L_KFS + (w >> 1) * 1024 + i16 * 64 + (w & 1) * 32 + g * 8, (u32x2){kfg[0], kfg[2]});
          sts8(L_KFS + 4096 + w * 32 + g * 8, (u32x2){kzg[0], kzg[2]});
          sts8(L_KFS + 4352 + (lane & 7) * 8, (u32x2){0u, 0u});
        }
        sts16f(redw + u * 32768 + 3072, a_[3]);
      }
      BAR_IF(cw, 1, 63);
      STAMP(18 + 2 * j);
      if (w == 4) {
        if (j == 0 && t == 0) SET_FLAG(WS_FB + (P * 8 + c) * 128);
        if (j == 3) SET_FLAG(fd);
        if (j == 4 && t != 0) SET_FLAG(WS_FC + (P * 8 + c) * 128);
        if ((j == 4 && gn == 5) || j == 5) SET_FLAG(fd + 1024);
      }
      if (j == 0 && t != 0) {
        kfg = ld16_sc1(l16, sws, WS_KF + c * 8192 + w * 1024);
        kzg = ld16_sc1(g * 16, sws, WS_KZ + c * 512 + w * 64);
      }
      u32x4 of[2];
      if (j == 2 && t != 0) {
        const int l32 = lane & 31, hh = lane >> 5;
        const bool own = ((lane >> 2) & 1) == hh;
        const u32 ka = own ? L_KFS + (2 * cw + hh) * 1024 + ((l32 & 3) + 4 * (l32 >> 3)) * 64 : L_KFS + 4352;
        const u32 zaa = own ? L_KFS + 4096 + (2 * cw + hh) * 64 : L_KFS + 4352;
        const u32 qa = L_WO1 + w * 4096 + l16;
        const f32x16 zero16 = {0, 0, 0, 0, 0, 0, 0, 0, 0, 0, 0, 0, 0, 0, 0, 0};
        f32x16 den = zero16, num = zero16;
#pragma unroll
        for (int a = 0; a < 4; ++a) {
          const u32x4 pq_ = lds16(qa + a * 1024);
          den = mfmaL(lds16(zaa + a * 16), pq_, den);
          num = mfmaL(lds16(ka + a * 16), pq_, num);
        }
        const float inv = __builtin_amdgcn_rcpf(den[0] + 1e-6f);
#pragma unroll
        for (int a = 0; a < 2; ++a) {
          of[a] = (u32x4){pk2(num[8 * a] * inv, num[8 * a + 1] * inv), pk2(num[8 * a + 2] * inv, num[8 * a + 3] * inv),
                          pk2(num[8 * a + 4] * inv, num[8 * a + 5] * inv), pk2(num[8 * a + 6] * inv, num[8 * a + 7] * inv)};
          PIN(of[a]);
        }
        STAMP(5);
      }
      if (j == 1) {
        if (cw == 0) {
          WAITV(0);
          PIN(gf);
          if (!__all(gf[0] == tlo && gf[1] == thi)) poll_flags((lane & 1) * 128, true, sws, f3s, tlo, thi);
          rsrc_t sb = __builtin_amdgcn_make_buffer_rsrc((void*)bsp, 0, 4096, 0x00020000);
          bs = ld2(i16 * 2, sb, (64 * p + 16 * rw) * 2);
        }
      }
      if ((j == 1 || j == 2) && cw == 0) {
#pragma unroll
        for (int v = 3 * j - 3; v < 3 * j; ++v)
          z1r[v] = ld8_sc1(l16, sws, WS_Z1 + ((2 * p + (rw >> 1)) * 43 + gs + TILE(min(v, gn - 1), gn)) * 1024 + (rw & 1) * 8);
      }
      const u32 ra_ = redr + ((j + 1) & 1) * 32768;
      f32x4 sm, r_[2];
#pragma unroll
      for (int i = 0; i < 8; ++i) {
        if (j >= 1 && (i & 1) == 0 && cw == 0) {
          if (i == 2) sm = r_[0] + r_[1];
          if (i > 2) sm += r_[0] + r_[1];
          r_[0] = lds16f(ra_ + i * 4096); r_[1] = lds16f(ra_ + (i + 1) * 4096);
        }
        SCHED();
        if (j < 3) bfr[u][i] = ld16_b(l16, sws, zb0 + TILE(j + 2, gn) * 65536 + i * 1024);
        else if (j == 3) ld16_b_if6(bfr[1][i], gn, l16, sws, zb0 + TILE(5, gn) * 65536 + i * 1024);
        SCHED();
      }
      if (j == 0) gf = ld8_sc1((lane & 1) * 128, sws, f3s);
      if (j == 2 && t != 0) {
        const u32 vo = ((lane >> 4) & 1) * 16384 + (lane >> 5) * 512 + i16 * 16;
        st16_sc1(of[0], vo, sws, WS_OF + ((8 * P + 2 * rw) * 16 + 2 * c + cw) * 1024);
        st16_sc1(of[1], vo, sws, WS_OF + ((8 * P + 2 * rw) * 16 + 2 * c + cw) * 1024 + 256);
      }
      if (j == 2) { PIN(bs); _Pragma("unroll") for (int v = 0; v < 3; ++v) PIN(z1r[v]); }
      if (j == 3) { _Pragma("unroll") for (int v = 3; v < 6; ++v) PIN(z1r[v]); }
      if (j >= 1 && cw == 0) {
        sm += r_[0] + r_[1];
        if (j == 1) sm0 = sm;
        if (j == 2) EMIT_H(0, sm0)
        if (j >= 2) EMIT_H(j >= 2 ? j - 1 : 0, sm)
      }
      if (j == 3) {
        if (t == 0) {
          VWO();
#pragma unroll
          for (int e = 2; e < 7; ++e) DMA_WOUT(e)
        }
#pragma unroll
        for (int e = 0; e < 4; ++e) DMA_WO(L_WO1, 0, e);
      }
      if (j == 3 && cw == 0) { WAITV(12); WAITV_IF(t, 0, 10); WAITV_IF(2 * gn + t, 11, 5); }
      if (j == 5) WAITV(1);
      BAR_IF(cw, 0, 63);
    }
  }
  {
    STAMP(10);
    const u32 bufA = L_RED + (gn & 1) * 32768, bufB = L_RED + 32768 - (gn & 1) * 32768;
    const u32 fv = (lane & 16) ? (P * 8 + (lane & 7)) * 128 : (WS_FD - WS_FC) + (p * 16 + ((i16 & 14) | cw)) * 128;
    gf = ld8_sc1(fv, sws, WS_FC);
    if (cw == 0) {
      const u32 ra_ = redr + (bufB - L_RED);
      const f32x4 sm = ((lds16f(ra_) + lds16f(ra_ + 4096)) + (lds16f(ra_ + 8192) + lds16f(ra_ + 12288))) +
           ((lds16f(ra_ + 16384) + lds16f(ra_ + 20480)) + (lds16f(ra_ + 24576) + lds16f(ra_ + 28672)));
      if (gn == 6) EMIT_H(5, sm) else EMIT_H(4, sm)
    }
    RELANE();
#pragma unroll
    for (int e = 0; e < 4; ++e) DMA_WO(bufA, 1, e);
    WAITV(5); WAITV_IF(cw, 1, 4);
    PIN(gf);
    const bool okf = gf[0] == tlo && gf[1] == thi;
    const bool n0 = (lane & 24) == 0;
    const bool all1 = __all(okf);
    if (!__all(!n0 || okf)) poll_flags(fv, n0, sws, WS_FC, tlo, thi);
    u32x4 hf[8], of[8];
#pragma unroll
    for (int e = 0; e < 4; ++e) hf[e] = ld16_b(l16, sws, WS_HF + ((4 * p + rw) * 16 + 2 * e + cw) * 1024);
    BARRIER();
    {
      VWO();
#pragma unroll
      for (int e = 7; e < 11; ++e) DMA_WOUT_AT(bufB + w * 4096, e)
    }
    if (!all1) poll_flags(fv, true, sws, WS_FC, tlo, thi);
#pragma unroll
    for (int e = 4; e < 8; ++e) hf[e] = ld16_b(l16, sws, WS_HF + ((4 * p + rw) * 16 + 2 * e + cw) * 1024);
    const u32 qb = ((lane >> 2) & 3) * 64 + ((((g & 1) << 1) | ((lane >> 1) & 1)) * 16) + (lane & 1) * 8;
    f32x4 a3[4];
#pragma unroll
    for (int ct = 0; ct < 4; ++ct) a3[ct] = zero4;
    u32x4 wf[3][4], hl[4][2];
#define RDW2(dst, lo0, hi0, offl, offh) _Pragma("unroll") for (int ct = 0; ct < 4; ++ct) dst[ct] = cat(ldstr(((lo0) ^ ((ct & 1) * 32)) + (offl) + (ct >> 1) * 512), ldstr(((hi0) ^ ((ct & 1) * 32)) + (offh) + (ct >> 1) * 512))
#define MM3(src, frag) _Pragma("unroll") for (int ct = 0; ct < 4; ++ct) a3[ct] = mfma32(src[ct], frag, a3[ct])
    const u32 lb = WS_HL + ((4 * p + rw) * 8 + cw) * 2048;
    const rsrc_t sxo = __builtin_amdgcn_make_buffer_rsrc((void*)(x + (size_t)p * 65536), 0, 65536, 0x00020000);
    const u32 w0 = opq(L_WOUT + qb + (g >> 1) * 1024 + (g & 1) * 256);
    const u32 wa = w0 + cw * 4096, wb = w0 + cw * 2048;
    const u32 oq = qb + g * 1024 + cw * 4096;
    const u32 oa = opq(L_WO1 + oq), ob = opq(bufA + oq);
    u32x4 xres;
    STAMP(11);
#define RDK(dst, k)                                                                                                 \
  if ((k) < 4) { RDW2(dst, wa, wa, (k) * 8192, (k) * 8192 + 2048); }                                                \
  else if ((k) < 8) { RDW2(dst, wb, w0, 32768 + ((k) - 4) * 4096, 49152 + ((k) - 4) * 2048); }                      \
  else if ((k) < 12) { RDW2(dst, oa, oa, ((k) - 8) * 8192, ((k) - 8) * 8192 + 256); }                               \
  else { RDW2(dst, ob, ob, ((k) - 12) * 8192, ((k) - 12) * 8192 + 256); }
    RDK(wf[0], 0)
    RDK(wf[1], 1)
#pragma unroll
    for (int k = 0; k < 16; ++k) {
      if (k == 0) WAITV(8);
      if (k == 4) WAITV(8);
      if (k == 8) WAITV(9);
      if (k == 10) { BARRIER(); STAMP(12); }
      if (k + 2 < 16) { RDK(wf[(k + 2) % 3], k + 2) }
      SCHED();
      if (k < 8) { PIN(hf[k]); MM3(wf[k % 3], hf[k]); }
      else { PIN(of[k - 8]); MM3(wf[k % 3], of[k - 8]); }
      SCHED();
      if (k < 4) {
        of[2 * k] = ld16_b(l16, sws, WS_OF + ((4 * p + rw) * 16 + 4 * k + cw) * 1024);
        of[2 * k + 1] = ld16_b(l16, sws, WS_OF + ((4 * p + rw) * 16 + 4 * k + 2 + cw) * 1024);
      } else if (k < 8) {
        if (k == 4) xres = ld16((tid >> 3) * 1024 + (tid & 7) * 16, sxo, c2 * 128);
        hl[k - 4][0] = ld16_sc1(l16, sws, lb + (k - 4) * 4096);
        hl[k - 4][1] = ld16_sc1(l16, sws, lb + (k - 4) * 4096 + 1024);
      }
    }
    STAMP(13);
    {
      const u32 wl_ = opq(bufB + (w0 - L_WOUT) + cw * 4096);
      u32x4 wg[4][4];
#pragma unroll
      for (int e = 0; e < 4; ++e) RDW2(wg[e], wl_, wl_, e * 8192, e * 8192 + 2048);
      WAITV(0);
      PIN(xres);
#pragma unroll
      for (int e = 0; e < 4; ++e) {
        PIN(hl[e][0]); PIN(hl[e][1]);
        poll_gran(hl[e][0], l16, sws, lb + e * 4096, tlo);
        poll_gran(hl[e][1], l16, sws, lb + e * 4096 + 1024, tlo);
        MM3(wg[e], ((u32x4){hl[e][0][0], hl[e][0][2], hl[e][1][0], hl[e][1][2]}));
      }
    }
    STAMP(14);
    const u32 fin = L_WOUT - 2048;
#pragma unroll
    for (int ct = 0; ct < 4; ++ct) sts16f(fin + 2048 + cw * 16384 + (16 * rw + i16) * 256 + (((4 * ct + g) ^ i16) * 16), a3[ct]);
    BARRIER();
    {
      const int row = tid >> 3;
      const u32 a = fin + 2048 + row * 256 + (((2 * (tid & 7)) ^ (row & 15)) * 16);
      const f32x4 s0 = lds16f(a) + lds16f(a + 16384), s1 = lds16f(a ^ 16) + lds16f((a ^ 16) + 16384);
      u32x4 o = {pk2(blo(xres[0]) + s0[0], bhi(xres[0]) + s0[1]), pk2(blo(xres[1]) + s0[2], bhi(xres[1]) + s0[3]),
                 pk2(blo(xres[2]) + s1[0], bhi(xres[2]) + s1[1]), pk2(blo(xres[3]) + s1[2], bhi(xres[3]) + s1[3])};
      *(u32x4*)(out + (size_t)(64 * p + row) * 1024 + c2 * 128 + (tid & 7) * 16) = o;
    }
  }
  }
  if (b == 0 && w == 0 && l16 == 0) *(u64*)(ws + WS_EP) = ((u64)thi << 32) | tlo;
  STAMP(15);
  STAMPC(31);
}

extern "C" void kernel_launch(void* const* d_in, const int* in_sizes, int n_in, void* d_out, int out_size, void* d_ws, size_t ws_size,
                              hipStream_t stream) {
  fused_kernel<<<256, 512, 0, stream>>>((const char*)d_in[0], (const char*)d_in[1], (const char*)d_in[2], (const char*)d_in[3],
                                        (const char*)d_in[5], (const char*)d_in[6], (const char*)d_in[7], (const char*)d_in[8], (char*)d_out,
                                        (char*)d_ws);
}
```

```cpp
#include <hip/hip_runtime.h>
#include <hip/hip_bf16.h>

typedef unsigned int u32;
typedef unsigned long long u64;
typedef short s16x4 __attribute__((ext_vector_type(4)));
typedef __bf16 bf16x2 __attribute__((ext_vector_type(2)));
typedef __bf16 bf16x8 __attribute__((ext_vector_type(8)));
typedef float f32x16 __attribute__((ext_vector_type(16)));
typedef float f32x4 __attribute__((ext_vector_type(4)));
typedef float f32x2 __attribute__((ext_vector_type(2)));
typedef u32 u32x4 __attribute__((ext_vector_type(4)));
typedef u32 u32x2 __attribute__((ext_vector_type(2)));
typedef __amdgpu_buffer_rsrc_t rsrc_t;
#define LDSP __attribute__((address_space(3)))
#define DI __device__ __forceinline__

#define STAMPS 0

constexpr u32 WS_EP = 0;
constexpr u32 WS_FA = 4096;
constexpr u32 WS_FB = WS_FA + 16384;
constexpr u32 WS_FC = WS_FB + 16384;
constexpr u32 WS_FD = WS_FC + 16384;
constexpr u32 WS_XG = WS_FD + 65536;
constexpr u32 WS_SG = WS_XG + 32768;
constexpr u32 WS_ZB = WS_SG + 524288;
constexpr u32 WS_Z1 = WS_ZB + 43 * 65536;
constexpr u32 KP_SZ = 17408;
constexpr u32 WS_KP = WS_Z1 + 64 * 43 * 1024;
constexpr u32 WS_KF = WS_KP + 128 * KP_SZ;
constexpr u32 WS_KZ = WS_KF + 65536;
constexpr u32 WS_OF = WS_KZ + 4096;
constexpr u32 WS_HF = WS_OF + 128 * 16384;
constexpr u32 WS_HL = WS_HF + 128 * 16 * 1024;
constexpr u32 WS_ST = WS_HL + 128 * 16 * 1024;
constexpr u32 WS_END = WS_ST + 256 * 8 * 32 * 8;

constexpr u32 LZ_W = 65536;
constexpr u32 LZ_U = 139264;
constexpr u32 LQ_M = 122880;
constexpr u32 LQ_A = 81920;
constexpr u32 LQ_PHI = 49152;
constexpr u32 LQ_V = 32768;
constexpr u32 L_TAB = 155648;
constexpr u32 L_XS = L_TAB;
constexpr u32 L_CS = L_TAB + 1024;
constexpr u32 L_ZST = L_TAB + 3072;
constexpr u32 L_WPHI = L_TAB + 3072;
constexpr u32 L_KFS = L_TAB + 2048;
constexpr u32 L_JUNK = L_TAB + 7168;
constexpr u32 L_WOUT = 0;
constexpr u32 L_RED = 57344;
constexpr u32 L_WO1 = 122880;

constexpr u64 Z2S = 0x26211B16110B0500ull;
constexpr u64 Z1S = 0x26211C16110C0600ull;
DI int grp_start(u64 tab, int c) { return c >= 8 ? 43 : (int)((tab >> (8 * c)) & 0xFF); }

#define SCHED() __builtin_amdgcn_sched_barrier(0)
#define TIC()
#define TOC(acc)
#define WAITV(n) do { TIC(); asm volatile("s_waitcnt vmcnt(%0)" ::"n"(n) : "memory"); TOC(acv); SCHED(); } while (0)
#define BARRIER() do { SCHED(); TIC(); asm volatile("s_waitcnt lgkmcnt(0)\n\ts_barrier" ::: "memory"); TOC(acb); SCHED(); } while (0)
#define WAITV_IF(g_, v, n) do { TIC(); asm volatile("s_cmp_lg_u32 %0, %1\n\ts_cbranch_scc1 1f\n\ts_waitcnt vmcnt(%2)\n1:" ::"s"(g_), "n"(v), "n"(n) : "scc", "memory"); TOC(acv); SCHED(); } while (0)
#define BAR_IF(g_, v, n) do { SCHED(); asm volatile("s_cmp_lg_u32 %0, %1\n\ts_cbranch_scc1 1f\n\ts_waitcnt vmcnt(%2) lgkmcnt(0)\n\ts_barrier\n1:" ::"s"(g_), "n"(v), "n"(n) : "scc", "memory"); SCHED(); } while (0)
#define WAITL0() asm volatile("s_waitcnt lgkmcnt(0)" ::: "memory")
#define PIN(x) asm volatile("" : "+v"(x))

DI void dma16(u32 lds, u32 voff, rsrc_t srd, u32 soff) {
  asm volatile("s_mov_b32 m0, %0\n\ts_nop 0\n\tbuffer_load_dwordx4 %1, %2, %3 offen lds" ::"s"(lds), "v"(voff), "s"(srd), "s"(soff) : "memory");
}
DI void touch(u32 lds, u32 voff, rsrc_t srd, u32 soff) {
  asm volatile("s_mov_b32 m0, %0\n\ts_nop 0\n\tbuffer_load_dword %1, %2, %3 offen lds" ::"s"(lds), "v"(voff), "s"(srd), "s"(soff) : "memory");
}
DI u32x4 ld16_sc1(u32 voff, rsrc_t srd, u32 soff) {
  u32x4 r;
  asm volatile("buffer_load_dwordx4 %0, %1, %2, %3 offen sc1" : "=v"(r) : "v"(voff), "s"(srd), "s"(soff) : "memory");
  return r;
}
template <int OFF> DI u32x4 ld16_sc1o(u32 voff, rsrc_t srd, u32 soff) {
  u32x4 r;
  asm volatile("buffer_load_dwordx4 %0, %1, %2, %3 offen offset:%4 sc1" : "=v"(r) : "v"(voff), "s"(srd), "s"(soff), "n"(OFF) : "memory");
  return r;
}
#define BPOL " sc1"
DI u32x4 ld16_b(u32 voff, rsrc_t srd, u32 soff) {
  u32x4 r;
  asm volatile("buffer_load_dwordx4 %0, %1, %2, %3 offen" BPOL : "=v"(r) : "v"(voff), "s"(srd), "s"(soff) : "memory");
  return r;
}
template <int OFF> DI u32x4 ld16_bo(u32 voff, rsrc_t srd, u32 soff) {
  u32x4 r;
  asm volatile("buffer_load_dwordx4 %0, %1, %2, %3 offen offset:%4" BPOL : "=v"(r) : "v"(voff), "s"(srd), "s"(soff), "n"(OFF) : "memory");
  return r;
}
DI void ld16_b_if6(u32x4& r, int gn, u32 voff, rsrc_t srd, u32 soff) {
  asm volatile("s_cmp_lg_u32 %4, 6\n\ts_cbranch_scc1 1f\n\tbuffer_load_dwordx4 %0, %1, %2, %3 offen" BPOL "\n1:" : "+v"(r) : "v"(voff), "s"(srd), "s"(soff), "s"(gn) : "scc", "memory");
}
DI u32x4 ld16(u32 voff, rsrc_t srd, u32 soff) {
  u32x4 r;
  asm volatile("buffer_load_dwordx4 %0, %1, %2, %3 offen" : "=v"(r) : "v"(voff), "s"(srd), "s"(soff) : "memory");
  return r;
}
DI u32x2 ld8_sc1(u32 voff, rsrc_t srd, u32 soff) {
  u32x2 r;
  asm volatile("buffer_load_dwordx2 %0, %1, %2, %3 offen sc1" : "=v"(r) : "v"(voff), "s"(srd), "s"(soff) : "memory");
  return r;
}
DI u32 ld2(u32 voff, rsrc_t srd, u32 soff) {
  u32 r;
  asm volatile("buffer_load_ushort %0, %1, %2, %3 offen" : "=v"(r) : "v"(voff), "s"(srd), "s"(soff) : "memory");
  return r;
}
DI void st16_sc1(u32x4 v, u32 voff, rsrc_t srd, u32 soff) {
  asm volatile("buffer_store_dwordx4 %0, %1, %2, %3 offen sc1\n\ts_nop 1" ::"v"(v), "v"(voff), "s"(srd), "s"(soff) : "memory");
}
DI void st8_sc1(u32x2 v, u32 voff, rsrc_t srd, u32 soff) {
  asm volatile("buffer_store_dwordx2 %0, %1, %2, %3 offen sc1" ::"v"(v), "v"(voff), "s"(srd), "s"(soff) : "memory");
}

DI u32x4 lds16(u32 a) { return *(const LDSP u32x4*)(size_t)a; }
DI u32x2 lds8(u32 a) { return *(const volatile LDSP u32x2*)(size_t)a; }
DI float lds4f(u32 a) { return *(const LDSP float*)(size_t)a; }
DI f32x4 lds16f(u32 a) { return *(const LDSP f32x4*)(size_t)a; }
DI void sts16f(u32 a, f32x4 v) { *(LDSP f32x4*)(size_t)a = v; }
DI void sts16(u32 a, u32x4 v) { *(LDSP u32x4*)(size_t)a = v; }
DI void sts8(u32 a, u32x2 v) { *(volatile LDSP u32x2*)(size_t)a = v; }
DI void sts4f(u32 a, float v) { *(LDSP float*)(size_t)a = v; }
DI u32x2 ldstr(u32 a) {
  return __builtin_bit_cast(u32x2, __builtin_amdgcn_ds_read_tr16_b64_v4i16((LDSP s16x4*)(size_t)a)); }
DI u32x4 cat(u32x2 lo, u32x2 hi) { return (u32x4){lo[0], lo[1], hi[0], hi[1]}; }
DI u32x4 ldfrag(u32 a, u32 d) { u32x2 lo = ldstr(a), hi = ldstr(a + d); return cat(lo, hi); }

DI f32x4 mfma32(u32x4 a, u32x4 b, f32x4 c) {
  return __builtin_amdgcn_mfma_f32_16x16x32_bf16(__builtin_bit_cast(bf16x8, a), __builtin_bit_cast(bf16x8, b), c, 0, 0, 0);
}
DI f32x16 mfmaL(u32x4 a, u32x4 b, f32x16 c) {
  return __builtin_amdgcn_mfma_f32_32x32x16_bf16(__builtin_bit_cast(bf16x8, a), __builtin_bit_cast(bf16x8, b), c, 0, 0, 0);
}
DI u32 pk2(float a, float b) { bf16x2 v = {(__bf16)a, (__bf16)b}; return __builtin_bit_cast(u32, v); }
DI float blo(u32 u) { return __uint_as_float(u << 16); }
DI float bhi(u32 u) { return __uint_as_float(u & 0xffff0000u); }
DI float rbf(float a) { return blo(pk2(a, a)); }
DI float dot2(u32 a, u32 b, float c) {
  return __builtin_amdgcn_fdot2_f32_bf16(__builtin_bit_cast(bf16x2, a), __builtin_bit_cast(bf16x2, b), c, false);
}
DI f32x2 fma2(f32x2 a, f32x2 b, f32x2 c) { return __builtin_elementwise_fma(a, b, c); }
DI f32x2 bc2(float a) { return (f32x2){a, a}; }
DI f32x2 lo2(f32x4 v) { return (f32x2){v[0], v[1]}; }
DI f32x2 hi2(f32x4 v) { return (f32x2){v[2], v[3]}; }
DI u32 pk2v(f32x2 v) { return pk2(v[0], v[1]); }
DI f32x2 gelu2(f32x2 x) {
  f32x2 u = fma2(x * x, bc2(-0.10294324f), bc2(-2.3022082f)) * x;
  f32x2 e = {__builtin_amdgcn_exp2f(u[0]), __builtin_amdgcn_exp2f(u[1])};
  e = e + bc2(1.0f);
  f32x2 r = {__builtin_amdgcn_rcpf(e[0]), __builtin_amdgcn_rcpf(e[1])};
  return x * r;
}
DI void red2g(float& a, float& b) {
  auto s = __builtin_amdgcn_permlane32_swap(__float_as_uint(a), __float_as_uint(b), false, false);
  float c = __uint_as_float(s[0]) + __uint_as_float(s[1]);
  auto s2 = __builtin_amdgcn_permlane16_swap(__float_as_uint(c), __float_as_uint(c), false, false);
  float d = __uint_as_float(s2[0]) + __uint_as_float(s2[1]);
  auto s3 = __builtin_amdgcn_permlane32_swap(__float_as_uint(d), __float_as_uint(d), false, false);
  a = __uint_as_float(s3[0]);
  b = __uint_as_float(s3[1]);
}
template <int CTRL> DI float dpp(float v) {
  return __uint_as_float(__builtin_amdgcn_update_dpp(0, __float_as_uint(v), CTRL, 0xF, 0xF, true));
}
DI void hedgehog16(const f32x16& p, float s, u32x4 (&o)[4]) {
  float mx = fmaxf(p[0], p[1]), mn = fminf(p[0], p[1]);
#pragma unroll
  for (int i = 2; i < 16; i += 2) { mx = fmaxf(fmaxf(mx, p[i]), p[i + 1]); mn = fminf(fminf(mn, p[i]), p[i + 1]); }
  const float c = s * 1.44269504f;
  const float a = -mx * c, b = mn * c;
  f32x2 ep[8], en[8];
#pragma unroll
  for (int i = 0; i < 8; ++i) {
    const f32x2 v = {p[2 * i], p[2 * i + 1]};
    const f32x2 u = fma2(v, bc2(c), bc2(a)), w = fma2(v, bc2(-c), bc2(b));
    ep[i] = (f32x2){__builtin_amdgcn_exp2f(u[0]), __builtin_amdgcn_exp2f(u[1])};
    en[i] = (f32x2){__builtin_amdgcn_exp2f(w[0]), __builtin_amdgcn_exp2f(w[1])};
  }
  const f32x2 sp = ((ep[0] + ep[1]) + (ep[2] + ep[3])) + ((ep[4] + ep[5]) + (ep[6] + ep[7]));
  const f32x2 sn = ((en[0] + en[1]) + (en[2] + en[3])) + ((en[4] + en[5]) + (en[6] + en[7]));
  const float ip = __builtin_amdgcn_rcpf(sp[0] + sp[1]), in = __builtin_amdgcn_rcpf(sn[0] + sn[1]);
#pragma unroll
  for (int i = 0; i < 8; ++i) {
    o[i >> 2][i & 3] = pk2v(ep[i] * bc2(ip));
    o[2 + (i >> 2)][i & 3] = pk2v(en[i] * bc2(in));
  }
}
DI void ln_fin(float s, float q, float invn, float& r, float& m) {
  m = s * invn;
  float v = fmaxf(q * invn - m * m, 0.0f);
  float sd = rbf(__builtin_amdgcn_sqrtf(rbf(rbf(v) + 1e-5f)));
  r = __builtin_amdgcn_rcpf(sd);
  m = rbf(m);
}

#define POLL_SLEEP 6
constexpr int SPIN_CAP = 1 << 17;
DI void poll_flags(u32 voff, bool act, rsrc_t srd, u32 soff, u32 tlo, u32 thi) {
#pragma nounroll
  for (int it = 0; it < SPIN_CAP; ++it) {
    u32x2 v;
    asm volatile("buffer_load_dwordx2 %0, %1, %2, %3 offen sc1\n\ts_waitcnt vmcnt(0)" : "=&v"(v) : "v"(voff), "s"(srd), "s"(soff) : "memory");
    bool ok = !act || (v[0] == tlo && v[1] == thi);
    if (__all(ok)) break;
    __builtin_amdgcn_s_sleep(POLL_SLEEP);
  }
}
DI bool tag_ok(u32x4 q, u32 t) { return q[1] == t && q[3] == t; }
DI void poll_gran(u32x4& q, u32 voff, rsrc_t srd, u32 soff, u32 tlo) {
#pragma nounroll
  for (int it = 0; it < SPIN_CAP; ++it) {
    if (__all(tag_ok(q, tlo))) break;
    __builtin_amdgcn_s_sleep(POLL_SLEEP);
    asm volatile("buffer_load_dwordx4 %0, %1, %2, %3 offen sc1\n\ts_waitcnt vmcnt(0)" : "=&v"(q) : "v"(voff), "s"(srd), "s"(soff) : "memory");
  }
}
DI u32 opq(u32 v) { asm volatile("" : "+v"(v)); return v; }

#define STAMP(i)
#define STAMPC(i)

#define GQ_N 3
#define PREFETCH 0
#define CODE_PF 44000
#define VM(x) do { x; ++vq; } while (0)
#define WAITQ(mark) WAITV(vq - (mark))

__global__ __launch_bounds__(512, 2) void fused_kernel(const char* __restrict__ x, const char* __restrict__ wqkv, const char* __restrict__ wphi,
                                                        const char* __restrict__ wo, const char* __restrict__ win, const char* __restrict__ wsp,
                                                        const char* __restrict__ bsp, const char* __restrict__ wout, char* __restrict__ out, char* ws) {
  __shared__ __attribute__((aligned(1024))) char smem[163840];
  const int tid = threadIdx.x, lane = tid & 63;
  const int w = __builtin_amdgcn_readfirstlane(tid >> 6);
  const int rw = w & 3, cw = w >> 2, i16 = lane & 15, g = lane >> 4;
  const int b = blockIdx.x, xcd = b & 7, r = b >> 3;
  const int Pl = r >> 3, t = (r >> 2) & 1, cl = r & 3;
  const int P = (xcd >> 1) * 4 + Pl, c = (xcd & 1) * 4 + cl;
  const int p = 2 * P + (cl & 1);
  const int c2 = (xcd & 1) * 4 + (t ? 1 + (cl >> 1) : 3 * (cl >> 1));
  const int rot = 2 * ((Pl + cl) & 3);
  u32 acv = 0, acb = 0; (void)acv; (void)acb;
  asm volatile("" ::"v"((u32)(size_t)(LDSP char*)smem));
  STAMP(0);
  STAMPC(30);

  {
    u64 pc;
    asm volatile("s_getpc_b64 %0" : "=s"(pc));
    if (w == 7) touch(L_JUNK, lane * 128, __builtin_amdgcn_make_buffer_rsrc((void*)(pc & ~255ull), 0, CODE_PF, 0x00020000), (r & 7) * 8192);
  }
  u64 ep;
  asm volatile("s_load_dwordx2 %0, %1, 0x0" : "=s"(ep) : "s"(ws) : "memory");
  const rsrc_t sx = __builtin_amdgcn_make_buffer_rsrc((void*)(x + (size_t)P * 131072), 0, 131072, 0x00020000);
  const u32 l16 = opq(lane * 16);
  const u32 vx0 = (16 * w + (lane >> 3)) * 1024 + (((lane & 7) ^ g) * 16);
  const u32 vx1 = (vx0 + 8192) ^ 64;
#define DMA_X(dst, n, e) dma16((dst) + w * 2048 + (e) * 1024, (e) ? vx1 : vx0, sx, (((n) + rot) & 7) * 128)
  DMA_X(0, 0, 0);
  DMA_X(0, 0, 1);
  const rsrc_t sa = __builtin_amdgcn_make_buffer_rsrc((void*)(wsp + (size_t)p * 262144), 0, 262144, 0x00020000);
  const rsrc_t sws = __builtin_amdgcn_make_buffer_rsrc((void*)ws, 0, WS_END, 0x00020000);
  const u32 vA0 = (lane >> 3) * 4096 + (((lane & 7) ^ g) * 16);
  const u32 vA1 = (vA0 + 32768) ^ 64;
#define DMA_U(u, base, e) dma16((base) + w * 2048 + (e) * 1024, (e) ? vA1 : vA0, sa, w * 512 + (((u) >> 1) & 3) * 128 + (((u) >> 3) * 32 + ((u) & 1) * 16) * 4096)
  const f32x4 zero4 = {0, 0, 0, 0};
  u32x4 ones4 = {0x3F803F80u, 0x3F803F80u, 0x3F803F80u, 0x3F803F80u};
  u32 tlo, thi;
  u32x4 af[4][8];
  u32x4 bfr[2][8];
  u32x2 gf;
  const u32 ard = w * 2048 + i16 * 128 + (((g >> 1) ^ (i16 >> 1)) * 16) + (g & 1) * 8;
#define READ_U(u, base)                                                                                             \
  {                                                                                                                 \
    const u32 ab_ = opq(ard + (base));                                                                              \
    _Pragma("unroll") for (int kk = 0; kk < 2; ++kk)                                                               \
      af[2 * ((u) >> 3) + ((u) & 1)][((u) & 6) + kk] = cat(lds8(ab_ ^ (64 * kk)), lds8(ab_ ^ (64 * kk + 32)));      \
  }
#define USE_EPOCH() { asm volatile("" : "+s"(ep)); ep += 1; tlo = (u32)ep; thi = (u32)(ep >> 32); PIN(ones4); }
#define EARLY_STATS(mark)                                                                                           \
  {                                                                                                                 \
    WAITQ(mark); PIN(xrow);                                                                                         \
    float s_ = 0, q_ = 0;                                                                                           \
    _Pragma("unroll") for (int e = 0; e < 4; ++e) { s_ = dot2(xrow[e], ones4[0], s_); q_ = dot2(xrow[e], xrow[e], q_); } \
    s_ += dpp<0x128>(s_); q_ += dpp<0x128>(q_);                                                                     \
    s_ += dpp<0x124>(s_); q_ += dpp<0x124>(q_);                                                                     \
    s_ += dpp<0x4E>(s_); q_ += dpp<0x4E>(q_);                                                                       \
    s_ += dpp<0xB1>(s_); q_ += dpp<0xB1>(q_);                                                                       \
    red2g(s_, q_);                                                                                                  \
    float r_, m_;                                                                                                   \
    ln_fin(s_, q_, 1.0f / 512, r_, m_);                                                                             \
    VM(st16_sc1((u32x4){__float_as_uint(r_), tlo, __float_as_uint(m_), tlo}, l16 << 22, sws, WS_XG + (P * 128 + (t * 8 + c) * 8 + w) * 16)); \
  }
#define GATHER_XS()                                                                                                 \
  WAITQ(mxg); PIN(xg);                                                                                              \
  poll_gran(xg, i16 * 16, sws, WS_XG + (P * 128 + 16 * w) * 16, tlo);                                               \
  sts4f(L_XS + (16 * w + i16) * 4, __uint_as_float(xg[0]));                                                         \
  sts4f(L_XS + 512 + (16 * w + i16) * 4, __uint_as_float(xg[2]));
#define RELANE() const int lane = opq(l16) >> 4, i16 = lane & 15, g = lane >> 4, tid = w * 64 + lane; (void)tid; (void)g; (void)i16
#define SET_FLAG(off) st8_sc1((u32x2){tlo, thi}, l16 << 22, sws, (off))
  const int gs = grp_start(Z2S, c2), gn = grp_start(Z2S, c2 + 1) - gs;
  const int trot = (p & 7) >= 5 ? (p & 7) - 5 : (p & 7);
#define TILE(j, n_) (((j) + trot) >= (n_) ? (j) + trot - (n_) : (j) + trot)
  const u32 zb0 = WS_ZB + (gs * 64 + 8 * w) * 1024;
#define DMA_WOUT(e)                                                                                                 \
  {                                                                                                                 \
    const rsrc_t so_ = __builtin_amdgcn_make_buffer_rsrc((void*)wout, 0, 688 * 1024, 0x00020000);                   \
    const int ws_ = grp_start(Z2S, w), wn_ = grp_start(Z2S, w + 1) - ws_;                                           \
    if ((e) < 6) dma16(L_WOUT + ((e) < 4 ? 2 * w + ((e) >> 1) : 16 + w) * 2048 + ((e) & 1) * 1024, vwo, so_, ((ws_ + TILE((e) >> 1, wn_)) * 2 + ((e) & 1)) * 8192); \
    else if ((e) == 6) dma16(L_WOUT + 24 * 2048 + w * 1024, vwo, so_, ((((w >> 1) == 0 ? 5 : ((w >> 1) == 1 ? 11 : ((w >> 1) == 2 ? 27 : 64))) + TILE(3, 6)) * 2 + (w & 1)) * 8192); \
  }
#define DMA_WOUT_AT(dst, e)                                                                                         \
  {                                                                                                                 \
    const rsrc_t so_ = __builtin_amdgcn_make_buffer_rsrc((void*)wout, 0, 688 * 1024, 0x00020000);                   \
    const int ws_ = grp_start(Z2S, w), wn_ = grp_start(Z2S, w + 1) - ws_;                                           \
    dma16((dst) + ((e) - 7) * 1024, vwo, so_, ((ws_ + TILE(wn_ - 1 - (((e) - 7) >> 1), wn_)) * 2 + (((e) - 7) & 1)) * 8192); \
  }
#define VWO() const u32 vwo = ((lane >> 2) & 7) * 1024 + c2 * 128 + (lane >> 5) * 64 + (((lane & 3) ^ (((lane >> 4) & 1) << 1)) * 16)
#define LOAD_B(u, so_)                                                                                              \
  bfr[u][0] = ld16_bo<0>(l16, sws, so_); bfr[u][1] = ld16_bo<1024>(l16, sws, so_);                                  \
  bfr[u][2] = ld16_bo<2048>(l16, sws, so_); bfr[u][3] = ld16_bo<3072>(l16, sws, so_);                               \
  bfr[u][4] = ld16_bo<0>(l16, sws, (so_) + 4096); bfr[u][5] = ld16_bo<1024>(l16, sws, (so_) + 4096);                \
  bfr[u][6] = ld16_bo<2048>(l16, sws, (so_) + 4096); bfr[u][7] = ld16_bo<3072>(l16, sws, (so_) + 4096);

  if (t == 0) {
    int vq = 2;
    const rsrc_t sw = __builtin_amdgcn_make_buffer_rsrc((void*)win, 0, 512 * 2752, 0x00020000);
    const int z2s = grp_start(Z2S, c), z2n = grp_start(Z2S, c + 1) - z2s;
    const int z1s = grp_start(Z1S, c), z1n = grp_start(Z1S, c + 1) - z1s;
    u32 vw[3];
    int wch[3];
#pragma unroll
    for (int e = 0; e < 3; ++e) {
      const int sg = 2 * e + (lane >> 5), hi = sg >= 3;
      wch[e] = (sg - 3 * hi) * 4 + ((lane & 3) ^ (hi << 1));
      vw[e] = wch[e] < 2 * z2n ? (16 * w + 8 * hi + ((lane >> 2) & 7)) * 2752 + 1376 + 32 * z2s + 16 * wch[e] : 0x7FFF0000u;
    }
#define DMA_W(bk, e) dma16(LZ_W + ((bk) % 3) * 24576 + w * 3072 + (e) * 1024, vw[e], sw, ((2 * (bk) + rot) & 7) * (64 * 2752))
#define UBASE(u) ((u) % 5 == 0 ? LZ_U : ((u) % 5 - 1) * 16384)
#define ISS_X(n) do { VM(DMA_X(((n) & 3) * 16384, n, 0)); VM(DMA_X(((n) & 3) * 16384, n, 1)); mx[n] = vq; } while (0)
#define ISS_U(u) do { VM(DMA_U(u, UBASE(u), 0)); VM(DMA_U(u, UBASE(u), 1)); mu[u] = vq; } while (0)
#define ISS_W(bk, e0, e1) do { _Pragma("unroll") for (int e_ = e0; e_ < e1; ++e_) VM(DMA_W(bk, e_)); mw[bk] = vq; } while (0)
    int mx[8], mw[8], mu[16];
    mx[0] = vq;
    ISS_W(0, 0, 3);
    u32x4 xrow;
    VM(xrow = ld16(l16, sx, (c * 8 + w) * 1024));
    const int mxr = vq;
    ISS_X(1);
    const u32 xa0 = opq((32 * rw + i16) * 128 + ((g ^ (i16 >> 1)) * 16)), xa1 = xa0 ^ 64;
    u32 ta[3];
    int tl[3];
    {
      const u32 trb = LZ_W + g * 1536 + ((lane >> 2) & 3) * 64 + ((((g & 1) << 1) | ((lane >> 1) & 1)) * 16) + (lane & 1) * 8;
#pragma unroll
      for (int j = 0; j < 3; ++j) {
        tl[j] = 3 * cw + (j + rw) % 3;
        ta[j] = (trb + (tl[j] >> 1) * 512) ^ ((tl[j] & 1) * 32);
      }
    }
#define NEXT_BLOCK(bk) _Pragma("unroll") for (int j = 0; j < 3; ++j) ta[j] = opq(ta[j] + ((bk) == 0 ? 0 : ((bk) % 3 == 0 ? -49152 : 24576)))
    f32x4 acc[2][3], csa = zero4;
#pragma unroll
    for (int j = 0; j < 3; ++j) acc[0][j] = acc[1][j] = zero4;
    u32x4 xf[16][2], wf[2][3];
    u32x4 xg;
    int mxg = 0;
    WAITQ(mw[0]);
    BARRIER();
    USE_EPOCH()
    STAMP(7);
#define P1_MEM(n, h)                                                                                                \
  {                                                                                                                 \
    if ((n) == 0) { if ((h) == 0) ISS_X(2); if ((h) == 1) ISS_W(1, 0, 2); if ((h) == 2) ISS_W(1, 2, 3); if ((h) == 3) ISS_X(3); } \
    if ((n) == 1) { if ((h) == 0) ISS_W(2, 0, 2); if ((h) == 1) ISS_W(2, 2, 3); if ((h) == 2) VM(DMA_X(0, 4, 0)); if ((h) == 3) { VM(DMA_X(0, 4, 1)); mx[4] = vq; } } \
    if ((n) == 2 || (n) == 4) { if ((h) < 2) { VM(DMA_X((((n) + 3) & 3) * 16384, (n) + 3, (h) & 1)); mx[(n) + 3] = vq; } else ISS_W((n) / 2 + 2, (h) - 2, (h) - 1); } \
    if ((n) == 3) { if ((h) == 0) ISS_W(3, 2, 3); else if ((h) < 3) { VM(DMA_X(32768, 6, (h) - 1)); mx[6] = vq; } else ISS_U(0); } \
    if ((n) == 5) { if ((h) == 0) ISS_W(4, 2, 3); if ((h) == 1) { VM(xg = ld16_sc1(i16 * 16, sws, WS_XG + (P * 128 + 16 * w) * 16)); mxg = vq; } \
                    if ((h) >= 2) { VM(DMA_U(1, UBASE(1), (h) - 2)); mu[1] = vq; } }                                 \
    if ((n) == 6) { if ((h) < 3) ISS_W(5, h, (h) + 1); else ISS_U(2); }                                             \
    if ((n) == 7) { if ((h) < 2) { VM(DMA_U(3, UBASE(3), h)); mu[3] = vq; } }                                       \
  }
#pragma unroll
    for (int ks = 0; ks <= 16; ++ks) {
      const int n = ks >> 1, kk = ks & 1;
      if (ks > 0 && ks < 16 && kk == 0) {
        { const int mk = (n & 1) ? mx[n] : (mx[n] > mw[n >> 1] ? mx[n] : mw[n >> 1]); WAITQ(mk); }
        BARRIER();
      }
      if (ks < 16 && (ks & 3) == 0) { NEXT_BLOCK(ks >> 2); }
      if (ks == 8) {
#pragma unroll
        for (int e = 0; e < 3; ++e)
          vw[e] = wch[e] < 2 * z1n ? (16 * w + 8 * (2 * e + (lane >> 5) >= 3) + ((lane >> 2) & 7)) * 2752 + 32 * z1s + 16 * wch[e] : 0x7FFF0000u;
      }
      const int k1 = ks > 0 ? ks - 1 : 0, q1 = k1 & 1;
#pragma unroll
      for (int m = 0; m < 7; ++m) {
        if (ks > 0) {
          if (m < 6) acc[m & 1][m >> 1] = mfma32(xf[k1][m & 1], wf[q1][m >> 1], acc[m & 1][m >> 1]);
          else csa = mfma32(ones4, wf[q1][0], csa);
          SCHED();
        }
        if (ks < 16) {
          if (m == 0) { P1_MEM(n, 2 * kk) xf[ks & 15][0] = lds16((kk ? xa1 : xa0) + (n & 3) * 16384); xf[ks & 15][1] = lds16((kk ? xa1 : xa0) + (n & 3) * 16384 + 2048); }
          else if (m < 4) wf[kk][m - 1] = ldfrag(ta[m - 1] + (ks & 3) * 6144, 256);
          if (m == 4) P1_MEM(n, 2 * kk + 1)
        }
        SCHED();
      }
      if (ks == 2) EARLY_STATS(mxr)
    }
    GATHER_XS()
    sts4f(L_CS + tl[0] * 64 + i16 * 4, csa[0]);
    WAITQ(mu[1] > mw[4] ? mu[1] : mw[4]);
    READ_U(0, UBASE(0)) READ_U(1, UBASE(1))
    BARRIER();
    STAMP(1);
    u32x4 zp[3];
    ISS_W(6, 0, 3);
    {
      f32x2 ps[4], pq[4];
      float cs[3];
#pragma unroll
      for (int j = 0; j < 3; ++j) cs[j] = -lds4f(L_CS + tl[j] * 64 + i16 * 4);
#pragma unroll
      for (int rt = 0; rt < 2; ++rt) {
        const f32x4 rr = lds16f(L_XS + (32 * rw + 16 * rt + 4 * g) * 4), mm = lds16f(L_XS + 512 + (32 * rw + 16 * rt + 4 * g) * 4);
        f32x2 z0[3], z1[3];
#pragma unroll
        for (int j = 0; j < 3; ++j) {
          z0[j] = gelu2(fma2(lo2(mm), bc2(cs[j]), lo2(acc[rt][j])) * lo2(rr));
          z1[j] = gelu2(fma2(hi2(mm), bc2(cs[j]), hi2(acc[rt][j])) * hi2(rr));
          zp[j][2 * rt] = pk2v(z0[j]); zp[j][2 * rt + 1] = pk2v(z1[j]);
        }
        ps[2 * rt] = z0[0] + z0[1] + z0[2]; ps[2 * rt + 1] = z1[0] + z1[1] + z1[2];
        pq[2 * rt] = fma2(z0[2], z0[2], fma2(z0[1], z0[1], z0[0] * z0[0]));
        pq[2 * rt + 1] = fma2(z1[2], z1[2], fma2(z1[1], z1[1], z1[0] * z1[0]));
        SCHED();
        if (rt == 0) { ISS_U(4); ISS_U(5); }
        else ISS_U(6);
        SCHED();
      }
      const bool b3 = lane & 8, b2 = lane & 4, b1 = lane & 2;
#pragma unroll
      for (int m = 0; m < 2; ++m)
#pragma unroll
        for (int e = 0; e < 2; ++e) {
          ps[m][e] = (b3 ? ps[m + 2][e] : ps[m][e]) + dpp<0x128>(b3 ? ps[m][e] : ps[m + 2][e]);
          pq[m][e] = (b3 ? pq[m + 2][e] : pq[m][e]) + dpp<0x128>(b3 ? pq[m][e] : pq[m + 2][e]);
        }
#pragma unroll
      for (int e = 0; e < 2; ++e) {
        ps[0][e] = (b2 ? ps[1][e] : ps[0][e]) + dpp<0x141>(b2 ? ps[0][e] : ps[1][e]);
        pq[0][e] = (b2 ? pq[1][e] : pq[0][e]) + dpp<0x141>(b2 ? pq[0][e] : pq[1][e]);
      }
      float s_ = (b1 ? ps[0][1] : ps[0][0]) + dpp<0x4E>(b1 ? ps[0][0] : ps[0][1]);
      float q_ = (b1 ? pq[0][1] : pq[0][0]) + dpp<0x4E>(b1 ? pq[0][0] : pq[0][1]);
      s_ += dpp<0xB1>(s_);
      q_ += dpp<0xB1>(q_);
      const int row = 32 * rw + 16 * (i16 >> 3) + 4 * g + ((i16 >> 1) & 3);
      VM(st16_sc1((u32x4){__float_as_uint(s_), tlo, __float_as_uint(q_), tlo}, row * 16 + ((lane & 1) << 30), sws, WS_SG + ((P * 8 + c) * 2 + cw) * 2048));
      PIN(zp[0]); PIN(zp[1]); PIN(zp[2]);
    }
    STAMP(2);
    {
    RELANE();
#pragma unroll
    for (int j = 0; j < 3; ++j) acc[0][j] = acc[1][j] = zero4;
    csa = zero4;
    u32x4 q0, q1, q2, q3;
    int mq = 0, mz = 0;
    const u32 gvo = (2 * g) * 4096 + (32 * rw + 16 * cw + i16) * 16;
    const u32 gso = WS_SG + P * 32768;
#pragma unroll
    for (int ks = 0; ks <= 16; ++ks) {
      if (ks == 8) {
        WAITQ(mq); PIN(q0); PIN(q1); PIN(q2); PIN(q3);
#pragma nounroll
        for (int it = 0; it < SPIN_CAP; ++it) {
          if (__all(tag_ok(q0, tlo) && tag_ok(q1, tlo) && tag_ok(q2, tlo) && tag_ok(q3, tlo))) break;
          __builtin_amdgcn_s_sleep(POLL_SLEEP);
          asm volatile(
              "buffer_load_dwordx4 %0, %4, %5, %6 offen sc1\n\t"
              "buffer_load_dwordx4 %1, %4, %5, %6 offen offset:2048 sc1\n\t"
              "buffer_load_dwordx4 %2, %4, %5, %7 offen sc1\n\t"
              "buffer_load_dwordx4 %3, %4, %5, %7 offen offset:2048 sc1\n\t"
              "s_waitcnt vmcnt(0)"
              : "=&v"(q0), "=&v"(q1), "=&v"(q2), "=&v"(q3)
              : "v"(gvo), "s"(sws), "s"(gso), "s"(gso + 4096)
              : "memory");
        }
        float s = (__uint_as_float(q0[0]) + __uint_as_float(q1[0])) + (__uint_as_float(q2[0]) + __uint_as_float(q3[0]));
        float q = (__uint_as_float(q0[2]) + __uint_as_float(q1[2])) + (__uint_as_float(q2[2]) + __uint_as_float(q3[2]));
        red2g(s, q);
        float r2, m2;
        ln_fin(s, q, 1.0f / 688, r2, m2);
        sts4f(L_ZST + (32 * rw + 16 * cw + i16) * 4, r2);
        sts4f(L_ZST + 512 + (32 * rw + 16 * cw + i16) * 4, -m2 * r2);
        STAMP(3);
      }
      if (ks < 16 && (ks & 3) == 0) {
        if (ks > 0) { WAITQ(ks == 12 ? mz : mw[4 + (ks >> 2)]); BARRIER(); }
        if (ks == 12) { if (w == 0) SET_FLAG(WS_FA + (P * 8 + c) * 128); STAMP(5); }
        NEXT_BLOCK(4 + (ks >> 2));
      }
      if (ks == 8) {
#pragma unroll
        for (int rt = 0; rt < 2; ++rt) {
          const u32 ra = L_ZST + (32 * rw + 16 * rt + 4 * g) * 4;
          const f32x4 r4 = lds16f(ra), m4 = lds16f(ra + 512);
#pragma unroll
          for (int j = 0; j < 3; ++j) {
            const u32 u0 = zp[j][2 * rt], u1 = zp[j][2 * rt + 1];
            zp[j][2 * rt] = pk2v(fma2((f32x2){blo(u0), bhi(u0)}, lo2(r4), lo2(m4)));
            zp[j][2 * rt + 1] = pk2v(fma2((f32x2){blo(u1), bhi(u1)}, hi2(r4), hi2(m4)));
          }
        }
#pragma unroll
        for (int j = 0; j < 3; ++j)
          VM(st16_sc1(zp[j], l16, sws, (tl[j] < z2n) ? WS_ZB + ((z2s + tl[j]) * 64 + 4 * P + rw) * 1024 : 0x7FFF0000u));
        mz = vq;
        STAMP(4);
        SCHED();
      }
      if (ks >= 3 && ks <= 9) { WAITQ(mu[ks - 1]); READ_U(ks - 1, UBASE(ks - 1)) }
      if (ks >= 9 && ks <= 15) { WAITQ(mu[ks]); READ_U(ks, UBASE(ks)) }
      const int k1 = ks > 0 ? ks - 1 : 0, q1_ = k1 & 1;
#pragma unroll
      for (int m = 0; m < 7; ++m) {
        if (ks > 0) {
          if (m < 6) acc[m & 1][m >> 1] = mfma32(wf[q1_][m >> 1], xf[k1][m & 1], acc[m & 1][m >> 1]);
          else csa = mfma32(ones4, wf[q1_][0], csa);
          SCHED();
        }
        if (ks < 16 && m >= 1 && m < 4) wf[ks & 1][m - 1] = ldfrag(ta[m - 1] + (ks & 3) * 6144, 256);
        if (m == 0 || m == 4) {
          const int h = m == 4;
          if (ks >= 4 && ks <= 12) { VM(DMA_U(ks + 3, UBASE(ks + 3), h)); mu[ks + 3] = vq; }
        }
        if (m == 6) {
          if (ks >= 4 && ks <= 6) ISS_W(7, ks - 4, ks - 3);
        }
        if (m == 2) {
          if (ks == 5) { VM(q0 = ld16_sc1o<0>(gvo, sws, gso)); VM(q1 = ld16_sc1o<2048>(gvo, sws, gso)); }
          if (ks == 6) { VM(q2 = ld16_sc1o<0>(gvo, sws, gso + 4096)); VM(q3 = ld16_sc1o<2048>(gvo, sws, gso + 4096)); mq = vq; }
        }
        SCHED();
      }
      if ((ks & 3) == 3 || ks == 16) {
#pragma unroll
        for (int j = 0; j < 3; ++j) { PIN(acc[0][j]); PIN(acc[1][j]); }
        PIN(csa);
      }
    }
    sts4f(L_CS + 512 + tl[0] * 64 + i16 * 4, csa[0]);
    gf = ld8_sc1((lane & 1) * 1024, sws, WS_FA + (16 * w + c2) * 128);
    BARRIER();
    VWO();
    DMA_WOUT(0) DMA_WOUT(1)
    WAITV(2);
    PIN(gf);
    if (!__all(gf[0] == tlo && gf[1] == thi)) poll_flags((lane & 1) * 1024, true, sws, WS_FA + (16 * w + c2) * 128, tlo, thi);
    STAMP(6);
    {
      float xr[2], xm[2];
#pragma unroll
      for (int rt = 0; rt < 2; ++rt) {
        xr[rt] = lds4f(L_XS + (32 * rw + 16 * rt + i16) * 4);
        xm[rt] = -lds4f(L_XS + 512 + (32 * rw + 16 * rt + i16) * 4);
      }
      const u32 so0 = zb0 + TILE(0, gn) * 65536;
#pragma unroll
      for (int j = 0; j < 3; ++j) {
        const f32x4 cc = lds16f(L_CS + 512 + tl[j] * 64 + g * 16);
        bfr[0][3 * j] = ld16_b(l16, sws, so0 + 3 * j * 1024);
        bfr[0][3 * j + 1] = ld16_b(l16, sws, so0 + (3 * j + 1) * 1024);
        if (j < 2) bfr[0][3 * j + 2] = ld16_b(l16, sws, so0 + (3 * j + 2) * 1024);
        u32x4 zz;
#pragma unroll
        for (int rt = 0; rt < 2; ++rt) {
          zz[2 * rt] = pk2v(gelu2(fma2(lo2(cc), bc2(xm[rt]), lo2(acc[rt][j])) * bc2(xr[rt])));
          zz[2 * rt + 1] = pk2v(gelu2(fma2(hi2(cc), bc2(xm[rt]), hi2(acc[rt][j])) * bc2(xr[rt])));
        }
        st16_sc1(zz, l16, sws, (tl[j] < z1n) ? WS_Z1 + ((4 * P + rw) * 43 + z1s + tl[j]) * 1024 : 0x7FFF0000u);
        SCHED();
      }
    }
    }
  } else {
    int vq = 2;
    const rsrc_t sw = __builtin_amdgcn_make_buffer_rsrc((void*)wqkv, 0, 512 * 3072, 0x00020000);
    const u32 vw = (8 * w + ((lane >> 2) & 7)) * 3072 + 128 * c + ((lane >> 5) * 4 + ((lane & 3) ^ ((w & 1) << 1))) * 16;
#define DMA_S(n, e)                                                                                                 \
  do {                                                                                                              \
    if ((e) < 2) DMA_X(((n) % 3) * 40960, n, (e) & 1);                                                              \
    else dma16(((n) % 3) * 40960 + 16384 + w * 3072 + ((e) - 2) * 1024, vw, sw, (((n) + rot) & 7) * (64 * 3072) + ((e) - 2) * 1024); \
  } while (0)
#define ISS_Q(u, base) do { VM(DMA_U(u, base, 0)); VM(DMA_U(u, base, 1)); mu[u] = vq; } while (0)
    int ms[8], mu[16];
    VM(DMA_S(0, 2)); VM(DMA_S(0, 3)); VM(DMA_S(0, 4)); ms[0] = vq;
    u32x4 xrow;
    VM(xrow = ld16(l16, sx, ((8 + c) * 8 + w) * 1024));
    const int mxr = vq;
#pragma unroll
    for (int e = 0; e < 5; ++e) { VM(DMA_S(1, e)); ms[1] = vq; }
    const int l32 = lane & 31, hh = lane >> 5;
    u32 xa = (32 * rw + l32) * 128 + ((hh ^ ((l32 >> 1) & 7)) * 16);
    u32 wa = 16384 + hh * 3072 + cw * 512 + ((lane >> 2) & 3) * 64 + (((((g & 1) << 1) | ((lane >> 1) & 1)) ^ (hh << 1)) * 16) + (lane & 1) * 8;
    u32 ca = 16384 + g * 3072 + min(w, 5) * 512 + ((lane >> 2) & 3) * 64 + ((((g & 1) << 1) | ((lane >> 1) & 1)) * 16) + (lane & 1) * 8;
    const f32x16 zero16 = {0, 0, 0, 0, 0, 0, 0, 0, 0, 0, 0, 0, 0, 0, 0, 0};
    f32x16 acc[3] = {zero16, zero16, zero16};
    f32x4 csa[2] = {zero4, zero4};
    u32x4 xg;
    int mxg = 0;
    WAITQ(ms[0]);
    BARRIER();
    USE_EPOCH()
    STAMP(7);
#pragma unroll
    for (int n = 0; n < 8; ++n) {
      u32x4 xf[4], wf[4][3], cf[4];
      {
        const int d = n == 0 ? 0 : (n % 3 == 0 ? -81920 : 40960);
        xa = opq(xa + d); wa = opq(wa + d); ca = opq(ca + d);
      }
      if (n == 4) { WAITQ(mu[1]); READ_U(0, LQ_M) READ_U(1, LQ_M + 16384) }
      if (n == 7) { WAITQ(mu[3]); READ_U(2, LQ_M) READ_U(3, LQ_M + 16384) }
#pragma unroll
      for (int sq = 0; sq < 4; ++sq) {
        if (n <= 5) { VM(DMA_S(n + 2, sq)); if (sq == 3) { VM(DMA_S(n + 2, 4)); ms[n + 2] = vq; } }
        if (n == 6 && sq < 2) ISS_Q(4 + sq, LQ_A + sq * 16384);
        if (n == 7 && sq < 2) ISS_Q(6 + sq, sq * 16384);
        SCHED();
        xf[sq] = lds16(xa ^ (32 * sq));
#pragma unroll
        for (int m = 0; m < 3; ++m) wf[sq][m] = ldfrag(wa + sq * 6144 + m * 1024, 256);
        cf[sq] = ldfrag((ca ^ (32 * (sq & 1))) + (sq >> 1) * 12288, 256);
        SCHED();
      }
      if (n == 0) {
        rsrc_t sp = __builtin_amdgcn_make_buffer_rsrc((void*)wphi, 0, 16384, 0x00020000);
        VM(dma16(L_WPHI + (w & 1) * 1024, l16, sp, c * 2048 + (w & 1) * 1024));
        EARLY_STATS(mxr)
      }
      if (n == 1 || n == 2) ISS_Q(n - 1, LQ_M + (n - 1) * 16384);
      if (n == 4) { VM(xg = ld16_sc1(i16 * 16, sws, WS_XG + (P * 128 + 16 * w) * 16)); mxg = vq; }
      if (n == 5) ISS_Q(2, LQ_M);
      if (n == 6) ISS_Q(3, LQ_M + 16384);
      if (n == 7) {
#pragma unroll
        for (int sq = 0; sq < 4; ++sq) csa[sq & 1] = mfma32(ones4, cf[sq], csa[sq & 1]);
        GATHER_XS()
        sts4f(L_CS + w * 128 + i16 * 4, csa[0][0]);
        sts4f(L_CS + w * 128 + 64 + i16 * 4, csa[1][0]);
      }
      const int nw = n < 7 ? vq - ms[n + 1] : 63;
      BAR_IF(cw, 1, nw);
#pragma unroll
      for (int sq = 0; sq < 4; ++sq) {
#pragma unroll
        for (int m = 0; m < 3; ++m) acc[m] = mfmaL(wf[sq][m], xf[sq], acc[m]);
        if (n < 7) csa[sq & 1] = mfma32(ones4, cf[sq], csa[sq & 1]);
      }
      BAR_IF(cw, 0, nw);
    }
    STAMP(1);
    ISS_Q(8, LQ_M);
    READ_U(4, LQ_A) READ_U(5, LQ_A + 16384)
    const float xr = lds4f(L_XS + (32 * rw + l32) * 4), xm = -lds4f(L_XS + 512 + (32 * rw + l32) * 4);
    u32x4 wp0, wp1;
    {
      const u32 a = L_WPHI + (2 * cw + (lane & 1)) * 512 + (4 * hh + ((lane >> 2) & 3)) * 32 + (2 * (g & 1) + ((lane >> 1) & 1)) * 8;
      const u32x4 f = ldfrag(a, 256);
      const bool own = (lane >> 2) & 1;
#pragma unroll
      for (int e = 0; e < 4; ++e) { wp0[e] = own ? 0u : f[e]; wp1[e] = own ? f[e] : 0u; }
    }
#define FEATURES(e_, dst)                                                                                           \
  {                                                                                                                 \
    u32x4 kp[2];                                                                                                    \
    _Pragma("unroll") for (int a = 0; a < 4; ++a) {                                                                \
      const f32x4 cc = lds16f(L_CS + (2 * (e_) + cw) * 128 + a * 32 + hh * 16);                                      \
      kp[a >> 1][2 * (a & 1)] = pk2v(fma2(lo2(cc), bc2(xm), (f32x2){acc[e_][4 * a], acc[e_][4 * a + 1]}));          \
      kp[a >> 1][2 * (a & 1) + 1] = pk2v(fma2(hi2(cc), bc2(xm), (f32x2){acc[e_][4 * a + 2], acc[e_][4 * a + 3]}));  \
    }                                                                                                               \
    f32x16 pl = mfmaL(wp0, kp[0], zero16);                                                                          \
    pl = mfmaL(wp1, kp[1], pl);                                                                                     \
    hedgehog16(pl, xr, dst);                                                                                        \
  }
    {
      const int s_ = 32 * rw + l32;
      const u32 key = (((s_ >> 1) & 3) ^ (((s_ >> 3) & 1) << 1)) * 16;
      ISS_Q(9, LQ_M + 16384);
      {
        const u32 va = LQ_V + (s_ >> 3) * 1024 + cw * 512 + (s_ & 7) * 64 + hh * 8;
#pragma unroll
        for (int a = 0; a < 4; ++a) {
          const f32x4 cc = lds16f(L_CS + (4 + cw) * 128 + a * 32 + hh * 16);
          sts8(va + ((a * 16) ^ key), (u32x2){pk2v(fma2(lo2(cc), bc2(xm), (f32x2){acc[2][4 * a], acc[2][4 * a + 1]}) * bc2(xr)),
                                              pk2v(fma2(hi2(cc), bc2(xm), (f32x2){acc[2][4 * a + 2], acc[2][4 * a + 3]}) * bc2(xr))});
        }
      }
      WAITL0();
      ISS_Q(10, LQ_A); ISS_Q(11, LQ_A + 16384);
      u32x4 ph[4];
      FEATURES(1, ph)
      const u32 pa = LQ_PHI + (s_ >> 3) * 2048 + (2 * cw + hh) * 512 + (s_ & 7) * 64;
#pragma unroll
      for (int a = 0; a < 4; ++a) sts16(pa + ((a * 16) ^ key), ph[a]);
    }
    WAITQ(mu[9]);
    READ_U(6, 0) READ_U(7, 16384) READ_U(8, LQ_M) READ_U(9, LQ_M + 16384)
    BARRIER();
    STAMP(2);
    {
      const u32 kx = (((lane >> 3) & 1) ^ ((g & 1) << 1)) * 16;
      const u32 pa = LQ_PHI + g * 2048 + (w >> 1) * 512 + ((lane >> 2) & 3) * 64 + (((2 * (w & 1) + ((lane >> 1) & 1)) * 16) ^ kx) + (lane & 1) * 8;
      const u32 va = LQ_V + g * 1024 + (w >> 2) * 512 + ((lane >> 2) & 3) * 64 + (((2 * ((w >> 1) & 1) + ((lane >> 1) & 1)) * 16) ^ kx) + (lane & 1) * 8;
      u32x4 pf[4], vf[4];
#pragma unroll
      for (int k = 0; k < 4; ++k) {
        pf[k] = cat(ldstr(pa + k * 8192), ldstr((pa ^ 32) + k * 8192 + 256));
        vf[k] = cat(ldstr(va + k * 4096), ldstr((va ^ 32) + k * 4096 + 256));
      }
      f32x4 kv = zero4, kz = zero4;
#pragma unroll
      for (int k = 0; k < 4; ++k) { kv = mfma32(pf[k], vf[k], kv); kz = mfma32(pf[k], ones4, kz); }
      const u32 so = WS_KP + (c * 16 + P) * KP_SZ;
      u32x4 g0 = {__float_as_uint(kv[0]), tlo, __float_as_uint(kv[1]), tlo}, g1 = {__float_as_uint(kv[2]), tlo, __float_as_uint(kv[3]), tlo};
      u32x4 g2 = {__float_as_uint((lane & 1) ? kz[2] : kz[0]), tlo, __float_as_uint((lane & 1) ? kz[3] : kz[1]), tlo};
      PIN(g0); PIN(g1); PIN(g2);
      asm volatile("s_nop 7" ::: "memory");
      VM(st16_sc1(g0, l16, sws, so + w * 2048));
      VM(st16_sc1(g1, l16, sws, so + w * 2048 + 1024));
      VM(st16_sc1(g2, g * 32 + (lane & 1) * 16 + ((i16 >> 1) << 26), sws, so + 16384 + w * 128));
    }
    STAMP(3);
    ISS_Q(12, LQ_M); ISS_Q(13, LQ_M + 16384);
    WAITQ(mu[11]);
    READ_U(10, LQ_A) READ_U(11, LQ_A + 16384)
    BARRIER();
    ISS_Q(14, LQ_A); ISS_Q(15, LQ_A + 16384);
    u32x4 phq[4];
    FEATURES(0, phq)
    PIN(phq[0]); PIN(phq[1]); PIN(phq[2]); PIN(phq[3]);
    STAMP(4);
    if (w >= 5) {
      const int id = 2 * P + (w == 7 ? (lane & 1) : w - 5), wq = id >> 2, gq = id & 3;
      const u32 vi0 = (4 * g) * KP_SZ + (w == 7 ? 16384 + wq * 128 + gq * 32 : wq * 2048 + (gq * 16 + i16) * 16);
      const u32 vi1 = vi0 + (w == 7 ? 16 : 1024);
      const u32 vo = (w == 7 ? (WS_KZ - WS_KF) + c * 512 + id * 16 + ((i16 >> 1) << 25) : c * 8192 + id * 256 + i16 * 16) + (g << 28);
      float sa[4];
#pragma nounroll
      for (int it = 0; it < SPIN_CAP; ++it) {
        u32x4 a[4][2];
#pragma unroll
        for (int o = 0; o < 4; ++o) {
          a[o][0] = ld16_sc1(vi0, sws, WS_KP + (c * 16 + o) * KP_SZ);
          a[o][1] = ld16_sc1(vi1, sws, WS_KP + (c * 16 + o) * KP_SZ);
        }
        WAITV(0);
        bool ok = true;
#pragma unroll
        for (int o = 0; o < 4; ++o) { PIN(a[o][0]); PIN(a[o][1]); ok = ok && tag_ok(a[o][0], tlo) && tag_ok(a[o][1], tlo); }
#pragma unroll
        for (int e = 0; e < 4; ++e)
          sa[e] = (__uint_as_float(a[0][e >> 1][2 * (e & 1)]) + __uint_as_float(a[1][e >> 1][2 * (e & 1)])) +
                  (__uint_as_float(a[2][e >> 1][2 * (e & 1)]) + __uint_as_float(a[3][e >> 1][2 * (e & 1)]));
        if (__all(ok)) break;
        __builtin_amdgcn_s_sleep(POLL_SLEEP);
      }
      red2g(sa[0], sa[1]);
      red2g(sa[2], sa[3]);
      st16_sc1((u32x4){pk2(sa[0], sa[1]), tlo, pk2(sa[2], sa[3]), tlo}, vo, sws, WS_KF);
    }
    {
      VWO();
#pragma unroll
      for (int e = 0; e < 7; ++e) DMA_WOUT(e)
    }
    gf = ld8_sc1((lane & 1) * 1024, sws, WS_FA + (16 * w + c2) * 128);
    WAITV(8);
    READ_U(12, LQ_M) READ_U(13, LQ_M + 16384) READ_U(14, LQ_A) READ_U(15, LQ_A + 16384)
    BARRIER();
#pragma unroll
    for (int a = 0; a < 4; ++a) sts16(L_WO1 + w * 4096 + a * 1024 + l16, phq[a]);
    WAITV(0);
    PIN(gf);
    if (!__all(gf[0] == tlo && gf[1] == thi)) poll_flags((lane & 1) * 1024, true, sws, WS_FA + (16 * w + c2) * 128, tlo, thi);
    STAMP(6);
    { const u32 so_ = zb0 + TILE(0, gn) * 65536; LOAD_B(0, so_) }
  }

  tlo = __builtin_amdgcn_readfirstlane(tlo);
  thi = __builtin_amdgcn_readfirstlane(thi);
  int w2 = w, c22 = c2, p2 = p, cc2 = c;
  asm volatile("" : "+s"(w2), "+s"(tlo), "+s"(thi), "+s"(c22), "+s"(p2), "+s"(cc2));
  {
  const int w = w2, rw = w & 3, cw = w >> 2, c2 = c22, p = p2, c = cc2, P = p >> 1;
  RELANE();
  { const u32 so_ = zb0 + TILE(1, gn) * 65536; LOAD_B(1, so_) }
  STAMP(9);
  const u32 redw = opq(L_RED + w * 4096 + lane * 16), redr = opq(L_RED + rw * 1024 + lane * 16);
  const u32 fd = WS_FD + (p * 16 + c2) * 128;
  const u32 f3s = WS_FB + (P * 8 + (c2 > 0 ? c2 - 1 : 0)) * 128;
  const u32 vw3 = ((lane >> 2) & 7) * 1024 + c2 * 128 + (lane >> 5) * 64;
  const rsrc_t swo = __builtin_amdgcn_make_buffer_rsrc((void*)wo, 0, 512 * 1024, 0x00020000);
#define DMA_WO(dst, half, e) dma16((dst) + w * 4096 + (e) * 1024, (vw3 + (lane & 3) * 16) ^ (((e) & 1) * 32), swo, ((w + 8 * (half)) * 4 + (e)) * 8192)
  u32x4 kfg, kzg;
  u32x2 z1r[6], hold;
  u32 bs;
  f32x4 sm0;
#define EMIT_H(jt, sm_)                                                                                             \
  {                                                                                                                 \
    const f32x2 bb = bc2(blo(bs));                                                                                  \
    const u32 h0 = pk2v((f32x2){blo(z1r[jt][0]), bhi(z1r[jt][0])} * (lo2(sm_) + bb));                               \
    const u32 h1 = pk2v((f32x2){blo(z1r[jt][1]), bhi(z1r[jt][1])} * (hi2(sm_) + bb));                               \
    const u32 hb_ = WS_HF + (((4 * p + rw) * 2 + ((jt) >> 1)) * 8 + c2) * 1024;                                     \
    if ((jt) == 0 || ((jt) == 2 && gn == 6)) hold = (u32x2){h0, h1};                                                \
    else if ((jt) == 1 || ((jt) == 3 && gn == 6)) st16_sc1((u32x4){hold[0], hold[1], h0, h1}, l16, sws, hb_);       \
    else if ((jt) == 2) st16_sc1((u32x4){h0, h1, 0u, 0u}, l16, sws, hb_);                                           \
    else st16_sc1((u32x4){h0, tlo, h1, tlo}, l16, sws, WS_HL + (((4 * p + rw) * 8 + c2) * 2 + (gn - 1 - (jt))) * 1024); \
  }
#pragma unroll
  for (int j = 0; j < 6; ++j) {
    if (j < 5 || gn == 6) {
      const int u = j & 1;
      if (j == 0 || j == 2) WAITV(8);
      if (j == 1) WAITV(9);
      if (j == 3) { WAITV(11); WAITV_IF(cw, 1, 10); WAITV_IF(t, 0, 9); WAITV_IF(2 * cw - t, 2, 8); }
      if (j == 4) { WAITV(12); WAITV_IF(t, 0, 9); WAITV_IF(2 * gn + t, 11, 4); }
      if (j == 5) { WAITV(5); WAITV_IF(cw, 1, 4); }
      STAMP(17 + 2 * j);
#pragma unroll
      for (int i = 0; i < 8; ++i) PIN(bfr[u][i]);
      {
        f32x4 a_[4];
#pragma unroll
        for (int rt = 0; rt < 4; ++rt) {
          a_[rt] = zero4;
#pragma unroll
          for (int i = 0; i < 8; ++i) a_[rt] = mfma32(bfr[u][i], af[rt][i], a_[rt]);
          SCHED();
          if (rt > 0) sts16f(redw + u * 32768 + (rt - 1) * 1024, a_[rt - 1]);
          SCHED();
        }
        if (j == 1 && t != 0) {
          PIN(kfg); PIN(kzg);
          poll_gran(kfg, l16, sws, WS_KF + c * 8192 + w * 1024, tlo);
          poll_gran(kzg, g * 16, sws, WS_KZ + c * 512 + w * 64, tlo);
          sts8(L_KFS + (w >> 1) * 1024 + i16 * 64 + (w & 1) * 32 + g * 8, (u32x2){kfg[0], kfg[2]});
          sts8(L_KFS + 4096 + w * 32 + g * 8, (u32x2){kzg[0], kzg[2]});
          sts8(L_KFS + 4352 + (lane & 7) * 8, (u32x2){0u, 0u});
        }
        sts16f(redw + u * 32768 + 3072, a_[3]);
      }
      BAR_IF(cw, 1, 63);
      STAMP(18 + 2 * j);
      if (w == 4) {
        if (j == 0 && t == 0) SET_FLAG(WS_FB + (P * 8 + c) * 128);
        if (j == 3) SET_FLAG(fd);
        if (j == 4 && t != 0) SET_FLAG(WS_FC + (P * 8 + c) * 128);
        if ((j == 4 && gn == 5) || j == 5) SET_FLAG(fd + 1024);
      }
      if (j == 0 && t != 0) {
        kfg = ld16_sc1(l16, sws, WS_KF + c * 8192 + w * 1024);
        kzg = ld16_sc1(g * 16, sws, WS_KZ + c * 512 + w * 64);
      }
      u32x4 of[2];
      if (j == 2 && t != 0) {
        const int l32 = lane & 31, hh = lane >> 5;
        const bool own = ((lane >> 2) & 1) == hh;
        const u32 ka = own ? L_KFS + (2 * cw + hh) * 1024 + ((l32 & 3) + 4 * (l32 >> 3)) * 64 : L_KFS + 4352;
        const u32 zaa = own ? L_KFS + 4096 + (2 * cw + hh) * 64 : L_KFS + 4352;
        const u32 qa = L_WO1 + w * 4096 + l16;
        const f32x16 zero16 = {0, 0, 0, 0, 0, 0, 0, 0, 0, 0, 0, 0, 0, 0, 0, 0};
        f32x16 den = zero16, num = zero16;
#pragma unroll
        for (int a = 0; a < 4; ++a) {
          const u32x4 pq_ = lds16(qa + a * 1024);
          den = mfmaL(lds16(zaa + a * 16), pq_, den);
          num = mfmaL(lds16(ka + a * 16), pq_, num);
        }
        const float inv = __builtin_amdgcn_rcpf(den[0] + 1e-6f);
#pragma unroll
        for (int a = 0; a < 2; ++a) {
          of[a] = (u32x4){pk2(num[8 * a] * inv, num[8 * a + 1] * inv), pk2(num[8 * a + 2] * inv, num[8 * a + 3] * inv),
                          pk2(num[8 * a + 4] * inv, num[8 * a + 5] * inv), pk2(num[8 * a + 6] * inv, num[8 * a + 7] * inv)};
          PIN(of[a]);
        }
        STAMP(5);
      }
      if (j == 1) {
        if (cw == 0) {
          WAITV(0);
          PIN(gf);
          if (!__all(gf[0] == tlo && gf[1] == thi)) poll_flags((lane & 1) * 128, true, sws, f3s, tlo, thi);
          rsrc_t sb = __builtin_amdgcn_make_buffer_rsrc((void*)bsp, 0, 4096, 0x00020000);
          bs = ld2(i16 * 2, sb, (64 * p + 16 * rw) * 2);
        }
      }
      if ((j == 1 || j == 2) && cw == 0) {
#pragma unroll
        for (int v = 3 * j - 3; v < 3 * j; ++v)
          z1r[v] = ld8_sc1(l16, sws, WS_Z1 + ((2 * p + (rw >> 1)) * 43 + gs + TILE(min(v, gn - 1), gn)) * 1024 + (rw & 1) * 8);
      }
      const u32 ra_ = redr + ((j + 1) & 1) * 32768;
      f32x4 sm, r_[2];
#pragma unroll
      for (int i = 0; i < 8; ++i) {
        if (j >= 1 && (i & 1) == 0 && cw == 0) {
          if (i == 2) sm = r_[0] + r_[1];
          if (i > 2) sm += r_[0] + r_[1];
          r_[0] = lds16f(ra_ + i * 4096); r_[1] = lds16f(ra_ + (i + 1) * 4096);
        }
        SCHED();
        if (j < 3) bfr[u][i] = ld16_b(l16, sws, zb0 + TILE(j + 2, gn) * 65536 + i * 1024);
        else if (j == 3) ld16_b_if6(bfr[1][i], gn, l16, sws, zb0 + TILE(5, gn) * 65536 + i * 1024);
        SCHED();
      }
      if (j == 0) gf = ld8_sc1((lane & 1) * 128, sws, f3s);
      if (j == 2 && t != 0) {
        const u32 vo = ((lane >> 4) & 1) * 16384 + (lane >> 5) * 512 + i16 * 16;
        st16_sc1(of[0], vo, sws, WS_OF + ((8 * P + 2 * rw) * 16 + 2 * c + cw) * 1024);
        st16_sc1(of[1], vo, sws, WS_OF + ((8 * P + 2 * rw) * 16 + 2 * c + cw) * 1024 + 256);
      }
      if (j == 2) { PIN(bs); _Pragma("unroll") for (int v = 0; v < 3; ++v) PIN(z1r[v]); }
      if (j == 3) { _Pragma("unroll") for (int v = 3; v < 6; ++v) PIN(z1r[v]); }
      if (j >= 1 && cw == 0) {
        sm += r_[0] + r_[1];
        if (j == 1) sm0 = sm;
        if (j == 2) EMIT_H(0, sm0)
        if (j >= 2) EMIT_H(j >= 2 ? j - 1 : 0, sm)
      }
      if (j == 3) {
        if (t == 0) {
          VWO();
#pragma unroll
          for (int e = 2; e < 7; ++e) DMA_WOUT(e)
        }
#pragma unroll
        for (int e = 0; e < 4; ++e) DMA_WO(L_WO1, 0, e);
      }
      if (j == 3 && cw == 0) { WAITV(12); WAITV_IF(t, 0, 10); WAITV_IF(2 * gn + t, 11, 5); }
      if (j == 5) WAITV(1);
      BAR_IF(cw, 0, 63);
    }
  }
  {
    STAMP(10);
    const u32 bufA = L_RED + (gn & 1) * 32768, bufB = L_RED + 32768 - (gn & 1) * 32768;
    const u32 fv = (lane & 16) ? (P * 8 + (lane & 7)) * 128 : (WS_FD - WS_FC) + (p * 16 + ((i16 & 14) | cw)) * 128;
    gf = ld8_sc1(fv, sws, WS_FC);
    if (cw == 0) {
      const u32 ra_ = redr + (bufB - L_RED);
      const f32x4 sm = ((lds16f(ra_) + lds16f(ra_ + 4096)) + (lds16f(ra_ + 8192) + lds16f(ra_ + 12288))) +
           ((lds16f(ra_ + 16384) + lds16f(ra_ + 20480)) + (lds16f(ra_ + 24576) + lds16f(ra_ + 28672)));
      if (gn == 6) EMIT_H(5, sm) else EMIT_H(4, sm)
    }
    RELANE();
#pragma unroll
    for (int e = 0; e < 4; ++e) DMA_WO(bufA, 1, e);
    WAITV(5); WAITV_IF(cw, 1, 4);
    PIN(gf);
    const bool okf = gf[0] == tlo && gf[1] == thi;
    const bool n0 = (lane & 24) == 0;
    const bool all1 = __all(okf);
    if (!__all(!n0 || okf)) poll_flags(fv, n0, sws, WS_FC, tlo, thi);
    u32x4 hf[8], of[8];
#pragma unroll
    for (int e = 0; e < 4; ++e) hf[e] = ld16_b(l16, sws, WS_HF + ((4 * p + rw) * 16 + 2 * e + cw) * 1024);
    BARRIER();
    {
      VWO();
#pragma unroll
      for (int e = 7; e < 11; ++e) DMA_WOUT_AT(bufB + w * 4096, e)
    }
    if (!all1) poll_flags(fv, true, sws, WS_FC, tlo, thi);
#pragma unroll
    for (int e = 4; e < 8; ++e) hf[e] = ld16_b(l16, sws, WS_HF + ((4 * p + rw) * 16 + 2 * e + cw) * 1024);
    const u32 qb = ((lane >> 2) & 3) * 64 + ((((g & 1) << 1) | ((lane >> 1) & 1)) * 16) + (lane & 1) * 8;
    f32x4 a3[4];
#pragma unroll
    for (int ct = 0; ct < 4; ++ct) a3[ct] = zero4;
    u32x4 wf[3][4], hl[4][2];
#define RDW2(dst, lo0, hi0, offl, offh) _Pragma("unroll") for (int ct = 0; ct < 4; ++ct) dst[ct] = cat(ldstr(((lo0) ^ ((ct & 1) * 32)) + (offl) + (ct >> 1) * 512), ldstr(((hi0) ^ ((ct & 1) * 32)) + (offh) + (ct >> 1) * 512))
#define MM3(src, frag) _Pragma("unroll") for (int ct = 0; ct < 4; ++ct) a3[ct] = mfma32(src[ct], frag, a3[ct])
    const u32 lb = WS_HL + ((4 * p + rw) * 8 + cw) * 2048;
    const rsrc_t sxo = __builtin_amdgcn_make_buffer_rsrc((void*)(x + (size_t)p * 65536), 0, 65536, 0x00020000);
    const u32 w0 = opq(L_WOUT + qb + (g >> 1) * 1024 + (g & 1) * 256);
    const u32 wa = w0 + cw * 4096, wb = w0 + cw * 2048;
    const u32 oq = qb + g * 1024 + cw * 4096;
    const u32 oa = opq(L_WO1 + oq), ob = opq(bufA + oq);
    u32x4 xres;
    STAMP(11);
#define RDK(dst, k)                                                                                                 \
  if ((k) < 4) { RDW2(dst, wa, wa, (k) * 8192, (k) * 8192 + 2048); }                                                \
  else if ((k) < 8) { RDW2(dst, wb, w0, 32768 + ((k) - 4) * 4096, 49152 + ((k) - 4) * 2048); }                      \
  else if ((k) < 12) { RDW2(dst, oa, oa, ((k) - 8) * 8192, ((k) - 8) * 8192 + 256); }                               \
  else { RDW2(dst, ob, ob, ((k) - 12) * 8192, ((k) - 12) * 8192 + 256); }
    RDK(wf[0], 0)
    RDK(wf[1], 1)
#pragma unroll
    for (int k = 0; k < 16; ++k) {
      if (k == 0) WAITV(8);
      if (k == 4) WAITV(8);
      if (k == 8) WAITV(9);
      if (k == 10) { BARRIER(); STAMP(12); }
      if (k + 2 < 16) { RDK(wf[(k + 2) % 3], k + 2) }
      SCHED();
      if (k < 8) { PIN(hf[k]); MM3(wf[k % 3], hf[k]); }
      else { PIN(of[k - 8]); MM3(wf[k % 3], of[k - 8]); }
      SCHED();
      if (k < 4) {
        of[2 * k] = ld16_b(l16, sws, WS_OF + ((4 * p + rw) * 16 + 4 * k + cw) * 1024);
        of[2 * k + 1] = ld16_b(l16, sws, WS_OF + ((4 * p + rw) * 16 + 4 * k + 2 + cw) * 1024);
      } else if (k < 8) {
        if (k == 4) xres = ld16((tid >> 3) * 1024 + (tid & 7) * 16, sxo, c2 * 128);
        hl[k - 4][0] = ld16_sc1(l16, sws, lb + (k - 4) * 4096);
        hl[k - 4][1] = ld16_sc1(l16, sws, lb + (k - 4) * 4096 + 1024);
      }
    }
    STAMP(13);
    {
      const u32 wl_ = opq(bufB + (w0 - L_WOUT) + cw * 4096);
      u32x4 wg[4][4];
#pragma unroll
      for (int e = 0; e < 4; ++e) RDW2(wg[e], wl_, wl_, e * 8192, e * 8192 + 2048);
      WAITV(0);
      PIN(xres);
#pragma unroll
      for (int e = 0; e < 4; ++e) {
        PIN(hl[e][0]); PIN(hl[e][1]);
        poll_gran(hl[e][0], l16, sws, lb + e * 4096, tlo);
        poll_gran(hl[e][1], l16, sws, lb + e * 4096 + 1024, tlo);
        MM3(wg[e], ((u32x4){hl[e][0][0], hl[e][0][2], hl[e][1][0], hl[e][1][2]}));
      }
    }
    STAMP(14);
    const u32 fin = L_WOUT - 2048;
#pragma unroll
    for (int ct = 0; ct < 4; ++ct) sts16f(fin + 2048 + cw * 16384 + (16 * rw + i16) * 256 + (((4 * ct + g) ^ i16) * 16), a3[ct]);
    BARRIER();
    {
      const int row = tid >> 3;
      const u32 a = fin + 2048 + row * 256 + (((2 * (tid & 7)) ^ (row & 15)) * 16);
      const f32x4 s0 = lds16f(a) + lds16f(a + 16384), s1 = lds16f(a ^ 16) + lds16f((a ^ 16) + 16384);
      u32x4 o = {pk2(blo(xres[0]) + s0[0], bhi(xres[0]) + s0[1]), pk2(blo(xres[1]) + s0[2], bhi(xres[1]) + s0[3]),
                 pk2(blo(xres[2]) + s1[0], bhi(xres[2]) + s1[1]), pk2(blo(xres[3]) + s1[2], bhi(xres[3]) + s1[3])};
      *(u32x4*)(out + (size_t)(64 * p + row) * 1024 + c2 * 128 + (tid & 7) * 16) = o;
    }
  }
  }
  if (b == 0 && w == 0 && l16 == 0) *(u64*)(ws + WS_EP) = ((u64)thi << 32) | tlo;
  STAMP(15);
  STAMPC(31);
}

extern "C" void kernel_launch(void* const* d_in, const int* in_sizes, int n_in, void* d_out, int out_size, void* d_ws, size_t ws_size,
                              hipStream_t stream) {
  fused_kernel<<<256, 512, 0, stream>>>((const char*)d_in[0], (const char*)d_in[1], (const char*)d_in[2], (const char*)d_in[3],
                                        (const char*)d_in[5], (const char*)d_in[6], (const char*)d_in[7], (const char*)d_in[8], (char*)d_out,
                                        (char*)d_ws);
}
```

```cpp
#include <hip/hip_runtime.h>
#include <hip/hip_bf16.h>

typedef unsigned int u32;
typedef unsigned long long u64;
typedef short s16x4 __attribute__((ext_vector_type(4)));
typedef __bf16 bf16x2 __attribute__((ext_vector_type(2)));
typedef __bf16 bf16x8 __attribute__((ext_vector_type(8)));
typedef float f32x16 __attribute__((ext_vector_type(16)));
typedef float f32x4 __attribute__((ext_vector_type(4)));
typedef float f32x2 __attribute__((ext_vector_type(2)));
typedef u32 u32x4 __attribute__((ext_vector_type(4)));
typedef u32 u32x2 __attribute__((ext_vector_type(2)));
typedef __amdgpu_buffer_rsrc_t rsrc_t;
#define LDSP __attribute__((address_space(3)))
#define DI __device__ __forceinline__

#define STAMPS 0

constexpr u32 WS_EP = 0;
constexpr u32 WS_FA = 4096;
constexpr u32 WS_FB = WS_FA + 16384;
constexpr u32 WS_FC = WS_FB + 16384;
constexpr u32 WS_FD = WS_FC + 16384;
constexpr u32 WS_XG = WS_FD + 65536;
constexpr u32 WS_SG = WS_XG + 32768;
constexpr u32 WS_ZB = WS_SG + 524288;
constexpr u32 WS_Z1 = WS_ZB + 43 * 65536;
constexpr u32 KP_SZ = 17408;
constexpr u32 WS_KP = WS_Z1 + 64 * 43 * 1024;
constexpr u32 WS_KF = WS_KP + 128 * KP_SZ;
constexpr u32 WS_KZ = WS_KF + 65536;
constexpr u32 WS_OF = WS_KZ + 4096;
constexpr u32 WS_HF = WS_OF + 128 * 16384;
constexpr u32 WS_HL = WS_HF + 128 * 16 * 1024;
constexpr u32 WS_ST = WS_HL + 128 * 16 * 1024;
constexpr u32 WS_END = WS_ST + 256 * 8 * 32 * 8;

constexpr u32 LZ_W = 65536;
constexpr u32 LZ_U = 139264;
constexpr u32 LQ_M = 122880;
constexpr u32 LQ_A = 81920;
constexpr u32 LQ_PHI = 49152;
constexpr u32 LQ_V = 32768;
constexpr u32 L_TAB = 155648;
constexpr u32 L_XS = L_TAB;
constexpr u32 L_CS = L_TAB + 1024;
constexpr u32 L_ZST = L_TAB + 3072;
constexpr u32 L_WPHI = L_TAB + 3072;
constexpr u32 L_KFS = L_TAB + 2048;
constexpr u32 L_JUNK = L_TAB + 7168;
constexpr u32 L_WOUT = 0;
constexpr u32 L_RED = 57344;
constexpr u32 L_WO1 = 122880;

constexpr u64 Z2S = 0x26211B16110B0500ull;
constexpr u64 Z1S = 0x26211C16110C0600ull;
DI int grp_start(u64 tab, int c) { return c >= 8 ? 43 : (int)((tab >> (8 * c)) & 0xFF); }

#define SCHED() __builtin_amdgcn_sched_barrier(0)
#define TIC()
#define TOC(acc)
#define WAITV(n) do { TIC(); asm volatile("s_waitcnt vmcnt(%0)" ::"n"(n) : "memory"); TOC(acv); SCHED(); } while (0)
#define BARRIER() do { SCHED(); TIC(); asm volatile("s_waitcnt lgkmcnt(0)\n\ts_barrier" ::: "memory"); TOC(acb); SCHED(); } while (0)
#define WAITV_IF(g_, v, n) do { TIC(); asm volatile("s_cmp_lg_u32 %0, %1\n\ts_cbranch_scc1 1f\n\ts_waitcnt vmcnt(%2)\n1:" ::"s"(g_), "n"(v), "n"(n) : "scc", "memory"); TOC(acv); SCHED(); } while (0)
#define BAR_IF(g_, v, n) do { SCHED(); asm volatile("s_cmp_lg_u32 %0, %1\n\ts_cbranch_scc1 1f\n\ts_waitcnt vmcnt(%2) lgkmcnt(0)\n\ts_barrier\n1:" ::"s"(g_), "n"(v), "n"(n) : "scc", "memory"); SCHED(); } while (0)
#define WAITL0() asm volatile("s_waitcnt lgkmcnt(0)" ::: "memory")
#define PIN(x) asm volatile("" : "+v"(x))

DI void dma16(u32 lds, u32 voff, rsrc_t srd, u32 soff) {
  asm volatile("s_mov_b32 m0, %0\n\ts_nop 0\n\tbuffer_load_dwordx4 %1, %2, %3 offen lds" ::"s"(lds), "v"(voff), "s"(srd), "s"(soff) : "memory");
}
DI void touch(u32 lds, u32 voff, rsrc_t srd, u32 soff) {
  asm volatile("s_mov_b32 m0, %0\n\ts_nop 0\n\tbuffer_load_dword %1, %2, %3 offen lds" ::"s"(lds), "v"(voff), "s"(srd), "s"(soff) : "memory");
}
DI u32x4 ld16_sc1(u32 voff, rsrc_t srd, u32 soff) {
  u32x4 r;
  asm volatile("buffer_load_dwordx4 %0, %1, %2, %3 offen sc1" : "=v"(r) : "v"(voff), "s"(srd), "s"(soff) : "memory");
  return r;
}
template <int OFF> DI u32x4 ld16_sc1o(u32 voff, rsrc_t srd, u32 soff) {
  u32x4 r;
  asm volatile("buffer_load_dwordx4 %0, %1, %2, %3 offen offset:%4 sc1" : "=v"(r) : "v"(voff), "s"(srd), "s"(soff), "n"(OFF) : "memory");
  return r;
}
#define BPOL " sc1"
DI u32x4 ld16_b(u32 voff, rsrc_t srd, u32 soff) {
  u32x4 r;
  asm volatile("buffer_load_dwordx4 %0, %1, %2, %3 offen" BPOL : "=v"(r) : "v"(voff), "s"(srd), "s"(soff) : "memory");
  return r;
}
template <int OFF> DI u32x4 ld16_bo(u32 voff, rsrc_t srd, u32 soff) {
  u32x4 r;
  asm volatile("buffer_load_dwordx4 %0, %1, %2, %3 offen offset:%4" BPOL : "=v"(r) : "v"(voff), "s"(srd), "s"(soff), "n"(OFF) : "memory");
  return r;
}
DI void ld16_b_if6(u32x4& r, int gn, u32 voff, rsrc_t srd, u32 soff) {
  asm volatile("s_cmp_lg_u32 %4, 6\n\ts_cbranch_scc1 1f\n\tbuffer_load_dwordx4 %0, %1, %2, %3 offen" BPOL "\n1:" : "+v"(r) : "v"(voff), "s"(srd), "s"(soff), "s"(gn) : "scc", "memory");
}
DI u32x4 ld16(u32 voff, rsrc_t srd, u32 soff) {
  u32x4 r;
  asm volatile("buffer_load_dwordx4 %0, %1, %2, %3 offen" : "=v"(r) : "v"(voff), "s"(srd), "s"(soff) : "memory");
  return r;
}
DI u32x2 ld8_sc1(u32 voff, rsrc_t srd, u32 soff) {
  u32x2 r;
  asm volatile("buffer_load_dwordx2 %0, %1, %2, %3 offen sc1" : "=v"(r) : "v"(voff), "s"(srd), "s"(soff) : "memory");
  return r;
}
DI u32 ld2(u32 voff, rsrc_t srd, u32 soff) {
  u32 r;
  asm volatile("buffer_load_ushort %0, %1, %2, %3 offen" : "=v"(r) : "v"(voff), "s"(srd), "s"(soff) : "memory");
  return r;
}
DI void st16_sc1(u32x4 v, u32 voff, rsrc_t srd, u32 soff) {
  asm volatile("buffer_store_dwordx4 %0, %1, %2, %3 offen sc1\n\ts_nop 1" ::"v"(v), "v"(voff), "s"(srd), "s"(soff) : "memory");
}
DI void st8_sc1(u32x2 v, u32 voff, rsrc_t srd, u32 soff) {
  asm volatile("buffer_store_dwordx2 %0, %1, %2, %3 offen sc1" ::"v"(v), "v"(voff), "s"(srd), "s"(soff) : "memory");
}

DI u32x4 lds16(u32 a) { return *(const LDSP u32x4*)(size_t)a; }
DI u32x2 lds8(u32 a) { return *(const volatile LDSP u32x2*)(size_t)a; }
DI float lds4f(u32 a) { return *(const LDSP float*)(size_t)a; }
DI f32x4 lds16f(u32 a) { return *(const LDSP f32x4*)(size_t)a; }
DI void sts16f(u32 a, f32x4 v) { *(LDSP f32x4*)(size_t)a = v; }
DI void sts16(u32 a, u32x4 v) { *(LDSP u32x4*)(size_t)a = v; }
DI void sts8(u32 a, u32x2 v) { *(volatile LDSP u32x2*)(size_t)a = v; }
DI void sts4f(u32 a, float v) { *(LDSP float*)(size_t)a = v; }
DI u32x2 ldstr(u32 a) {
  return __builtin_bit_cast(u32x2, __builtin_amdgcn_ds_read_tr16_b64_v4i16((LDSP s16x4*)(size_t)a)); }
DI u32x4 cat(u32x2 lo, u32x2 hi) { return (u32x4){lo[0], lo[1], hi[0], hi[1]}; }
DI u32x4 ldfrag(u32 a, u32 d) { u32x2 lo = ldstr(a), hi = ldstr(a + d); return cat(lo, hi); }

DI f32x4 mfma32(u32x4 a, u32x4 b, f32x4 c) {
  return __builtin_amdgcn_mfma_f32_16x16x32_bf16(__builtin_bit_cast(bf16x8, a), __builtin_bit_cast(bf16x8, b), c, 0, 0, 0);
}
DI f32x16 mfmaL(u32x4 a, u32x4 b, f32x16 c) {
  return __builtin_amdgcn_mfma_f32_32x32x16_bf16(__builtin_bit_cast(bf16x8, a), __builtin_bit_cast(bf16x8, b), c, 0, 0, 0);
}
DI u32 pk2(float a, float b) { bf16x2 v = {(__bf16)a, (__bf16)b}; return __builtin_bit_cast(u32, v); }
DI float blo(u32 u) { return __uint_as_float(u << 16); }
DI float bhi(u32 u) { return __uint_as_float(u & 0xffff0000u); }
DI float rbf(float a) { return blo(pk2(a, a)); }
DI float dot2(u32 a, u32 b, float c) {
  return __builtin_amdgcn_fdot2_f32_bf16(__builtin_bit_cast(bf16x2, a), __builtin_bit_cast(bf16x2, b), c, false);
}
DI f32x2 fma2(f32x2 a, f32x2 b, f32x2 c) { return __builtin_elementwise_fma(a, b, c); }
DI f32x2 bc2(float a) { return (f32x2){a, a}; }
DI f32x2 lo2(f32x4 v) { return (f32x2){v[0], v[1]}; }
DI f32x2 hi2(f32x4 v) { return (f32x2){v[2], v[3]}; }
DI u32 pk2v(f32x2 v) { return pk2(v[0], v[1]); }
DI f32x2 gelu2(f32x2 x) {
  f32x2 u = fma2(x * x, bc2(-0.10294324f), bc2(-2.3022082f)) * x;
  f32x2 e = {__builtin_amdgcn_exp2f(u[0]), __builtin_amdgcn_exp2f(u[1])};
  e = e + bc2(1.0f);
  f32x2 r = {__builtin_amdgcn_rcpf(e[0]), __builtin_amdgcn_rcpf(e[1])};
  return x * r;
}
DI void red2g(float& a, float& b) {
  auto s = __builtin_amdgcn_permlane32_swap(__float_as_uint(a), __float_as_uint(b), false, false);
  float c = __uint_as_float(s[0]) + __uint_as_float(s[1]);
  auto s2 = __builtin_amdgcn_permlane16_swap(__float_as_uint(c), __float_as_uint(c), false, false);
  float d = __uint_as_float(s2[0]) + __uint_as_float(s2[1]);
  auto s3 = __builtin_amdgcn_permlane32_swap(__float_as_uint(d), __float_as_uint(d), false, false);
  a = __uint_as_float(s3[0]);
  b = __uint_as_float(s3[1]);
}
template <int CTRL> DI float dpp(float v) {
  return __uint_as_float(__builtin_amdgcn_update_dpp(0, __float_as_uint(v), CTRL, 0xF, 0xF, true));
}
DI void hedgehog16(const f32x16& p, float s, u32x4 (&o)[4]) {
  float mx = fmaxf(p[0], p[1]), mn = fminf(p[0], p[1]);
#pragma unroll
  for (int i = 2; i < 16; i += 2) { mx = fmaxf(fmaxf(mx, p[i]), p[i + 1]); mn = fminf(fminf(mn, p[i]), p[i + 1]); }
  const float c = s * 1.44269504f;
  const float a = -mx * c, b = mn * c;
  f32x2 ep[8], en[8];
#pragma unroll
  for (int i = 0; i < 8; ++i) {
    const f32x2 v = {p[2 * i], p[2 * i + 1]};
    const f32x2 u = fma2(v, bc2(c), bc2(a)), w = fma2(v, bc2(-c), bc2(b));
    ep[i] = (f32x2){__builtin_amdgcn_exp2f(u[0]), __builtin_amdgcn_exp2f(u[1])};
    en[i] = (f32x2){__builtin_amdgcn_exp2f(w[0]), __builtin_amdgcn_exp2f(w[1])};
  }
  const f32x2 sp = ((ep[0] + ep[1]) + (ep[2] + ep[3])) + ((ep[4] + ep[5]) + (ep[6] + ep[7]));
  const f32x2 sn = ((en[0] + en[1]) + (en[2] + en[3])) + ((en[4] + en[5]) + (en[6] + en[7]));
  const float ip = __builtin_amdgcn_rcpf(sp[0] + sp[1]), in = __builtin_amdgcn_rcpf(sn[0] + sn[1]);
#pragma unroll
  for (int i = 0; i < 8; ++i) {
    o[i >> 2][i & 3] = pk2v(ep[i] * bc2(ip));
    o[2 + (i >> 2)][i & 3] = pk2v(en[i] * bc2(in));
  }
}
DI void ln_fin(float s, float q, float invn, float& r, float& m) {
  m = s * invn;
  float v = fmaxf(q * invn - m * m, 0.0f);
  float sd = rbf(__builtin_amdgcn_sqrtf(rbf(rbf(v) + 1e-5f)));
  r = __builtin_amdgcn_rcpf(sd);
  m = rbf(m);
}

#define POLL_SLEEP 6
constexpr int SPIN_CAP = 1 << 17;
DI void poll_flags(u32 voff, bool act, rsrc_t srd, u32 soff, u32 tlo, u32 thi) {
#pragma nounroll
  for (int it = 0; it < SPIN_CAP; ++it) {
    u32x2 v;
    asm volatile("buffer_load_dwordx2 %0, %1, %2, %3 offen sc1\n\ts_waitcnt vmcnt(0)" : "=&v"(v) : "v"(voff), "s"(srd), "s"(soff) : "memory");
    bool ok = !act || (v[0] == tlo && v[1] == thi);
    if (__all(ok)) break;
    __builtin_amdgcn_s_sleep(POLL_SLEEP);
  }
}
DI bool tag_ok(u32x4 q, u32 t) { return q[1] == t && q[3] == t; }
DI void poll_gran(u32x4& q, u32 voff, rsrc_t srd, u32 soff, u32 tlo) {
#pragma nounroll
  for (int it = 0; it < SPIN_CAP; ++it) {
    if (__all(tag_ok(q, tlo))) break;
    __builtin_amdgcn_s_sleep(POLL_SLEEP);
    asm volatile("buffer_load_dwordx4 %0, %1, %2, %3 offen sc1\n\ts_waitcnt vmcnt(0)" : "=&v"(q) : "v"(voff), "s"(srd), "s"(soff) : "memory");
  }
}
DI u32 opq(u32 v) { asm volatile("" : "+v"(v)); return v; }

#define STAMP(i)
#define STAMPC(i)

#define GQ_N 3
#define PREFETCH 0
#define CODE_PF 44000
#define OUT_SC1 1
#define PRIO 0
#define VM(x) do { x; ++vq; } while (0)
#define WAITQ(mark) WAITV(vq - (mark))

__global__ __launch_bounds__(512, 2) void fused_kernel(const char* __restrict__ x, const char* __restrict__ wqkv, const char* __restrict__ wphi,
                                                        const char* __restrict__ wo, const char* __restrict__ win, const char* __restrict__ wsp,
                                                        const char* __restrict__ bsp, const char* __restrict__ wout, char* __restrict__ out, char* ws) {
  __shared__ __attribute__((aligned(1024))) char smem[163840];
  const int tid = threadIdx.x, lane = tid & 63;
  const int w = __builtin_amdgcn_readfirstlane(tid >> 6);
  const int rw = w & 3, cw = w >> 2, i16 = lane & 15, g = lane >> 4;
  const int b = blockIdx.x, xcd = b & 7, r = b >> 3;
  const int Pl = r >> 3, t = (r >> 2) & 1, cl = r & 3;
  const int P = (xcd >> 1) * 4 + Pl, c = (xcd & 1) * 4 + cl;
  const int p = 2 * P + (cl & 1);
  const int c2 = (xcd & 1) * 4 + (t ? 1 + (cl >> 1) : 3 * (cl >> 1));
  const int rot = 2 * ((Pl + cl) & 3);
  u32 acv = 0, acb = 0; (void)acv; (void)acb;
  asm volatile("" ::"v"((u32)(size_t)(LDSP char*)smem));
  STAMP(0);
  STAMPC(30);

  {
    u64 pc;
    asm volatile("s_getpc_b64 %0" : "=s"(pc));
    if (w == 7) touch(L_JUNK, lane * 128, __builtin_amdgcn_make_buffer_rsrc((void*)(pc & ~255ull), 0, CODE_PF, 0x00020000), (r & 7) * 8192);
  }
  u64 ep;
  asm volatile("s_load_dwordx2 %0, %1, 0x0" : "=s"(ep) : "s"(ws) : "memory");
  const rsrc_t sx = __builtin_amdgcn_make_buffer_rsrc((void*)(x + (size_t)P * 131072), 0, 131072, 0x00020000);
  const u32 l16 = opq(lane * 16);
  const u32 vx0 = (16 * w + (lane >> 3)) * 1024 + (((lane & 7) ^ g) * 16);
  const u32 vx1 = (vx0 + 8192) ^ 64;
#define DMA_X(dst, n, e) dma16((dst) + w * 2048 + (e) * 1024, (e) ? vx1 : vx0, sx, (((n) + rot) & 7) * 128)
  DMA_X(0, 0, 0);
  DMA_X(0, 0, 1);
  const rsrc_t sa = __builtin_amdgcn_make_buffer_rsrc((void*)(wsp + (size_t)p * 262144), 0, 262144, 0x00020000);
  const rsrc_t sws = __builtin_amdgcn_make_buffer_rsrc((void*)ws, 0, WS_END, 0x00020000);
  const u32 vA0 = (lane >> 3) * 4096 + (((lane & 7) ^ g) * 16);
  const u32 vA1 = (vA0 + 32768) ^ 64;
#define DMA_U(u, base, e) dma16((base) + w * 2048 + (e) * 1024, (e) ? vA1 : vA0, sa, w * 512 + (((u) >> 1) & 3) * 128 + (((u) >> 3) * 32 + ((u) & 1) * 16) * 4096)
  const f32x4 zero4 = {0, 0, 0, 0};
  u32x4 ones4 = {0x3F803F80u, 0x3F803F80u, 0x3F803F80u, 0x3F803F80u};
  u32 tlo, thi;
  u32x4 af[4][8];
  u32x4 bfr[2][8];
  u32x2 gf;
  const u32 ard = w * 2048 + i16 * 128 + (((g >> 1) ^ (i16 >> 1)) * 16) + (g & 1) * 8;
#define READ_U(u, base)                                                                                             \
  {                                                                                                                 \
    const u32 ab_ = opq(ard + (base));                                                                              \
    _Pragma("unroll") for (int kk = 0; kk < 2; ++kk)                                                               \
      af[2 * ((u) >> 3) + ((u) & 1)][((u) & 6) + kk] = cat(lds8(ab_ ^ (64 * kk)), lds8(ab_ ^ (64 * kk + 32)));      \
  }
#define USE_EPOCH() { asm volatile("" : "+s"(ep)); ep += 1; tlo = (u32)ep; thi = (u32)(ep >> 32); PIN(ones4); }
#define EARLY_STATS(mark)                                                                                           \
  {                                                                                                                 \
    WAITQ(mark); PIN(xrow);                                                                                         \
    float s_ = 0, q_ = 0;                                                                                           \
    _Pragma("unroll") for (int e = 0; e < 4; ++e) { s_ = dot2(xrow[e], ones4[0], s_); q_ = dot2(xrow[e], xrow[e], q_); } \
    s_ += dpp<0x128>(s_); q_ += dpp<0x128>(q_);                                                                     \
    s_ += dpp<0x124>(s_); q_ += dpp<0x124>(q_);                                                                     \
    s_ += dpp<0x4E>(s_); q_ += dpp<0x4E>(q_);                                                                       \
    s_ += dpp<0xB1>(s_); q_ += dpp<0xB1>(q_);                                                                       \
    red2g(s_, q_);                                                                                                  \
    float r_, m_;                                                                                                   \
    ln_fin(s_, q_, 1.0f / 512, r_, m_);                                                                             \
    VM(st16_sc1((u32x4){__float_as_uint(r_), tlo, __float_as_uint(m_), tlo}, l16 << 22, sws, WS_XG + (P * 128 + (t * 8 + c) * 8 + w) * 16)); \
  }
#define GATHER_XS()                                                                                                 \
  WAITQ(mxg); PIN(xg);                                                                                              \
  poll_gran(xg, i16 * 16, sws, WS_XG + (P * 128 + 16 * w) * 16, tlo);                                               \
  sts4f(L_XS + (16 * w + i16) * 4, __uint_as_float(xg[0]));                                                         \
  sts4f(L_XS + 512 + (16 * w + i16) * 4, __uint_as_float(xg[2]));
#define RELANE() const int lane = opq(l16) >> 4, i16 = lane & 15, g = lane >> 4, tid = w * 64 + lane; (void)tid; (void)g; (void)i16
#define SET_FLAG(off) st8_sc1((u32x2){tlo, thi}, l16 << 22, sws, (off))
  const int gs = grp_start(Z2S, c2), gn = grp_start(Z2S, c2 + 1) - gs;
  const int trot = (p & 7) >= 5 ? (p & 7) - 5 : (p & 7);
#define TILE(j, n_) (((j) + trot) >= (n_) ? (j) + trot - (n_) : (j) + trot)
  const u32 zb0 = WS_ZB + (gs * 64 + 8 * w) * 1024;
#define DMA_WOUT(e)                                                                                                 \
  {                                                                                                                 \
    const rsrc_t so_ = __builtin_amdgcn_make_buffer_rsrc((void*)wout, 0, 688 * 1024, 0x00020000);                   \
    const int ws_ = grp_start(Z2S, w), wn_ = grp_start(Z2S, w + 1) - ws_;                                           \
    if ((e) < 6) dma16(L_WOUT + ((e) < 4 ? 2 * w + ((e) >> 1) : 16 + w) * 2048 + ((e) & 1) * 1024, vwo, so_, ((ws_ + TILE((e) >> 1, wn_)) * 2 + ((e) & 1)) * 8192); \
    else if ((e) == 6) dma16(L_WOUT + 24 * 2048 + w * 1024, vwo, so_, ((((w >> 1) == 0 ? 5 : ((w >> 1) == 1 ? 11 : ((w >> 1) == 2 ? 27 : 64))) + TILE(3, 6)) * 2 + (w & 1)) * 8192); \
  }
#define DMA_WOUT_AT(dst, e)                                                                                         \
  {                                                                                                                 \
    const rsrc_t so_ = __builtin_amdgcn_make_buffer_rsrc((void*)wout, 0, 688 * 1024, 0x00020000);                   \
    const int ws_ = grp_start(Z2S, w), wn_ = grp_start(Z2S, w + 1) - ws_;                                           \
    dma16((dst) + ((e) - 7) * 1024, vwo, so_, ((ws_ + TILE(wn_ - 1 - (((e) - 7) >> 1), wn_)) * 2 + (((e) - 7) & 1)) * 8192); \
  }
#define VWO() const u32 vwo = ((lane >> 2) & 7) * 1024 + c2 * 128 + (lane >> 5) * 64 + (((lane & 3) ^ (((lane >> 4) & 1) << 1)) * 16)
#define LOAD_B(u, so_)                                                                                              \
  bfr[u][0] = ld16_bo<0>(l16, sws, so_); bfr[u][1] = ld16_bo<1024>(l16, sws, so_);                                  \
  bfr[u][2] = ld16_bo<2048>(l16, sws, so_); bfr[u][3] = ld16_bo<3072>(l16, sws, so_);                               \
  bfr[u][4] = ld16_bo<0>(l16, sws, (so_) + 4096); bfr[u][5] = ld16_bo<1024>(l16, sws, (so_) + 4096);                \
  bfr[u][6] = ld16_bo<2048>(l16, sws, (so_) + 4096); bfr[u][7] = ld16_bo<3072>(l16, sws, (so_) + 4096);

  if (t == 0) {
    int vq = 2;
    const rsrc_t sw = __builtin_amdgcn_make_buffer_rsrc((void*)win, 0, 512 * 2752, 0x00020000);
    const int z2s = grp_start(Z2S, c), z2n = grp_start(Z2S, c + 1) - z2s;
    const int z1s = grp_start(Z1S, c), z1n = grp_start(Z1S, c + 1) - z1s;
    u32 vw[3];
    int wch[3];
#pragma unroll
    for (int e = 0; e < 3; ++e) {
      const int sg = 2 * e + (lane >> 5), hi = sg >= 3;
      wch[e] = (sg - 3 * hi) * 4 + ((lane & 3) ^ (hi << 1));
      vw[e] = wch[e] < 2 * z2n ? (16 * w + 8 * hi + ((lane >> 2) & 7)) * 2752 + 1376 + 32 * z2s + 16 * wch[e] : 0x7FFF0000u;
    }
#define DMA_W(bk, e) dma16(LZ_W + ((bk) % 3) * 24576 + w * 3072 + (e) * 1024, vw[e], sw, ((2 * (bk) + rot) & 7) * (64 * 2752))
#define UBASE(u) ((u) % 5 == 0 ? LZ_U : ((u) % 5 - 1) * 16384)
#define ISS_X(n) do { VM(DMA_X(((n) & 3) * 16384, n, 0)); VM(DMA_X(((n) & 3) * 16384, n, 1)); mx[n] = vq; } while (0)
#define ISS_U(u) do { VM(DMA_U(u, UBASE(u), 0)); VM(DMA_U(u, UBASE(u), 1)); mu[u] = vq; } while (0)
#define ISS_W(bk, e0, e1) do { _Pragma("unroll") for (int e_ = e0; e_ < e1; ++e_) VM(DMA_W(bk, e_)); mw[bk] = vq; } while (0)
    int mx[8], mw[8], mu[16];
    mx[0] = vq;
    ISS_W(0, 0, 3);
    u32x4 xrow;
    VM(xrow = ld16(l16, sx, (c * 8 + w) * 1024));
    const int mxr = vq;
    ISS_X(1);
    const u32 xa0 = opq((32 * rw + i16) * 128 + ((g ^ (i16 >> 1)) * 16)), xa1 = xa0 ^ 64;
    u32 ta[3];
    int tl[3];
    {
      const u32 trb = LZ_W + g * 1536 + ((lane >> 2) & 3) * 64 + ((((g & 1) << 1) | ((lane >> 1) & 1)) * 16) + (lane & 1) * 8;
#pragma unroll
      for (int j = 0; j < 3; ++j) {
        tl[j] = 3 * cw + (j + rw) % 3;
        ta[j] = (trb + (tl[j] >> 1) * 512) ^ ((tl[j] & 1) * 32);
      }
    }
#define NEXT_BLOCK(bk) _Pragma("unroll") for (int j = 0; j < 3; ++j) ta[j] = opq(ta[j] + ((bk) == 0 ? 0 : ((bk) % 3 == 0 ? -49152 : 24576)))
    f32x4 acc[2][3], csa = zero4;
#pragma unroll
    for (int j = 0; j < 3; ++j) acc[0][j] = acc[1][j] = zero4;
    u32x4 xf[16][2], wf[2][3];
    u32x4 xg;
    int mxg = 0;
    WAITQ(mw[0]);
    BARRIER();
    USE_EPOCH()
    STAMP(7);
#define P1_MEM(n, h)                                                                                                \
  {                                                                                                                 \
    if ((n) == 0) { if ((h) == 0) ISS_X(2); if ((h) == 1) ISS_W(1, 0, 2); if ((h) == 2) ISS_W(1, 2, 3); if ((h) == 3) ISS_X(3); } \
    if ((n) == 1) { if ((h) == 0) ISS_W(2, 0, 2); if ((h) == 1) ISS_W(2, 2, 3); if ((h) == 2) VM(DMA_X(0, 4, 0)); if ((h) == 3) { VM(DMA_X(0, 4, 1)); mx[4] = vq; } } \
    if ((n) == 2 || (n) == 4) { if ((h) < 2) { VM(DMA_X((((n) + 3) & 3) * 16384, (n) + 3, (h) & 1)); mx[(n) + 3] = vq; } else ISS_W((n) / 2 + 2, (h) - 2, (h) - 1); } \
    if ((n) == 3) { if ((h) == 0) ISS_W(3, 2, 3); else if ((h) < 3) { VM(DMA_X(32768, 6, (h) - 1)); mx[6] = vq; } else ISS_U(0); } \
    if ((n) == 5) { if ((h) == 0) ISS_W(4, 2, 3); if ((h) == 1) { VM(xg = ld16_sc1(i16 * 16, sws, WS_XG + (P * 128 + 16 * w) * 16)); mxg = vq; } \
                    if ((h) >= 2) { VM(DMA_U(1, UBASE(1), (h) - 2)); mu[1] = vq; } }                                 \
    if ((n) == 6) { if ((h) < 3) ISS_W(5, h, (h) + 1); else ISS_U(2); }                                             \
    if ((n) == 7) { if ((h) < 2) { VM(DMA_U(3, UBASE(3), h)); mu[3] = vq; } }                                       \
  }
#pragma unroll
    for (int ks = 0; ks <= 16; ++ks) {
      const int n = ks >> 1, kk = ks & 1;
      if (ks > 0 && ks < 16 && kk == 0) {
        { const int mk = (n & 1) ? mx[n] : (mx[n] > mw[n >> 1] ? mx[n] : mw[n >> 1]); WAITQ(mk); }
        BARRIER();
      }
      if (ks < 16 && (ks & 3) == 0) { NEXT_BLOCK(ks >> 2); }
      if (ks == 8) {
#pragma unroll
        for (int e = 0; e < 3; ++e)
          vw[e] = wch[e] < 2 * z1n ? (16 * w + 8 * (2 * e + (lane >> 5) >= 3) + ((lane >> 2) & 7)) * 2752 + 32 * z1s + 16 * wch[e] : 0x7FFF0000u;
      }
      const int k1 = ks > 0 ? ks - 1 : 0, q1 = k1 & 1;
#pragma unroll
      for (int m = 0; m < 7; ++m) {
        if (ks > 0) {
          if (m < 6) acc[m & 1][m >> 1] = mfma32(xf[k1][m & 1], wf[q1][m >> 1], acc[m & 1][m >> 1]);
          else csa = mfma32(ones4, wf[q1][0], csa);
          SCHED();
        }
        if (ks < 16) {
          if (m == 0) { P1_MEM(n, 2 * kk) xf[ks & 15][0] = lds16((kk ? xa1 : xa0) + (n & 3) * 16384); xf[ks & 15][1] = lds16((kk ? xa1 : xa0) + (n & 3) * 16384 + 2048); }
          else if (m < 4) wf[kk][m - 1] = ldfrag(ta[m - 1] + (ks & 3) * 6144, 256);
          if (m == 4) P1_MEM(n, 2 * kk + 1)
        }
        SCHED();
      }
      if (ks == 2) EARLY_STATS(mxr)
    }
    GATHER_XS()
    sts4f(L_CS + tl[0] * 64 + i16 * 4, csa[0]);
    WAITQ(mu[1] > mw[4] ? mu[1] : mw[4]);
    READ_U(0, UBASE(0)) READ_U(1, UBASE(1))
    BARRIER();
    STAMP(1);
    u32x4 zp[3];
    ISS_W(6, 0, 3);
    {
      f32x2 ps[4], pq[4];
      float cs[3];
#pragma unroll
      for (int j = 0; j < 3; ++j) cs[j] = -lds4f(L_CS + tl[j] * 64 + i16 * 4);
#pragma unroll
      for (int rt = 0; rt < 2; ++rt) {
        const f32x4 rr = lds16f(L_XS + (32 * rw + 16 * rt + 4 * g) * 4), mm = lds16f(L_XS + 512 + (32 * rw + 16 * rt + 4 * g) * 4);
        f32x2 z0[3], z1[3];
#pragma unroll
        for (int j = 0; j < 3; ++j) {
          z0[j] = gelu2(fma2(lo2(mm), bc2(cs[j]), lo2(acc[rt][j])) * lo2(rr));
          z1[j] = gelu2(fma2(hi2(mm), bc2(cs[j]), hi2(acc[rt][j])) * hi2(rr));
          zp[j][2 * rt] = pk2v(z0[j]); zp[j][2 * rt + 1] = pk2v(z1[j]);
        }
        ps[2 * rt] = z0[0] + z0[1] + z0[2]; ps[2 * rt + 1] = z1[0] + z1[1] + z1[2];
        pq[2 * rt] = fma2(z0[2], z0[2], fma2(z0[1], z0[1], z0[0] * z0[0]));
        pq[2 * rt + 1] = fma2(z1[2], z1[2], fma2(z1[1], z1[1], z1[0] * z1[0]));
        SCHED();
        if (rt == 0) { ISS_U(4); ISS_U(5); }
        else ISS_U(6);
        SCHED();
      }
      const bool b3 = lane & 8, b2 = lane & 4, b1 = lane & 2;
#pragma unroll
      for (int m = 0; m < 2; ++m)
#pragma unroll
        for (int e = 0; e < 2; ++e) {
          ps[m][e] = (b3 ? ps[m + 2][e] : ps[m][e]) + dpp<0x128>(b3 ? ps[m][e] : ps[m + 2][e]);
          pq[m][e] = (b3 ? pq[m + 2][e] : pq[m][e]) + dpp<0x128>(b3 ? pq[m][e] : pq[m + 2][e]);
        }
#pragma unroll
      for (int e = 0; e < 2; ++e) {
        ps[0][e] = (b2 ? ps[1][e] : ps[0][e]) + dpp<0x141>(b2 ? ps[0][e] : ps[1][e]);
        pq[0][e] = (b2 ? pq[1][e] : pq[0][e]) + dpp<0x141>(b2 ? pq[0][e] : pq[1][e]);
      }
      float s_ = (b1 ? ps[0][1] : ps[0][0]) + dpp<0x4E>(b1 ? ps[0][0] : ps[0][1]);
      float q_ = (b1 ? pq[0][1] : pq[0][0]) + dpp<0x4E>(b1 ? pq[0][0] : pq[0][1]);
      s_ += dpp<0xB1>(s_);
      q_ += dpp<0xB1>(q_);
      const int row = 32 * rw + 16 * (i16 >> 3) + 4 * g + ((i16 >> 1) & 3);
      VM(st16_sc1((u32x4){__float_as_uint(s_), tlo, __float_as_uint(q_), tlo}, row * 16 + ((lane & 1) << 30), sws, WS_SG + ((P * 8 + c) * 2 + cw) * 2048));
      PIN(zp[0]); PIN(zp[1]); PIN(zp[2]);
    }
    STAMP(2);
    {
    RELANE();
#pragma unroll
    for (int j = 0; j < 3; ++j) acc[0][j] = acc[1][j] = zero4;
    csa = zero4;
    u32x4 q0, q1, q2, q3;
    int mq = 0, mz = 0;
    const u32 gvo = (2 * g) * 4096 + (32 * rw + 16 * cw + i16) * 16;
    const u32 gso = WS_SG + P * 32768;
#pragma unroll
    for (int ks = 0; ks <= 16; ++ks) {
      if (ks == 8) {
        WAITQ(mq); PIN(q0); PIN(q1); PIN(q2); PIN(q3);
#pragma nounroll
        for (int it = 0; it < SPIN_CAP; ++it) {
          if (__all(tag_ok(q0, tlo) && tag_ok(q1, tlo) && tag_ok(q2, tlo) && tag_ok(q3, tlo))) break;
          __builtin_amdgcn_s_sleep(POLL_SLEEP);
          asm volatile(
              "buffer_load_dwordx4 %0, %4, %5, %6 offen sc1\n\t"
              "buffer_load_dwordx4 %1, %4, %5, %6 offen offset:2048 sc1\n\t"
              "buffer_load_dwordx4 %2, %4, %5, %7 offen sc1\n\t"
              "buffer_load_dwordx4 %3, %4, %5, %7 offen offset:2048 sc1\n\t"
              "s_waitcnt vmcnt(0)"
              : "=&v"(q0), "=&v"(q1), "=&v"(q2), "=&v"(q3)
              : "v"(gvo), "s"(sws), "s"(gso), "s"(gso + 4096)
              : "memory");
        }
        float s = (__uint_as_float(q0[0]) + __uint_as_float(q1[0])) + (__uint_as_float(q2[0]) + __uint_as_float(q3[0]));
        float q = (__uint_as_float(q0[2]) + __uint_as_float(q1[2])) + (__uint_as_float(q2[2]) + __uint_as_float(q3[2]));
        red2g(s, q);
        float r2, m2;
        ln_fin(s, q, 1.0f / 688, r2, m2);
        sts4f(L_ZST + (32 * rw + 16 * cw + i16) * 4, r2);
        sts4f(L_ZST + 512 + (32 * rw + 16 * cw + i16) * 4, -m2 * r2);
        STAMP(3);
      }
      if (ks < 16 && (ks & 3) == 0) {
        if (ks > 0) { WAITQ(ks == 12 ? mz : mw[4 + (ks >> 2)]); BARRIER(); }
        if (ks == 12) { if (w == 0) SET_FLAG(WS_FA + (P * 8 + c) * 128); STAMP(5); }
        NEXT_BLOCK(4 + (ks >> 2));
      }
      if (ks == 8) {
#pragma unroll
        for (int rt = 0; rt < 2; ++rt) {
          const u32 ra = L_ZST + (32 * rw + 16 * rt + 4 * g) * 4;
          const f32x4 r4 = lds16f(ra), m4 = lds16f(ra + 512);
#pragma unroll
          for (int j = 0; j < 3; ++j) {
            const u32 u0 = zp[j][2 * rt], u1 = zp[j][2 * rt + 1];
            zp[j][2 * rt] = pk2v(fma2((f32x2){blo(u0), bhi(u0)}, lo2(r4), lo2(m4)));
            zp[j][2 * rt + 1] = pk2v(fma2((f32x2){blo(u1), bhi(u1)}, hi2(r4), hi2(m4)));
          }
        }
#pragma unroll
        for (int j = 0; j < 3; ++j)
          VM(st16_sc1(zp[j], l16, sws, (tl[j] < z2n) ? WS_ZB + ((z2s + tl[j]) * 64 + 4 * P + rw) * 1024 : 0x7FFF0000u));
        mz = vq;
        STAMP(4);
        SCHED();
      }
      if (ks >= 3 && ks <= 9) { WAITQ(mu[ks - 1]); READ_U(ks - 1, UBASE(ks - 1)) }
      if (ks >= 9 && ks <= 15) { WAITQ(mu[ks]); READ_U(ks, UBASE(ks)) }
      const int k1 = ks > 0 ? ks - 1 : 0, q1_ = k1 & 1;
#pragma unroll
      for (int m = 0; m < 7; ++m) {
        if (ks > 0) {
          if (m < 6) acc[m & 1][m >> 1] = mfma32(wf[q1_][m >> 1], xf[k1][m & 1], acc[m & 1][m >> 1]);
          else csa = mfma32(ones4, wf[q1_][0], csa);
          SCHED();
        }
        if (ks < 16 && m >= 1 && m < 4) wf[ks & 1][m - 1] = ldfrag(ta[m - 1] + (ks & 3) * 6144, 256);
        if (m == 0 || m == 4) {
          const int h = m == 4;
          if (ks >= 4 && ks <= 12) { VM(DMA_U(ks + 3, UBASE(ks + 3), h)); mu[ks + 3] = vq; }
        }
        if (m == 6) {
          if (ks >= 4 && ks <= 6) ISS_W(7, ks - 4, ks - 3);
        }
        if (m == 2) {
          if (ks == 5) { VM(q0 = ld16_sc1o<0>(gvo, sws, gso)); VM(q1 = ld16_sc1o<2048>(gvo, sws, gso)); }
          if (ks == 6) { VM(q2 = ld16_sc1o<0>(gvo, sws, gso + 4096)); VM(q3 = ld16_sc1o<2048>(gvo, sws, gso + 4096)); mq = vq; }
        }
        SCHED();
      }
      if ((ks & 3) == 3 || ks == 16) {
#pragma unroll
        for (int j = 0; j < 3; ++j) { PIN(acc[0][j]); PIN(acc[1][j]); }
        PIN(csa);
      }
    }
    sts4f(L_CS + 512 + tl[0] * 64 + i16 * 4, csa[0]);
    gf = ld8_sc1((lane >> 5) * 1024, sws, WS_FA + (16 * w + c2) * 128);
    BARRIER();
    VWO();
    DMA_WOUT(0) DMA_WOUT(1)
    WAITV(2);
    PIN(gf);
    if (!__all(gf[0] == tlo && gf[1] == thi)) poll_flags((lane >> 5) * 1024, true, sws, WS_FA + (16 * w + c2) * 128, tlo, thi);
    STAMP(6);
    {
      float xr[2], xm[2];
#pragma unroll
      for (int rt = 0; rt < 2; ++rt) {
        xr[rt] = lds4f(L_XS + (32 * rw + 16 * rt + i16) * 4);
        xm[rt] = -lds4f(L_XS + 512 + (32 * rw + 16 * rt + i16) * 4);
      }
      const u32 so0 = zb0 + TILE(0, gn) * 65536;
#pragma unroll
      for (int j = 0; j < 3; ++j) {
        const f32x4 cc = lds16f(L_CS + 512 + tl[j] * 64 + g * 16);
        bfr[0][3 * j] = ld16_b(l16, sws, so0 + 3 * j * 1024);
        bfr[0][3 * j + 1] = ld16_b(l16, sws, so0 + (3 * j + 1) * 1024);
        if (j < 2) bfr[0][3 * j + 2] = ld16_b(l16, sws, so0 + (3 * j + 2) * 1024);
        u32x4 zz;
#pragma unroll
        for (int rt = 0; rt < 2; ++rt) {
          zz[2 * rt] = pk2v(gelu2(fma2(lo2(cc), bc2(xm[rt]), lo2(acc[rt][j])) * bc2(xr[rt])));
          zz[2 * rt + 1] = pk2v(gelu2(fma2(hi2(cc), bc2(xm[rt]), hi2(acc[rt][j])) * bc2(xr[rt])));
        }
        st16_sc1(zz, l16, sws, (tl[j] < z1n) ? WS_Z1 + ((4 * P + rw) * 43 + z1s + tl[j]) * 1024 : 0x7FFF0000u);
        SCHED();
      }
    }
    }
  } else {
    int vq = 2;
    const rsrc_t sw = __builtin_amdgcn_make_buffer_rsrc((void*)wqkv, 0, 512 * 3072, 0x00020000);
    const u32 vw = (8 * w + ((lane >> 2) & 7)) * 3072 + 128 * c + ((lane >> 5) * 4 + ((lane & 3) ^ ((w & 1) << 1))) * 16;
#define DMA_S(n, e)                                                                                                 \
  do {                                                                                                              \
    if ((e) < 2) DMA_X(((n) % 3) * 40960, n, (e) & 1);                                                              \
    else dma16(((n) % 3) * 40960 + 16384 + w * 3072 + ((e) - 2) * 1024, vw, sw, (((n) + rot) & 7) * (64 * 3072) + ((e) - 2) * 1024); \
  } while (0)
#define ISS_Q(u, base) do { VM(DMA_U(u, base, 0)); VM(DMA_U(u, base, 1)); mu[u] = vq; } while (0)
    int ms[8], mu[16];
    VM(DMA_S(0, 2)); VM(DMA_S(0, 3)); VM(DMA_S(0, 4)); ms[0] = vq;
    u32x4 xrow;
    VM(xrow = ld16(l16, sx, ((8 + c) * 8 + w) * 1024));
    const int mxr = vq;
#pragma unroll
    for (int e = 0; e < 5; ++e) { VM(DMA_S(1, e)); ms[1] = vq; }
    const int l32 = lane & 31, hh = lane >> 5;
    u32 xa = (32 * rw + l32) * 128 + ((hh ^ ((l32 >> 1) & 7)) * 16);
    u32 wa = 16384 + hh * 3072 + cw * 512 + ((lane >> 2) & 3) * 64 + (((((g & 1) << 1) | ((lane >> 1) & 1)) ^ (hh << 1)) * 16) + (lane & 1) * 8;
    u32 ca = 16384 + g * 3072 + min(w, 5) * 512 + ((lane >> 2) & 3) * 64 + ((((g & 1) << 1) | ((lane >> 1) & 1)) * 16) + (lane & 1) * 8;
    const f32x16 zero16 = {0, 0, 0, 0, 0, 0, 0, 0, 0, 0, 0, 0, 0, 0, 0, 0};
    f32x16 acc[3] = {zero16, zero16, zero16};
    f32x4 csa[2] = {zero4, zero4};
    u32x4 xg;
    int mxg = 0;
    WAITQ(ms[0]);
    BARRIER();
    USE_EPOCH()
    STAMP(7);
#pragma unroll
    for (int n = 0; n < 8; ++n) {
      u32x4 xf[4], wf[4][3], cf[4];
      {
        const int d = n == 0 ? 0 : (n % 3 == 0 ? -81920 : 40960);
        xa = opq(xa + d); wa = opq(wa + d); ca = opq(ca + d);
      }
      if (n == 4) { WAITQ(mu[1]); READ_U(0, LQ_M) READ_U(1, LQ_M + 16384) }
      if (n == 7) { WAITQ(mu[3]); READ_U(2, LQ_M) READ_U(3, LQ_M + 16384) }
#pragma unroll
      for (int sq = 0; sq < 4; ++sq) {
        if (n <= 5) { VM(DMA_S(n + 2, sq)); if (sq == 3) { VM(DMA_S(n + 2, 4)); ms[n + 2] = vq; } }
        if (n == 6 && sq < 2) ISS_Q(4 + sq, LQ_A + sq * 16384);
        if (n == 7 && sq < 2) ISS_Q(6 + sq, sq * 16384);
        SCHED();
        xf[sq] = lds16(xa ^ (32 * sq));
#pragma unroll
        for (int m = 0; m < 3; ++m) wf[sq][m] = ldfrag(wa + sq * 6144 + m * 1024, 256);
        cf[sq] = ldfrag((ca ^ (32 * (sq & 1))) + (sq >> 1) * 12288, 256);
        SCHED();
      }
      if (n == 0) {
        rsrc_t sp = __builtin_amdgcn_make_buffer_rsrc((void*)wphi, 0, 16384, 0x00020000);
        VM(dma16(L_WPHI + (w & 1) * 1024, l16, sp, c * 2048 + (w & 1) * 1024));
        EARLY_STATS(mxr)
      }
      if (n == 1 || n == 2) ISS_Q(n - 1, LQ_M + (n - 1) * 16384);
      if (n == 4) { VM(xg = ld16_sc1(i16 * 16, sws, WS_XG + (P * 128 + 16 * w) * 16)); mxg = vq; }
      if (n == 5) ISS_Q(2, LQ_M);
      if (n == 6) ISS_Q(3, LQ_M + 16384);
      if (n == 7) {
#pragma unroll
        for (int sq = 0; sq < 4; ++sq) csa[sq & 1] = mfma32(ones4, cf[sq], csa[sq & 1]);
        GATHER_XS()
        sts4f(L_CS + w * 128 + i16 * 4, csa[0][0]);
        sts4f(L_CS + w * 128 + 64 + i16 * 4, csa[1][0]);
      }
      const int nw = n < 7 ? vq - ms[n + 1] : 63;
      BAR_IF(cw, 1, nw);
#pragma unroll
      for (int sq = 0; sq < 4; ++sq) {
#pragma unroll
        for (int m = 0; m < 3; ++m) acc[m] = mfmaL(wf[sq][m], xf[sq], acc[m]);
        if (n < 7) csa[sq & 1] = mfma32(ones4, cf[sq], csa[sq & 1]);
      }
      BAR_IF(cw, 0, nw);
    }
    STAMP(1);
    ISS_Q(8, LQ_M);
    READ_U(4, LQ_A) READ_U(5, LQ_A + 16384)
    const float xr = lds4f(L_XS + (32 * rw + l32) * 4), xm = -lds4f(L_XS + 512 + (32 * rw + l32) * 4);
    u32x4 wp0, wp1;
    {
      const u32 a = L_WPHI + (2 * cw + (lane & 1)) * 512 + (4 * hh + ((lane >> 2) & 3)) * 32 + (2 * (g & 1) + ((lane >> 1) & 1)) * 8;
      const u32x4 f = ldfrag(a, 256);
      const bool own = (lane >> 2) & 1;
#pragma unroll
      for (int e = 0; e < 4; ++e) { wp0[e] = own ? 0u : f[e]; wp1[e] = own ? f[e] : 0u; }
    }
#define FEATURES(e_, dst)                                                                                           \
  {                                                                                                                 \
    u32x4 kp[2];                                                                                                    \
    _Pragma("unroll") for (int a = 0; a < 4; ++a) {                                                                \
      const f32x4 cc = lds16f(L_CS + (2 * (e_) + cw) * 128 + a * 32 + hh * 16);                                      \
      kp[a >> 1][2 * (a & 1)] = pk2v(fma2(lo2(cc), bc2(xm), (f32x2){acc[e_][4 * a], acc[e_][4 * a + 1]}));          \
      kp[a >> 1][2 * (a & 1) + 1] = pk2v(fma2(hi2(cc), bc2(xm), (f32x2){acc[e_][4 * a + 2], acc[e_][4 * a + 3]}));  \
    }                                                                                                               \
    f32x16 pl = mfmaL(wp0, kp[0], zero16);                                                                          \
    pl = mfmaL(wp1, kp[1], pl);                                                                                     \
    hedgehog16(pl, xr, dst);                                                                                        \
  }
    {
      const int s_ = 32 * rw + l32;
      const u32 key = (((s_ >> 1) & 3) ^ (((s_ >> 3) & 1) << 1)) * 16;
      ISS_Q(9, LQ_M + 16384);
      {
        const u32 va = LQ_V + (s_ >> 3) * 1024 + cw * 512 + (s_ & 7) * 64 + hh * 8;
#pragma unroll
        for (int a = 0; a < 4; ++a) {
          const f32x4 cc = lds16f(L_CS + (4 + cw) * 128 + a * 32 + hh * 16);
          sts8(va + ((a * 16) ^ key), (u32x2){pk2v(fma2(lo2(cc), bc2(xm), (f32x2){acc[2][4 * a], acc[2][4 * a + 1]}) * bc2(xr)),
                                              pk2v(fma2(hi2(cc), bc2(xm), (f32x2){acc[2][4 * a + 2], acc[2][4 * a + 3]}) * bc2(xr))});
        }
      }
      WAITL0();
      ISS_Q(10, LQ_A); ISS_Q(11, LQ_A + 16384);
      u32x4 ph[4];
      FEATURES(1, ph)
      const u32 pa = LQ_PHI + (s_ >> 3) * 2048 + (2 * cw + hh) * 512 + (s_ & 7) * 64;
#pragma unroll
      for (int a = 0; a < 4; ++a) sts16(pa + ((a * 16) ^ key), ph[a]);
    }
    WAITQ(mu[9]);
    READ_U(6, 0) READ_U(7, 16384) READ_U(8, LQ_M) READ_U(9, LQ_M + 16384)
    BARRIER();
    STAMP(2);
    {
      const u32 kx = (((lane >> 3) & 1) ^ ((g & 1) << 1)) * 16;
      const u32 pa = LQ_PHI + g * 2048 + (w >> 1) * 512 + ((lane >> 2) & 3) * 64 + (((2 * (w & 1) + ((lane >> 1) & 1)) * 16) ^ kx) + (lane & 1) * 8;
      const u32 va = LQ_V + g * 1024 + (w >> 2) * 512 + ((lane >> 2) & 3) * 64 + (((2 * ((w >> 1) & 1) + ((lane >> 1) & 1)) * 16) ^ kx) + (lane & 1) * 8;
      u32x4 pf[4], vf[4];
#pragma unroll
      for (int k = 0; k < 4; ++k) {
        pf[k] = cat(ldstr(pa + k * 8192), ldstr((pa ^ 32) + k * 8192 + 256));
        vf[k] = cat(ldstr(va + k * 4096), ldstr((va ^ 32) + k * 4096 + 256));
      }
      f32x4 kv = zero4, kz = zero4;
#pragma unroll
      for (int k = 0; k < 4; ++k) { kv = mfma32(pf[k], vf[k], kv); kz = mfma32(pf[k], ones4, kz); }
      const u32 so = WS_KP + (c * 16 + P) * KP_SZ;
      u32x4 g0 = {__float_as_uint(kv[0]), tlo, __float_as_uint(kv[1]), tlo}, g1 = {__float_as_uint(kv[2]), tlo, __float_as_uint(kv[3]), tlo};
      u32x4 g2 = {__float_as_uint((lane & 1) ? kz[2] : kz[0]), tlo, __float_as_uint((lane & 1) ? kz[3] : kz[1]), tlo};
      PIN(g0); PIN(g1); PIN(g2);
      asm volatile("s_nop 7" ::: "memory");
      VM(st16_sc1(g0, l16, sws, so + w * 2048));
      VM(st16_sc1(g1, l16, sws, so + w * 2048 + 1024));
      VM(st16_sc1(g2, g * 32 + (lane & 1) * 16 + ((i16 >> 1) << 26), sws, so + 16384 + w * 128));
    }
    STAMP(3);
    ISS_Q(12, LQ_M); ISS_Q(13, LQ_M + 16384);
    WAITQ(mu[11]);
    READ_U(10, LQ_A) READ_U(11, LQ_A + 16384)
    BARRIER();
    ISS_Q(14, LQ_A); ISS_Q(15, LQ_A + 16384);
    u32x4 phq[4];
    FEATURES(0, phq)
    PIN(phq[0]); PIN(phq[1]); PIN(phq[2]); PIN(phq[3]);
    STAMP(4);
    if (w >= 5) {
      const int id = 2 * P + (w == 7 ? (lane & 1) : w - 5), wq = id >> 2, gq = id & 3;
      const u32 vi0 = (4 * g) * KP_SZ + (w == 7 ? 16384 + wq * 128 + gq * 32 : wq * 2048 + (gq * 16 + i16) * 16);
      const u32 vi1 = vi0 + (w == 7 ? 16 : 1024);
      const u32 vo = (w == 7 ? (WS_KZ - WS_KF) + c * 512 + id * 16 + ((i16 >> 1) << 25) : c * 8192 + id * 256 + i16 * 16) + (g << 28);
      float sa[4];
#pragma nounroll
      for (int it = 0; it < SPIN_CAP; ++it) {
        u32x4 a[4][2];
#pragma unroll
        for (int o = 0; o < 4; ++o) {
          a[o][0] = ld16_sc1(vi0, sws, WS_KP + (c * 16 + o) * KP_SZ);
          a[o][1] = ld16_sc1(vi1, sws, WS_KP + (c * 16 + o) * KP_SZ);
        }
        WAITV(0);
        bool ok = true;
#pragma unroll
        for (int o = 0; o < 4; ++o) { PIN(a[o][0]); PIN(a[o][1]); ok = ok && tag_ok(a[o][0], tlo) && tag_ok(a[o][1], tlo); }
#pragma unroll
        for (int e = 0; e < 4; ++e)
          sa[e] = (__uint_as_float(a[0][e >> 1][2 * (e & 1)]) + __uint_as_float(a[1][e >> 1][2 * (e & 1)])) +
                  (__uint_as_float(a[2][e >> 1][2 * (e & 1)]) + __uint_as_float(a[3][e >> 1][2 * (e & 1)]));
        if (__all(ok)) break;
        __builtin_amdgcn_s_sleep(POLL_SLEEP);
      }
      red2g(sa[0], sa[1]);
      red2g(sa[2], sa[3]);
      st16_sc1((u32x4){pk2(sa[0], sa[1]), tlo, pk2(sa[2], sa[3]), tlo}, vo, sws, WS_KF);
    }
    {
      VWO();
#pragma unroll
      for (int e = 0; e < 7; ++e) DMA_WOUT(e)
    }
    gf = ld8_sc1((lane >> 5) * 1024, sws, WS_FA + (16 * w + c2) * 128);
    WAITV(8);
    READ_U(12, LQ_M) READ_U(13, LQ_M + 16384) READ_U(14, LQ_A) READ_U(15, LQ_A + 16384)
    BARRIER();
#pragma unroll
    for (int a = 0; a < 4; ++a) sts16(L_WO1 + w * 4096 + a * 1024 + l16, phq[a]);
    WAITV(0);
    PIN(gf);
    if (!__all(gf[0] == tlo && gf[1] == thi)) poll_flags((lane >> 5) * 1024, true, sws, WS_FA + (16 * w + c2) * 128, tlo, thi);
    STAMP(6);
    { const u32 so_ = zb0 + TILE(0, gn) * 65536; LOAD_B(0, so_) }
  }

  tlo = __builtin_amdgcn_readfirstlane(tlo);
  thi = __builtin_amdgcn_readfirstlane(thi);
  int w2 = w, c22 = c2, p2 = p, cc2 = c;
  asm volatile("" : "+s"(w2), "+s"(tlo), "+s"(thi), "+s"(c22), "+s"(p2), "+s"(cc2));
  {
  const int w = w2, rw = w & 3, cw = w >> 2, c2 = c22, p = p2, c = cc2, P = p >> 1;
  RELANE();
  { const u32 so_ = zb0 + TILE(1, gn) * 65536; LOAD_B(1, so_) }
  STAMP(9);
  const u32 redw = opq(L_RED + w * 4096 + lane * 16), redr = opq(L_RED + rw * 1024 + lane * 16);
  const u32 fd = WS_FD + (p * 16 + c2) * 128;
  const u32 f3s = WS_FB + (P * 8 + (c2 > 0 ? c2 - 1 : 0)) * 128;
  const u32 vw3 = ((lane >> 2) & 7) * 1024 + c2 * 128 + (lane >> 5) * 64;
  const rsrc_t swo = __builtin_amdgcn_make_buffer_rsrc((void*)wo, 0, 512 * 1024, 0x00020000);
#define DMA_WO(dst, half, e) dma16((dst) + w * 4096 + (e) * 1024, (vw3 + (lane & 3) * 16) ^ (((e) & 1) * 32), swo, ((w + 8 * (half)) * 4 + (e)) * 8192)
  u32x4 kfg, kzg;
  u32x2 z1r[6], hold;
  u32 bs;
  f32x4 sm0;
#define EMIT_H(jt, sm_)                                                                                             \
  {                                                                                                                 \
    const f32x2 bb = bc2(blo(bs));                                                                                  \
    const u32 h0 = pk2v((f32x2){blo(z1r[jt][0]), bhi(z1r[jt][0])} * (lo2(sm_) + bb));                               \
    const u32 h1 = pk2v((f32x2){blo(z1r[jt][1]), bhi(z1r[jt][1])} * (hi2(sm_) + bb));                               \
    const u32 hb_ = WS_HF + (((4 * p + rw) * 2 + ((jt) >> 1)) * 8 + c2) * 1024;                                     \
    if ((jt) == 0 || ((jt) == 2 && gn == 6)) hold = (u32x2){h0, h1};                                                \
    else if ((jt) == 1 || ((jt) == 3 && gn == 6)) st16_sc1((u32x4){hold[0], hold[1], h0, h1}, l16, sws, hb_);       \
    else if ((jt) == 2) st16_sc1((u32x4){h0, h1, 0u, 0u}, l16, sws, hb_);                                           \
    else st16_sc1((u32x4){h0, tlo, h1, tlo}, l16, sws, WS_HL + (((4 * p + rw) * 8 + c2) * 2 + (gn - 1 - (jt))) * 1024); \
  }
#pragma unroll
  for (int j = 0; j < 6; ++j) {
    if (j < 5 || gn == 6) {
      const int u = j & 1;
      if (j == 0 || j == 2) WAITV(8);
      if (j == 1) WAITV(9);
      if (j == 3) { WAITV(11); WAITV_IF(cw, 1, 10); WAITV_IF(t, 0, 9); WAITV_IF(2 * cw - t, 2, 8); }
      if (j == 4) { WAITV(12); WAITV_IF(t, 0, 9); WAITV_IF(2 * gn + t, 11, 4); }
      if (j == 5) { WAITV(5); WAITV_IF(cw, 1, 4); }
      STAMP(17 + 2 * j);
#pragma unroll
      for (int i = 0; i < 8; ++i) PIN(bfr[u][i]);
      {
        f32x4 a_[4];
#pragma unroll
        for (int rt = 0; rt < 4; ++rt) {
          a_[rt] = zero4;
#pragma unroll
          for (int i = 0; i < 8; ++i) a_[rt] = mfma32(bfr[u][i], af[rt][i], a_[rt]);
          SCHED();
          if (rt > 0) sts16f(redw + u * 32768 + (rt - 1) * 1024, a_[rt - 1]);
          SCHED();
        }
        if (j == 1 && t != 0) {
          PIN(kfg); PIN(kzg);
          poll_gran(kfg, l16, sws, WS_KF + c * 8192 + w * 1024, tlo);
          poll_gran(kzg, g * 16, sws, WS_KZ + c * 512 + w * 64, tlo);
          sts8(L_KFS + (w >> 1) * 1024 + i16 * 64 + (w & 1) * 32 + g * 8, (u32x2){kfg[0], kfg[2]});
          sts8(L_KFS + 4096 + w * 32 + g * 8, (u32x2){kzg[0], kzg[2]});
          sts8(L_KFS + 4352 + (lane & 7) * 8, (u32x2){0u, 0u});
        }
        sts16f(redw + u * 32768 + 3072, a_[3]);
      }
      BAR_IF(cw, 1, 63);
      STAMP(18 + 2 * j);
      if (w == 4) {
        if (j == 0 && t == 0) SET_FLAG(WS_FB + (P * 8 + c) * 128);
        if (j == 3) SET_FLAG(fd);
        if (j == 4 && t != 0) SET_FLAG(WS_FC + (P * 8 + c) * 128);
        if ((j == 4 && gn == 5) || j == 5) SET_FLAG(fd + 1024);
      }
      if (j == 0 && t != 0) {
        kfg = ld16_sc1(l16, sws, WS_KF + c * 8192 + w * 1024);
        kzg = ld16_sc1(g * 16, sws, WS_KZ + c * 512 + w * 64);
      }
      u32x4 of[2];
      if (j == 2 && t != 0) {
        const int l32 = lane & 31, hh = lane >> 5;
        const bool own = ((lane >> 2) & 1) == hh;
        const u32 ka = own ? L_KFS + (2 * cw + hh) * 1024 + ((l32 & 3) + 4 * (l32 >> 3)) * 64 : L_KFS + 4352;
        const u32 zaa = own ? L_KFS + 4096 + (2 * cw + hh) * 64 : L_KFS + 4352;
        const u32 qa = L_WO1 + w * 4096 + l16;
        const f32x16 zero16 = {0, 0, 0, 0, 0, 0, 0, 0, 0, 0, 0, 0, 0, 0, 0, 0};
        f32x16 den = zero16, num = zero16;
#pragma unroll
        for (int a = 0; a < 4; ++a) {
          const u32x4 pq_ = lds16(qa + a * 1024);
          den = mfmaL(lds16(zaa + a * 16), pq_, den);
          num = mfmaL(lds16(ka + a * 16), pq_, num);
        }
        const float inv = __builtin_amdgcn_rcpf(den[0] + 1e-6f);
#pragma unroll
        for (int a = 0; a < 2; ++a) {
          of[a] = (u32x4){pk2(num[8 * a] * inv, num[8 * a + 1] * inv), pk2(num[8 * a + 2] * inv, num[8 * a + 3] * inv),
                          pk2(num[8 * a + 4] * inv, num[8 * a + 5] * inv), pk2(num[8 * a + 6] * inv, num[8 * a + 7] * inv)};
          PIN(of[a]);
        }
        STAMP(5);
      }
      if (j == 1) {
        if (cw == 0) {
          WAITV(0);
          PIN(gf);
          if (!__all(gf[0] == tlo && gf[1] == thi)) poll_flags((lane >> 5) * 128, true, sws, f3s, tlo, thi);
          rsrc_t sb = __builtin_amdgcn_make_buffer_rsrc((void*)bsp, 0, 4096, 0x00020000);
          bs = ld2(i16 * 2, sb, (64 * p + 16 * rw) * 2);
        }
      }
      if ((j == 1 || j == 2) && cw == 0) {
#pragma unroll
        for (int v = 3 * j - 3; v < 3 * j; ++v)
          z1r[v] = ld8_sc1(l16, sws, WS_Z1 + ((2 * p + (rw >> 1)) * 43 + gs + TILE(min(v, gn - 1), gn)) * 1024 + (rw & 1) * 8);
      }
      const u32 ra_ = redr + ((j + 1) & 1) * 32768;
      f32x4 sm, r_[2];
#pragma unroll
      for (int i = 0; i < 8; ++i) {
        if (j >= 1 && (i & 1) == 0 && cw == 0) {
          if (i == 2) sm = r_[0] + r_[1];
          if (i > 2) sm += r_[0] + r_[1];
          r_[0] = lds16f(ra_ + i * 4096); r_[1] = lds16f(ra_ + (i + 1) * 4096);
        }
        SCHED();
        if (j < 3) bfr[u][i] = ld16_b(l16, sws, zb0 + TILE(j + 2, gn) * 65536 + i * 1024);
        else if (j == 3) ld16_b_if6(bfr[1][i], gn, l16, sws, zb0 + TILE(5, gn) * 65536 + i * 1024);
        SCHED();
      }
      if (j == 0) gf = ld8_sc1((lane >> 5) * 128, sws, f3s);
      if (j == 2 && t != 0) {
        const u32 vo = ((lane >> 4) & 1) * 16384 + (lane >> 5) * 512 + i16 * 16;
        st16_sc1(of[0], vo, sws, WS_OF + ((8 * P + 2 * rw) * 16 + 2 * c + cw) * 1024);
        st16_sc1(of[1], vo, sws, WS_OF + ((8 * P + 2 * rw) * 16 + 2 * c + cw) * 1024 + 256);
      }
      if (j == 2) { PIN(bs); _Pragma("unroll") for (int v = 0; v < 3; ++v) PIN(z1r[v]); }
      if (j == 3) { _Pragma("unroll") for (int v = 3; v < 6; ++v) PIN(z1r[v]); }
      if (j >= 1 && cw == 0) {
        sm += r_[0] + r_[1];
        if (j == 1) sm0 = sm;
        if (j == 2) EMIT_H(0, sm0)
        if (j >= 2) EMIT_H(j >= 2 ? j - 1 : 0, sm)
      }
      if (j == 3) {
        if (t == 0) {
          VWO();
#pragma unroll
          for (int e = 2; e < 7; ++e) DMA_WOUT(e)
        }
#pragma unroll
        for (int e = 0; e < 4; ++e) DMA_WO(L_WO1, 0, e);
      }
      if (j == 3 && cw == 0) { WAITV(12); WAITV_IF(t, 0, 10); WAITV_IF(2 * gn + t, 11, 5); }
      if (j == 5) WAITV(1);
      BAR_IF(cw, 0, 63);
    }
  }
  {
    STAMP(10);
    const u32 bufA = L_RED + (gn & 1) * 32768, bufB = L_RED + 32768 - (gn & 1) * 32768;
    const u32 fv = (lane & 32) ? (P * 8 + ((lane >> 2) & 7)) * 128 : (WS_FD - WS_FC) + (p * 16 + (((lane >> 1) & 14) | cw)) * 128;
    gf = ld8_sc1(fv, sws, WS_FC);
    if (cw == 0) {
      const u32 ra_ = redr + (bufB - L_RED);
      const f32x4 sm = ((lds16f(ra_) + lds16f(ra_ + 4096)) + (lds16f(ra_ + 8192) + lds16f(ra_ + 12288))) +
           ((lds16f(ra_ + 16384) + lds16f(ra_ + 20480)) + (lds16f(ra_ + 24576) + lds16f(ra_ + 28672)));
      if (gn == 6) EMIT_H(5, sm) else EMIT_H(4, sm)
    }
    RELANE();
#pragma unroll
    for (int e = 0; e < 4; ++e) DMA_WO(bufA, 1, e);
    WAITV(5); WAITV_IF(cw, 1, 4);
    PIN(gf);
    const bool okf = gf[0] == tlo && gf[1] == thi;
    const bool n0 = lane < 16;
    const bool all1 = __all(okf);
    if (!__all(!n0 || okf)) poll_flags(fv, n0, sws, WS_FC, tlo, thi);
    u32x4 hf[8], of[8];
#pragma unroll
    for (int e = 0; e < 4; ++e) hf[e] = ld16_b(l16, sws, WS_HF + ((4 * p + rw) * 16 + 2 * e + cw) * 1024);
    BARRIER();
    {
      VWO();
#pragma unroll
      for (int e = 7; e < 11; ++e) DMA_WOUT_AT(bufB + w * 4096, e)
    }
    if (!all1) poll_flags(fv, true, sws, WS_FC, tlo, thi);
#pragma unroll
    for (int e = 4; e < 8; ++e) hf[e] = ld16_b(l16, sws, WS_HF + ((4 * p + rw) * 16 + 2 * e + cw) * 1024);
    const u32 qb = ((lane >> 2) & 3) * 64 + ((((g & 1) << 1) | ((lane >> 1) & 1)) * 16) + (lane & 1) * 8;
    f32x4 a3[4];
#pragma unroll
    for (int ct = 0; ct < 4; ++ct) a3[ct] = zero4;
    u32x4 wf[3][4], hl[4][2];
#define RDW2(dst, lo0, hi0, offl, offh) _Pragma("unroll") for (int ct = 0; ct < 4; ++ct) dst[ct] = cat(ldstr(((lo0) ^ ((ct & 1) * 32)) + (offl) + (ct >> 1) * 512), ldstr(((hi0) ^ ((ct & 1) * 32)) + (offh) + (ct >> 1) * 512))
#define MM3(src, frag) _Pragma("unroll") for (int ct = 0; ct < 4; ++ct) a3[ct] = mfma32(src[ct], frag, a3[ct])
    const u32 lb = WS_HL + ((4 * p + rw) * 8 + cw) * 2048;
    const rsrc_t sxo = __builtin_amdgcn_make_buffer_rsrc((void*)(x + (size_t)p * 65536), 0, 65536, 0x00020000);
    const u32 w0 = opq(L_WOUT + qb + (g >> 1) * 1024 + (g & 1) * 256);
    const u32 wa = w0 + cw * 4096, wb = w0 + cw * 2048;
    const u32 oq = qb + g * 1024 + cw * 4096;
    const u32 oa = opq(L_WO1 + oq), ob = opq(bufA + oq);
    u32x4 xres;
    STAMP(11);
#define RDK(dst, k)                                                                                                 \
  if ((k) < 4) { RDW2(dst, wa, wa, (k) * 8192, (k) * 8192 + 2048); }                                                \
  else if ((k) < 8) { RDW2(dst, wb, w0, 32768 + ((k) - 4) * 4096, 49152 + ((k) - 4) * 2048); }                      \
  else if ((k) < 12) { RDW2(dst, oa, oa, ((k) - 8) * 8192, ((k) - 8) * 8192 + 256); }                               \
  else { RDW2(dst, ob, ob, ((k) - 12) * 8192, ((k) - 12) * 8192 + 256); }
    RDK(wf[0], 0)
    RDK(wf[1], 1)
#pragma unroll
    for (int k = 0; k < 16; ++k) {
      if (k == 0) WAITV(8);
      if (k == 4) WAITV(8);
      if (k == 8) WAITV(9);
      if (k == 10) { BARRIER(); STAMP(12); }
      if (k + 2 < 16) { RDK(wf[(k + 2) % 3], k + 2) }
      SCHED();
      if (k < 8) { PIN(hf[k]); MM3(wf[k % 3], hf[k]); }
      else { PIN(of[k - 8]); MM3(wf[k % 3], of[k - 8]); }
      SCHED();
      if (k < 4) {
        of[2 * k] = ld16_b(l16, sws, WS_OF + ((4 * p + rw) * 16 + 4 * k + cw) * 1024);
        of[2 * k + 1] = ld16_b(l16, sws, WS_OF + ((4 * p + rw) * 16 + 4 * k + 2 + cw) * 1024);
      } else if (k < 8) {
        if (k == 4) xres = ld16((tid >> 3) * 1024 + (tid & 7) * 16, sxo, c2 * 128);
        hl[k - 4][0] = ld16_sc1(l16, sws, lb + (k - 4) * 4096);
        hl[k - 4][1] = ld16_sc1(l16, sws, lb + (k - 4) * 4096 + 1024);
      }
    }
    STAMP(13);
    {
      const u32 wl_ = opq(bufB + (w0 - L_WOUT) + cw * 4096);
      u32x4 wg[4][4];
#pragma unroll
      for (int e = 0; e < 4; ++e) RDW2(wg[e], wl_, wl_, e * 8192, e * 8192 + 2048);
      WAITV(0);
      PIN(xres);
#pragma unroll
      for (int e = 0; e < 4; ++e) {
        PIN(hl[e][0]); PIN(hl[e][1]);
        poll_gran(hl[e][0], l16, sws, lb + e * 4096, tlo);
        poll_gran(hl[e][1], l16, sws, lb + e * 4096 + 1024, tlo);
        MM3(wg[e], ((u32x4){hl[e][0][0], hl[e][0][2], hl[e][1][0], hl[e][1][2]}));
      }
    }
    STAMP(14);
    const u32 fin = L_WOUT - 2048;
#pragma unroll
    for (int ct = 0; ct < 4; ++ct) sts16f(fin + 2048 + cw * 16384 + (16 * rw + i16) * 256 + (((4 * ct + g) ^ i16) * 16), a3[ct]);
    BARRIER();
    {
      const int row = tid >> 3;
      const u32 a = fin + 2048 + row * 256 + (((2 * (tid & 7)) ^ (row & 15)) * 16);
      const f32x4 s0 = lds16f(a) + lds16f(a + 16384), s1 = lds16f(a ^ 16) + lds16f((a ^ 16) + 16384);
      u32x4 o = {pk2(blo(xres[0]) + s0[0], bhi(xres[0]) + s0[1]), pk2(blo(xres[1]) + s0[2], bhi(xres[1]) + s0[3]),
                 pk2(blo(xres[2]) + s1[0], bhi(xres[2]) + s1[1]), pk2(blo(xres[3]) + s1[2], bhi(xres[3]) + s1[3])};
      st16_sc1(o, row * 1024 + (tid & 7) * 16, __builtin_amdgcn_make_buffer_rsrc((void*)(out + (size_t)p * 65536), 0, 65536, 0x00020000), c2 * 128);
    }
  }
  }
  if (b == 0 && w == 0 && l16 == 0) *(u64*)(ws + WS_EP) = ((u64)thi << 32) | tlo;
  STAMP(15);
  STAMPC(31);
}

extern "C" void kernel_launch(void* const* d_in, const int* in_sizes, int n_in, void* d_out, int out_size, void* d_ws, size_t ws_size,
                              hipStream_t stream) {
  fused_kernel<<<256, 512, 0, stream>>>((const char*)d_in[0], (const char*)d_in[1], (const char*)d_in[2], (const char*)d_in[3],
                                        (const char*)d_in[5], (const char*)d_in[6], (const char*)d_in[7], (const char*)d_in[8], (char*)d_out,
                                        (char*)d_ws);
}
```

```cpp
#include <hip/hip_runtime.h>
#include <hip/hip_bf16.h>

typedef unsigned int u32;
typedef unsigned long long u64;
typedef short s16x4 __attribute__((ext_vector_type(4)));
typedef __bf16 bf16x2 __attribute__((ext_vector_type(2)));
typedef __bf16 bf16x8 __attribute__((ext_vector_type(8)));
typedef float f32x16 __attribute__((ext_vector_type(16)));
typedef float f32x4 __attribute__((ext_vector_type(4)));
typedef float f32x2 __attribute__((ext_vector_type(2)));
typedef u32 u32x4 __attribute__((ext_vector_type(4)));
typedef u32 u32x2 __attribute__((ext_vector_type(2)));
typedef __amdgpu_buffer_rsrc_t rsrc_t;
#define LDSP __attribute__((address_space(3)))
#define DI __device__ __forceinline__

#define STAMPS 0

constexpr u32 WS_EP = 0;
constexpr u32 WS_FA = 8192;
constexpr u32 WS_FB = WS_FA + 16384;
constexpr u32 WS_FC = WS_FB + 16384;
constexpr u32 WS_FD = WS_FC + 16384;
constexpr u32 WS_XG = WS_FD + 65536;
constexpr u32 WS_SG = WS_XG + 32768;
constexpr u32 WS_ZB = WS_SG + 524288;
constexpr u32 WS_Z1 = WS_ZB + 43 * 65536;
constexpr u32 KP_SZ = 17408;
constexpr u32 WS_KP = WS_Z1 + 64 * 43 * 1024;
constexpr u32 WS_KF = WS_KP + 128 * KP_SZ;
constexpr u32 WS_KZ = WS_KF + 65536;
constexpr u32 WS_OF = WS_KZ + 4096;
constexpr u32 WS_HF = WS_OF + 128 * 16384;
constexpr u32 WS_HL = WS_HF + 128 * 16 * 1024;
constexpr u32 WS_ST = WS_HL + 128 * 16 * 1024;
constexpr u32 WS_END = WS_ST + 256 * 8 * 32 * 8;

constexpr u32 LZ_W = 65536;
constexpr u32 LZ_U = 139264;
constexpr u32 LQ_M = 122880;
constexpr u32 LQ_A = 81920;
constexpr u32 LQ_PHI = 49152;
constexpr u32 LQ_V = 32768;
constexpr u32 L_TAB = 155648;
constexpr u32 L_XS = L_TAB;
constexpr u32 L_CS = L_TAB + 1024;
constexpr u32 L_ZST = L_TAB + 3072;
constexpr u32 L_WPHI = L_TAB + 3072;
constexpr u32 L_KFS = L_TAB + 2048;
constexpr u32 L_JUNK = L_TAB + 7168;
constexpr u32 L_WOUT = 0;
constexpr u32 L_RED = 57344;
constexpr u32 L_WO1 = 122880;

constexpr u64 Z2S = 0x26211B16110B0500ull;
constexpr u64 Z1S = 0x26211C16110C0600ull;
DI int grp_start(u64 tab, int c) { return c >= 8 ? 43 : (int)((tab >> (8 * c)) & 0xFF); }

#define SCHED() __builtin_amdgcn_sched_barrier(0)
#define TIC()
#define TOC(acc)
#define WAITV(n) do { TIC(); asm volatile("s_waitcnt vmcnt(%0)" ::"n"(n) : "memory"); TOC(acv); SCHED(); } while (0)
#define BARRIER() do { SCHED(); TIC(); asm volatile("s_waitcnt lgkmcnt(0)\n\ts_barrier" ::: "memory"); TOC(acb); SCHED(); } while (0)
#define WAITV_IF(g_, v, n) do { TIC(); asm volatile("s_cmp_lg_u32 %0, %1\n\ts_cbranch_scc1 1f\n\ts_waitcnt vmcnt(%2)\n1:" ::"s"(g_), "n"(v), "n"(n) : "scc", "memory"); TOC(acv); SCHED(); } while (0)
#define BAR_IF(g_, v, n) do { SCHED(); asm volatile("s_cmp_lg_u32 %0, %1\n\ts_cbranch_scc1 1f\n\ts_waitcnt vmcnt(%2) lgkmcnt(0)\n\ts_barrier\n1:" ::"s"(g_), "n"(v), "n"(n) : "scc", "memory"); SCHED(); } while (0)
#define WAITL0() asm volatile("s_waitcnt lgkmcnt(0)" ::: "memory")
#define PIN(x) asm volatile("" : "+v"(x))

DI void dma16(u32 lds, u32 voff, rsrc_t srd, u32 soff) {
  asm volatile("s_mov_b32 m0, %0\n\ts_nop 0\n\tbuffer_load_dwordx4 %1, %2, %3 offen lds" ::"s"(lds), "v"(voff), "s"(srd), "s"(soff) : "memory");
}
DI void touch(u32 lds, u32 voff, rsrc_t srd, u32 soff) {
  asm volatile("s_mov_b32 m0, %0\n\ts_nop 0\n\tbuffer_load_dword %1, %2, %3 offen lds" ::"s"(lds), "v"(voff), "s"(srd), "s"(soff) : "memory");
}
DI u32x4 ld16_sc1(u32 voff, rsrc_t srd, u32 soff) {
  u32x4 r;
  asm volatile("buffer_load_dwordx4 %0, %1, %2, %3 offen sc1" : "=v"(r) : "v"(voff), "s"(srd), "s"(soff) : "memory");
  return r;
}
template <int OFF> DI u32x4 ld16_sc1o(u32 voff, rsrc_t srd, u32 soff) {
  u32x4 r;
  asm volatile("buffer_load_dwordx4 %0, %1, %2, %3 offen offset:%4 sc1" : "=v"(r) : "v"(voff), "s"(srd), "s"(soff), "n"(OFF) : "memory");
  return r;
}
#define BPOL " sc1"
DI u32x4 ld16_b(u32 voff, rsrc_t srd, u32 soff) {
  u32x4 r;
  asm volatile("buffer_load_dwordx4 %0, %1, %2, %3 offen" BPOL : "=v"(r) : "v"(voff), "s"(srd), "s"(soff) : "memory");
  return r;
}
template <int OFF> DI u32x4 ld16_bo(u32 voff, rsrc_t srd, u32 soff) {
  u32x4 r;
  asm volatile("buffer_load_dwordx4 %0, %1, %2, %3 offen offset:%4" BPOL : "=v"(r) : "v"(voff), "s"(srd), "s"(soff), "n"(OFF) : "memory");
  return r;
}
DI void ld16_b_if6(u32x4& r, int gn, u32 voff, rsrc_t srd, u32 soff) {
  asm volatile("s_cmp_lg_u32 %4, 6\n\ts_cbranch_scc1 1f\n\tbuffer_load_dwordx4 %0, %1, %2, %3 offen" BPOL "\n1:" : "+v"(r) : "v"(voff), "s"(srd), "s"(soff), "s"(gn) : "scc", "memory");
}
DI u32x4 ld16(u32 voff, rsrc_t srd, u32 soff) {
  u32x4 r;
  asm volatile("buffer_load_dwordx4 %0, %1, %2, %3 offen" : "=v"(r) : "v"(voff), "s"(srd), "s"(soff) : "memory");
  return r;
}
DI u32x2 ld8_sc1(u32 voff, rsrc_t srd, u32 soff) {
  u32x2 r;
  asm volatile("buffer_load_dwordx2 %0, %1, %2, %3 offen sc1" : "=v"(r) : "v"(voff), "s"(srd), "s"(soff) : "memory");
  return r;
}
DI u32 ld2(u32 voff, rsrc_t srd, u32 soff) {
  u32 r;
  asm volatile("buffer_load_ushort %0, %1, %2, %3 offen" : "=v"(r) : "v"(voff), "s"(srd), "s"(soff) : "memory");
  return r;
}
DI void st16_sc1(u32x4 v, u32 voff, rsrc_t srd, u32 soff) {
  asm volatile("buffer_store_dwordx4 %0, %1, %2, %3 offen sc1\n\ts_nop 1" ::"v"(v), "v"(voff), "s"(srd), "s"(soff) : "memory");
}
DI void st8_sc1(u32x2 v, u32 voff, rsrc_t srd, u32 soff) {
  asm volatile("buffer_store_dwordx2 %0, %1, %2, %3 offen sc1" ::"v"(v), "v"(voff), "s"(srd), "s"(soff) : "memory");
}

DI u32x4 lds16(u32 a) { return *(const LDSP u32x4*)(size_t)a; }
DI u32x2 lds8(u32 a) { return *(const volatile LDSP u32x2*)(size_t)a; }
DI float lds4f(u32 a) { return *(const LDSP float*)(size_t)a; }
DI f32x4 lds16f(u32 a) { return *(const LDSP f32x4*)(size_t)a; }
DI void sts16f(u32 a, f32x4 v) { *(LDSP f32x4*)(size_t)a = v; }
DI void sts16(u32 a, u32x4 v) { *(LDSP u32x4*)(size_t)a = v; }
DI void sts8(u32 a, u32x2 v) { *(volatile LDSP u32x2*)(size_t)a = v; }
DI void sts4f(u32 a, float v) { *(LDSP float*)(size_t)a = v; }
DI u32x2 ldstr(u32 a) {
  return __builtin_bit_cast(u32x2, __builtin_amdgcn_ds_read_tr16_b64_v4i16((LDSP s16x4*)(size_t)a)); }
DI u32x4 cat(u32x2 lo, u32x2 hi) { return (u32x4){lo[0], lo[1], hi[0], hi[1]}; }
DI u32x4 ldfrag(u32 a, u32 d) { u32x2 lo = ldstr(a), hi = ldstr(a + d); return cat(lo, hi); }

DI f32x4 mfma32(u32x4 a, u32x4 b, f32x4 c) {
  return __builtin_amdgcn_mfma_f32_16x16x32_bf16(__builtin_bit_cast(bf16x8, a), __builtin_bit_cast(bf16x8, b), c, 0, 0, 0);
}
DI f32x16 mfmaL(u32x4 a, u32x4 b, f32x16 c) {
  return __builtin_amdgcn_mfma_f32_32x32x16_bf16(__builtin_bit_cast(bf16x8, a), __builtin_bit_cast(bf16x8, b), c, 0, 0, 0);
}
DI u32 pk2(float a, float b) { bf16x2 v = {(__bf16)a, (__bf16)b}; return __builtin_bit_cast(u32, v); }
DI float blo(u32 u) { return __uint_as_float(u << 16); }
DI float bhi(u32 u) { return __uint_as_float(u & 0xffff0000u); }
DI float rbf(float a) { return blo(pk2(a, a)); }
DI float dot2(u32 a, u32 b, float c) {
  return __builtin_amdgcn_fdot2_f32_bf16(__builtin_bit_cast(bf16x2, a), __builtin_bit_cast(bf16x2, b), c, false);
}
DI f32x2 fma2(f32x2 a, f32x2 b, f32x2 c) { return __builtin_elementwise_fma(a, b, c); }
DI f32x2 bc2(float a) { return (f32x2){a, a}; }
DI f32x2 lo2(f32x4 v) { return (f32x2){v[0], v[1]}; }
DI f32x2 hi2(f32x4 v) { return (f32x2){v[2], v[3]}; }
DI u32 pk2v(f32x2 v) { return pk2(v[0], v[1]); }
DI f32x2 gelu2(f32x2 x) {
  f32x2 u = fma2(x * x, bc2(-0.10294324f), bc2(-2.3022082f)) * x;
  f32x2 e = {__builtin_amdgcn_exp2f(u[0]), __builtin_amdgcn_exp2f(u[1])};
  e = e + bc2(1.0f);
  f32x2 r = {__builtin_amdgcn_rcpf(e[0]), __builtin_amdgcn_rcpf(e[1])};
  return x * r;
}
DI void red2g(float& a, float& b) {
  auto s = __builtin_amdgcn_permlane32_swap(__float_as_uint(a), __float_as_uint(b), false, false);
  float c = __uint_as_float(s[0]) + __uint_as_float(s[1]);
  auto s2 = __builtin_amdgcn_permlane16_swap(__float_as_uint(c), __float_as_uint(c), false, false);
  float d = __uint_as_float(s2[0]) + __uint_as_float(s2[1]);
  auto s3 = __builtin_amdgcn_permlane32_swap(__float_as_uint(d), __float_as_uint(d), false, false);
  a = __uint_as_float(s3[0]);
  b = __uint_as_float(s3[1]);
}
template <int CTRL> DI float dpp(float v) {
  return __uint_as_float(__builtin_amdgcn_update_dpp(0, __float_as_uint(v), CTRL, 0xF, 0xF, true));
}
DI void hedgehog16(const f32x16& p, float s, u32x4 (&o)[4]) {
  float mx = fmaxf(p[0], p[1]), mn = fminf(p[0], p[1]);
#pragma unroll
  for (int i = 2; i < 16; i += 2) { mx = fmaxf(fmaxf(mx, p[i]), p[i + 1]); mn = fminf(fminf(mn, p[i]), p[i + 1]); }
  const float c = s * 1.44269504f;
  const float a = -mx * c, b = mn * c;
  f32x2 ep[8], en[8];
#pragma unroll
  for (int i = 0; i < 8; ++i) {
    const f32x2 v = {p[2 * i], p[2 * i + 1]};
    const f32x2 u = fma2(v, bc2(c), bc2(a)), w = fma2(v, bc2(-c), bc2(b));
    ep[i] = (f32x2){__builtin_amdgcn_exp2f(u[0]), __builtin_amdgcn_exp2f(u[1])};
    en[i] = (f32x2){__builtin_amdgcn_exp2f(w[0]), __builtin_amdgcn_exp2f(w[1])};
  }
  const f32x2 sp = ((ep[0] + ep[1]) + (ep[2] + ep[3])) + ((ep[4] + ep[5]) + (ep[6] + ep[7]));
  const f32x2 sn = ((en[0] + en[1]) + (en[2] + en[3])) + ((en[4] + en[5]) + (en[6] + en[7]));
  const float ip = __builtin_amdgcn_rcpf(sp[0] + sp[1]), in = __builtin_amdgcn_rcpf(sn[0] + sn[1]);
#pragma unroll
  for (int i = 0; i < 8; ++i) {
    o[i >> 2][i & 3] = pk2v(ep[i] * bc2(ip));
    o[2 + (i >> 2)][i & 3] = pk2v(en[i] * bc2(in));
  }
}
DI void ln_fin(float s, float q, float invn, float& r, float& m) {
  m = s * invn;
  float v = fmaxf(q * invn - m * m, 0.0f);
  float sd = rbf(__builtin_amdgcn_sqrtf(rbf(rbf(v) + 1e-5f)));
  r = __builtin_amdgcn_rcpf(sd);
  m = rbf(m);
}

#define POLL_SLEEP 6
constexpr int SPIN_CAP = 1 << 17;
DI void poll_flags(u32 voff, bool act, rsrc_t srd, u32 soff, u32 tlo, u32 thi) {
#pragma nounroll
  for (int it = 0; it < SPIN_CAP; ++it) {
    u32x2 v;
    asm volatile("buffer_load_dwordx2 %0, %1, %2, %3 offen sc1\n\ts_waitcnt vmcnt(0)" : "=&v"(v) : "v"(voff), "s"(srd), "s"(soff) : "memory");
    bool ok = !act || (v[0] == tlo && v[1] == thi);
    if (__all(ok)) break;
    __builtin_amdgcn_s_sleep(POLL_SLEEP);
  }
}
DI bool tag_ok(u32x4 q, u32 t) { return q[1] == t && q[3] == t; }
DI void poll_gran(u32x4& q, u32 voff, rsrc_t srd, u32 soff, u32 tlo) {
#pragma nounroll
  for (int it = 0; it < SPIN_CAP; ++it) {
    if (__all(tag_ok(q, tlo))) break;
    __builtin_amdgcn_s_sleep(POLL_SLEEP);
    asm volatile("buffer_load_dwordx4 %0, %1, %2, %3 offen sc1\n\ts_waitcnt vmcnt(0)" : "=&v"(q) : "v"(voff), "s"(srd), "s"(soff) : "memory");
  }
}
DI u32 opq(u32 v) { asm volatile("" : "+v"(v)); return v; }

#define STAMP(i)
#define STAMPC(i)

#define GQ_N 3
#define PREFETCH 0
#define CODE_PF 44000
#define OUT_SC1 1
#define PRIO 0
#define UROT 0
static_assert(WS_ZB % 8192 == 0, "");
#define VM(x) do { x; ++vq; } while (0)
#define WAITQ(mark) WAITV(vq - (mark))

__global__ __launch_bounds__(512, 2) void fused_kernel(const char* __restrict__ x, const char* __restrict__ wqkv, const char* __restrict__ wphi,
                                                        const char* __restrict__ wo, const char* __restrict__ win, const char* __restrict__ wsp,
                                                        const char* __restrict__ bsp, const char* __restrict__ wout, char* __restrict__ out, char* ws) {
  __shared__ __attribute__((aligned(1024))) char smem[163840];
  const int tid = threadIdx.x, lane = tid & 63;
  const int w = __builtin_amdgcn_readfirstlane(tid >> 6);
  const int rw = w & 3, cw = w >> 2, i16 = lane & 15, g = lane >> 4;
  const int b = blockIdx.x, xcd = b & 7, r = b >> 3;
  const int Pl = r >> 3, t = (r >> 2) & 1, cl = r & 3;
  const int P = (xcd >> 1) * 4 + Pl, c = (xcd & 1) * 4 + cl;
  const int p = 2 * P + (cl & 1);
  const int c2 = (xcd & 1) * 4 + (t ? 1 + (cl >> 1) : 3 * (cl >> 1));
  const int rot = 2 * ((Pl + cl) & 3);
  u32 acv = 0, acb = 0; (void)acv; (void)acb;
  asm volatile("" ::"v"((u32)(size_t)(LDSP char*)smem));
  STAMP(0);
  STAMPC(30);

  {
    u64 pc;
    asm volatile("s_getpc_b64 %0" : "=s"(pc));
    if (w == 7) touch(L_JUNK, lane * 128, __builtin_amdgcn_make_buffer_rsrc((void*)(pc & ~255ull), 0, CODE_PF, 0x00020000), (r & 7) * 8192);
  }
  u64 ep;
  asm volatile("s_load_dwordx2 %0, %1, 0x0" : "=s"(ep) : "s"(ws) : "memory");
  const rsrc_t sx = __builtin_amdgcn_make_buffer_rsrc((void*)(x + (size_t)P * 131072), 0, 131072, 0x00020000);
  const u32 l16 = opq(lane * 16);
  const u32 vx0 = (16 * w + (lane >> 3)) * 1024 + (((lane & 7) ^ g) * 16);
  const u32 vx1 = (vx0 + 8192) ^ 64;
#define DMA_X(dst, n, e) dma16((dst) + w * 2048 + (e) * 1024, (e) ? vx1 : vx0, sx, (((n) + rot) & 7) * 128)
  DMA_X(0, 0, 0);
  DMA_X(0, 0, 1);
  const rsrc_t sa = __builtin_amdgcn_make_buffer_rsrc((void*)(wsp + (size_t)p * 262144), 0, 262144, 0x00020000);
  const rsrc_t sws = __builtin_amdgcn_make_buffer_rsrc((void*)ws, 0, WS_END, 0x00020000);
  const u32 vA0 = (lane >> 3) * 4096 + (((lane & 7) ^ g) * 16);
  const u32 vA1 = (vA0 + 32768) ^ 64;
  const int ux0 = UROT ? (cl >> 1) : 0, ux1 = UROT ? t : 0;
  const u32 uxs = w * 512 + ux1 * 256 + ux0 * 131072;
#define DMA_U(u, base, e) dma16((base) + w * 2048 + (e) * 1024, (e) ? vA1 : vA0, sa, uxs ^ ((((u) >> 1) & 3) * 128 + (((u) >> 3) * 32 + ((u) & 1) * 16) * 4096))
  const f32x4 zero4 = {0, 0, 0, 0};
  u32x4 ones4 = {0x3F803F80u, 0x3F803F80u, 0x3F803F80u, 0x3F803F80u};
  u32 tlo, thi;
  u32x4 af[4][8];
  u32x4 bfr[2][8];
  u32x2 gf;
  const u32 ard = w * 2048 + i16 * 128 + (((g >> 1) ^ (i16 >> 1)) * 16) + (g & 1) * 8;
#define READ_U(u, base)                                                                                             \
  {                                                                                                                 \
    const u32 ab_ = opq(ard + (base));                                                                              \
    _Pragma("unroll") for (int kk = 0; kk < 2; ++kk)                                                               \
      af[2 * ((u) >> 3) + ((u) & 1)][((u) & 6) + kk] = cat(lds8(ab_ ^ (64 * kk)), lds8(ab_ ^ (64 * kk + 32)));      \
  }
#define USE_EPOCH() { asm volatile("" : "+s"(ep)); ep += 1; tlo = (u32)ep; thi = (u32)(ep >> 32); PIN(ones4); }
#define EARLY_STATS(mark)                                                                                           \
  {                                                                                                                 \
    WAITQ(mark); PIN(xrow);                                                                                         \
    float s_ = 0, q_ = 0;                                                                                           \
    _Pragma("unroll") for (int e = 0; e < 4; ++e) { s_ = dot2(xrow[e], ones4[0], s_); q_ = dot2(xrow[e], xrow[e], q_); } \
    s_ += dpp<0x128>(s_); q_ += dpp<0x128>(q_);                                                                     \
    s_ += dpp<0x124>(s_); q_ += dpp<0x124>(q_);                                                                     \
    s_ += dpp<0x4E>(s_); q_ += dpp<0x4E>(q_);                                                                       \
    s_ += dpp<0xB1>(s_); q_ += dpp<0xB1>(q_);                                                                       \
    red2g(s_, q_);                                                                                                  \
    float r_, m_;                                                                                                   \
    ln_fin(s_, q_, 1.0f / 512, r_, m_);                                                                             \
    VM(st16_sc1((u32x4){__float_as_uint(r_), tlo, __float_as_uint(m_), tlo}, l16 << 22, sws, WS_XG + (P * 128 + (t * 8 + c) * 8 + w) * 16)); \
  }
#define GATHER_XS()                                                                                                 \
  WAITQ(mxg); PIN(xg);                                                                                              \
  poll_gran(xg, i16 * 16, sws, WS_XG + (P * 128 + 16 * w) * 16, tlo);                                               \
  sts4f(L_XS + (16 * w + i16) * 4, __uint_as_float(xg[0]));                                                         \
  sts4f(L_XS + 512 + (16 * w + i16) * 4, __uint_as_float(xg[2]));
#define RELANE() const int lane = opq(l16) >> 4, i16 = lane & 15, g = lane >> 4, tid = w * 64 + lane; (void)tid; (void)g; (void)i16
#define SET_FLAG(off) st8_sc1((u32x2){tlo, thi}, l16 << 22, sws, (off))
  const int gs = grp_start(Z2S, c2), gn = grp_start(Z2S, c2 + 1) - gs;
  const int trot = (p & 7) >= 5 ? (p & 7) - 5 : (p & 7);
#define TILE(j, n_) (((j) + trot) >= (n_) ? (j) + trot - (n_) : (j) + trot)
  const u32 zb0 = WS_ZB + (gs * 64 + 8 * w) * 1024 + ux1 * 4096;
#define DMA_WOUT(e)                                                                                                 \
  {                                                                                                                 \
    const rsrc_t so_ = __builtin_amdgcn_make_buffer_rsrc((void*)wout, 0, 688 * 1024, 0x00020000);                   \
    const int ws_ = grp_start(Z2S, w), wn_ = grp_start(Z2S, w + 1) - ws_;                                           \
    if ((e) < 6) dma16(L_WOUT + ((e) < 4 ? 2 * w + ((e) >> 1) : 16 + w) * 2048 + ((e) & 1) * 1024, vwo, so_, ((ws_ + TILE((e) >> 1, wn_)) * 2 + ((e) & 1)) * 8192); \
    else if ((e) == 6) dma16(L_WOUT + 24 * 2048 + w * 1024, vwo, so_, ((((w >> 1) == 0 ? 5 : ((w >> 1) == 1 ? 11 : ((w >> 1) == 2 ? 27 : 64))) + TILE(3, 6)) * 2 + (w & 1)) * 8192); \
  }
#define DMA_WOUT_AT(dst, e)                                                                                         \
  {                                                                                                                 \
    const rsrc_t so_ = __builtin_amdgcn_make_buffer_rsrc((void*)wout, 0, 688 * 1024, 0x00020000);                   \
    const int ws_ = grp_start(Z2S, w), wn_ = grp_start(Z2S, w + 1) - ws_;                                           \
    dma16((dst) + ((e) - 7) * 1024, vwo, so_, ((ws_ + TILE(wn_ - 1 - (((e) - 7) >> 1), wn_)) * 2 + (((e) - 7) & 1)) * 8192); \
  }
#define VWO() const u32 vwo = ((lane >> 2) & 7) * 1024 + c2 * 128 + (lane >> 5) * 64 + (((lane & 3) ^ (((lane >> 4) & 1) << 1)) * 16)
#define LOAD_B(u, so_)                                                                                              \
  bfr[u][0] = ld16_bo<0>(l16, sws, so_); bfr[u][1] = ld16_bo<1024>(l16, sws, so_);                                  \
  bfr[u][2] = ld16_bo<2048>(l16, sws, so_); bfr[u][3] = ld16_bo<3072>(l16, sws, so_);                               \
  bfr[u][4] = ld16_bo<0>(l16, sws, (so_) ^ 4096); bfr[u][5] = ld16_bo<1024>(l16, sws, (so_) ^ 4096);                \
  bfr[u][6] = ld16_bo<2048>(l16, sws, (so_) ^ 4096); bfr[u][7] = ld16_bo<3072>(l16, sws, (so_) ^ 4096);

  if (t == 0) {
    int vq = 2;
    const rsrc_t sw = __builtin_amdgcn_make_buffer_rsrc((void*)win, 0, 512 * 2752, 0x00020000);
    const int z2s = grp_start(Z2S, c), z2n = grp_start(Z2S, c + 1) - z2s;
    const int z1s = grp_start(Z1S, c), z1n = grp_start(Z1S, c + 1) - z1s;
    u32 vw[3];
    int wch[3];
#pragma unroll
    for (int e = 0; e < 3; ++e) {
      const int sg = 2 * e + (lane >> 5), hi = sg >= 3;
      wch[e] = (sg - 3 * hi) * 4 + ((lane & 3) ^ (hi << 1));
      vw[e] = wch[e] < 2 * z2n ? (16 * w + 8 * hi + ((lane >> 2) & 7)) * 2752 + 1376 + 32 * z2s + 16 * wch[e] : 0x7FFF0000u;
    }
#define DMA_W(bk, e) dma16(LZ_W + ((bk) % 3) * 24576 + w * 3072 + (e) * 1024, vw[e], sw, ((2 * (bk) + rot) & 7) * (64 * 2752))
#define UBASE(u) ((u) % 5 == 0 ? LZ_U : ((u) % 5 - 1) * 16384)
#define ISS_X(n) do { VM(DMA_X(((n) & 3) * 16384, n, 0)); VM(DMA_X(((n) & 3) * 16384, n, 1)); mx[n] = vq; } while (0)
#define ISS_U(u) do { VM(DMA_U(u, UBASE(u), 0)); VM(DMA_U(u, UBASE(u), 1)); mu[u] = vq; } while (0)
#define ISS_W(bk, e0, e1) do { _Pragma("unroll") for (int e_ = e0; e_ < e1; ++e_) VM(DMA_W(bk, e_)); mw[bk] = vq; } while (0)
    int mx[8], mw[8], mu[16];
    mx[0] = vq;
    ISS_W(0, 0, 3);
    u32x4 xrow;
    VM(xrow = ld16(l16, sx, (c * 8 + w) * 1024));
    const int mxr = vq;
    ISS_X(1);
    const u32 xa0 = opq((32 * rw + i16) * 128 + ((g ^ (i16 >> 1)) * 16)), xa1 = xa0 ^ 64;
    u32 ta[3];
    int tl[3];
    {
      const u32 trb = LZ_W + g * 1536 + ((lane >> 2) & 3) * 64 + ((((g & 1) << 1) | ((lane >> 1) & 1)) * 16) + (lane & 1) * 8;
#pragma unroll
      for (int j = 0; j < 3; ++j) {
        tl[j] = 3 * cw + (j + rw) % 3;
        ta[j] = (trb + (tl[j] >> 1) * 512) ^ ((tl[j] & 1) * 32);
      }
    }
#define NEXT_BLOCK(bk) _Pragma("unroll") for (int j = 0; j < 3; ++j) ta[j] = opq(ta[j] + ((bk) == 0 ? 0 : ((bk) % 3 == 0 ? -49152 : 24576)))
    f32x4 acc[2][3], csa = zero4;
#pragma unroll
    for (int j = 0; j < 3; ++j) acc[0][j] = acc[1][j] = zero4;
    u32x4 xf[16][2], wf[2][3];
    u32x4 xg;
    int mxg = 0;
    WAITQ(mw[0]);
    BARRIER();
    USE_EPOCH()
    STAMP(7);
#define P1_MEM(n, h)                                                                                                \
  {                                                                                                                 \
    if ((n) == 0) { if ((h) == 0) ISS_X(2); if ((h) == 1) ISS_W(1, 0, 2); if ((h) == 2) ISS_W(1, 2, 3); if ((h) == 3) ISS_X(3); } \
    if ((n) == 1) { if ((h) == 0) ISS_W(2, 0, 2); if ((h) == 1) ISS_W(2, 2, 3); if ((h) == 2) VM(DMA_X(0, 4, 0)); if ((h) == 3) { VM(DMA_X(0, 4, 1)); mx[4] = vq; } } \
    if ((n) == 2 || (n) == 4) { if ((h) < 2) { VM(DMA_X((((n) + 3) & 3) * 16384, (n) + 3, (h) & 1)); mx[(n) + 3] = vq; } else ISS_W((n) / 2 + 2, (h) - 2, (h) - 1); } \
    if ((n) == 3) { if ((h) == 0) ISS_W(3, 2, 3); else if ((h) < 3) { VM(DMA_X(32768, 6, (h) - 1)); mx[6] = vq; } else ISS_U(0); } \
    if ((n) == 5) { if ((h) == 0) ISS_W(4, 2, 3); if ((h) == 1) { VM(xg = ld16_sc1(i16 * 16, sws, WS_XG + (P * 128 + 16 * w) * 16)); mxg = vq; } \
                    if ((h) >= 2) { VM(DMA_U(1, UBASE(1), (h) - 2)); mu[1] = vq; } }                                 \
    if ((n) == 6) { if ((h) < 3) ISS_W(5, h, (h) + 1); else ISS_U(2); }                                             \
    if ((n) == 7) { if ((h) < 2) { VM(DMA_U(3, UBASE(3), h)); mu[3] = vq; } }                                       \
  }
#pragma unroll
    for (int ks = 0; ks <= 16; ++ks) {
      const int n = ks >> 1, kk = ks & 1;
      if (ks > 0 && ks < 16 && kk == 0) {
        { const int mk = (n & 1) ? mx[n] : (mx[n] > mw[n >> 1] ? mx[n] : mw[n >> 1]); WAITQ(mk); }
        BARRIER();
      }
      if (ks < 16 && (ks & 3) == 0) { NEXT_BLOCK(ks >> 2); }
      if (ks == 8) {
#pragma unroll
        for (int e = 0; e < 3; ++e)
          vw[e] = wch[e] < 2 * z1n ? (16 * w + 8 * (2 * e + (lane >> 5) >= 3) + ((lane >> 2) & 7)) * 2752 + 32 * z1s + 16 * wch[e] : 0x7FFF0000u;
      }
      const int k1 = ks > 0 ? ks - 1 : 0, q1 = k1 & 1;
#pragma unroll
      for (int m = 0; m < 7; ++m) {
        if (ks > 0) {
          if (m < 6) acc[m & 1][m >> 1] = mfma32(xf[k1][m & 1], wf[q1][m >> 1], acc[m & 1][m >> 1]);
          else csa = mfma32(ones4, wf[q1][0], csa);
          SCHED();
        }
        if (ks < 16) {
          if (m == 0) { P1_MEM(n, 2 * kk) xf[ks & 15][0] = lds16((kk ? xa1 : xa0) + (n & 3) * 16384); xf[ks & 15][1] = lds16((kk ? xa1 : xa0) + (n & 3) * 16384 + 2048); }
          else if (m < 4) wf[kk][m - 1] = ldfrag(ta[m - 1] + (ks & 3) * 6144, 256);
          if (m == 4) P1_MEM(n, 2 * kk + 1)
        }
        SCHED();
      }
      if (ks == 2) EARLY_STATS(mxr)
    }
    GATHER_XS()
    sts4f(L_CS + tl[0] * 64 + i16 * 4, csa[0]);
    WAITQ(mu[1] > mw[4] ? mu[1] : mw[4]);
    READ_U(0, UBASE(0)) READ_U(1, UBASE(1))
    BARRIER();
    STAMP(1);
    u32x4 zp[3];
    ISS_W(6, 0, 3);
    {
      f32x2 ps[4], pq[4];
      float cs[3];
#pragma unroll
      for (int j = 0; j < 3; ++j) cs[j] = -lds4f(L_CS + tl[j] * 64 + i16 * 4);
#pragma unroll
      for (int rt = 0; rt < 2; ++rt) {
        const f32x4 rr = lds16f(L_XS + (32 * rw + 16 * rt + 4 * g) * 4), mm = lds16f(L_XS + 512 + (32 * rw + 16 * rt + 4 * g) * 4);
        f32x2 z0[3], z1[3];
#pragma unroll
        for (int j = 0; j < 3; ++j) {
          z0[j] = gelu2(fma2(lo2(mm), bc2(cs[j]), lo2(acc[rt][j])) * lo2(rr));
          z1[j] = gelu2(fma2(hi2(mm), bc2(cs[j]), hi2(acc[rt][j])) * hi2(rr));
          zp[j][2 * rt] = pk2v(z0[j]); zp[j][2 * rt + 1] = pk2v(z1[j]);
        }
        ps[2 * rt] = z0[0] + z0[1] + z0[2]; ps[2 * rt + 1] = z1[0] + z1[1] + z1[2];
        pq[2 * rt] = fma2(z0[2], z0[2], fma2(z0[1], z0[1], z0[0] * z0[0]));
        pq[2 * rt + 1] = fma2(z1[2], z1[2], fma2(z1[1], z1[1], z1[0] * z1[0]));
        SCHED();
        if (rt == 0) { ISS_U(4); ISS_U(5); }
        else ISS_U(6);
        SCHED();
      }
      const bool b3 = lane & 8, b2 = lane & 4, b1 = lane & 2;
#pragma unroll
      for (int m = 0; m < 2; ++m)
#pragma unroll
        for (int e = 0; e < 2; ++e) {
          ps[m][e] = (b3 ? ps[m + 2][e] : ps[m][e]) + dpp<0x128>(b3 ? ps[m][e] : ps[m + 2][e]);
          pq[m][e] = (b3 ? pq[m + 2][e] : pq[m][e]) + dpp<0x128>(b3 ? pq[m][e] : pq[m + 2][e]);
        }
#pragma unroll
      for (int e = 0; e < 2; ++e) {
        ps[0][e] = (b2 ? ps[1][e] : ps[0][e]) + dpp<0x141>(b2 ? ps[0][e] : ps[1][e]);
        pq[0][e] = (b2 ? pq[1][e] : pq[0][e]) + dpp<0x141>(b2 ? pq[0][e] : pq[1][e]);
      }
      float s_ = (b1 ? ps[0][1] : ps[0][0]) + dpp<0x4E>(b1 ? ps[0][0] : ps[0][1]);
      float q_ = (b1 ? pq[0][1] : pq[0][0]) + dpp<0x4E>(b1 ? pq[0][0] : pq[0][1]);
      s_ += dpp<0xB1>(s_);
      q_ += dpp<0xB1>(q_);
      const int row = 32 * rw + 16 * (i16 >> 3) + 4 * g + ((i16 >> 1) & 3);
      VM(st16_sc1((u32x4){__float_as_uint(s_), tlo, __float_as_uint(q_), tlo}, row * 16 + ((lane & 1) << 30), sws, WS_SG + ((P * 8 + c) * 2 + cw) * 2048));
      PIN(zp[0]); PIN(zp[1]); PIN(zp[2]);
    }
    STAMP(2);
    {
    RELANE();
#pragma unroll
    for (int j = 0; j < 3; ++j) acc[0][j] = acc[1][j] = zero4;
    csa = zero4;
    u32x4 q0, q1, q2, q3;
    int mq = 0, mz = 0;
    const u32 gvo = (2 * g) * 4096 + (32 * rw + 16 * cw + i16) * 16;
    const u32 gso = WS_SG + P * 32768;
#pragma unroll
    for (int ks = 0; ks <= 16; ++ks) {
      if (ks == 8) {
        WAITQ(mq); PIN(q0); PIN(q1); PIN(q2); PIN(q3);
#pragma nounroll
        for (int it = 0; it < SPIN_CAP; ++it) {
          if (__all(tag_ok(q0, tlo) && tag_ok(q1, tlo) && tag_ok(q2, tlo) && tag_ok(q3, tlo))) break;
          __builtin_amdgcn_s_sleep(POLL_SLEEP);
          asm volatile(
              "buffer_load_dwordx4 %0, %4, %5, %6 offen sc1\n\t"
              "buffer_load_dwordx4 %1, %4, %5, %6 offen offset:2048 sc1\n\t"
              "buffer_load_dwordx4 %2, %4, %5, %7 offen sc1\n\t"
              "buffer_load_dwordx4 %3, %4, %5, %7 offen offset:2048 sc1\n\t"
              "s_waitcnt vmcnt(0)"
              : "=&v"(q0), "=&v"(q1), "=&v"(q2), "=&v"(q3)
              : "v"(gvo), "s"(sws), "s"(gso), "s"(gso + 4096)
              : "memory");
        }
        float s = (__uint_as_float(q0[0]) + __uint_as_float(q1[0])) + (__uint_as_float(q2[0]) + __uint_as_float(q3[0]));
        float q = (__uint_as_float(q0[2]) + __uint_as_float(q1[2])) + (__uint_as_float(q2[2]) + __uint_as_float(q3[2]));
        red2g(s, q);
        float r2, m2;
        ln_fin(s, q, 1.0f / 688, r2, m2);
        sts4f(L_ZST + (32 * rw + 16 * cw + i16) * 4, r2);
        sts4f(L_ZST + 512 + (32 * rw + 16 * cw + i16) * 4, -m2 * r2);
        STAMP(3);
      }
      if (ks < 16 && (ks & 3) == 0) {
        if (ks > 0) { WAITQ(ks == 12 ? mz : mw[4 + (ks >> 2)]); BARRIER(); }
        if (ks == 12) { if (w == 0) SET_FLAG(WS_FA + (P * 8 + c) * 128); STAMP(5); }
        NEXT_BLOCK(4 + (ks >> 2));
      }
      if (ks == 8) {
#pragma unroll
        for (int rt = 0; rt < 2; ++rt) {
          const u32 ra = L_ZST + (32 * rw + 16 * rt + 4 * g) * 4;
          const f32x4 r4 = lds16f(ra), m4 = lds16f(ra + 512);
#pragma unroll
          for (int j = 0; j < 3; ++j) {
            const u32 u0 = zp[j][2 * rt], u1 = zp[j][2 * rt + 1];
            zp[j][2 * rt] = pk2v(fma2((f32x2){blo(u0), bhi(u0)}, lo2(r4), lo2(m4)));
            zp[j][2 * rt + 1] = pk2v(fma2((f32x2){blo(u1), bhi(u1)}, hi2(r4), hi2(m4)));
          }
        }
#pragma unroll
        for (int j = 0; j < 3; ++j)
          VM(st16_sc1(zp[j], l16, sws, (tl[j] < z2n) ? WS_ZB + ((z2s + tl[j]) * 64 + 4 * P + rw) * 1024 : 0x7FFF0000u));
        mz = vq;
        STAMP(4);
        SCHED();
      }
      if (ks >= 3 && ks <= 9) { WAITQ(mu[ks - 1]); READ_U(ks - 1, UBASE(ks - 1)) }
      if (ks >= 9 && ks <= 15) { WAITQ(mu[ks]); READ_U(ks, UBASE(ks)) }
      const int k1 = ks > 0 ? ks - 1 : 0, q1_ = k1 & 1;
#pragma unroll
      for (int m = 0; m < 7; ++m) {
        if (ks > 0) {
          if (m < 6) acc[m & 1][m >> 1] = mfma32(wf[q1_][m >> 1], xf[k1][m & 1], acc[m & 1][m >> 1]);
          else csa = mfma32(ones4, wf[q1_][0], csa);
          SCHED();
        }
        if (ks < 16 && m >= 1 && m < 4) wf[ks & 1][m - 1] = ldfrag(ta[m - 1] + (ks & 3) * 6144, 256);
        if (m == 0 || m == 4) {
          const int h = m == 4;
          if (ks >= 4 && ks <= 12) { VM(DMA_U(ks + 3, UBASE(ks + 3), h)); mu[ks + 3] = vq; }
        }
        if (m == 6) {
          if (ks >= 4 && ks <= 6) ISS_W(7, ks - 4, ks - 3);
        }
        if (m == 2) {
          if (ks == 5) { VM(q0 = ld16_sc1o<0>(gvo, sws, gso)); VM(q1 = ld16_sc1o<2048>(gvo, sws, gso)); }
          if (ks == 6) { VM(q2 = ld16_sc1o<0>(gvo, sws, gso + 4096)); VM(q3 = ld16_sc1o<2048>(gvo, sws, gso + 4096)); mq = vq; }
        }
        SCHED();
      }
      if ((ks & 3) == 3 || ks == 16) {
#pragma unroll
        for (int j = 0; j < 3; ++j) { PIN(acc[0][j]); PIN(acc[1][j]); }
        PIN(csa);
      }
    }
    sts4f(L_CS + 512 + tl[0] * 64 + i16 * 4, csa[0]);
    gf = ld8_sc1((lane >> 5) * 1024, sws, WS_FA + (16 * w + c2) * 128);
    BARRIER();
    VWO();
    DMA_WOUT(0) DMA_WOUT(1)
    WAITV(2);
    PIN(gf);
    if (!__all(gf[0] == tlo && gf[1] == thi)) poll_flags((lane >> 5) * 1024, true, sws, WS_FA + (16 * w + c2) * 128, tlo, thi);
    STAMP(6);
    {
      float xr[2], xm[2];
#pragma unroll
      for (int rt = 0; rt < 2; ++rt) {
        xr[rt] = lds4f(L_XS + (32 * rw + 16 * rt + i16) * 4);
        xm[rt] = -lds4f(L_XS + 512 + (32 * rw + 16 * rt + i16) * 4);
      }
      const u32 so0 = zb0 + TILE(0, gn) * 65536;
#pragma unroll
      for (int j = 0; j < 3; ++j) {
        const f32x4 cc = lds16f(L_CS + 512 + tl[j] * 64 + g * 16);
        bfr[0][3 * j] = ld16_b(l16, sws, so0 ^ (3 * j * 1024));
        bfr[0][3 * j + 1] = ld16_b(l16, sws, so0 ^ ((3 * j + 1) * 1024));
        if (j < 2) bfr[0][3 * j + 2] = ld16_b(l16, sws, so0 ^ ((3 * j + 2) * 1024));
        u32x4 zz;
#pragma unroll
        for (int rt = 0; rt < 2; ++rt) {
          zz[2 * rt] = pk2v(gelu2(fma2(lo2(cc), bc2(xm[rt]), lo2(acc[rt][j])) * bc2(xr[rt])));
          zz[2 * rt + 1] = pk2v(gelu2(fma2(hi2(cc), bc2(xm[rt]), hi2(acc[rt][j])) * bc2(xr[rt])));
        }
        st16_sc1(zz, l16, sws, (tl[j] < z1n) ? WS_Z1 + ((4 * P + rw) * 43 + z1s + tl[j]) * 1024 : 0x7FFF0000u);
        SCHED();
      }
    }
    }
  } else {
    int vq = 2;
    const rsrc_t sw = __builtin_amdgcn_make_buffer_rsrc((void*)wqkv, 0, 512 * 3072, 0x00020000);
    const u32 vw = (8 * w + ((lane >> 2) & 7)) * 3072 + 128 * c + ((lane >> 5) * 4 + ((lane & 3) ^ ((w & 1) << 1))) * 16;
#define DMA_S(n, e)                                                                                                 \
  do {                                                                                                              \
    if ((e) < 2) DMA_X(((n) % 3) * 40960, n, (e) & 1);                                                              \
    else dma16(((n) % 3) * 40960 + 16384 + w * 3072 + ((e) - 2) * 1024, vw, sw, (((n) + rot) & 7) * (64 * 3072) + ((e) - 2) * 1024); \
  } while (0)
#define ISS_Q(u, base) do { VM(DMA_U(u, base, 0)); VM(DMA_U(u, base, 1)); mu[u] = vq; } while (0)
    int ms[8], mu[16];
    VM(DMA_S(0, 2)); VM(DMA_S(0, 3)); VM(DMA_S(0, 4)); ms[0] = vq;
    u32x4 xrow;
    VM(xrow = ld16(l16, sx, ((8 + c) * 8 + w) * 1024));
    const int mxr = vq;
#pragma unroll
    for (int e = 0; e < 5; ++e) { VM(DMA_S(1, e)); ms[1] = vq; }
    const int l32 = lane & 31, hh = lane >> 5;
    u32 xa = (32 * rw + l32) * 128 + ((hh ^ ((l32 >> 1) & 7)) * 16);
    u32 wa = 16384 + hh * 3072 + cw * 512 + ((lane >> 2) & 3) * 64 + (((((g & 1) << 1) | ((lane >> 1) & 1)) ^ (hh << 1)) * 16) + (lane & 1) * 8;
    u32 ca = 16384 + g * 3072 + min(w, 5) * 512 + ((lane >> 2) & 3) * 64 + ((((g & 1) << 1) | ((lane >> 1) & 1)) * 16) + (lane & 1) * 8;
    const f32x16 zero16 = {0, 0, 0, 0, 0, 0, 0, 0, 0, 0, 0, 0, 0, 0, 0, 0};
    f32x16 acc[3] = {zero16, zero16, zero16};
    f32x4 csa[2] = {zero4, zero4};
    u32x4 xg;
    int mxg = 0;
    WAITQ(ms[0]);
    BARRIER();
    USE_EPOCH()
    STAMP(7);
#pragma unroll
    for (int n = 0; n < 8; ++n) {
      u32x4 xf[4], wf[4][3], cf[4];
      {
        const int d = n == 0 ? 0 : (n % 3 == 0 ? -81920 : 40960);
        xa = opq(xa + d); wa = opq(wa + d); ca = opq(ca + d);
      }
      if (n == 4) { WAITQ(mu[1]); READ_U(0, LQ_M) READ_U(1, LQ_M + 16384) }
      if (n == 7) { WAITQ(mu[3]); READ_U(2, LQ_M) READ_U(3, LQ_M + 16384) }
#pragma unroll
      for (int sq = 0; sq < 4; ++sq) {
        if (n <= 5) { VM(DMA_S(n + 2, sq)); if (sq == 3) { VM(DMA_S(n + 2, 4)); ms[n + 2] = vq; } }
        if (n == 6 && sq < 2) ISS_Q(4 + sq, LQ_A + sq * 16384);
        if (n == 7 && sq < 2) ISS_Q(6 + sq, sq * 16384);
        SCHED();
        xf[sq] = lds16(xa ^ (32 * sq));
#pragma unroll
        for (int m = 0; m < 3; ++m) wf[sq][m] = ldfrag(wa + sq * 6144 + m * 1024, 256);
        cf[sq] = ldfrag((ca ^ (32 * (sq & 1))) + (sq >> 1) * 12288, 256);
        SCHED();
      }
      if (n == 0) {
        rsrc_t sp = __builtin_amdgcn_make_buffer_rsrc((void*)wphi, 0, 16384, 0x00020000);
        VM(dma16(L_WPHI + (w & 1) * 1024, l16, sp, c * 2048 + (w & 1) * 1024));
        EARLY_STATS(mxr)
      }
      if (n == 1 || n == 2) ISS_Q(n - 1, LQ_M + (n - 1) * 16384);
      if (n == 4) { VM(xg = ld16_sc1(i16 * 16, sws, WS_XG + (P * 128 + 16 * w) * 16)); mxg = vq; }
      if (n == 5) ISS_Q(2, LQ_M);
      if (n == 6) ISS_Q(3, LQ_M + 16384);
      if (n == 7) {
#pragma unroll
        for (int sq = 0; sq < 4; ++sq) csa[sq & 1] = mfma32(ones4, cf[sq], csa[sq & 1]);
        GATHER_XS()
        sts4f(L_CS + w * 128 + i16 * 4, csa[0][0]);
        sts4f(L_CS + w * 128 + 64 + i16 * 4, csa[1][0]);
      }
      const int nw = n < 7 ? vq - ms[n + 1] : 63;
      BAR_IF(cw, 1, nw);
#pragma unroll
      for (int sq = 0; sq < 4; ++sq) {
#pragma unroll
        for (int m = 0; m < 3; ++m) acc[m] = mfmaL(wf[sq][m], xf[sq], acc[m]);
        if (n < 7) csa[sq & 1] = mfma32(ones4, cf[sq], csa[sq & 1]);
      }
      BAR_IF(cw, 0, nw);
    }
    STAMP(1);
    ISS_Q(8, LQ_M);
    READ_U(4, LQ_A) READ_U(5, LQ_A + 16384)
    const float xr = lds4f(L_XS + (32 * rw + l32) * 4), xm = -lds4f(L_XS + 512 + (32 * rw + l32) * 4);
    u32x4 wp0, wp1;
    {
      const u32 a = L_WPHI + (2 * cw + (lane & 1)) * 512 + (4 * hh + ((lane >> 2) & 3)) * 32 + (2 * (g & 1) + ((lane >> 1) & 1)) * 8;
      const u32x4 f = ldfrag(a, 256);
      const bool own = (lane >> 2) & 1;
#pragma unroll
      for (int e = 0; e < 4; ++e) { wp0[e] = own ? 0u : f[e]; wp1[e] = own ? f[e] : 0u; }
    }
#define FEATURES(e_, dst)                                                                                           \
  {                                                                                                                 \
    u32x4 kp[2];                                                                                                    \
    _Pragma("unroll") for (int a = 0; a < 4; ++a) {                                                                \
      const f32x4 cc = lds16f(L_CS + (2 * (e_) + cw) * 128 + a * 32 + hh * 16);                                      \
      kp[a >> 1][2 * (a & 1)] = pk2v(fma2(lo2(cc), bc2(xm), (f32x2){acc[e_][4 * a], acc[e_][4 * a + 1]}));          \
      kp[a >> 1][2 * (a & 1) + 1] = pk2v(fma2(hi2(cc), bc2(xm), (f32x2){acc[e_][4 * a + 2], acc[e_][4 * a + 3]}));  \
    }                                                                                                               \
    f32x16 pl = mfmaL(wp0, kp[0], zero16);                                                                          \
    pl = mfmaL(wp1, kp[1], pl);                                                                                     \
    hedgehog16(pl, xr, dst);                                                                                        \
  }
    {
      const int s_ = 32 * rw + l32;
      const u32 key = (((s_ >> 1) & 3) ^ (((s_ >> 3) & 1) << 1)) * 16;
      ISS_Q(9, LQ_M + 16384);
      {
        const u32 va = LQ_V + (s_ >> 3) * 1024 + cw * 512 + (s_ & 7) * 64 + hh * 8;
#pragma unroll
        for (int a = 0; a < 4; ++a) {
          const f32x4 cc = lds16f(L_CS + (4 + cw) * 128 + a * 32 + hh * 16);
          sts8(va + ((a * 16) ^ key), (u32x2){pk2v(fma2(lo2(cc), bc2(xm), (f32x2){acc[2][4 * a], acc[2][4 * a + 1]}) * bc2(xr)),
                                              pk2v(fma2(hi2(cc), bc2(xm), (f32x2){acc[2][4 * a + 2], acc[2][4 * a + 3]}) * bc2(xr))});
        }
      }
      WAITL0();
      ISS_Q(10, LQ_A); ISS_Q(11, LQ_A + 16384);
      u32x4 ph[4];
      FEATURES(1, ph)
      const u32 pa = LQ_PHI + (s_ >> 3) * 2048 + (2 * cw + hh) * 512 + (s_ & 7) * 64;
#pragma unroll
      for (int a = 0; a < 4; ++a) sts16(pa + ((a * 16) ^ key), ph[a]);
    }
    WAITQ(mu[9]);
    READ_U(6, 0) READ_U(7, 16384) READ_U(8, LQ_M) READ_U(9, LQ_M + 16384)
    BARRIER();
    STAMP(2);
    {
      const u32 kx = (((lane >> 3) & 1) ^ ((g & 1) << 1)) * 16;
      const u32 pa = LQ_PHI + g * 2048 + (w >> 1) * 512 + ((lane >> 2) & 3) * 64 + (((2 * (w & 1) + ((lane >> 1) & 1)) * 16) ^ kx) + (lane & 1) * 8;
      const u32 va = LQ_V + g * 1024 + (w >> 2) * 512 + ((lane >> 2) & 3) * 64 + (((2 * ((w >> 1) & 1) + ((lane >> 1) & 1)) * 16) ^ kx) + (lane & 1) * 8;
      u32x4 pf[4], vf[4];
#pragma unroll
      for (int k = 0; k < 4; ++k) {
        pf[k] = cat(ldstr(pa + k * 8192), ldstr((pa ^ 32) + k * 8192 + 256));
        vf[k] = cat(ldstr(va + k * 4096), ldstr((va ^ 32) + k * 4096 + 256));
      }
      f32x4 kv = zero4, kz = zero4;
#pragma unroll
      for (int k = 0; k < 4; ++k) { kv = mfma32(pf[k], vf[k], kv); kz = mfma32(pf[k], ones4, kz); }
      const u32 so = WS_KP + (c * 16 + P) * KP_SZ;
      u32x4 g0 = {__float_as_uint(kv[0]), tlo, __float_as_uint(kv[1]), tlo}, g1 = {__float_as_uint(kv[2]), tlo, __float_as_uint(kv[3]), tlo};
      u32x4 g2 = {__float_as_uint((lane & 1) ? kz[2] : kz[0]), tlo, __float_as_uint((lane & 1) ? kz[3] : kz[1]), tlo};
      PIN(g0); PIN(g1); PIN(g2);
      asm volatile("s_nop 7" ::: "memory");
      VM(st16_sc1(g0, l16, sws, so + w * 2048));
      VM(st16_sc1(g1, l16, sws, so + w * 2048 + 1024));
      VM(st16_sc1(g2, g * 32 + (lane & 1) * 16 + ((i16 >> 1) << 26), sws, so + 16384 + w * 128));
    }
    STAMP(3);
    ISS_Q(12, LQ_M); ISS_Q(13, LQ_M + 16384);
    WAITQ(mu[11]);
    READ_U(10, LQ_A) READ_U(11, LQ_A + 16384)
    BARRIER();
    ISS_Q(14, LQ_A); ISS_Q(15, LQ_A + 16384);
    u32x4 phq[4];
    FEATURES(0, phq)
    PIN(phq[0]); PIN(phq[1]); PIN(phq[2]); PIN(phq[3]);
    STAMP(4);
    if (w >= 5) {
      const int id = 2 * P + (w == 7 ? (lane & 1) : w - 5), wq = id >> 2, gq = id & 3;
      const u32 vi0 = (4 * g) * KP_SZ + (w == 7 ? 16384 + wq * 128 + gq * 32 : wq * 2048 + (gq * 16 + i16) * 16);
      const u32 vi1 = vi0 + (w == 7 ? 16 : 1024);
      const u32 vo = (w == 7 ? (WS_KZ - WS_KF) + c * 512 + id * 16 + ((i16 >> 1) << 25) : c * 8192 + id * 256 + i16 * 16) + (g << 28);
      float sa[4];
#pragma nounroll
      for (int it = 0; it < SPIN_CAP; ++it) {
        u32x4 a[4][2];
#pragma unroll
        for (int o = 0; o < 4; ++o) {
          a[o][0] = ld16_sc1(vi0, sws, WS_KP + (c * 16 + o) * KP_SZ);
          a[o][1] = ld16_sc1(vi1, sws, WS_KP + (c * 16 + o) * KP_SZ);
        }
        WAITV(0);
        bool ok = true;
#pragma unroll
        for (int o = 0; o < 4; ++o) { PIN(a[o][0]); PIN(a[o][1]); ok = ok && tag_ok(a[o][0], tlo) && tag_ok(a[o][1], tlo); }
#pragma unroll
        for (int e = 0; e < 4; ++e)
          sa[e] = (__uint_as_float(a[0][e >> 1][2 * (e & 1)]) + __uint_as_float(a[1][e >> 1][2 * (e & 1)])) +
                  (__uint_as_float(a[2][e >> 1][2 * (e & 1)]) + __uint_as_float(a[3][e >> 1][2 * (e & 1)]));
        if (__all(ok)) break;
        __builtin_amdgcn_s_sleep(POLL_SLEEP);
      }
      red2g(sa[0], sa[1]);
      red2g(sa[2], sa[3]);
      st16_sc1((u32x4){pk2(sa[0], sa[1]), tlo, pk2(sa[2], sa[3]), tlo}, vo, sws, WS_KF);
    }
    {
      VWO();
#pragma unroll
      for (int e = 0; e < 7; ++e) DMA_WOUT(e)
    }
    gf = ld8_sc1((lane >> 5) * 1024, sws, WS_FA + (16 * w + c2) * 128);
    WAITV(8);
    READ_U(12, LQ_M) READ_U(13, LQ_M + 16384) READ_U(14, LQ_A) READ_U(15, LQ_A + 16384)
    BARRIER();
#pragma unroll
    for (int a = 0; a < 4; ++a) sts16(L_WO1 + w * 4096 + a * 1024 + l16, phq[a]);
    WAITV(0);
    PIN(gf);
    if (!__all(gf[0] == tlo && gf[1] == thi)) poll_flags((lane >> 5) * 1024, true, sws, WS_FA + (16 * w + c2) * 128, tlo, thi);
    STAMP(6);
    { const u32 so_ = zb0 + TILE(0, gn) * 65536; LOAD_B(0, so_) }
  }

  tlo = __builtin_amdgcn_readfirstlane(tlo);
  thi = __builtin_amdgcn_readfirstlane(thi);
  int w2 = w, c22 = c2, p2 = p, cc2 = c;
  asm volatile("" : "+s"(w2), "+s"(tlo), "+s"(thi), "+s"(c22), "+s"(p2), "+s"(cc2));
  {
  const int w = w2, rw = w & 3, cw = w >> 2, c2 = c22, p = p2, c = cc2, P = p >> 1;
  RELANE();
  { const u32 so_ = zb0 + TILE(1, gn) * 65536; LOAD_B(1, so_) }
  STAMP(9);
  const u32 redw = opq(L_RED + w * 4096 + lane * 16), redr = opq(L_RED + (rw ^ (2 * ux0)) * 1024 + lane * 16);
  const u32 fd = WS_FD + (p * 16 + c2) * 128;
  const u32 f3s = WS_FB + (P * 8 + (c2 > 0 ? c2 - 1 : 0)) * 128;
  const u32 vw3 = ((lane >> 2) & 7) * 1024 + c2 * 128 + (lane >> 5) * 64;
  const rsrc_t swo = __builtin_amdgcn_make_buffer_rsrc((void*)wo, 0, 512 * 1024, 0x00020000);
#define DMA_WO(dst, half, e) dma16((dst) + w * 4096 + (e) * 1024, (vw3 + (lane & 3) * 16) ^ (((e) & 1) * 32), swo, ((w + 8 * (half)) * 4 + (e)) * 8192)
  u32x4 kfg, kzg;
  u32x2 z1r[6], hold;
  u32 bs;
  f32x4 sm0;
#define EMIT_H(jt, sm_)                                                                                             \
  {                                                                                                                 \
    const f32x2 bb = bc2(blo(bs));                                                                                  \
    const u32 h0 = pk2v((f32x2){blo(z1r[jt][0]), bhi(z1r[jt][0])} * (lo2(sm_) + bb));                               \
    const u32 h1 = pk2v((f32x2){blo(z1r[jt][1]), bhi(z1r[jt][1])} * (hi2(sm_) + bb));                               \
    const u32 hb_ = WS_HF + (((4 * p + rw) * 2 + ((jt) >> 1)) * 8 + c2) * 1024;                                     \
    if ((jt) == 0 || ((jt) == 2 && gn == 6)) hold = (u32x2){h0, h1};                                                \
    else if ((jt) == 1 || ((jt) == 3 && gn == 6)) st16_sc1((u32x4){hold[0], hold[1], h0, h1}, l16, sws, hb_);       \
    else if ((jt) == 2) st16_sc1((u32x4){h0, h1, 0u, 0u}, l16, sws, hb_);                                           \
    else st16_sc1((u32x4){h0, tlo, h1, tlo}, l16, sws, WS_HL + (((4 * p + rw) * 8 + c2) * 2 + (gn - 1 - (jt))) * 1024); \
  }
#pragma unroll
  for (int j = 0; j < 6; ++j) {
    if (j < 5 || gn == 6) {
      const int u = j & 1;
      if (j == 0 || j == 2) WAITV(8);
      if (j == 1) WAITV(9);
      if (j == 3) { WAITV(11); WAITV_IF(cw, 1, 10); WAITV_IF(t, 0, 9); WAITV_IF(2 * cw - t, 2, 8); }
      if (j == 4) { WAITV(12); WAITV_IF(t, 0, 9); WAITV_IF(2 * gn + t, 11, 4); }
      if (j == 5) { WAITV(5); WAITV_IF(cw, 1, 4); }
      STAMP(17 + 2 * j);
#pragma unroll
      for (int i = 0; i < 8; ++i) PIN(bfr[u][i]);
      {
        f32x4 a_[4];
#pragma unroll
        for (int rt = 0; rt < 4; ++rt) {
          a_[rt] = zero4;
#pragma unroll
          for (int i = 0; i < 8; ++i) a_[rt] = mfma32(bfr[u][i], af[rt][i], a_[rt]);
          SCHED();
          if (rt > 0) sts16f(redw + u * 32768 + (rt - 1) * 1024, a_[rt - 1]);
          SCHED();
        }
        if (j == 1 && t != 0) {
          PIN(kfg); PIN(kzg);
          poll_gran(kfg, l16, sws, WS_KF + c * 8192 + w * 1024, tlo);
          poll_gran(kzg, g * 16, sws, WS_KZ + c * 512 + w * 64, tlo);
          sts8(L_KFS + (w >> 1) * 1024 + i16 * 64 + (w & 1) * 32 + g * 8, (u32x2){kfg[0], kfg[2]});
          sts8(L_KFS + 4096 + w * 32 + g * 8, (u32x2){kzg[0], kzg[2]});
          sts8(L_KFS + 4352 + (lane & 7) * 8, (u32x2){0u, 0u});
        }
        sts16f(redw + u * 32768 + 3072, a_[3]);
      }
      BAR_IF(cw, 1, 63);
      STAMP(18 + 2 * j);
      if (w == 4) {
        if (j == 0 && t == 0) SET_FLAG(WS_FB + (P * 8 + c) * 128);
        if (j == 3) SET_FLAG(fd);
        if (j == 4 && t != 0) SET_FLAG(WS_FC + (P * 8 + c) * 128);
        if ((j == 4 && gn == 5) || j == 5) SET_FLAG(fd + 1024);
      }
      if (j == 0 && t != 0) {
        kfg = ld16_sc1(l16, sws, WS_KF + c * 8192 + w * 1024);
        kzg = ld16_sc1(g * 16, sws, WS_KZ + c * 512 + w * 64);
      }
      u32x4 of[2];
      if (j == 2 && t != 0) {
        const int l32 = lane & 31, hh = lane >> 5;
        const bool own = ((lane >> 2) & 1) == hh;
        const u32 ka = own ? L_KFS + (2 * cw + hh) * 1024 + ((l32 & 3) + 4 * (l32 >> 3)) * 64 : L_KFS + 4352;
        const u32 zaa = own ? L_KFS + 4096 + (2 * cw + hh) * 64 : L_KFS + 4352;
        const u32 qa = L_WO1 + w * 4096 + l16;
        const f32x16 zero16 = {0, 0, 0, 0, 0, 0, 0, 0, 0, 0, 0, 0, 0, 0, 0, 0};
        f32x16 den = zero16, num = zero16;
#pragma unroll
        for (int a = 0; a < 4; ++a) {
          const u32x4 pq_ = lds16(qa + a * 1024);
          den = mfmaL(lds16(zaa + a * 16), pq_, den);
          num = mfmaL(lds16(ka + a * 16), pq_, num);
        }
        const float inv = __builtin_amdgcn_rcpf(den[0] + 1e-6f);
#pragma unroll
        for (int a = 0; a < 2; ++a) {
          of[a] = (u32x4){pk2(num[8 * a] * inv, num[8 * a + 1] * inv), pk2(num[8 * a + 2] * inv, num[8 * a + 3] * inv),
                          pk2(num[8 * a + 4] * inv, num[8 * a + 5] * inv), pk2(num[8 * a + 6] * inv, num[8 * a + 7] * inv)};
          PIN(of[a]);
        }
        STAMP(5);
      }
      if (j == 1) {
        if (cw == 0) {
          WAITV(0);
          PIN(gf);
          if (!__all(gf[0] == tlo && gf[1] == thi)) poll_flags((lane >> 5) * 128, true, sws, f3s, tlo, thi);
          rsrc_t sb = __builtin_amdgcn_make_buffer_rsrc((void*)bsp, 0, 4096, 0x00020000);
          bs = ld2(i16 * 2, sb, (64 * p + 16 * rw) * 2);
        }
      }
      if ((j == 1 || j == 2) && cw == 0) {
#pragma unroll
        for (int v = 3 * j - 3; v < 3 * j; ++v)
          z1r[v] = ld8_sc1(l16, sws, WS_Z1 + ((2 * p + (rw >> 1)) * 43 + gs + TILE(min(v, gn - 1), gn)) * 1024 + (rw & 1) * 8);
      }
      const u32 ra_ = redr + ((j + 1) & 1) * 32768;
      f32x4 sm, r_[2];
#pragma unroll
      for (int i = 0; i < 8; ++i) {
        if (j >= 1 && (i & 1) == 0 && cw == 0) {
          if (i == 2) sm = r_[0] + r_[1];
          if (i > 2) sm += r_[0] + r_[1];
          r_[0] = lds16f(ra_ + i * 4096); r_[1] = lds16f(ra_ + (i + 1) * 4096);
        }
        SCHED();
        if (j < 3) bfr[u][i] = ld16_b(l16, sws, (zb0 + TILE(j + 2, gn) * 65536) ^ (i * 1024));
        else if (j == 3) ld16_b_if6(bfr[1][i], gn, l16, sws, (zb0 + TILE(5, gn) * 65536) ^ (i * 1024));
        SCHED();
      }
      if (j == 0) gf = ld8_sc1((lane >> 5) * 128, sws, f3s);
      if (j == 2 && t != 0) {
        const u32 vo = ((lane >> 4) & 1) * 16384 + (lane >> 5) * 512 + i16 * 16;
        st16_sc1(of[0], vo, sws, WS_OF + ((8 * P + 2 * rw) * 16 + 2 * c + cw) * 1024);
        st16_sc1(of[1], vo, sws, WS_OF + ((8 * P + 2 * rw) * 16 + 2 * c + cw) * 1024 + 256);
      }
      if (j == 2) { PIN(bs); _Pragma("unroll") for (int v = 0; v < 3; ++v) PIN(z1r[v]); }
      if (j == 3) { _Pragma("unroll") for (int v = 3; v < 6; ++v) PIN(z1r[v]); }
      if (j >= 1 && cw == 0) {
        sm += r_[0] + r_[1];
        if (j == 1) sm0 = sm;
        if (j == 2) EMIT_H(0, sm0)
        if (j >= 2) EMIT_H(j >= 2 ? j - 1 : 0, sm)
      }
      if (j == 3) {
        if (t == 0) {
          VWO();
#pragma unroll
          for (int e = 2; e < 7; ++e) DMA_WOUT(e)
        }
#pragma unroll
        for (int e = 0; e < 4; ++e) DMA_WO(L_WO1, 0, e);
      }
      if (j == 3 && cw == 0) { WAITV(12); WAITV_IF(t, 0, 10); WAITV_IF(2 * gn + t, 11, 5); }
      if (j == 5) WAITV(1);
      BAR_IF(cw, 0, 63);
    }
  }
  {
    STAMP(10);
    const u32 bufA = L_RED + (gn & 1) * 32768, bufB = L_RED + 32768 - (gn & 1) * 32768;
    const u32 fv = (lane & 32) ? (P * 8 + ((lane >> 2) & 7)) * 128 : (WS_FD - WS_FC) + (p * 16 + (((lane >> 1) & 14) | cw)) * 128;
    gf = ld8_sc1(fv, sws, WS_FC);
    if (cw == 0) {
      const u32 ra_ = redr + (bufB - L_RED);
      const f32x4 sm = ((lds16f(ra_) + lds16f(ra_ + 4096)) + (lds16f(ra_ + 8192) + lds16f(ra_ + 12288))) +
           ((lds16f(ra_ + 16384) + lds16f(ra_ + 20480)) + (lds16f(ra_ + 24576) + lds16f(ra_ + 28672)));
      if (gn == 6) EMIT_H(5, sm) else EMIT_H(4, sm)
    }
    RELANE();
#pragma unroll
    for (int e = 0; e < 4; ++e) DMA_WO(bufA, 1, e);
    WAITV(5); WAITV_IF(cw, 1, 4);
    PIN(gf);
    const bool okf = gf[0] == tlo && gf[1] == thi;
    const bool n0 = lane < 16;
    const bool all1 = __all(okf);
    if (!__all(!n0 || okf)) poll_flags(fv, n0, sws, WS_FC, tlo, thi);
    u32x4 hf[8], of[8];
#pragma unroll
    for (int e = 0; e < 4; ++e) hf[e] = ld16_b(l16, sws, WS_HF + ((4 * p + rw) * 16 + 2 * e + cw) * 1024);
    BARRIER();
    {
      VWO();
#pragma unroll
      for (int e = 7; e < 11; ++e) DMA_WOUT_AT(bufB + w * 4096, e)
    }
    if (!all1) poll_flags(fv, true, sws, WS_FC, tlo, thi);
#pragma unroll
    for (int e = 4; e < 8; ++e) hf[e] = ld16_b(l16, sws, WS_HF + ((4 * p + rw) * 16 + 2 * e + cw) * 1024);
    const u32 qb = ((lane >> 2) & 3) * 64 + ((((g & 1) << 1) | ((lane >> 1) & 1)) * 16) + (lane & 1) * 8;
    f32x4 a3[4];
#pragma unroll
    for (int ct = 0; ct < 4; ++ct) a3[ct] = zero4;
    u32x4 wf[3][4], hl[4][2];
#define RDW2(dst, lo0, hi0, offl, offh) _Pragma("unroll") for (int ct = 0; ct < 4; ++ct) dst[ct] = cat(ldstr(((lo0) ^ ((ct & 1) * 32)) + (offl) + (ct >> 1) * 512), ldstr(((hi0) ^ ((ct & 1) * 32)) + (offh) + (ct >> 1) * 512))
#define MM3(src, frag) _Pragma("unroll") for (int ct = 0; ct < 4; ++ct) a3[ct] = mfma32(src[ct], frag, a3[ct])
    const u32 lb = WS_HL + ((4 * p + rw) * 8 + cw) * 2048;
    const rsrc_t sxo = __builtin_amdgcn_make_buffer_rsrc((void*)(x + (size_t)p * 65536), 0, 65536, 0x00020000);
    const u32 w0 = opq(L_WOUT + qb + (g >> 1) * 1024 + (g & 1) * 256);
    const u32 wa = w0 + cw * 4096, wb = w0 + cw * 2048;
    const u32 oq = qb + g * 1024 + cw * 4096;
    const u32 oa = opq(L_WO1 + oq), ob = opq(bufA + oq);
    u32x4 xres;
    STAMP(11);
#define RDK(dst, k)                                                                                                 \
  if ((k) < 4) { RDW2(dst, wa, wa, (k) * 8192, (k) * 8192 + 2048); }                                                \
  else if ((k) < 8) { RDW2(dst, wb, w0, 32768 + ((k) - 4) * 4096, 49152 + ((k) - 4) * 2048); }                      \
  else if ((k) < 12) { RDW2(dst, oa, oa, ((k) - 8) * 8192, ((k) - 8) * 8192 + 256); }                               \
  else { RDW2(dst, ob, ob, ((k) - 12) * 8192, ((k) - 12) * 8192 + 256); }
    RDK(wf[0], 0)
    RDK(wf[1], 1)
#pragma unroll
    for (int k = 0; k < 16; ++k) {
      if (k == 0) WAITV(8);
      if (k == 4) WAITV(8);
      if (k == 8) WAITV(9);
      if (k == 10) { BARRIER(); STAMP(12); }
      if (k + 2 < 16) { RDK(wf[(k + 2) % 3], k + 2) }
      SCHED();
      if (k < 8) { PIN(hf[k]); MM3(wf[k % 3], hf[k]); }
      else { PIN(of[k - 8]); MM3(wf[k % 3], of[k - 8]); }
      SCHED();
      if (k < 4) {
        of[2 * k] = ld16_b(l16, sws, WS_OF + ((4 * p + rw) * 16 + 4 * k + cw) * 1024);
        of[2 * k + 1] = ld16_b(l16, sws, WS_OF + ((4 * p + rw) * 16 + 4 * k + 2 + cw) * 1024);
      } else if (k < 8) {
        if (k == 4) xres = ld16((tid >> 3) * 1024 + (tid & 7) * 16, sxo, c2 * 128);
        hl[k - 4][0] = ld16_sc1(l16, sws, lb + (k - 4) * 4096);
        hl[k - 4][1] = ld16_sc1(l16, sws, lb + (k - 4) * 4096 + 1024);
      }
    }
    STAMP(13);
    {
      const u32 wl_ = opq(bufB + (w0 - L_WOUT) + cw * 4096);
      u32x4 wg[4][4];
#pragma unroll
      for (int e = 0; e < 4; ++e) RDW2(wg[e], wl_, wl_, e * 8192, e * 8192 + 2048);
      WAITV(0);
      PIN(xres);
#pragma unroll
      for (int e = 0; e < 4; ++e) {
        PIN(hl[e][0]); PIN(hl[e][1]);
        poll_gran(hl[e][0], l16, sws, lb + e * 4096, tlo);
        poll_gran(hl[e][1], l16, sws, lb + e * 4096 + 1024, tlo);
        MM3(wg[e], ((u32x4){hl[e][0][0], hl[e][0][2], hl[e][1][0], hl[e][1][2]}));
      }
    }
    STAMP(14);
    const u32 fin = L_WOUT - 2048;
#pragma unroll
    for (int ct = 0; ct < 4; ++ct) sts16f(fin + 2048 + cw * 16384 + (16 * rw + i16) * 256 + (((4 * ct + g) ^ i16) * 16), a3[ct]);
    BARRIER();
    {
      const int row = tid >> 3;
      const u32 a = fin + 2048 + row * 256 + (((2 * (tid & 7)) ^ (row & 15)) * 16);
      const f32x4 s0 = lds16f(a) + lds16f(a + 16384), s1 = lds16f(a ^ 16) + lds16f((a ^ 16) + 16384);
      u32x4 o = {pk2(blo(xres[0]) + s0[0], bhi(xres[0]) + s0[1]), pk2(blo(xres[1]) + s0[2], bhi(xres[1]) + s0[3]),
                 pk2(blo(xres[2]) + s1[0], bhi(xres[2]) + s1[1]), pk2(blo(xres[3]) + s1[2], bhi(xres[3]) + s1[3])};
      st16_sc1(o, row * 1024 + (tid & 7) * 16, __builtin_amdgcn_make_buffer_rsrc((void*)(out + (size_t)p * 65536), 0, 65536, 0x00020000), c2 * 128);
    }
  }
  }
  if (b == 0 && w == 0 && l16 == 0) *(u64*)(ws + WS_EP) = ((u64)thi << 32) | tlo;
  STAMP(15);
  STAMPC(31);
}

extern "C" void kernel_launch(void* const* d_in, const int* in_sizes, int n_in, void* d_out, int out_size, void* d_ws, size_t ws_size,
                              hipStream_t stream) {
  fused_kernel<<<256, 512, 0, stream>>>((const char*)d_in[0], (const char*)d_in[1], (const char*)d_in[2], (const char*)d_in[3],
                                        (const char*)d_in[5], (const char*)d_in[6], (const char*)d_in[7], (const char*)d_in[8], (char*)d_out,
                                        (char*)d_ws);
}
```

```cpp
#include <hip/hip_runtime.h>
#include <hip/hip_bf16.h>

typedef unsigned int u32;
typedef unsigned long long u64;
typedef short s16x4 __attribute__((ext_vector_type(4)));
typedef __bf16 bf16x2 __attribute__((ext_vector_type(2)));
typedef __bf16 bf16x8 __attribute__((ext_vector_type(8)));
typedef float f32x16 __attribute__((ext_vector_type(16)));
typedef float f32x4 __attribute__((ext_vector_type(4)));
typedef float f32x2 __attribute__((ext_vector_type(2)));
typedef u32 u32x4 __attribute__((ext_vector_type(4)));
typedef u32 u32x2 __attribute__((ext_vector_type(2)));
typedef __amdgpu_buffer_rsrc_t rsrc_t;
#define LDSP __attribute__((address_space(3)))
#define DI __device__ __forceinline__

#define STAMPS 0

constexpr u32 WS_EP = 0;
constexpr u32 WS_FA = 8192;
constexpr u32 WS_FB = WS_FA + 16384;
constexpr u32 WS_FC = WS_FB + 16384;
constexpr u32 WS_FD = WS_FC + 16384;
constexpr u32 WS_XG = WS_FD + 65536;
constexpr u32 WS_SG = WS_XG + 32768;
constexpr u32 WS_ZB = WS_SG + 524288;
constexpr u32 WS_Z1 = WS_ZB + 43 * 65536;
constexpr u32 KP_SZ = 17408;
constexpr u32 WS_KP = WS_Z1 + 64 * 43 * 1024;
constexpr u32 WS_KF = WS_KP + 128 * KP_SZ;
constexpr u32 WS_KZ = WS_KF + 65536;
constexpr u32 WS_OF = WS_KZ + 4096;
constexpr u32 WS_HF = WS_OF + 128 * 16384;
constexpr u32 WS_HL = WS_HF + 128 * 16 * 1024;
constexpr u32 WS_ST = WS_HL + 128 * 16 * 1024;
constexpr u32 WS_END = WS_ST + 256 * 8 * 32 * 8;

constexpr u32 LZ_W = 65536;
constexpr u32 LZ_U = 139264;
constexpr u32 LQ_M = 122880;
constexpr u32 LQ_A = 81920;
constexpr u32 LQ_PHI = 49152;
constexpr u32 LQ_V = 32768;
constexpr u32 L_TAB = 155648;
constexpr u32 L_XS = L_TAB;
constexpr u32 L_CS = L_TAB + 1024;
constexpr u32 L_ZST = L_TAB + 3072;
constexpr u32 L_WPHI = L_TAB + 3072;
constexpr u32 L_KFS = L_TAB + 2048;
constexpr u32 L_JUNK = L_TAB + 7168;
constexpr u32 L_WOUT = 0;
constexpr u32 L_RED = 57344;
constexpr u32 L_WO1 = 122880;

constexpr u64 Z2S = 0x26211B16110B0500ull;
constexpr u64 Z1S = 0x26211C16110C0600ull;
DI int grp_start(u64 tab, int c) { return c >= 8 ? 43 : (int)((tab >> (8 * c)) & 0xFF); }

#define SCHED() __builtin_amdgcn_sched_barrier(0)
#define TIC()
#define TOC(acc)
#define WAITV(n) do { TIC(); asm volatile("s_waitcnt vmcnt(%0)" ::"n"(n) : "memory"); TOC(acv); SCHED(); } while (0)
#define BARRIER() do { SCHED(); TIC(); asm volatile("s_waitcnt lgkmcnt(0)\n\ts_barrier" ::: "memory"); TOC(acb); SCHED(); } while (0)
#define WAITV_IF(g_, v, n) do { TIC(); asm volatile("s_cmp_lg_u32 %0, %1\n\ts_cbranch_scc1 1f\n\ts_waitcnt vmcnt(%2)\n1:" ::"s"(g_), "n"(v), "n"(n) : "scc", "memory"); TOC(acv); SCHED(); } while (0)
#define BAR_IF(g_, v, n) do { SCHED(); asm volatile("s_cmp_lg_u32 %0, %1\n\ts_cbranch_scc1 1f\n\ts_waitcnt vmcnt(%2) lgkmcnt(0)\n\ts_barrier\n1:" ::"s"(g_), "n"(v), "n"(n) : "scc", "memory"); SCHED(); } while (0)
#define WAITL0() asm volatile("s_waitcnt lgkmcnt(0)" ::: "memory")
#define PIN(x) asm volatile("" : "+v"(x))

DI void dma16(u32 lds, u32 voff, rsrc_t srd, u32 soff) {
  asm volatile("s_mov_b32 m0, %0\n\ts_nop 0\n\tbuffer_load_dwordx4 %1, %2, %3 offen lds" ::"s"(lds), "v"(voff), "s"(srd), "s"(soff) : "memory");
}
DI void touch(u32 lds, u32 voff, rsrc_t srd, u32 soff) {
  asm volatile("s_mov_b32 m0, %0\n\ts_nop 0\n\tbuffer_load_dword %1, %2, %3 offen lds" ::"s"(lds), "v"(voff), "s"(srd), "s"(soff) : "memory");
}
DI u32x4 ld16_sc1(u32 voff, rsrc_t srd, u32 soff) {
  u32x4 r;
  asm volatile("buffer_load_dwordx4 %0, %1, %2, %3 offen sc1" : "=v"(r) : "v"(voff), "s"(srd), "s"(soff) : "memory");
  return r;
}
template <int OFF> DI u32x4 ld16_sc1o(u32 voff, rsrc_t srd, u32 soff) {
  u32x4 r;
  asm volatile("buffer_load_dwordx4 %0, %1, %2, %3 offen offset:%4 sc1" : "=v"(r) : "v"(voff), "s"(srd), "s"(soff), "n"(OFF) : "memory");
  return r;
}
#define BPOL " sc1"
DI u32x4 ld16_b(u32 voff, rsrc_t srd, u32 soff) {
  u32x4 r;
  asm volatile("buffer_load_dwordx4 %0, %1, %2, %3 offen" BPOL : "=v"(r) : "v"(voff), "s"(srd), "s"(soff) : "memory");
  return r;
}
template <int OFF> DI u32x4 ld16_bo(u32 voff, rsrc_t srd, u32 soff) {
  u32x4 r;
  asm volatile("buffer_load_dwordx4 %0, %1, %2, %3 offen offset:%4" BPOL : "=v"(r) : "v"(voff), "s"(srd), "s"(soff), "n"(OFF) : "memory");
  return r;
}
DI void ld16_b_if6(u32x4& r, int gn, u32 voff, rsrc_t srd, u32 soff) {
  asm volatile("s_cmp_lg_u32 %4, 6\n\ts_cbranch_scc1 1f\n\tbuffer_load_dwordx4 %0, %1, %2, %3 offen" BPOL "\n1:" : "+v"(r) : "v"(voff), "s"(srd), "s"(soff), "s"(gn) : "scc", "memory");
}
DI u32x4 ld16(u32 voff, rsrc_t srd, u32 soff) {
  u32x4 r;
  asm volatile("buffer_load_dwordx4 %0, %1, %2, %3 offen" : "=v"(r) : "v"(voff), "s"(srd), "s"(soff) : "memory");
  return r;
}
DI u32x2 ld8_sc1(u32 voff, rsrc_t srd, u32 soff) {
  u32x2 r;
  asm volatile("buffer_load_dwordx2 %0, %1, %2, %3 offen sc1" : "=v"(r) : "v"(voff), "s"(srd), "s"(soff) : "memory");
  return r;
}
DI u32 ld2(u32 voff, rsrc_t srd, u32 soff) {
  u32 r;
  asm volatile("buffer_load_ushort %0, %1, %2, %3 offen" : "=v"(r) : "v"(voff), "s"(srd), "s"(soff) : "memory");
  return r;
}
DI void st16_sc1(u32x4 v, u32 voff, rsrc_t srd, u32 soff) {
  asm volatile("buffer_store_dwordx4 %0, %1, %2, %3 offen sc1\n\ts_nop 1" ::"v"(v), "v"(voff), "s"(srd), "s"(soff) : "memory");
}
DI void st8_sc1(u32x2 v, u32 voff, rsrc_t srd, u32 soff) {
  asm volatile("buffer_store_dwordx2 %0, %1, %2, %3 offen sc1" ::"v"(v), "v"(voff), "s"(srd), "s"(soff) : "memory");
}

DI u32x4 lds16(u32 a) { return *(const LDSP u32x4*)(size_t)a; }
DI u32x2 lds8(u32 a) { return *(const volatile LDSP u32x2*)(size_t)a; }
DI float lds4f(u32 a) { return *(const LDSP float*)(size_t)a; }
DI f32x4 lds16f(u32 a) { return *(const LDSP f32x4*)(size_t)a; }
DI void sts16f(u32 a, f32x4 v) { *(LDSP f32x4*)(size_t)a = v; }
DI void sts16(u32 a, u32x4 v) { *(LDSP u32x4*)(size_t)a = v; }
DI void sts8(u32 a, u32x2 v) { *(volatile LDSP u32x2*)(size_t)a = v; }
DI void sts4f(u32 a, float v) { *(LDSP float*)(size_t)a = v; }
DI u32x2 ldstr(u32 a) {
  return __builtin_bit_cast(u32x2, __builtin_amdgcn_ds_read_tr16_b64_v4i16((LDSP s16x4*)(size_t)a)); }
DI u32x4 cat(u32x2 lo, u32x2 hi) { return (u32x4){lo[0], lo[1], hi[0], hi[1]}; }
DI u32x4 ldfrag(u32 a, u32 d) { u32x2 lo = ldstr(a), hi = ldstr(a + d); return cat(lo, hi); }

DI f32x4 mfma32(u32x4 a, u32x4 b, f32x4 c) {
  return __builtin_amdgcn_mfma_f32_16x16x32_bf16(__builtin_bit_cast(bf16x8, a), __builtin_bit_cast(bf16x8, b), c, 0, 0, 0);
}
DI f32x16 mfmaL(u32x4 a, u32x4 b, f32x16 c) {
  return __builtin_amdgcn_mfma_f32_32x32x16_bf16(__builtin_bit_cast(bf16x8, a), __builtin_bit_cast(bf16x8, b), c, 0, 0, 0);
}
DI u32 pk2(float a, float b) { bf16x2 v = {(__bf16)a, (__bf16)b}; return __builtin_bit_cast(u32, v); }
DI float blo(u32 u) { return __uint_as_float(u << 16); }
DI float bhi(u32 u) { return __uint_as_float(u & 0xffff0000u); }
DI float rbf(float a) { return blo(pk2(a, a)); }
DI float dot2(u32 a, u32 b, float c) {
  return __builtin_amdgcn_fdot2_f32_bf16(__builtin_bit_cast(bf16x2, a), __builtin_bit_cast(bf16x2, b), c, false);
}
DI f32x2 fma2(f32x2 a, f32x2 b, f32x2 c) { return __builtin_elementwise_fma(a, b, c); }
DI f32x2 bc2(float a) { return (f32x2){a, a}; }
DI f32x2 lo2(f32x4 v) { return (f32x2){v[0], v[1]}; }
DI f32x2 hi2(f32x4 v) { return (f32x2){v[2], v[3]}; }
DI u32 pk2v(f32x2 v) { return pk2(v[0], v[1]); }
DI f32x2 gelu2(f32x2 x) {
  f32x2 u = fma2(x * x, bc2(-0.10294324f), bc2(-2.3022082f)) * x;
  f32x2 e = {__builtin_amdgcn_exp2f(u[0]), __builtin_amdgcn_exp2f(u[1])};
  e = e + bc2(1.0f);
  f32x2 r = {__builtin_amdgcn_rcpf(e[0]), __builtin_amdgcn_rcpf(e[1])};
  return x * r;
}
DI void red2g(float& a, float& b) {
  auto s = __builtin_amdgcn_permlane32_swap(__float_as_uint(a), __float_as_uint(b), false, false);
  float c = __uint_as_float(s[0]) + __uint_as_float(s[1]);
  auto s2 = __builtin_amdgcn_permlane16_swap(__float_as_uint(c), __float_as_uint(c), false, false);
  float d = __uint_as_float(s2[0]) + __uint_as_float(s2[1]);
  auto s3 = __builtin_amdgcn_permlane32_swap(__float_as_uint(d), __float_as_uint(d), false, false);
  a = __uint_as_float(s3[0]);
  b = __uint_as_float(s3[1]);
}
template <int CTRL> DI float dpp(float v) {
  return __uint_as_float(__builtin_amdgcn_update_dpp(0, __float_as_uint(v), CTRL, 0xF, 0xF, true));
}
DI void hedgehog16(const f32x16& p, float s, u32x4 (&o)[4]) {
  float mx = fmaxf(p[0], p[1]), mn = fminf(p[0], p[1]);
#pragma unroll
  for (int i = 2; i < 16; i += 2) { mx = fmaxf(fmaxf(mx, p[i]), p[i + 1]); mn = fminf(fminf(mn, p[i]), p[i + 1]); }
  const float c = s * 1.44269504f;
  const float a = -mx * c, b = mn * c;
  f32x2 ep[8], en[8];
#pragma unroll
  for (int i = 0; i < 8; ++i) {
    const f32x2 v = {p[2 * i], p[2 * i + 1]};
    const f32x2 u = fma2(v, bc2(c), bc2(a)), w = fma2(v, bc2(-c), bc2(b));
    ep[i] = (f32x2){__builtin_amdgcn_exp2f(u[0]), __builtin_amdgcn_exp2f(u[1])};
    en[i] = (f32x2){__builtin_amdgcn_exp2f(w[0]), __builtin_amdgcn_exp2f(w[1])};
  }
  const f32x2 sp = ((ep[0] + ep[1]) + (ep[2] + ep[3])) + ((ep[4] + ep[5]) + (ep[6] + ep[7]));
  const f32x2 sn = ((en[0] + en[1]) + (en[2] + en[3])) + ((en[4] + en[5]) + (en[6] + en[7]));
  const float ip = __builtin_amdgcn_rcpf(sp[0] + sp[1]), in = __builtin_amdgcn_rcpf(sn[0] + sn[1]);
#pragma unroll
  for (int i = 0; i < 8; ++i) {
    o[i >> 2][i & 3] = pk2v(ep[i] * bc2(ip));
    o[2 + (i >> 2)][i & 3] = pk2v(en[i] * bc2(in));
  }
}
DI void ln_fin(float s, float q, float invn, float& r, float& m) {
  m = s * invn;
  float v = fmaxf(q * invn - m * m, 0.0f);
  float sd = rbf(__builtin_amdgcn_sqrtf(rbf(rbf(v) + 1e-5f)));
  r = __builtin_amdgcn_rcpf(sd);
  m = rbf(m);
}

#define POLL_SLEEP 6
constexpr int SPIN_CAP = 1 << 17;
DI void poll_flags(u32 voff, bool act, rsrc_t srd, u32 soff, u32 tlo, u32 thi) {
#pragma nounroll
  for (int it = 0; it < SPIN_CAP; ++it) {
    u32x2 v;
    asm volatile("buffer_load_dwordx2 %0, %1, %2, %3 offen sc1\n\ts_waitcnt vmcnt(0)" : "=&v"(v) : "v"(voff), "s"(srd), "s"(soff) : "memory");
    bool ok = !act || (v[0] == tlo && v[1] == thi);
    if (__all(ok)) break;
    __builtin_amdgcn_s_sleep(POLL_SLEEP);
  }
}
DI bool tag_ok(u32x4 q, u32 t) { return q[1] == t && q[3] == t; }
DI void poll_gran(u32x4& q, u32 voff, rsrc_t srd, u32 soff, u32 tlo) {
#pragma nounroll
  for (int it = 0; it < SPIN_CAP; ++it) {
    if (__all(tag_ok(q, tlo))) break;
    __builtin_amdgcn_s_sleep(POLL_SLEEP);
    asm volatile("buffer_load_dwordx4 %0, %1, %2, %3 offen sc1\n\ts_waitcnt vmcnt(0)" : "=&v"(q) : "v"(voff), "s"(srd), "s"(soff) : "memory");
  }
}
DI u32 opq(u32 v) { asm volatile("" : "+v"(v)); return v; }

#define STAMP(i)
#define STAMPC(i)

#define GQ_N 3
#define PREFETCH 0
#define CODE_PF 44000
#define OUT_SC1 1
#define PRIO 0
#define UROT 1
static_assert(WS_ZB % 8192 == 0, "");
#define VM(x) do { x; ++vq; } while (0)
#define WAITQ(mark) WAITV(vq - (mark))

__global__ __launch_bounds__(512, 2) void fused_kernel(const char* __restrict__ x, const char* __restrict__ wqkv, const char* __restrict__ wphi,
                                                        const char* __restrict__ wo, const char* __restrict__ win, const char* __restrict__ wsp,
                                                        const char* __restrict__ bsp, const char* __restrict__ wout, char* __restrict__ out, char* ws) {
  __shared__ __attribute__((aligned(1024))) char smem[163840];
  const int tid = threadIdx.x, lane = tid & 63;
  const int w = __builtin_amdgcn_readfirstlane(tid >> 6);
  const int rw = w & 3, cw = w >> 2, i16 = lane & 15, g = lane >> 4;
  const int b = blockIdx.x, xcd = b & 7, r = b >> 3;
  const int Pl = r >> 3, t = (r >> 2) & 1, cl = r & 3;
  const int P = (xcd >> 1) * 4 + Pl, c = (xcd & 1) * 4 + cl;
  const int p = 2 * P + (cl & 1);
  const int c2 = (xcd & 1) * 4 + (t ? 3 * (cl >> 1) : 1 + (cl >> 1));
  const int rot = 2 * ((Pl + cl) & 3);
  u32 acv = 0, acb = 0; (void)acv; (void)acb;
  asm volatile("" ::"v"((u32)(size_t)(LDSP char*)smem));
  STAMP(0);
  STAMPC(30);

  {
    u64 pc;
    asm volatile("s_getpc_b64 %0" : "=s"(pc));
    if (w == 7) touch(L_JUNK, lane * 128, __builtin_amdgcn_make_buffer_rsrc((void*)(pc & ~255ull), 0, CODE_PF, 0x00020000), (r & 7) * 8192);
  }
  u64 ep;
  asm volatile("s_load_dwordx2 %0, %1, 0x0" : "=s"(ep) : "s"(ws) : "memory");
  const rsrc_t sx = __builtin_amdgcn_make_buffer_rsrc((void*)(x + (size_t)P * 131072), 0, 131072, 0x00020000);
  const u32 l16 = opq(lane * 16);
  const u32 vx0 = (16 * w + (lane >> 3)) * 1024 + (((lane & 7) ^ g) * 16);
  const u32 vx1 = (vx0 + 8192) ^ 64;
#define DMA_X(dst, n, e) dma16((dst) + w * 2048 + (e) * 1024, (e) ? vx1 : vx0, sx, (((n) + rot) & 7) * 128)
  DMA_X(0, 0, 0);
  DMA_X(0, 0, 1);
  const rsrc_t sa = __builtin_amdgcn_make_buffer_rsrc((void*)(wsp + (size_t)p * 262144), 0, 262144, 0x00020000);
  const rsrc_t sws = __builtin_amdgcn_make_buffer_rsrc((void*)ws, 0, WS_END, 0x00020000);
  const u32 vA0 = (lane >> 3) * 4096 + (((lane & 7) ^ g) * 16);
  const u32 vA1 = (vA0 + 32768) ^ 64;
  const int ux0 = UROT ? (cl >> 1) : 0, ux1 = UROT ? t : 0;
  const u32 uxs = w * 512 + ux1 * 256 + ux0 * 131072;
#define DMA_U(u, base, e) dma16((base) + w * 2048 + (e) * 1024, (e) ? vA1 : vA0, sa, uxs ^ ((((u) >> 1) & 3) * 128 + (((u) >> 3) * 32 + ((u) & 1) * 16) * 4096))
  const f32x4 zero4 = {0, 0, 0, 0};
  u32x4 ones4 = {0x3F803F80u, 0x3F803F80u, 0x3F803F80u, 0x3F803F80u};
  u32 tlo, thi;
  u32x4 af[4][8];
  u32x4 bfr[2][8];
  u32x2 gf;
  const u32 ard = w * 2048 + i16 * 128 + (((g >> 1) ^ (i16 >> 1)) * 16) + (g & 1) * 8;
#define READ_U(u, base)                                                                                             \
  {                                                                                                                 \
    const u32 ab_ = opq(ard + (base));                                                                              \
    _Pragma("unroll") for (int kk = 0; kk < 2; ++kk)                                                               \
      af[2 * ((u) >> 3) + ((u) & 1)][((u) & 6) + kk] = cat(lds8(ab_ ^ (64 * kk)), lds8(ab_ ^ (64 * kk + 32)));      \
  }
#define USE_EPOCH() { asm volatile("" : "+s"(ep)); ep += 1; tlo = (u32)ep; thi = (u32)(ep >> 32); PIN(ones4); }
#define EARLY_STATS(mark)                                                                                           \
  {                                                                                                                 \
    WAITQ(mark); PIN(xrow);                                                                                         \
    float s_ = 0, q_ = 0;                                                                                           \
    _Pragma("unroll") for (int e = 0; e < 4; ++e) { s_ = dot2(xrow[e], ones4[0], s_); q_ = dot2(xrow[e], xrow[e], q_); } \
    s_ += dpp<0x128>(s_); q_ += dpp<0x128>(q_);                                                                     \
    s_ += dpp<0x124>(s_); q_ += dpp<0x124>(q_);                                                                     \
    s_ += dpp<0x4E>(s_); q_ += dpp<0x4E>(q_);                                                                       \
    s_ += dpp<0xB1>(s_); q_ += dpp<0xB1>(q_);                                                                       \
    red2g(s_, q_);                                                                                                  \
    float r_, m_;                                                                                                   \
    ln_fin(s_, q_, 1.0f / 512, r_, m_);                                                                             \
    VM(st16_sc1((u32x4){__float_as_uint(r_), tlo, __float_as_uint(m_), tlo}, l16 << 22, sws, WS_XG + (P * 128 + (t * 8 + c) * 8 + w) * 16)); \
  }
#define GATHER_XS()                                                                                                 \
  WAITQ(mxg); PIN(xg);                                                                                              \
  poll_gran(xg, i16 * 16, sws, WS_XG + (P * 128 + 16 * w) * 16, tlo);                                               \
  sts4f(L_XS + (16 * w + i16) * 4, __uint_as_float(xg[0]));                                                         \
  sts4f(L_XS + 512 + (16 * w + i16) * 4, __uint_as_float(xg[2]));
#define RELANE() const int lane = opq(l16) >> 4, i16 = lane & 15, g = lane >> 4, tid = w * 64 + lane; (void)tid; (void)g; (void)i16
#define SET_FLAG(off) st8_sc1((u32x2){tlo, thi}, l16 << 22, sws, (off))
  const int gs = grp_start(Z2S, c2), gn = grp_start(Z2S, c2 + 1) - gs;
  const int trot = (p & 7) >= 5 ? (p & 7) - 5 : (p & 7);
#define TILE(j, n_) (((j) + trot) >= (n_) ? (j) + trot - (n_) : (j) + trot)
  const u32 zb0 = WS_ZB + (gs * 64 + 8 * w) * 1024 + ux1 * 4096;
#define DMA_WOUT(e)                                                                                                 \
  {                                                                                                                 \
    const rsrc_t so_ = __builtin_amdgcn_make_buffer_rsrc((void*)wout, 0, 688 * 1024, 0x00020000);                   \
    const int ws_ = grp_start(Z2S, w), wn_ = grp_start(Z2S, w + 1) - ws_;                                           \
    if ((e) < 6) dma16(L_WOUT + ((e) < 4 ? 2 * w + ((e) >> 1) : 16 + w) * 2048 + ((e) & 1) * 1024, vwo, so_, ((ws_ + TILE((e) >> 1, wn_)) * 2 + ((e) & 1)) * 8192); \
    else if ((e) == 6) dma16(L_WOUT + 24 * 2048 + w * 1024, vwo, so_, ((((w >> 1) == 0 ? 5 : ((w >> 1) == 1 ? 11 : ((w >> 1) == 2 ? 27 : 64))) + TILE(3, 6)) * 2 + (w & 1)) * 8192); \
  }
#define DMA_WOUT_AT(dst, e)                                                                                         \
  {                                                                                                                 \
    const rsrc_t so_ = __builtin_amdgcn_make_buffer_rsrc((void*)wout, 0, 688 * 1024, 0x00020000);                   \
    const int ws_ = grp_start(Z2S, w), wn_ = grp_start(Z2S, w + 1) - ws_;                                           \
    dma16((dst) + ((e) - 7) * 1024, vwo, so_, ((ws_ + TILE(wn_ - 1 - (((e) - 7) >> 1), wn_)) * 2 + (((e) - 7) & 1)) * 8192); \
  }
#define VWO() const u32 vwo = ((lane >> 2) & 7) * 1024 + c2 * 128 + (lane >> 5) * 64 + (((lane & 3) ^ (((lane >> 4) & 1) << 1)) * 16)
#define LOAD_B(u, so_)                                                                                              \
  bfr[u][0] = ld16_bo<0>(l16, sws, so_); bfr[u][1] = ld16_bo<1024>(l16, sws, so_);                                  \
  bfr[u][2] = ld16_bo<2048>(l16, sws, so_); bfr[u][3] = ld16_bo<3072>(l16, sws, so_);                               \
  bfr[u][4] = ld16_bo<0>(l16, sws, (so_) ^ 4096); bfr[u][5] = ld16_bo<1024>(l16, sws, (so_) ^ 4096);                \
  bfr[u][6] = ld16_bo<2048>(l16, sws, (so_) ^ 4096); bfr[u][7] = ld16_bo<3072>(l16, sws, (so_) ^ 4096);

  if (t == 0) {
    int vq = 2;
    const rsrc_t sw = __builtin_amdgcn_make_buffer_rsrc((void*)win, 0, 512 * 2752, 0x00020000);
    const int z2s = grp_start(Z2S, c), z2n = grp_start(Z2S, c + 1) - z2s;
    const int z1s = grp_start(Z1S, c), z1n = grp_start(Z1S, c + 1) - z1s;
    u32 vw[3];
    int wch[3];
#pragma unroll
    for (int e = 0; e < 3; ++e) {
      const int sg = 2 * e + (lane >> 5), hi = sg >= 3;
      wch[e] = (sg - 3 * hi) * 4 + ((lane & 3) ^ (hi << 1));
      vw[e] = wch[e] < 2 * z2n ? (16 * w + 8 * hi + ((lane >> 2) & 7)) * 2752 + 1376 + 32 * z2s + 16 * wch[e] : 0x7FFF0000u;
    }
#define DMA_W(bk, e) dma16(LZ_W + ((bk) % 3) * 24576 + w * 3072 + (e) * 1024, vw[e], sw, ((2 * (bk) + rot) & 7) * (64 * 2752))
#define UBASE(u) ((u) % 5 == 0 ? LZ_U : ((u) % 5 - 1) * 16384)
#define ISS_X(n) do { VM(DMA_X(((n) & 3) * 16384, n, 0)); VM(DMA_X(((n) & 3) * 16384, n, 1)); mx[n] = vq; } while (0)
#define ISS_U(u) do { VM(DMA_U(u, UBASE(u), 0)); VM(DMA_U(u, UBASE(u), 1)); mu[u] = vq; } while (0)
#define ISS_W(bk, e0, e1) do { _Pragma("unroll") for (int e_ = e0; e_ < e1; ++e_) VM(DMA_W(bk, e_)); mw[bk] = vq; } while (0)
    int mx[8], mw[8], mu[16];
    mx[0] = vq;
    ISS_W(0, 0, 3);
    u32x4 xrow;
    VM(xrow = ld16(l16, sx, (c * 8 + w) * 1024));
    const int mxr = vq;
    ISS_X(1);
    const u32 xa0 = opq((32 * rw + i16) * 128 + ((g ^ (i16 >> 1)) * 16)), xa1 = xa0 ^ 64;
    u32 ta[3];
    int tl[3];
    {
      const u32 trb = LZ_W + g * 1536 + ((lane >> 2) & 3) * 64 + ((((g & 1) << 1) | ((lane >> 1) & 1)) * 16) + (lane & 1) * 8;
#pragma unroll
      for (int j = 0; j < 3; ++j) {
        tl[j] = 3 * cw + (j + rw) % 3;
        ta[j] = (trb + (tl[j] >> 1) * 512) ^ ((tl[j] & 1) * 32);
      }
    }
#define NEXT_BLOCK(bk) _Pragma("unroll") for (int j = 0; j < 3; ++j) ta[j] = opq(ta[j] + ((bk) == 0 ? 0 : ((bk) % 3 == 0 ? -49152 : 24576)))
    f32x4 acc[2][3], csa = zero4;
#pragma unroll
    for (int j = 0; j < 3; ++j) acc[0][j] = acc[1][j] = zero4;
    u32x4 xf[16][2], wf[2][3];
    u32x4 xg;
    int mxg = 0;
    WAITQ(mw[0]);
    BARRIER();
    USE_EPOCH()
    STAMP(7);
#define P1_MEM(n, h)                                                                                                \
  {                                                                                                                 \
    if ((n) == 0) { if ((h) == 0) ISS_X(2); if ((h) == 1) ISS_W(1, 0, 2); if ((h) == 2) ISS_W(1, 2, 3); if ((h) == 3) ISS_X(3); } \
    if ((n) == 1) { if ((h) == 0) ISS_W(2, 0, 2); if ((h) == 1) ISS_W(2, 2, 3); if ((h) == 2) VM(DMA_X(0, 4, 0)); if ((h) == 3) { VM(DMA_X(0, 4, 1)); mx[4] = vq; } } \
    if ((n) == 2 || (n) == 4) { if ((h) < 2) { VM(DMA_X((((n) + 3) & 3) * 16384, (n) + 3, (h) & 1)); mx[(n) + 3] = vq; } else ISS_W((n) / 2 + 2, (h) - 2, (h) - 1); } \
    if ((n) == 3) { if ((h) == 0) ISS_W(3, 2, 3); else if ((h) < 3) { VM(DMA_X(32768, 6, (h) - 1)); mx[6] = vq; } else ISS_U(0); } \
    if ((n) == 5) { if ((h) == 0) ISS_W(4, 2, 3); if ((h) == 1) { VM(xg = ld16_sc1(i16 * 16, sws, WS_XG + (P * 128 + 16 * w) * 16)); mxg = vq; } \
                    if ((h) >= 2) { VM(DMA_U(1, UBASE(1), (h) - 2)); mu[1] = vq; } }                                 \
    if ((n) == 6) { if ((h) < 3) ISS_W(5, h, (h) + 1); else ISS_U(2); }                                             \
    if ((n) == 7) { if ((h) < 2) { VM(DMA_U(3, UBASE(3), h)); mu[3] = vq; } }                                       \
  }
#pragma unroll
    for (int ks = 0; ks <= 16; ++ks) {
      const int n = ks >> 1, kk = ks & 1;
      if (ks > 0 && ks < 16 && kk == 0) {
        { const int mk = (n & 1) ? mx[n] : (mx[n] > mw[n >> 1] ? mx[n] : mw[n >> 1]); WAITQ(mk); }
        BARRIER();
      }
      if (ks < 16 && (ks & 3) == 0) { NEXT_BLOCK(ks >> 2); }
      if (ks == 8) {
#pragma unroll
        for (int e = 0; e < 3; ++e)
          vw[e] = wch[e] < 2 * z1n ? (16 * w + 8 * (2 * e + (lane >> 5) >= 3) + ((lane >> 2) & 7)) * 2752 + 32 * z1s + 16 * wch[e] : 0x7FFF0000u;
      }
      const int k1 = ks > 0 ? ks - 1 : 0, q1 = k1 & 1;
#pragma unroll
      for (int m = 0; m < 7; ++m) {
        if (ks > 0) {
          if (m < 6) acc[m & 1][m >> 1] = mfma32(xf[k1][m & 1], wf[q1][m >> 1], acc[m & 1][m >> 1]);
          else csa = mfma32(ones4, wf[q1][0], csa);
          SCHED();
        }
        if (ks < 16) {
          if (m == 0) { P1_MEM(n, 2 * kk) xf[ks & 15][0] = lds16((kk ? xa1 : xa0) + (n & 3) * 16384); xf[ks & 15][1] = lds16((kk ? xa1 : xa0) + (n & 3) * 16384 + 2048); }
          else if (m < 4) wf[kk][m - 1] = ldfrag(ta[m - 1] + (ks & 3) * 6144, 256);
          if (m == 4) P1_MEM(n, 2 * kk + 1)
        }
        SCHED();
      }
      if (ks == 2) EARLY_STATS(mxr)
    }
    GATHER_XS()
    sts4f(L_CS + tl[0] * 64 + i16 * 4, csa[0]);
    WAITQ(mu[1] > mw[4] ? mu[1] : mw[4]);
    READ_U(0, UBASE(0)) READ_U(1, UBASE(1))
    BARRIER();
    STAMP(1);
    u32x4 zp[3];
    ISS_W(6, 0, 3);
    {
      f32x2 ps[4], pq[4];
      float cs[3];
#pragma unroll
      for (int j = 0; j < 3; ++j) cs[j] = -lds4f(L_CS + tl[j] * 64 + i16 * 4);
#pragma unroll
      for (int rt = 0; rt < 2; ++rt) {
        const f32x4 rr = lds16f(L_XS + (32 * rw + 16 * rt + 4 * g) * 4), mm = lds16f(L_XS + 512 + (32 * rw + 16 * rt + 4 * g) * 4);
        f32x2 z0[3], z1[3];
#pragma unroll
        for (int j = 0; j < 3; ++j) {
          z0[j] = gelu2(fma2(lo2(mm), bc2(cs[j]), lo2(acc[rt][j])) * lo2(rr));
          z1[j] = gelu2(fma2(hi2(mm), bc2(cs[j]), hi2(acc[rt][j])) * hi2(rr));
          zp[j][2 * rt] = pk2v(z0[j]); zp[j][2 * rt + 1] = pk2v(z1[j]);
        }
        ps[2 * rt] = z0[0] + z0[1] + z0[2]; ps[2 * rt + 1] = z1[0] + z1[1] + z1[2];
        pq[2 * rt] = fma2(z0[2], z0[2], fma2(z0[1], z0[1], z0[0] * z0[0]));
        pq[2 * rt + 1] = fma2(z1[2], z1[2], fma2(z1[1], z1[1], z1[0] * z1[0]));
        SCHED();
        if (rt == 0) { ISS_U(4); ISS_U(5); }
        else ISS_U(6);
        SCHED();
      }
      const bool b3 = lane & 8, b2 = lane & 4, b1 = lane & 2;
#pragma unroll
      for (int m = 0; m < 2; ++m)
#pragma unroll
        for (int e = 0; e < 2; ++e) {
          ps[m][e] = (b3 ? ps[m + 2][e] : ps[m][e]) + dpp<0x128>(b3 ? ps[m][e] : ps[m + 2][e]);
          pq[m][e] = (b3 ? pq[m + 2][e] : pq[m][e]) + dpp<0x128>(b3 ? pq[m][e] : pq[m + 2][e]);
        }
#pragma unroll
      for (int e = 0; e < 2; ++e) {
        ps[0][e] = (b2 ? ps[1][e] : ps[0][e]) + dpp<0x141>(b2 ? ps[0][e] : ps[1][e]);
        pq[0][e] = (b2 ? pq[1][e] : pq[0][e]) + dpp<0x141>(b2 ? pq[0][e] : pq[1][e]);
      }
      float s_ = (b1 ? ps[0][1] : ps[0][0]) + dpp<0x4E>(b1 ? ps[0][0] : ps[0][1]);
      float q_ = (b1 ? pq[0][1] : pq[0][0]) + dpp<0x4E>(b1 ? pq[0][0] : pq[0][1]);
      s_ += dpp<0xB1>(s_);
      q_ += dpp<0xB1>(q_);
      const int row = 32 * rw + 16 * (i16 >> 3) + 4 * g + ((i16 >> 1) & 3);
      VM(st16_sc1((u32x4){__float_as_uint(s_), tlo, __float_as_uint(q_), tlo}, row * 16 + ((lane & 1) << 30), sws, WS_SG + ((P * 8 + c) * 2 + cw) * 2048));
      PIN(zp[0]); PIN(zp[1]); PIN(zp[2]);
    }
    STAMP(2);
    {
    RELANE();
#pragma unroll
    for (int j = 0; j < 3; ++j) acc[0][j] = acc[1][j] = zero4;
    csa = zero4;
    u32x4 q0, q1, q2, q3;
    int mq = 0, mz = 0;
    const u32 gvo = (2 * g) * 4096 + (32 * rw + 16 * cw + i16) * 16;
    const u32 gso = WS_SG + P * 32768;
#pragma unroll
    for (int ks = 0; ks <= 16; ++ks) {
      if (ks == 8) {
        WAITQ(mq); PIN(q0); PIN(q1); PIN(q2); PIN(q3);
#pragma nounroll
        for (int it = 0; it < SPIN_CAP; ++it) {
          if (__all(tag_ok(q0, tlo) && tag_ok(q1, tlo) && tag_ok(q2, tlo) && tag_ok(q3, tlo))) break;
          __builtin_amdgcn_s_sleep(POLL_SLEEP);
          asm volatile(
              "buffer_load_dwordx4 %0, %4, %5, %6 offen sc1\n\t"
              "buffer_load_dwordx4 %1, %4, %5, %6 offen offset:2048 sc1\n\t"
              "buffer_load_dwordx4 %2, %4, %5, %7 offen sc1\n\t"
              "buffer_load_dwordx4 %3, %4, %5, %7 offen offset:2048 sc1\n\t"
              "s_waitcnt vmcnt(0)"
              : "=&v"(q0), "=&v"(q1), "=&v"(q2), "=&v"(q3)
              : "v"(gvo), "s"(sws), "s"(gso), "s"(gso + 4096)
              : "memory");
        }
        float s = (__uint_as_float(q0[0]) + __uint_as_float(q1[0])) + (__uint_as_float(q2[0]) + __uint_as_float(q3[0]));
        float q = (__uint_as_float(q0[2]) + __uint_as_float(q1[2])) + (__uint_as_float(q2[2]) + __uint_as_float(q3[2]));
        red2g(s, q);
        float r2, m2;
        ln_fin(s, q, 1.0f / 688, r2, m2);
        sts4f(L_ZST + (32 * rw + 16 * cw + i16) * 4, r2);
        sts4f(L_ZST + 512 + (32 * rw + 16 * cw + i16) * 4, -m2 * r2);
        STAMP(3);
      }
      if (ks < 16 && (ks & 3) == 0) {
        if (ks > 0) { WAITQ(ks == 12 ? mz : mw[4 + (ks >> 2)]); BARRIER(); }
        if (ks == 12) { if (w == 0) SET_FLAG(WS_FA + (P * 8 + c) * 128); STAMP(5); }
        NEXT_BLOCK(4 + (ks >> 2));
      }
      if (ks == 8) {
#pragma unroll
        for (int rt = 0; rt < 2; ++rt) {
          const u32 ra = L_ZST + (32 * rw + 16 * rt + 4 * g) * 4;
          const f32x4 r4 = lds16f(ra), m4 = lds16f(ra + 512);
#pragma unroll
          for (int j = 0; j < 3; ++j) {
            const u32 u0 = zp[j][2 * rt], u1 = zp[j][2 * rt + 1];
            zp[j][2 * rt] = pk2v(fma2((f32x2){blo(u0), bhi(u0)}, lo2(r4), lo2(m4)));
            zp[j][2 * rt + 1] = pk2v(fma2((f32x2){blo(u1), bhi(u1)}, hi2(r4), hi2(m4)));
          }
        }
#pragma unroll
        for (int j = 0; j < 3; ++j)
          VM(st16_sc1(zp[j], l16, sws, (tl[j] < z2n) ? WS_ZB + ((z2s + tl[j]) * 64 + 4 * P + rw) * 1024 : 0x7FFF0000u));
        mz = vq;
        STAMP(4);
        SCHED();
      }
      if (ks >= 3 && ks <= 9) { WAITQ(mu[ks - 1]); READ_U(ks - 1, UBASE(ks - 1)) }
      if (ks >= 9 && ks <= 15) { WAITQ(mu[ks]); READ_U(ks, UBASE(ks)) }
      const int k1 = ks > 0 ? ks - 1 : 0, q1_ = k1 & 1;
#pragma unroll
      for (int m = 0; m < 7; ++m) {
        if (ks > 0) {
          if (m < 6) acc[m & 1][m >> 1] = mfma32(wf[q1_][m >> 1], xf[k1][m & 1], acc[m & 1][m >> 1]);
          else csa = mfma32(ones4, wf[q1_][0], csa);
          SCHED();
        }
        if (ks < 16 && m >= 1 && m < 4) wf[ks & 1][m - 1] = ldfrag(ta[m - 1] + (ks & 3) * 6144, 256);
        if (m == 0 || m == 4) {
          const int h = m == 4;
          if (ks >= 4 && ks <= 12) { VM(DMA_U(ks + 3, UBASE(ks + 3), h)); mu[ks + 3] = vq; }
        }
        if (m == 6) {
          if (ks >= 4 && ks <= 6) ISS_W(7, ks - 4, ks - 3);
        }
        if (m == 2) {
          if (ks == 5) { VM(q0 = ld16_sc1o<0>(gvo, sws, gso)); VM(q1 = ld16_sc1o<2048>(gvo, sws, gso)); }
          if (ks == 6) { VM(q2 = ld16_sc1o<0>(gvo, sws, gso + 4096)); VM(q3 = ld16_sc1o<2048>(gvo, sws, gso + 4096)); mq = vq; }
        }
        SCHED();
      }
      if ((ks & 3) == 3 || ks == 16) {
#pragma unroll
        for (int j = 0; j < 3; ++j) { PIN(acc[0][j]); PIN(acc[1][j]); }
        PIN(csa);
      }
    }
    sts4f(L_CS + 512 + tl[0] * 64 + i16 * 4, csa[0]);
    gf = ld8_sc1((lane >> 5) * 1024, sws, WS_FA + (16 * w + c2) * 128);
    BARRIER();
    VWO();
    DMA_WOUT(0) DMA_WOUT(1)
    WAITV(2);
    PIN(gf);
    if (!__all(gf[0] == tlo && gf[1] == thi)) poll_flags((lane >> 5) * 1024, true, sws, WS_FA + (16 * w + c2) * 128, tlo, thi);
    STAMP(6);
    {
      float xr[2], xm[2];
#pragma unroll
      for (int rt = 0; rt < 2; ++rt) {
        xr[rt] = lds4f(L_XS + (32 * rw + 16 * rt + i16) * 4);
        xm[rt] = -lds4f(L_XS + 512 + (32 * rw + 16 * rt + i16) * 4);
      }
      const u32 so0 = zb0 + TILE(0, gn) * 65536;
#pragma unroll
      for (int j = 0; j < 3; ++j) {
        const f32x4 cc = lds16f(L_CS + 512 + tl[j] * 64 + g * 16);
        bfr[0][3 * j] = ld16_b(l16, sws, so0 ^ (3 * j * 1024));
        bfr[0][3 * j + 1] = ld16_b(l16, sws, so0 ^ ((3 * j + 1) * 1024));
        if (j < 2) bfr[0][3 * j + 2] = ld16_b(l16, sws, so0 ^ ((3 * j + 2) * 1024));
        u32x4 zz;
#pragma unroll
        for (int rt = 0; rt < 2; ++rt) {
          zz[2 * rt] = pk2v(gelu2(fma2(lo2(cc), bc2(xm[rt]), lo2(acc[rt][j])) * bc2(xr[rt])));
          zz[2 * rt + 1] = pk2v(gelu2(fma2(hi2(cc), bc2(xm[rt]), hi2(acc[rt][j])) * bc2(xr[rt])));
        }
        st16_sc1(zz, l16, sws, (tl[j] < z1n) ? WS_Z1 + ((4 * P + rw) * 43 + z1s + tl[j]) * 1024 : 0x7FFF0000u);
        SCHED();
      }
    }
    }
  } else {
    int vq = 2;
    const rsrc_t sw = __builtin_amdgcn_make_buffer_rsrc((void*)wqkv, 0, 512 * 3072, 0x00020000);
    const u32 vw = (8 * w + ((lane >> 2) & 7)) * 3072 + 128 * c + ((lane >> 5) * 4 + ((lane & 3) ^ ((w & 1) << 1))) * 16;
#define DMA_S(n, e)                                                                                                 \
  do {                                                                                                              \
    if ((e) < 2) DMA_X(((n) % 3) * 40960, n, (e) & 1);                                                              \
    else dma16(((n) % 3) * 40960 + 16384 + w * 3072 + ((e) - 2) * 1024, vw, sw, (((n) + rot) & 7) * (64 * 3072) + ((e) - 2) * 1024); \
  } while (0)
#define ISS_Q(u, base) do { VM(DMA_U(u, base, 0)); VM(DMA_U(u, base, 1)); mu[u] = vq; } while (0)
    int ms[8], mu[16];
    VM(DMA_S(0, 2)); VM(DMA_S(0, 3)); VM(DMA_S(0, 4)); ms[0] = vq;
    u32x4 xrow;
    VM(xrow = ld16(l16, sx, ((8 + c) * 8 + w) * 1024));
    const int mxr = vq;
#pragma unroll
    for (int e = 0; e < 5; ++e) { VM(DMA_S(1, e)); ms[1] = vq; }
    const int l32 = lane & 31, hh = lane >> 5;
    u32 xa = (32 * rw + l32) * 128 + ((hh ^ ((l32 >> 1) & 7)) * 16);
    u32 wa = 16384 + hh * 3072 + cw * 512 + ((lane >> 2) & 3) * 64 + (((((g & 1) << 1) | ((lane >> 1) & 1)) ^ (hh << 1)) * 16) + (lane & 1) * 8;
    u32 ca = 16384 + g * 3072 + min(w, 5) * 512 + ((lane >> 2) & 3) * 64 + ((((g & 1) << 1) | ((lane >> 1) & 1)) * 16) + (lane & 1) * 8;
    const f32x16 zero16 = {0, 0, 0, 0, 0, 0, 0, 0, 0, 0, 0, 0, 0, 0, 0, 0};
    f32x16 acc[3] = {zero16, zero16, zero16};
    f32x4 csa[2] = {zero4, zero4};
    u32x4 xg;
    int mxg = 0;
    WAITQ(ms[0]);
    BARRIER();
    USE_EPOCH()
    STAMP(7);
    {
      u32x4 xf[2], wf[2][4];
#pragma unroll
      for (int n = 0; n <= 8; ++n)
#pragma unroll
      for (int sq = 0; sq < 4; ++sq) {
        const int s = 4 * n + sq;
        if (s > 32) continue;
        if (s < 32 && sq == 0) {
          if (n > 0) {
            WAITQ(ms[n]);
            BARRIER();
          }
          const int d = n == 0 ? 0 : (n % 3 == 0 ? -81920 : 40960);
          xa = opq(xa + d); wa = opq(wa + d); ca = opq(ca + d);
        }
        const int q1 = (s + 1) & 1, q0 = s & 1;
#pragma unroll
        for (int m = 0; m < 4; ++m) {
          if (s > 0) {
            if (m < 3) acc[m] = mfmaL(wf[q1][m], xf[q1], acc[m]);
            else csa[q1] = mfma32(ones4, wf[q1][3], csa[q1]);
            SCHED();
          }
          if (s < 32) {
            if (m == 0) { xf[q0] = lds16(xa ^ (32 * sq)); wf[q0][0] = ldfrag(wa + sq * 6144, 256); }
            if (m == 1) { wf[q0][1] = ldfrag(wa + sq * 6144 + 1024, 256); wf[q0][2] = ldfrag(wa + sq * 6144 + 2048, 256); }
            if (m == 2) wf[q0][3] = ldfrag((ca ^ (32 * (sq & 1))) + (sq >> 1) * 12288, 256);
            if (m == 0 || m == 2) {
              const int h = 2 * sq + (m == 2);
              if (n <= 5 && h < 5) { VM(DMA_S(n + 2, h)); ms[n + 2] = vq; }
              if (n == 0 && h == 5) {
                rsrc_t sp = __builtin_amdgcn_make_buffer_rsrc((void*)wphi, 0, 16384, 0x00020000);
                VM(dma16(L_WPHI + (w & 1) * 1024, l16, sp, c * 2048 + (w & 1) * 1024));
              }
              if (n == 4 && h == 5) { VM(xg = ld16_sc1(i16 * 16, sws, WS_XG + (P * 128 + 16 * w) * 16)); mxg = vq; }
              if (n == 6) { VM(DMA_U(sq, (sq < 2 ? LQ_M : LQ_A) + (sq & 1) * 16384, h & 1)); mu[sq] = vq; }
              if (n == 7 && sq < 2) { VM(DMA_U(4 + sq, sq * 16384, h & 1)); mu[4 + sq] = vq; }
            }
          }
          SCHED();
        }
        if (s == 3) EARLY_STATS(mxr)
      }
    }
    GATHER_XS()
    sts4f(L_CS + w * 128 + i16 * 4, csa[0][0]);
    sts4f(L_CS + w * 128 + 64 + i16 * 4, csa[1][0]);
    BARRIER();
    STAMP(1);
    WAITQ(mu[3]);
    READ_U(0, LQ_M) READ_U(1, LQ_M + 16384) READ_U(2, LQ_A) READ_U(3, LQ_A + 16384)
    const float xr = lds4f(L_XS + (32 * rw + l32) * 4), xm = -lds4f(L_XS + 512 + (32 * rw + l32) * 4);
    u32x4 wp0, wp1;
    {
      const u32 a = L_WPHI + (2 * cw + (lane & 1)) * 512 + (4 * hh + ((lane >> 2) & 3)) * 32 + (2 * (g & 1) + ((lane >> 1) & 1)) * 8;
      const u32x4 f = ldfrag(a, 256);
      const bool own = (lane >> 2) & 1;
#pragma unroll
      for (int e = 0; e < 4; ++e) { wp0[e] = own ? 0u : f[e]; wp1[e] = own ? f[e] : 0u; }
    }
#define FEATURES(e_, dst)                                                                                           \
  {                                                                                                                 \
    u32x4 kp[2];                                                                                                    \
    _Pragma("unroll") for (int a = 0; a < 4; ++a) {                                                                \
      const f32x4 cc = lds16f(L_CS + (2 * (e_) + cw) * 128 + a * 32 + hh * 16);                                      \
      kp[a >> 1][2 * (a & 1)] = pk2v(fma2(lo2(cc), bc2(xm), (f32x2){acc[e_][4 * a], acc[e_][4 * a + 1]}));          \
      kp[a >> 1][2 * (a & 1) + 1] = pk2v(fma2(hi2(cc), bc2(xm), (f32x2){acc[e_][4 * a + 2], acc[e_][4 * a + 3]}));  \
    }                                                                                                               \
    f32x16 pl = mfmaL(wp0, kp[0], zero16);                                                                          \
    pl = mfmaL(wp1, kp[1], pl);                                                                                     \
    hedgehog16(pl, xr, dst);                                                                                        \
  }
    {
      const int s_ = 32 * rw + l32;
      const u32 key = (((s_ >> 1) & 3) ^ (((s_ >> 3) & 1) << 1)) * 16;
      {
        const u32 va = LQ_V + (s_ >> 3) * 1024 + cw * 512 + (s_ & 7) * 64 + hh * 8;
#pragma unroll
        for (int a = 0; a < 4; ++a) {
          const f32x4 cc = lds16f(L_CS + (4 + cw) * 128 + a * 32 + hh * 16);
          sts8(va + ((a * 16) ^ key), (u32x2){pk2v(fma2(lo2(cc), bc2(xm), (f32x2){acc[2][4 * a], acc[2][4 * a + 1]}) * bc2(xr)),
                                              pk2v(fma2(hi2(cc), bc2(xm), (f32x2){acc[2][4 * a + 2], acc[2][4 * a + 3]}) * bc2(xr))});
        }
      }
      WAITQ(mu[5]);
      READ_U(4, 0) READ_U(5, 16384)
      WAITL0();
      ISS_Q(6, LQ_M); ISS_Q(7, LQ_M + 16384); ISS_Q(8, LQ_A); ISS_Q(9, LQ_A + 16384); ISS_Q(10, 0); ISS_Q(11, 16384);
      u32x4 ph[4];
      FEATURES(1, ph)
      const u32 pa = LQ_PHI + (s_ >> 3) * 2048 + (2 * cw + hh) * 512 + (s_ & 7) * 64;
#pragma unroll
      for (int a = 0; a < 4; ++a) sts16(pa + ((a * 16) ^ key), ph[a]);
    }
    WAITQ(mu[9]);
    READ_U(6, LQ_M) READ_U(7, LQ_M + 16384) READ_U(8, LQ_A) READ_U(9, LQ_A + 16384)
    BARRIER();
    STAMP(2);
    {
      const u32 kx = (((lane >> 3) & 1) ^ ((g & 1) << 1)) * 16;
      const u32 pa = LQ_PHI + g * 2048 + (w >> 1) * 512 + ((lane >> 2) & 3) * 64 + (((2 * (w & 1) + ((lane >> 1) & 1)) * 16) ^ kx) + (lane & 1) * 8;
      const u32 va = LQ_V + g * 1024 + (w >> 2) * 512 + ((lane >> 2) & 3) * 64 + (((2 * ((w >> 1) & 1) + ((lane >> 1) & 1)) * 16) ^ kx) + (lane & 1) * 8;
      u32x4 pf[4], vf[4];
#pragma unroll
      for (int k = 0; k < 4; ++k) {
        pf[k] = cat(ldstr(pa + k * 8192), ldstr((pa ^ 32) + k * 8192 + 256));
        vf[k] = cat(ldstr(va + k * 4096), ldstr((va ^ 32) + k * 4096 + 256));
      }
      f32x4 kv = zero4, kz = zero4;
#pragma unroll
      for (int k = 0; k < 4; ++k) { kv = mfma32(pf[k], vf[k], kv); kz = mfma32(pf[k], ones4, kz); }
      const u32 so = WS_KP + (c * 16 + P) * KP_SZ;
      u32x4 g0 = {__float_as_uint(kv[0]), tlo, __float_as_uint(kv[1]), tlo}, g1 = {__float_as_uint(kv[2]), tlo, __float_as_uint(kv[3]), tlo};
      u32x4 g2 = {__float_as_uint((lane & 1) ? kz[2] : kz[0]), tlo, __float_as_uint((lane & 1) ? kz[3] : kz[1]), tlo};
      PIN(g0); PIN(g1); PIN(g2);
      asm volatile("s_nop 7" ::: "memory");
      VM(st16_sc1(g0, l16, sws, so + w * 2048));
      VM(st16_sc1(g1, l16, sws, so + w * 2048 + 1024));
      VM(st16_sc1(g2, g * 32 + (lane & 1) * 16 + ((i16 >> 1) << 26), sws, so + 16384 + w * 128));
    }
    STAMP(3);
    ISS_Q(12, LQ_M); ISS_Q(13, LQ_M + 16384); ISS_Q(14, LQ_A); ISS_Q(15, LQ_A + 16384);
    WAITQ(mu[11]);
    READ_U(10, 0) READ_U(11, 16384)
    BARRIER();
    {
      VWO();
#pragma unroll
      for (int e = 0; e < 7; ++e) DMA_WOUT(e)
    }
    u32x4 phq[4];
    FEATURES(0, phq)
    PIN(phq[0]); PIN(phq[1]); PIN(phq[2]); PIN(phq[3]);
    STAMP(4);
    if (w >= 5) {
      const int id = 2 * P + (w == 7 ? (lane & 1) : w - 5), wq = id >> 2, gq = id & 3;
      const u32 vi0 = (4 * g) * KP_SZ + (w == 7 ? 16384 + wq * 128 + gq * 32 : wq * 2048 + (gq * 16 + i16) * 16);
      const u32 vi1 = vi0 + (w == 7 ? 16 : 1024);
      const u32 vo = (w == 7 ? (WS_KZ - WS_KF) + c * 512 + id * 16 + ((i16 >> 1) << 25) : c * 8192 + id * 256 + i16 * 16) + (g << 28);
      float sa[4];
#pragma nounroll
      for (int it = 0; it < SPIN_CAP; ++it) {
        u32x4 a[4][2];
#pragma unroll
        for (int o = 0; o < 4; ++o) {
          a[o][0] = ld16_sc1(vi0, sws, WS_KP + (c * 16 + o) * KP_SZ);
          a[o][1] = ld16_sc1(vi1, sws, WS_KP + (c * 16 + o) * KP_SZ);
        }
        WAITV(0);
        bool ok = true;
#pragma unroll
        for (int o = 0; o < 4; ++o) { PIN(a[o][0]); PIN(a[o][1]); ok = ok && tag_ok(a[o][0], tlo) && tag_ok(a[o][1], tlo); }
#pragma unroll
        for (int e = 0; e < 4; ++e)
          sa[e] = (__uint_as_float(a[0][e >> 1][2 * (e & 1)]) + __uint_as_float(a[1][e >> 1][2 * (e & 1)])) +
                  (__uint_as_float(a[2][e >> 1][2 * (e & 1)]) + __uint_as_float(a[3][e >> 1][2 * (e & 1)]));
        if (__all(ok)) break;
        __builtin_amdgcn_s_sleep(POLL_SLEEP);
      }
      red2g(sa[0], sa[1]);
      red2g(sa[2], sa[3]);
      st16_sc1((u32x4){pk2(sa[0], sa[1]), tlo, pk2(sa[2], sa[3]), tlo}, vo, sws, WS_KF);
    }
    gf = ld8_sc1((lane >> 5) * 1024, sws, WS_FA + (16 * w + c2) * 128);
    WAITV(8);
    READ_U(12, LQ_M) READ_U(13, LQ_M + 16384) READ_U(14, LQ_A) READ_U(15, LQ_A + 16384)
    BARRIER();
#pragma unroll
    for (int a = 0; a < 4; ++a) sts16(L_WO1 + w * 4096 + a * 1024 + l16, phq[a]);
    WAITV(0);
    PIN(gf);
    if (!__all(gf[0] == tlo && gf[1] == thi)) poll_flags((lane >> 5) * 1024, true, sws, WS_FA + (16 * w + c2) * 128, tlo, thi);
    STAMP(6);
    { const u32 so_ = zb0 + TILE(0, gn) * 65536; LOAD_B(0, so_) }
  }

  tlo = __builtin_amdgcn_readfirstlane(tlo);
  thi = __builtin_amdgcn_readfirstlane(thi);
  int w2 = w, c22 = c2, p2 = p, cc2 = c;
  asm volatile("" : "+s"(w2), "+s"(tlo), "+s"(thi), "+s"(c22), "+s"(p2), "+s"(cc2));
  {
  const int w = w2, rw = w & 3, cw = w >> 2, c2 = c22, p = p2, c = cc2, P = p >> 1;
  RELANE();
  u32x4 kfg, kzg;
  u32 kfo = WS_KF + c * 8192 + w * 1024, kzo = WS_KZ + c * 512 + w * 64;
  asm volatile("" : "+s"(kfo), "+s"(kzo));
  if (t != 0) {
    kfg = ld16_sc1(l16, sws, kfo);
    kzg = ld16_sc1(g * 16, sws, kzo);
  }
  { const u32 so_ = zb0 + TILE(1, gn) * 65536; LOAD_B(1, so_) }
  STAMP(9);
  const u32 redw = opq(L_RED + w * 4096 + lane * 16), redr = opq(L_RED + (rw ^ (2 * ux0)) * 1024 + lane * 16);
  const u32 fd = WS_FD + (p * 16 + c2) * 128;
  const u32 f3s = WS_FB + (P * 8 + (c2 > 0 ? c2 - 1 : 0)) * 128;
  const u32 vw3 = ((lane >> 2) & 7) * 1024 + c2 * 128 + (lane >> 5) * 64;
  const rsrc_t swo = __builtin_amdgcn_make_buffer_rsrc((void*)wo, 0, 512 * 1024, 0x00020000);
#define DMA_WO(dst, half, e) dma16((dst) + w * 4096 + (e) * 1024, (vw3 + (lane & 3) * 16) ^ (((e) & 1) * 32), swo, ((w + 8 * (half)) * 4 + (e)) * 8192)
#define FV() ((lane & 32) ? (P * 8 + ((lane >> 2) & 7)) * 128 : (WS_FD - WS_FC) + (p * 16 + (((lane >> 1) & 14) | cw)) * 128)
  u32x2 z1r[6], hold;
  u32 bs;
  f32x4 sm0;
#define EMIT_H(jt, sm_)                                                                                             \
  {                                                                                                                 \
    const f32x2 bb = bc2(blo(bs));                                                                                  \
    const u32 h0 = pk2v((f32x2){blo(z1r[jt][0]), bhi(z1r[jt][0])} * (lo2(sm_) + bb));                               \
    const u32 h1 = pk2v((f32x2){blo(z1r[jt][1]), bhi(z1r[jt][1])} * (hi2(sm_) + bb));                               \
    const u32 hb_ = WS_HF + (((4 * p + rw) * 2 + ((jt) >> 1)) * 8 + c2) * 1024;                                     \
    if ((jt) == 0 || ((jt) == 2 && gn == 6)) hold = (u32x2){h0, h1};                                                \
    else if ((jt) == 1 || ((jt) == 3 && gn == 6)) st16_sc1((u32x4){hold[0], hold[1], h0, h1}, l16, sws, hb_);       \
    else if ((jt) == 2) st16_sc1((u32x4){h0, h1, 0u, 0u}, l16, sws, hb_);                                           \
    else st16_sc1((u32x4){h0, tlo, h1, tlo}, l16, sws, WS_HL + (((4 * p + rw) * 8 + c2) * 2 + (gn - 1 - (jt))) * 1024); \
  }
#pragma unroll
  for (int j = 0; j < 6; ++j) {
    if (j < 5 || gn == 6) {
      const int u = j & 1;
      if (j == 0) WAITV(8);
      if (j == 2) { WAITV(10); WAITV_IF(t, 0, 8); }
      if (j == 1) { WAITV(9); WAITV_IF(cw, 1, 8); }
      if (j == 3) { WAITV(9); WAITV_IF(cw, 1, 8); }
      if (j == 4) { WAITV(12); WAITV_IF(gn, 5, 4); }
      if (j == 5) { WAITV(10); WAITV_IF(cw, 1, 9); }
#pragma unroll
      for (int i = 0; i < 8; ++i) PIN(bfr[u][i]);
      {
        f32x4 a_[4];
#pragma unroll
        for (int rt = 0; rt < 4; ++rt) {
          a_[rt] = zero4;
#pragma unroll
          for (int i = 0; i < 8; ++i) a_[rt] = mfma32(bfr[u][i], af[rt][i], a_[rt]);
          SCHED();
          if (rt > 0) sts16f(redw + u * 32768 + (rt - 1) * 1024, a_[rt - 1]);
          SCHED();
        }
        if (j == 0 && t != 0) {
          PIN(kfg); PIN(kzg);
          poll_gran(kfg, l16, sws, kfo, tlo);
          poll_gran(kzg, g * 16, sws, kzo, tlo);
          sts8(L_KFS + (w >> 1) * 1024 + i16 * 64 + (w & 1) * 32 + g * 8, (u32x2){kfg[0], kfg[2]});
          sts8(L_KFS + 4096 + w * 32 + g * 8, (u32x2){kzg[0], kzg[2]});
          sts8(L_KFS + 4352 + (lane & 7) * 8, (u32x2){0u, 0u});
        }
        sts16f(redw + u * 32768 + 3072, a_[3]);
      }
      u32x4 of[2];
      if (j == 1 && t != 0) {
        const int l32 = lane & 31, hh = lane >> 5;
        const bool own = ((lane >> 2) & 1) == hh;
        const u32 ka = own ? L_KFS + (2 * cw + hh) * 1024 + ((l32 & 3) + 4 * (l32 >> 3)) * 64 : L_KFS + 4352;
        const u32 zaa = own ? L_KFS + 4096 + (2 * cw + hh) * 64 : L_KFS + 4352;
        const u32 qa = L_WO1 + w * 4096 + l16;
        const f32x16 zero16 = {0, 0, 0, 0, 0, 0, 0, 0, 0, 0, 0, 0, 0, 0, 0, 0};
        f32x16 den = zero16, num = zero16;
#pragma unroll
        for (int a = 0; a < 4; ++a) {
          const u32x4 pq_ = lds16(qa + a * 1024);
          den = mfmaL(lds16(zaa + a * 16), pq_, den);
          num = mfmaL(lds16(ka + a * 16), pq_, num);
        }
        const float inv = __builtin_amdgcn_rcpf(den[0] + 1e-6f);
#pragma unroll
        for (int a = 0; a < 2; ++a) {
          of[a] = (u32x4){pk2(num[8 * a] * inv, num[8 * a + 1] * inv), pk2(num[8 * a + 2] * inv, num[8 * a + 3] * inv),
                          pk2(num[8 * a + 4] * inv, num[8 * a + 5] * inv), pk2(num[8 * a + 6] * inv, num[8 * a + 7] * inv)};
          PIN(of[a]);
        }
        STAMP(5);
      }
      BAR_IF(cw, 1, 63);
      if (w == 4) {
        if (j == 0 && t == 0) SET_FLAG(WS_FB + (P * 8 + c) * 128);
        if (j == 3) SET_FLAG(fd);
        if (j == 3 && t != 0) SET_FLAG(WS_FC + (P * 8 + c) * 128);
        if ((j == 4 && gn == 5) || j == 5) SET_FLAG(fd + 1024);
      }
      if (j == 1) {
        if (cw == 0) {
          WAITV(0);
          PIN(gf);
          if (!__all(gf[0] == tlo && gf[1] == thi)) poll_flags((lane >> 5) * 128, true, sws, f3s, tlo, thi);
          rsrc_t sb = __builtin_amdgcn_make_buffer_rsrc((void*)bsp, 0, 4096, 0x00020000);
          bs = ld2(i16 * 2, sb, (64 * p + 16 * rw) * 2);
        }
      }
      if ((j == 1 || j == 2) && cw == 0) {
#pragma unroll
        for (int v = 3 * j - 3; v < 3 * j; ++v)
          z1r[v] = ld8_sc1(l16, sws, WS_Z1 + ((2 * p + (rw >> 1)) * 43 + gs + TILE(min(v, gn - 1), gn)) * 1024 + (rw & 1) * 8);
      }
      if ((j == 4 && gn == 5) || j == 5) gf = ld8_sc1(FV(), sws, WS_FC);
      if (j == 4 && t == 0) {
        VWO();
#pragma unroll
        for (int e = 2; e < 7; ++e) DMA_WOUT(e)
      }
      const u32 ra_ = redr + ((j + 1) & 1) * 32768;
      f32x4 sm, r_[2];
#pragma unroll
      for (int i = 0; i < 8; ++i) {
        if (j >= 1 && (i & 1) == 0 && cw == 0) {
          if (i == 2) sm = r_[0] + r_[1];
          if (i > 2) sm += r_[0] + r_[1];
          r_[0] = lds16f(ra_ + i * 4096); r_[1] = lds16f(ra_ + (i + 1) * 4096);
        }
        SCHED();
        if (j < 3) bfr[u][i] = ld16_b(l16, sws, (zb0 + TILE(j + 2, gn) * 65536) ^ (i * 1024));
        else if (j == 3) ld16_b_if6(bfr[1][i], gn, l16, sws, (zb0 + TILE(5, gn) * 65536) ^ (i * 1024));
        SCHED();
      }
      if (j == 0 && cw == 0) gf = ld8_sc1((lane >> 5) * 128, sws, f3s);
      if (j == 1 && t != 0) {
        const u32 vo = ((lane >> 4) & 1) * 16384 + (lane >> 5) * 512 + i16 * 16;
        st16_sc1(of[0], vo, sws, WS_OF + ((8 * P + 2 * rw) * 16 + 2 * c + cw) * 1024);
        st16_sc1(of[1], vo, sws, WS_OF + ((8 * P + 2 * rw) * 16 + 2 * c + cw) * 1024 + 256);
      }
      if (j == 2) { PIN(bs); _Pragma("unroll") for (int v = 0; v < 3; ++v) PIN(z1r[v]); }
      if (j == 3) { _Pragma("unroll") for (int v = 3; v < 6; ++v) PIN(z1r[v]); }
      if (j >= 1 && cw == 0) {
        sm += r_[0] + r_[1];
        if (j == 1) sm0 = sm;
        if (j == 2) EMIT_H(0, sm0)
        if (j >= 2) EMIT_H(j >= 2 ? j - 1 : 0, sm)
      }
      if (j == 3) {
#pragma unroll
        for (int e = 0; e < 4; ++e) DMA_WO(L_WO1, 0, e);
      }
      if (j == 3 && cw == 0) { WAITV(12); WAITV_IF(gn, 5, 5); }
      if (j == 5) WAITV(2);
      BAR_IF(cw, 0, 63);
    }
  }
  {
    STAMP(10);
    const u32 bufA = L_RED + (gn & 1) * 32768, bufB = L_RED + 32768 - (gn & 1) * 32768;
    const u32 fv = FV();
    if (cw == 0) {
      const u32 ra_ = redr + (bufB - L_RED);
      const f32x4 sm = ((lds16f(ra_) + lds16f(ra_ + 4096)) + (lds16f(ra_ + 8192) + lds16f(ra_ + 12288))) +
           ((lds16f(ra_ + 16384) + lds16f(ra_ + 20480)) + (lds16f(ra_ + 24576) + lds16f(ra_ + 28672)));
      if (gn == 6) EMIT_H(5, sm) else EMIT_H(4, sm)
    }
    WAITV(2); WAITV_IF(cw, 1, 0);
    PIN(gf);
    {
      const bool n0 = (lane & 48) != 16;
      if (!__all(!n0 || (gf[0] == tlo && gf[1] == thi))) poll_flags(fv, n0, sws, WS_FC, tlo, thi);
    }
    RELANE();
    u32x4 hf[8], of[8];
    const u32 ofb = WS_OF + ((4 * p + rw) * 16 + cw) * 1024, hfb = WS_HF + ((4 * p + rw) * 16 + cw) * 1024;
#pragma unroll
    for (int e = 0; e < 4; ++e) of[e] = ld16_b(l16, sws, ofb + e * 2048);
#pragma unroll
    for (int e = 0; e < 4; ++e) hf[e] = ld16_b(l16, sws, hfb + e * 2048);
    BARRIER();
    const u32 qb = ((lane >> 2) & 3) * 64 + ((((g & 1) << 1) | ((lane >> 1) & 1)) * 16) + (lane & 1) * 8;
    f32x4 a3[4];
#pragma unroll
    for (int ct = 0; ct < 4; ++ct) a3[ct] = zero4;
    u32x4 wf[3][4], hl[4][2];
#define RDW2(dst, lo0, hi0, offl, offh) _Pragma("unroll") for (int ct = 0; ct < 4; ++ct) dst[ct] = cat(ldstr(((lo0) ^ ((ct & 1) * 32)) + (offl) + (ct >> 1) * 512), ldstr(((hi0) ^ ((ct & 1) * 32)) + (offh) + (ct >> 1) * 512))
#define MM3(src, frag) _Pragma("unroll") for (int ct = 0; ct < 4; ++ct) a3[ct] = mfma32(src[ct], frag, a3[ct])
    const u32 lb = WS_HL + ((4 * p + rw) * 8 + cw) * 2048;
    const rsrc_t sxo = __builtin_amdgcn_make_buffer_rsrc((void*)(x + (size_t)p * 65536), 0, 65536, 0x00020000);
    const u32 w0 = opq(L_WOUT + qb + (g >> 1) * 1024 + (g & 1) * 256);
    const u32 wa = w0 + cw * 4096, wb = w0 + cw * 2048;
    const u32 oq = qb + g * 1024 + cw * 4096;
    const u32 oa = opq(L_WO1 + oq), ob = opq(bufA + oq);
    u32x4 xres;
    VWO();
#pragma unroll
    for (int e = 0; e < 4; ++e) DMA_WO(bufA, 1, e);
    STAMP(11);
#define RDK(dst, k)                                                                                                 \
  if ((k) < 4) { RDW2(dst, oa, oa, (k) * 8192, (k) * 8192 + 256); }                                                 \
  else if ((k) < 8) { RDW2(dst, wa, wa, ((k) - 4) * 8192, ((k) - 4) * 8192 + 2048); }                               \
  else if ((k) < 12) { RDW2(dst, ob, ob, ((k) - 8) * 8192, ((k) - 8) * 8192 + 256); }                               \
  else { RDW2(dst, wb, w0, 32768 + ((k) - 12) * 4096, 49152 + ((k) - 12) * 2048); }
    RDK(wf[0], 0)
    RDK(wf[1], 1)
#pragma unroll
    for (int k = 0; k < 16; ++k) {
      if (k == 0) WAITV(8);
      if (k == 4) WAITV(13);
      if (k == 6) {
        WAITV(0);
        PIN(gf);
        if (!__all(gf[0] == tlo && gf[1] == thi)) poll_flags(fv, true, sws, WS_FC, tlo, thi);
#pragma unroll
        for (int e = 4; e < 8; ++e) hf[e] = ld16_b(l16, sws, hfb + e * 2048);
        BARRIER();
        STAMP(12);
      }
      if (k == 12) WAITV(9);
      if (k + 2 < 16) { RDK(wf[(k + 2) % 3], k + 2) }
      SCHED();
      if (k < 4) { PIN(of[k]); MM3(wf[k % 3], of[k]); }
      else if (k < 8) { PIN(hf[k - 4]); MM3(wf[k % 3], hf[k - 4]); }
      else if (k < 12) { PIN(of[k - 4]); MM3(wf[k % 3], of[k - 4]); }
      else { PIN(hf[k - 8]); MM3(wf[k % 3], hf[k - 8]); }
      SCHED();
      if (k < 4) {
        of[4 + k] = ld16_b(l16, sws, ofb + (4 + k) * 2048);
        DMA_WOUT_AT(bufB + w * 4096, 7 + k)
        if (k == 3) gf = ld8_sc1(fv, sws, WS_FC);
      } else if (k >= 6 && k < 10) {
        hl[k - 6][0] = ld16_sc1(l16, sws, lb + (k - 6) * 4096);
        hl[k - 6][1] = ld16_sc1(l16, sws, lb + (k - 6) * 4096 + 1024);
      } else if (k == 10) {
        xres = ld16((tid >> 3) * 1024 + (tid & 7) * 16, sxo, c2 * 128);
      }
    }
    STAMP(13);
    {
      const u32 wl_ = opq(bufB + (w0 - L_WOUT) + cw * 4096);
      u32x4 wg[4][4];
#pragma unroll
      for (int e = 0; e < 4; ++e) RDW2(wg[e], wl_, wl_, e * 8192, e * 8192 + 2048);
      WAITV(0);
      PIN(xres);
#pragma unroll
      for (int e = 0; e < 4; ++e) { PIN(hl[e][0]); PIN(hl[e][1]); }
#pragma nounroll
      for (int it = 0; it < SPIN_CAP; ++it) {
        u32 bad = 0;
#pragma unroll
        for (int e = 0; e < 8; ++e) bad |= (hl[e >> 1][e & 1][1] ^ tlo) | (hl[e >> 1][e & 1][3] ^ tlo);
        if (__all(bad == 0)) break;
        __builtin_amdgcn_s_sleep(2);
        asm volatile(
            "buffer_load_dwordx4 %0, %8, %9, %10 offen sc1\n\tbuffer_load_dwordx4 %1, %8, %9, %10 offen offset:1024 sc1\n\t"
            "buffer_load_dwordx4 %2, %8, %9, %11 offen sc1\n\tbuffer_load_dwordx4 %3, %8, %9, %11 offen offset:1024 sc1\n\t"
            "buffer_load_dwordx4 %4, %8, %9, %12 offen sc1\n\tbuffer_load_dwordx4 %5, %8, %9, %12 offen offset:1024 sc1\n\t"
            "buffer_load_dwordx4 %6, %8, %9, %13 offen sc1\n\tbuffer_load_dwordx4 %7, %8, %9, %13 offen offset:1024 sc1\n\t"
            "s_waitcnt vmcnt(0)"
            : "=&v"(hl[0][0]), "=&v"(hl[0][1]), "=&v"(hl[1][0]), "=&v"(hl[1][1]), "=&v"(hl[2][0]), "=&v"(hl[2][1]), "=&v"(hl[3][0]), "=&v"(hl[3][1])
            : "v"(l16), "s"(sws), "s"(lb), "s"(lb + 4096), "s"(lb + 8192), "s"(lb + 12288)
            : "memory");
      }
#pragma unroll
      for (int e = 0; e < 4; ++e) MM3(wg[e], ((u32x4){hl[e][0][0], hl[e][0][2], hl[e][1][0], hl[e][1][2]}));
    }
    STAMP(14);
    const u32 fin = L_WO1 - 2048;
#pragma unroll
    for (int ct = 0; ct < 4; ++ct) sts16f(fin + 2048 + cw * 16384 + (16 * rw + i16) * 256 + (((4 * ct + g) ^ i16) * 16), a3[ct]);
    BARRIER();
    {
      const int row = tid >> 3;
      const u32 a = fin + 2048 + row * 256 + (((2 * (tid & 7)) ^ (row & 15)) * 16);
      const f32x4 s0 = lds16f(a) + lds16f(a + 16384), s1 = lds16f(a ^ 16) + lds16f((a ^ 16) + 16384);
      u32x4 o = {pk2(blo(xres[0]) + s0[0], bhi(xres[0]) + s0[1]), pk2(blo(xres[1]) + s0[2], bhi(xres[1]) + s0[3]),
                 pk2(blo(xres[2]) + s1[0], bhi(xres[2]) + s1[1]), pk2(blo(xres[3]) + s1[2], bhi(xres[3]) + s1[3])};
      st16_sc1(o, row * 1024 + (tid & 7) * 16, __builtin_amdgcn_make_buffer_rsrc((void*)(out + (size_t)p * 65536), 0, 65536, 0x00020000), c2 * 128);
    }
  }
  }
  if (b == 0 && w == 0 && l16 == 0) *(u64*)(ws + WS_EP) = ((u64)thi << 32) | tlo;
  STAMP(15);
  STAMPC(31);
}

extern "C" void kernel_launch(void* const* d_in, const int* in_sizes, int n_in, void* d_out, int out_size, void* d_ws, size_t ws_size,
                              hipStream_t stream) {
  fused_kernel<<<256, 512, 0, stream>>>((const char*)d_in[0], (const char*)d_in[1], (const char*)d_in[2], (const char*)d_in[3],
                                        (const char*)d_in[5], (const char*)d_in[6], (const char*)d_in[7], (const char*)d_in[8], (char*)d_out,
                                        (char*)d_ws);
}
```

```cpp
#include <hip/hip_runtime.h>
#include <hip/hip_bf16.h>

typedef unsigned int u32;
typedef unsigned long long u64;
typedef short s16x4 __attribute__((ext_vector_type(4)));
typedef __bf16 bf16x2 __attribute__((ext_vector_type(2)));
typedef __bf16 bf16x8 __attribute__((ext_vector_type(8)));
typedef float f32x16 __attribute__((ext_vector_type(16)));
typedef float f32x4 __attribute__((ext_vector_type(4)));
typedef float f32x2 __attribute__((ext_vector_type(2)));
typedef u32 u32x4 __attribute__((ext_vector_type(4)));
typedef u32 u32x2 __attribute__((ext_vector_type(2)));
typedef __amdgpu_buffer_rsrc_t rsrc_t;
#define LDSP __attribute__((address_space(3)))
#define DI __device__ __forceinline__

#define STAMPS 0

constexpr u32 WS_EP = 0;
constexpr u32 WS_FA = 8192;
constexpr u32 WS_FB = WS_FA + 16384;
constexpr u32 WS_FC = WS_FB + 16384;
constexpr u32 WS_FD = WS_FC + 16384;
constexpr u32 WS_XG = WS_FD + 65536;
constexpr u32 WS_SG = WS_XG + 32768;
constexpr u32 WS_ZB = WS_SG + 524288;
constexpr u32 WS_Z1 = WS_ZB + 43 * 65536;
constexpr u32 KP_SZ = 17408;
constexpr u32 WS_KP = WS_Z1 + 64 * 43 * 1024;
constexpr u32 WS_KF = WS_KP + 128 * KP_SZ;
constexpr u32 WS_KZ = WS_KF + 65536;
constexpr u32 WS_OF = WS_KZ + 4096;
constexpr u32 WS_HF = WS_OF + 128 * 16384;
constexpr u32 WS_HL = WS_HF + 128 * 16 * 1024;
constexpr u32 WS_ST = WS_HL + 128 * 16 * 1024;
constexpr u32 WS_END = WS_ST + 256 * 8 * 32 * 8;

constexpr u32 LZ_W = 65536;
constexpr u32 LZ_U = 139264;
constexpr u32 LQ_M = 122880;
constexpr u32 LQ_A = 81920;
constexpr u32 LQ_PHI = 49152;
constexpr u32 LQ_V = 32768;
constexpr u32 L_TAB = 155648;
constexpr u32 L_XS = L_TAB;
constexpr u32 L_CS = L_TAB + 1024;
constexpr u32 L_ZST = L_TAB + 3072;
constexpr u32 L_WPHI = L_TAB + 3072;
constexpr u32 L_KFS = L_TAB + 2048;
constexpr u32 L_JUNK = L_TAB + 7168;
constexpr u32 L_WOUT = 0;
constexpr u32 L_RED = 57344;
constexpr u32 L_WO1 = 122880;

constexpr u64 Z2S = 0x26211B16110B0500ull;
constexpr u64 Z1S = 0x26211C16110C0600ull;
DI int grp_start(u64 tab, int c) { return c >= 8 ? 43 : (int)((tab >> (8 * c)) & 0xFF); }

#define SCHED() __builtin_amdgcn_sched_barrier(0)
#define TIC()
#define TOC(acc)
#define WAITV(n) do { TIC(); asm volatile("s_waitcnt vmcnt(%0)" ::"n"(n) : "memory"); TOC(acv); SCHED(); } while (0)
#define BARRIER() do { SCHED(); TIC(); asm volatile("s_waitcnt lgkmcnt(0)\n\ts_barrier" ::: "memory"); TOC(acb); SCHED(); } while (0)
#define WAITV_IF(g_, v, n) do { TIC(); asm volatile("s_cmp_lg_u32 %0, %1\n\ts_cbranch_scc1 1f\n\ts_waitcnt vmcnt(%2)\n1:" ::"s"(g_), "n"(v), "n"(n) : "scc", "memory"); TOC(acv); SCHED(); } while (0)
#define BAR_IF(g_, v, n) do { SCHED(); asm volatile("s_cmp_lg_u32 %0, %1\n\ts_cbranch_scc1 1f\n\ts_waitcnt vmcnt(%2) lgkmcnt(0)\n\ts_barrier\n1:" ::"s"(g_), "n"(v), "n"(n) : "scc", "memory"); SCHED(); } while (0)
#define WAITL0() asm volatile("s_waitcnt lgkmcnt(0)" ::: "memory")
#define PIN(x) asm volatile("" : "+v"(x))

DI void dma16(u32 lds, u32 voff, rsrc_t srd, u32 soff) {
  asm volatile("s_mov_b32 m0, %0\n\ts_nop 0\n\tbuffer_load_dwordx4 %1, %2, %3 offen lds" ::"s"(lds), "v"(voff), "s"(srd), "s"(soff) : "memory");
}
DI void touch(u32 lds, u32 voff, rsrc_t srd, u32 soff) {
  asm volatile("s_mov_b32 m0, %0\n\ts_nop 0\n\tbuffer_load_dword %1, %2, %3 offen lds" ::"s"(lds), "v"(voff), "s"(srd), "s"(soff) : "memory");
}
DI u32x4 ld16_sc1(u32 voff, rsrc_t srd, u32 soff) {
  u32x4 r;
  asm volatile("buffer_load_dwordx4 %0, %1, %2, %3 offen sc1" : "=v"(r) : "v"(voff), "s"(srd), "s"(soff) : "memory");
  return r;
}
template <int OFF> DI u32x4 ld16_sc1o(u32 voff, rsrc_t srd, u32 soff) {
  u32x4 r;
  asm volatile("buffer_load_dwordx4 %0, %1, %2, %3 offen offset:%4 sc1" : "=v"(r) : "v"(voff), "s"(srd), "s"(soff), "n"(OFF) : "memory");
  return r;
}
#define BPOL " sc1"
DI u32x4 ld16_b(u32 voff, rsrc_t srd, u32 soff) {
  u32x4 r;
  asm volatile("buffer_load_dwordx4 %0, %1, %2, %3 offen" BPOL : "=v"(r) : "v"(voff), "s"(srd), "s"(soff) : "memory");
  return r;
}
template <int OFF> DI u32x4 ld16_bo(u32 voff, rsrc_t srd, u32 soff) {
  u32x4 r;
  asm volatile("buffer_load_dwordx4 %0, %1, %2, %3 offen offset:%4" BPOL : "=v"(r) : "v"(voff), "s"(srd), "s"(soff), "n"(OFF) : "memory");
  return r;
}
DI void ld16_b_if6(u32x4& r, int gn, u32 voff, rsrc_t srd, u32 soff) {
  asm volatile("s_cmp_lg_u32 %4, 6\n\ts_cbranch_scc1 1f\n\tbuffer_load_dwordx4 %0, %1, %2, %3 offen" BPOL "\n1:" : "+v"(r) : "v"(voff), "s"(srd), "s"(soff), "s"(gn) : "scc", "memory");
}
DI u32x4 ld16(u32 voff, rsrc_t srd, u32 soff) {
  u32x4 r;
  asm volatile("buffer_load_dwordx4 %0, %1, %2, %3 offen" : "=v"(r) : "v"(voff), "s"(srd), "s"(soff) : "memory");
  return r;
}
DI u32x2 ld8_sc1(u32 voff, rsrc_t srd, u32 soff) {
  u32x2 r;
  asm volatile("buffer_load_dwordx2 %0, %1, %2, %3 offen sc1" : "=v"(r) : "v"(voff), "s"(srd), "s"(soff) : "memory");
  return r;
}
DI u32 ld2(u32 voff, rsrc_t srd, u32 soff) {
  u32 r;
  asm volatile("buffer_load_ushort %0, %1, %2, %3 offen" : "=v"(r) : "v"(voff), "s"(srd), "s"(soff) : "memory");
  return r;
}
DI void st16_sc1(u32x4 v, u32 voff, rsrc_t srd, u32 soff) {
  asm volatile("buffer_store_dwordx4 %0, %1, %2, %3 offen sc1\n\ts_nop 1" ::"v"(v), "v"(voff), "s"(srd), "s"(soff) : "memory");
}
DI void st8_sc1(u32x2 v, u32 voff, rsrc_t srd, u32 soff) {
  asm volatile("buffer_store_dwordx2 %0, %1, %2, %3 offen sc1" ::"v"(v), "v"(voff), "s"(srd), "s"(soff) : "memory");
}

DI u32x4 lds16(u32 a) { return *(const LDSP u32x4*)(size_t)a; }
DI u32x2 lds8(u32 a) { return *(const volatile LDSP u32x2*)(size_t)a; }
DI float lds4f(u32 a) { return *(const LDSP float*)(size_t)a; }
DI f32x4 lds16f(u32 a) { return *(const LDSP f32x4*)(size_t)a; }
DI void sts16f(u32 a, f32x4 v) { *(LDSP f32x4*)(size_t)a = v; }
DI void sts16(u32 a, u32x4 v) { *(LDSP u32x4*)(size_t)a = v; }
DI void sts8(u32 a, u32x2 v) { *(volatile LDSP u32x2*)(size_t)a = v; }
DI void sts4f(u32 a, float v) { *(LDSP float*)(size_t)a = v; }
DI u32x2 ldstr(u32 a) {
  return __builtin_bit_cast(u32x2, __builtin_amdgcn_ds_read_tr16_b64_v4i16((LDSP s16x4*)(size_t)a)); }
DI u32x4 cat(u32x2 lo, u32x2 hi) { return (u32x4){lo[0], lo[1], hi[0], hi[1]}; }
DI u32x4 ldfrag(u32 a, u32 d) { u32x2 lo = ldstr(a), hi = ldstr(a + d); return cat(lo, hi); }

DI f32x4 mfma32(u32x4 a, u32x4 b, f32x4 c) {
  return __builtin_amdgcn_mfma_f32_16x16x32_bf16(__builtin_bit_cast(bf16x8, a), __builtin_bit_cast(bf16x8, b), c, 0, 0, 0);
}
DI f32x16 mfmaL(u32x4 a, u32x4 b, f32x16 c) {
  return __builtin_amdgcn_mfma_f32_32x32x16_bf16(__builtin_bit_cast(bf16x8, a), __builtin_bit_cast(bf16x8, b), c, 0, 0, 0);
}
DI u32 pk2(float a, float b) { bf16x2 v = {(__bf16)a, (__bf16)b}; return __builtin_bit_cast(u32, v); }
DI float blo(u32 u) { return __uint_as_float(u << 16); }
DI float bhi(u32 u) { return __uint_as_float(u & 0xffff0000u); }
DI float rbf(float a) { return blo(pk2(a, a)); }
DI float dot2(u32 a, u32 b, float c) {
  return __builtin_amdgcn_fdot2_f32_bf16(__builtin_bit_cast(bf16x2, a), __builtin_bit_cast(bf16x2, b), c, false);
}
DI f32x2 fma2(f32x2 a, f32x2 b, f32x2 c) { return __builtin_elementwise_fma(a, b, c); }
DI f32x2 bc2(float a) { return (f32x2){a, a}; }
DI f32x2 lo2(f32x4 v) { return (f32x2){v[0], v[1]}; }
DI f32x2 hi2(f32x4 v) { return (f32x2){v[2], v[3]}; }
DI u32 pk2v(f32x2 v) { return pk2(v[0], v[1]); }
DI f32x2 gelu2(f32x2 x) {
  f32x2 u = fma2(x * x, bc2(-0.10294324f), bc2(-2.3022082f)) * x;
  f32x2 e = {__builtin_amdgcn_exp2f(u[0]), __builtin_amdgcn_exp2f(u[1])};
  e = e + bc2(1.0f);
  f32x2 r = {__builtin_amdgcn_rcpf(e[0]), __builtin_amdgcn_rcpf(e[1])};
  return x * r;
}
DI void red2g(float& a, float& b) {
  auto s = __builtin_amdgcn_permlane32_swap(__float_as_uint(a), __float_as_uint(b), false, false);
  float c = __uint_as_float(s[0]) + __uint_as_float(s[1]);
  auto s2 = __builtin_amdgcn_permlane16_swap(__float_as_uint(c), __float_as_uint(c), false, false);
  float d = __uint_as_float(s2[0]) + __uint_as_float(s2[1]);
  auto s3 = __builtin_amdgcn_permlane32_swap(__float_as_uint(d), __float_as_uint(d), false, false);
  a = __uint_as_float(s3[0]);
  b = __uint_as_float(s3[1]);
}
template <int CTRL> DI float dpp(float v) {
  return __uint_as_float(__builtin_amdgcn_update_dpp(0, __float_as_uint(v), CTRL, 0xF, 0xF, true));
}
DI void hedgehog16(const f32x16& p, float s, u32x4 (&o)[4]) {
  float mx = fmaxf(p[0], p[1]), mn = fminf(p[0], p[1]);
#pragma unroll
  for (int i = 2; i < 16; i += 2) { mx = fmaxf(fmaxf(mx, p[i]), p[i + 1]); mn = fminf(fminf(mn, p[i]), p[i + 1]); }
  const float c = s * 1.44269504f;
  const float a = -mx * c, b = mn * c;
  f32x2 ep[8], en[8];
#pragma unroll
  for (int i = 0; i < 8; ++i) {
    const f32x2 v = {p[2 * i], p[2 * i + 1]};
    const f32x2 u = fma2(v, bc2(c), bc2(a)), w = fma2(v, bc2(-c), bc2(b));
    ep[i] = (f32x2){__builtin_amdgcn_exp2f(u[0]), __builtin_amdgcn_exp2f(u[1])};
    en[i] = (f32x2){__builtin_amdgcn_exp2f(w[0]), __builtin_amdgcn_exp2f(w[1])};
  }
  const f32x2 sp = ((ep[0] + ep[1]) + (ep[2] + ep[3])) + ((ep[4] + ep[5]) + (ep[6] + ep[7]));
  const f32x2 sn = ((en[0] + en[1]) + (en[2] + en[3])) + ((en[4] + en[5]) + (en[6] + en[7]));
  const float ip = __builtin_amdgcn_rcpf(sp[0] + sp[1]), in = __builtin_amdgcn_rcpf(sn[0] + sn[1]);
#pragma unroll
  for (int i = 0; i < 8; ++i) {
    o[i >> 2][i & 3] = pk2v(ep[i] * bc2(ip));
    o[2 + (i >> 2)][i & 3] = pk2v(en[i] * bc2(in));
  }
}
DI void ln_fin(float s, float q, float invn, float& r, float& m) {
  m = s * invn;
  float v = fmaxf(q * invn - m * m, 0.0f);
  float sd = rbf(__builtin_amdgcn_sqrtf(rbf(rbf(v) + 1e-5f)));
  r = __builtin_amdgcn_rcpf(sd);
  m = rbf(m);
}

#define POLL_SLEEP 6
constexpr int SPIN_CAP = 1 << 17;
DI void poll_flags(u32 voff, bool act, rsrc_t srd, u32 soff, u32 tlo, u32 thi) {
#pragma nounroll
  for (int it = 0; it < SPIN_CAP; ++it) {
    u32x2 v;
    asm volatile("buffer_load_dwordx2 %0, %1, %2, %3 offen sc1\n\ts_waitcnt vmcnt(0)" : "=&v"(v) : "v"(voff), "s"(srd), "s"(soff) : "memory");
    bool ok = !act || (v[0] == tlo && v[1] == thi);
    if (__all(ok)) break;
    __builtin_amdgcn_s_sleep(POLL_SLEEP);
  }
}
DI bool tag_ok(u32x4 q, u32 t) { return q[1] == t && q[3] == t; }
DI void poll_gran(u32x4& q, u32 voff, rsrc_t srd, u32 soff, u32 tlo) {
#pragma nounroll
  for (int it = 0; it < SPIN_CAP; ++it) {
    if (__all(tag_ok(q, tlo))) break;
    __builtin_amdgcn_s_sleep(POLL_SLEEP);
    asm volatile("buffer_load_dwordx4 %0, %1, %2, %3 offen sc1\n\ts_waitcnt vmcnt(0)" : "=&v"(q) : "v"(voff), "s"(srd), "s"(soff) : "memory");
  }
}
DI u32 opq(u32 v) { asm volatile("" : "+v"(v)); return v; }

#define STAMP(i)
#define STAMPC(i)

#define GQ_N 3
#define PREFETCH 0
#define CODE_PF 44000
#define OUT_SC1 1
#define PRIO 0
#define UROT 1
static_assert(WS_ZB % 8192 == 0, "");
#define TLB_PF 1
#define VM(x) do { x; ++vq; } while (0)
#define WAITQ(mark) WAITV(vq - (mark))

__global__ __launch_bounds__(512, 2) void fused_kernel(const char* __restrict__ x, const char* __restrict__ wqkv, const char* __restrict__ wphi,
                                                        const char* __restrict__ wo, const char* __restrict__ win, const char* __restrict__ wsp,
                                                        const char* __restrict__ bsp, const char* __restrict__ wout, char* __restrict__ out, char* ws) {
  __shared__ __attribute__((aligned(1024))) char smem[163840];
  const int tid = threadIdx.x, lane = tid & 63;
  const int w = __builtin_amdgcn_readfirstlane(tid >> 6);
  const int rw = w & 3, cw = w >> 2, i16 = lane & 15, g = lane >> 4;
  const int b = blockIdx.x, xcd = b & 7, r = b >> 3;
  const int Pl = r >> 3, t = (r >> 2) & 1, cl = r & 3;
  const int P = (xcd >> 1) * 4 + Pl, c = (xcd & 1) * 4 + cl;
  const int p = 2 * P + (cl & 1);
  const int c2 = (xcd & 1) * 4 + (t ? 3 * (cl >> 1) : 1 + (cl >> 1));
  const int rot = 2 * ((Pl + cl) & 3);
  u32 acv = 0, acb = 0; (void)acv; (void)acb;
  asm volatile("" ::"v"((u32)(size_t)(LDSP char*)smem));
  STAMP(0);
  STAMPC(30);

  {
    u64 pc;
    asm volatile("s_getpc_b64 %0" : "=s"(pc));
    if (w == 7) touch(L_JUNK, lane * 128, __builtin_amdgcn_make_buffer_rsrc((void*)(pc & ~255ull), 0, CODE_PF, 0x00020000), (r & 7) * 8192);
  }
  u64 ep;
  asm volatile("s_load_dwordx2 %0, %1, 0x0" : "=s"(ep) : "s"(ws) : "memory");
  const rsrc_t sx = __builtin_amdgcn_make_buffer_rsrc((void*)(x + (size_t)P * 131072), 0, 131072, 0x00020000);
  const u32 l16 = opq(lane * 16);
  const u32 vx0 = (16 * w + (lane >> 3)) * 1024 + (((lane & 7) ^ g) * 16);
  const u32 vx1 = (vx0 + 8192) ^ 64;
#define DMA_X(dst, n, e) dma16((dst) + w * 2048 + (e) * 1024, (e) ? vx1 : vx0, sx, (((n) + rot) & 7) * 128)
  DMA_X(0, 0, 0);
  DMA_X(0, 0, 1);
  const rsrc_t sa = __builtin_amdgcn_make_buffer_rsrc((void*)(wsp + (size_t)p * 262144), 0, 262144, 0x00020000);
  const rsrc_t sws = __builtin_amdgcn_make_buffer_rsrc((void*)ws, 0, WS_END, 0x00020000);
  if (w == 6) touch(L_JUNK, lane * 262144 + 131072 + 64, sws, 0);
  if (w == 5) touch(L_JUNK, lane * 131072, __builtin_amdgcn_make_buffer_rsrc((void*)wsp, 0, 8388608, 0x00020000), 0);
  if (w == 4) touch(L_JUNK, lane * 65536, __builtin_amdgcn_make_buffer_rsrc((void*)out, 0, 2097152, 0x00020000), 0);
  if (w == 3) touch(L_JUNK, lane * 65536, __builtin_amdgcn_make_buffer_rsrc((void*)wout, 0, 688 * 1024, 0x00020000), 0);
  if (w == 2) touch(L_JUNK, lane * 65536, __builtin_amdgcn_make_buffer_rsrc((void*)wo, 0, 512 * 1024, 0x00020000), 0);
  if (w == 1) touch(L_JUNK, lane * 65536, __builtin_amdgcn_make_buffer_rsrc((void*)bsp, 0, 4096, 0x00020000), 0);
  const u32 vA0 = (lane >> 3) * 4096 + (((lane & 7) ^ g) * 16);
  const u32 vA1 = (vA0 + 32768) ^ 64;
  const int ux0 = UROT ? (cl >> 1) : 0, ux1 = UROT ? t : 0;
  const u32 uxs = w * 512 + ux1 * 256 + ux0 * 131072;
#define DMA_U(u, base, e) dma16((base) + w * 2048 + (e) * 1024, (e) ? vA1 : vA0, sa, uxs ^ ((((u) >> 1) & 3) * 128 + (((u) >> 3) * 32 + ((u) & 1) * 16) * 4096))
  const f32x4 zero4 = {0, 0, 0, 0};
  u32x4 ones4 = {0x3F803F80u, 0x3F803F80u, 0x3F803F80u, 0x3F803F80u};
  u32 tlo, thi;
  u32x4 af[4][8];
  u32x4 bfr[2][8];
  u32x2 gf;
  const u32 ard = w * 2048 + i16 * 128 + (((g >> 1) ^ (i16 >> 1)) * 16) + (g & 1) * 8;
#define READ_U(u, base)                                                                                             \
  {                                                                                                                 \
    const u32 ab_ = opq(ard + (base));                                                                              \
    _Pragma("unroll") for (int kk = 0; kk < 2; ++kk)                                                               \
      af[2 * ((u) >> 3) + ((u) & 1)][((u) & 6) + kk] = cat(lds8(ab_ ^ (64 * kk)), lds8(ab_ ^ (64 * kk + 32)));      \
  }
#define USE_EPOCH() { asm volatile("" : "+s"(ep)); ep += 1; tlo = (u32)ep; thi = (u32)(ep >> 32); PIN(ones4); }
#define EARLY_STATS(mark)                                                                                           \
  {                                                                                                                 \
    WAITQ(mark); PIN(xrow);                                                                                         \
    float s_ = 0, q_ = 0;                                                                                           \
    _Pragma("unroll") for (int e = 0; e < 4; ++e) { s_ = dot2(xrow[e], ones4[0], s_); q_ = dot2(xrow[e], xrow[e], q_); } \
    s_ += dpp<0x128>(s_); q_ += dpp<0x128>(q_);                                                                     \
    s_ += dpp<0x124>(s_); q_ += dpp<0x124>(q_);                                                                     \
    s_ += dpp<0x4E>(s_); q_ += dpp<0x4E>(q_);                                                                       \
    s_ += dpp<0xB1>(s_); q_ += dpp<0xB1>(q_);                                                                       \
    red2g(s_, q_);                                                                                                  \
    float r_, m_;                                                                                                   \
    ln_fin(s_, q_, 1.0f / 512, r_, m_);                                                                             \
    VM(st16_sc1((u32x4){__float_as_uint(r_), tlo, __float_as_uint(m_), tlo}, l16 << 22, sws, WS_XG + (P * 128 + (t * 8 + c) * 8 + w) * 16)); \
  }
#define GATHER_XS()                                                                                                 \
  WAITQ(mxg); PIN(xg);                                                                                              \
  poll_gran(xg, i16 * 16, sws, WS_XG + (P * 128 + 16 * w) * 16, tlo);                                               \
  sts4f(L_XS + (16 * w + i16) * 4, __uint_as_float(xg[0]));                                                         \
  sts4f(L_XS + 512 + (16 * w + i16) * 4, __uint_as_float(xg[2]));
#define RELANE() const int lane = opq(l16) >> 4, i16 = lane & 15, g = lane >> 4, tid = w * 64 + lane; (void)tid; (void)g; (void)i16
#define SET_FLAG(off) st8_sc1((u32x2){tlo, thi}, l16 << 22, sws, (off))
  const int gs = grp_start(Z2S, c2), gn = grp_start(Z2S, c2 + 1) - gs;
  const int trot = (p & 7) >= 5 ? (p & 7) - 5 : (p & 7);
#define TILE(j, n_) (((j) + trot) >= (n_) ? (j) + trot - (n_) : (j) + trot)
  const u32 zb0 = WS_ZB + (gs * 64 + 8 * w) * 1024 + ux1 * 4096;
#define DMA_WOUT(e)                                                                                                 \
  {                                                                                                                 \
    const rsrc_t so_ = __builtin_amdgcn_make_buffer_rsrc((void*)wout, 0, 688 * 1024, 0x00020000);                   \
    const int ws_ = grp_start(Z2S, w), wn_ = grp_start(Z2S, w + 1) - ws_;                                           \
    if ((e) < 6) dma16(L_WOUT + ((e) < 4 ? 2 * w + ((e) >> 1) : 16 + w) * 2048 + ((e) & 1) * 1024, vwo, so_, ((ws_ + TILE((e) >> 1, wn_)) * 2 + ((e) & 1)) * 8192); \
    else if ((e) == 6) dma16(L_WOUT + 24 * 2048 + w * 1024, vwo, so_, ((((w >> 1) == 0 ? 5 : ((w >> 1) == 1 ? 11 : ((w >> 1) == 2 ? 27 : 64))) + TILE(3, 6)) * 2 + (w & 1)) * 8192); \
  }
#define DMA_WOUT_AT(dst, e)                                                                                         \
  {                                                                                                                 \
    const rsrc_t so_ = __builtin_amdgcn_make_buffer_rsrc((void*)wout, 0, 688 * 1024, 0x00020000);                   \
    const int ws_ = grp_start(Z2S, w), wn_ = grp_start(Z2S, w + 1) - ws_;                                           \
    dma16((dst) + ((e) - 7) * 1024, vwo, so_, ((ws_ + TILE(wn_ - 1 - (((e) - 7) >> 1), wn_)) * 2 + (((e) - 7) & 1)) * 8192); \
  }
#define VWO() const u32 vwo = ((lane >> 2) & 7) * 1024 + c2 * 128 + (lane >> 5) * 64 + (((lane & 3) ^ (((lane >> 4) & 1) << 1)) * 16)
#define LOAD_B(u, so_)                                                                                              \
  bfr[u][0] = ld16_bo<0>(l16, sws, so_); bfr[u][1] = ld16_bo<1024>(l16, sws, so_);                                  \
  bfr[u][2] = ld16_bo<2048>(l16, sws, so_); bfr[u][3] = ld16_bo<3072>(l16, sws, so_);                               \
  bfr[u][4] = ld16_bo<0>(l16, sws, (so_) ^ 4096); bfr[u][5] = ld16_bo<1024>(l16, sws, (so_) ^ 4096);                \
  bfr[u][6] = ld16_bo<2048>(l16, sws, (so_) ^ 4096); bfr[u][7] = ld16_bo<3072>(l16, sws, (so_) ^ 4096);

  if (t == 0) {
    int vq = 2;
    const rsrc_t sw = __builtin_amdgcn_make_buffer_rsrc((void*)win, 0, 512 * 2752, 0x00020000);
    const int z2s = grp_start(Z2S, c), z2n = grp_start(Z2S, c + 1) - z2s;
    const int z1s = grp_start(Z1S, c), z1n = grp_start(Z1S, c + 1) - z1s;
    u32 vw[3];
    int wch[3];
#pragma unroll
    for (int e = 0; e < 3; ++e) {
      const int sg = 2 * e + (lane >> 5), hi = sg >= 3;
      wch[e] = (sg - 3 * hi) * 4 + ((lane & 3) ^ (hi << 1));
      vw[e] = wch[e] < 2 * z2n ? (16 * w + 8 * hi + ((lane >> 2) & 7)) * 2752 + 1376 + 32 * z2s + 16 * wch[e] : 0x7FFF0000u;
    }
#define DMA_W(bk, e) dma16(LZ_W + ((bk) % 3) * 24576 + w * 3072 + (e) * 1024, vw[e], sw, ((2 * (bk) + rot) & 7) * (64 * 2752))
#define UBASE(u) ((u) % 5 == 0 ? LZ_U : ((u) % 5 - 1) * 16384)
#define ISS_X(n) do { VM(DMA_X(((n) & 3) * 16384, n, 0)); VM(DMA_X(((n) & 3) * 16384, n, 1)); mx[n] = vq; } while (0)
#define ISS_U(u) do { VM(DMA_U(u, UBASE(u), 0)); VM(DMA_U(u, UBASE(u), 1)); mu[u] = vq; } while (0)
#define ISS_W(bk, e0, e1) do { _Pragma("unroll") for (int e_ = e0; e_ < e1; ++e_) VM(DMA_W(bk, e_)); mw[bk] = vq; } while (0)
    int mx[8], mw[8], mu[16];
    mx[0] = vq;
    ISS_W(0, 0, 3);
    u32x4 xrow;
    VM(xrow = ld16(l16, sx, (c * 8 + w) * 1024));
    const int mxr = vq;
    ISS_X(1);
    const u32 xa0 = opq((32 * rw + i16) * 128 + ((g ^ (i16 >> 1)) * 16)), xa1 = xa0 ^ 64;
    u32 ta[3];
    int tl[3];
    {
      const u32 trb = LZ_W + g * 1536 + ((lane >> 2) & 3) * 64 + ((((g & 1) << 1) | ((lane >> 1) & 1)) * 16) + (lane & 1) * 8;
#pragma unroll
      for (int j = 0; j < 3; ++j) {
        tl[j] = 3 * cw + (j + rw) % 3;
        ta[j] = (trb + (tl[j] >> 1) * 512) ^ ((tl[j] & 1) * 32);
      }
    }
#define NEXT_BLOCK(bk) _Pragma("unroll") for (int j = 0; j < 3; ++j) ta[j] = opq(ta[j] + ((bk) == 0 ? 0 : ((bk) % 3 == 0 ? -49152 : 24576)))
    f32x4 acc[2][3], csa = zero4;
#pragma unroll
    for (int j = 0; j < 3; ++j) acc[0][j] = acc[1][j] = zero4;
    u32x4 xf[16][2], wf[2][3];
    u32x4 xg;
    int mxg = 0;
    WAITQ(mw[0]);
    BARRIER();
    USE_EPOCH()
    STAMP(7);
#define P1_MEM(n, h)                                                                                                \
  {                                                                                                                 \
    if ((n) == 0) { if ((h) == 0) ISS_X(2); if ((h) == 1) ISS_W(1, 0, 2); if ((h) == 2) ISS_W(1, 2, 3); if ((h) == 3) ISS_X(3); } \
    if ((n) == 1) { if ((h) == 0) ISS_W(2, 0, 2); if ((h) == 1) ISS_W(2, 2, 3); if ((h) == 2) VM(DMA_X(0, 4, 0)); if ((h) == 3) { VM(DMA_X(0, 4, 1)); mx[4] = vq; } } \
    if ((n) == 2 || (n) == 4) { if ((h) < 2) { VM(DMA_X((((n) + 3) & 3) * 16384, (n) + 3, (h) & 1)); mx[(n) + 3] = vq; } else ISS_W((n) / 2 + 2, (h) - 2, (h) - 1); } \
    if ((n) == 3) { if ((h) == 0) ISS_W(3, 2, 3); else if ((h) < 3) { VM(DMA_X(32768, 6, (h) - 1)); mx[6] = vq; } else ISS_U(0); } \
    if ((n) == 5) { if ((h) == 0) ISS_W(4, 2, 3); if ((h) == 1) { VM(xg = ld16_sc1(i16 * 16, sws, WS_XG + (P * 128 + 16 * w) * 16)); mxg = vq; } \
                    if ((h) >= 2) { VM(DMA_U(1, UBASE(1), (h) - 2)); mu[1] = vq; } }                                 \
    if ((n) == 6) { if ((h) < 3) ISS_W(5, h, (h) + 1); else ISS_U(2); }                                             \
    if ((n) == 7) { if ((h) < 2) { VM(DMA_U(3, UBASE(3), h)); mu[3] = vq; } }                                       \
  }
#pragma unroll
    for (int ks = 0; ks <= 16; ++ks) {
      const int n = ks >> 1, kk = ks & 1;
      if (ks > 0 && ks < 16 && kk == 0) {
        { const int mk = (n & 1) ? mx[n] : (mx[n] > mw[n >> 1] ? mx[n] : mw[n >> 1]); WAITQ(mk); }
        BARRIER();
      }
      if (ks < 16 && (ks & 3) == 0) { NEXT_BLOCK(ks >> 2); }
      if (ks == 8) {
#pragma unroll
        for (int e = 0; e < 3; ++e)
          vw[e] = wch[e] < 2 * z1n ? (16 * w + 8 * (2 * e + (lane >> 5) >= 3) + ((lane >> 2) & 7)) * 2752 + 32 * z1s + 16 * wch[e] : 0x7FFF0000u;
      }
      const int k1 = ks > 0 ? ks - 1 : 0, q1 = k1 & 1;
#pragma unroll
      for (int m = 0; m < 7; ++m) {
        if (ks > 0) {
          if (m < 6) acc[m & 1][m >> 1] = mfma32(xf[k1][m & 1], wf[q1][m >> 1], acc[m & 1][m >> 1]);
          else csa = mfma32(ones4, wf[q1][0], csa);
          SCHED();
        }
        if (ks < 16) {
          if (m == 0) { P1_MEM(n, 2 * kk) xf[ks & 15][0] = lds16((kk ? xa1 : xa0) + (n & 3) * 16384); xf[ks & 15][1] = lds16((kk ? xa1 : xa0) + (n & 3) * 16384 + 2048); }
          else if (m < 4) wf[kk][m - 1] = ldfrag(ta[m - 1] + (ks & 3) * 6144, 256);
          if (m == 4) P1_MEM(n, 2 * kk + 1)
        }
        SCHED();
      }
      if (ks == 2) EARLY_STATS(mxr)
    }
    GATHER_XS()
    sts4f(L_CS + tl[0] * 64 + i16 * 4, csa[0]);
    WAITQ(mu[1] > mw[4] ? mu[1] : mw[4]);
    READ_U(0, UBASE(0)) READ_U(1, UBASE(1))
    BARRIER();
    STAMP(1);
    u32x4 zp[3];
    ISS_W(6, 0, 3);
    {
      f32x2 ps[4], pq[4];
      float cs[3];
#pragma unroll
      for (int j = 0; j < 3; ++j) cs[j] = -lds4f(L_CS + tl[j] * 64 + i16 * 4);
#pragma unroll
      for (int rt = 0; rt < 2; ++rt) {
        const f32x4 rr = lds16f(L_XS + (32 * rw + 16 * rt + 4 * g) * 4), mm = lds16f(L_XS + 512 + (32 * rw + 16 * rt + 4 * g) * 4);
        f32x2 z0[3], z1[3];
#pragma unroll
        for (int j = 0; j < 3; ++j) {
          z0[j] = gelu2(fma2(lo2(mm), bc2(cs[j]), lo2(acc[rt][j])) * lo2(rr));
          z1[j] = gelu2(fma2(hi2(mm), bc2(cs[j]), hi2(acc[rt][j])) * hi2(rr));
          zp[j][2 * rt] = pk2v(z0[j]); zp[j][2 * rt + 1] = pk2v(z1[j]);
        }
        ps[2 * rt] = z0[0] + z0[1] + z0[2]; ps[2 * rt + 1] = z1[0] + z1[1] + z1[2];
        pq[2 * rt] = fma2(z0[2], z0[2], fma2(z0[1], z0[1], z0[0] * z0[0]));
        pq[2 * rt + 1] = fma2(z1[2], z1[2], fma2(z1[1], z1[1], z1[0] * z1[0]));
        SCHED();
        if (rt == 0) { ISS_U(4); ISS_U(5); }
        else ISS_U(6);
        SCHED();
      }
      const bool b3 = lane & 8, b2 = lane & 4, b1 = lane & 2;
#pragma unroll
      for (int m = 0; m < 2; ++m)
#pragma unroll
        for (int e = 0; e < 2; ++e) {
          ps[m][e] = (b3 ? ps[m + 2][e] : ps[m][e]) + dpp<0x128>(b3 ? ps[m][e] : ps[m + 2][e]);
          pq[m][e] = (b3 ? pq[m + 2][e] : pq[m][e]) + dpp<0x128>(b3 ? pq[m][e] : pq[m + 2][e]);
        }
#pragma unroll
      for (int e = 0; e < 2; ++e) {
        ps[0][e] = (b2 ? ps[1][e] : ps[0][e]) + dpp<0x141>(b2 ? ps[0][e] : ps[1][e]);
        pq[0][e] = (b2 ? pq[1][e] : pq[0][e]) + dpp<0x141>(b2 ? pq[0][e] : pq[1][e]);
      }
      float s_ = (b1 ? ps[0][1] : ps[0][0]) + dpp<0x4E>(b1 ? ps[0][0] : ps[0][1]);
      float q_ = (b1 ? pq[0][1] : pq[0][0]) + dpp<0x4E>(b1 ? pq[0][0] : pq[0][1]);
      s_ += dpp<0xB1>(s_);
      q_ += dpp<0xB1>(q_);
      const int row = 32 * rw + 16 * (i16 >> 3) + 4 * g + ((i16 >> 1) & 3);
      VM(st16_sc1((u32x4){__float_as_uint(s_), tlo, __float_as_uint(q_), tlo}, row * 16 + ((lane & 1) << 30), sws, WS_SG + ((P * 8 + c) * 2 + cw) * 2048));
      PIN(zp[0]); PIN(zp[1]); PIN(zp[2]);
    }
    STAMP(2);
    {
    RELANE();
#pragma unroll
    for (int j = 0; j < 3; ++j) acc[0][j] = acc[1][j] = zero4;
    csa = zero4;
    u32x4 q0, q1, q2, q3;
    int mq = 0, mz = 0;
    const u32 gvo = (2 * g) * 4096 + (32 * rw + 16 * cw + i16) * 16;
    const u32 gso = WS_SG + P * 32768;
#pragma unroll
    for (int ks = 0; ks <= 16; ++ks) {
      if (ks == 8) {
        WAITQ(mq); PIN(q0); PIN(q1); PIN(q2); PIN(q3);
#pragma nounroll
        for (int it = 0; it < SPIN_CAP; ++it) {
          if (__all(tag_ok(q0, tlo) && tag_ok(q1, tlo) && tag_ok(q2, tlo) && tag_ok(q3, tlo))) break;
          __builtin_amdgcn_s_sleep(POLL_SLEEP);
          asm volatile(
              "buffer_load_dwordx4 %0, %4, %5, %6 offen sc1\n\t"
              "buffer_load_dwordx4 %1, %4, %5, %6 offen offset:2048 sc1\n\t"
              "buffer_load_dwordx4 %2, %4, %5, %7 offen sc1\n\t"
              "buffer_load_dwordx4 %3, %4, %5, %7 offen offset:2048 sc1\n\t"
              "s_waitcnt vmcnt(0)"
              : "=&v"(q0), "=&v"(q1), "=&v"(q2), "=&v"(q3)
              : "v"(gvo), "s"(sws), "s"(gso), "s"(gso + 4096)
              : "memory");
        }
        float s = (__uint_as_float(q0[0]) + __uint_as_float(q1[0])) + (__uint_as_float(q2[0]) + __uint_as_float(q3[0]));
        float q = (__uint_as_float(q0[2]) + __uint_as_float(q1[2])) + (__uint_as_float(q2[2]) + __uint_as_float(q3[2]));
        red2g(s, q);
        float r2, m2;
        ln_fin(s, q, 1.0f / 688, r2, m2);
        sts4f(L_ZST + (32 * rw + 16 * cw + i16) * 4, r2);
        sts4f(L_ZST + 512 + (32 * rw + 16 * cw + i16) * 4, -m2 * r2);
        STAMP(3);
      }
      if (ks < 16 && (ks & 3) == 0) {
        if (ks > 0) { WAITQ(ks == 12 ? mz : mw[4 + (ks >> 2)]); BARRIER(); }
        if (ks == 12) { if (w == 0) SET_FLAG(WS_FA + (P * 8 + c) * 128); STAMP(5); }
        NEXT_BLOCK(4 + (ks >> 2));
      }
      if (ks == 8) {
#pragma unroll
        for (int rt = 0; rt < 2; ++rt) {
          const u32 ra = L_ZST + (32 * rw + 16 * rt + 4 * g) * 4;
          const f32x4 r4 = lds16f(ra), m4 = lds16f(ra + 512);
#pragma unroll
          for (int j = 0; j < 3; ++j) {
            const u32 u0 = zp[j][2 * rt], u1 = zp[j][2 * rt + 1];
            zp[j][2 * rt] = pk2v(fma2((f32x2){blo(u0), bhi(u0)}, lo2(r4), lo2(m4)));
            zp[j][2 * rt + 1] = pk2v(fma2((f32x2){blo(u1), bhi(u1)}, hi2(r4), hi2(m4)));
          }
        }
#pragma unroll
        for (int j = 0; j < 3; ++j)
          VM(st16_sc1(zp[j], l16, sws, (tl[j] < z2n) ? WS_ZB + ((z2s + tl[j]) * 64 + 4 * P + rw) * 1024 : 0x7FFF0000u));
        mz = vq;
        STAMP(4);
        SCHED();
      }
      if (ks >= 3 && ks <= 9) { WAITQ(mu[ks - 1]); READ_U(ks - 1, UBASE(ks - 1)) }
      if (ks >= 9 && ks <= 15) { WAITQ(mu[ks]); READ_U(ks, UBASE(ks)) }
      const int k1 = ks > 0 ? ks - 1 : 0, q1_ = k1 & 1;
#pragma unroll
      for (int m = 0; m < 7; ++m) {
        if (ks > 0) {
          if (m < 6) acc[m & 1][m >> 1] = mfma32(wf[q1_][m >> 1], xf[k1][m & 1], acc[m & 1][m >> 1]);
          else csa = mfma32(ones4, wf[q1_][0], csa);
          SCHED();
        }
        if (ks < 16 && m >= 1 && m < 4) wf[ks & 1][m - 1] = ldfrag(ta[m - 1] + (ks & 3) * 6144, 256);
        if (m == 0 || m == 4) {
          const int h = m == 4;
          if (ks >= 4 && ks <= 12) { VM(DMA_U(ks + 3, UBASE(ks + 3), h)); mu[ks + 3] = vq; }
        }
        if (m == 6) {
          if (ks >= 4 && ks <= 6) ISS_W(7, ks - 4, ks - 3);
        }
        if (m == 2) {
          if (ks == 5) { VM(q0 = ld16_sc1o<0>(gvo, sws, gso)); VM(q1 = ld16_sc1o<2048>(gvo, sws, gso)); }
          if (ks == 6) { VM(q2 = ld16_sc1o<0>(gvo, sws, gso + 4096)); VM(q3 = ld16_sc1o<2048>(gvo, sws, gso + 4096)); mq = vq; }
        }
        SCHED();
      }
      if ((ks & 3) == 3 || ks == 16) {
#pragma unroll
        for (int j = 0; j < 3; ++j) { PIN(acc[0][j]); PIN(acc[1][j]); }
        PIN(csa);
      }
    }
    sts4f(L_CS + 512 + tl[0] * 64 + i16 * 4, csa[0]);
    gf = ld8_sc1((lane >> 5) * 1024, sws, WS_FA + (16 * w + c2) * 128);
    BARRIER();
    VWO();
    DMA_WOUT(0) DMA_WOUT(1)
    WAITV(2);
    PIN(gf);
    if (!__all(gf[0] == tlo && gf[1] == thi)) poll_flags((lane >> 5) * 1024, true, sws, WS_FA + (16 * w + c2) * 128, tlo, thi);
    STAMP(6);
    {
      float xr[2], xm[2];
#pragma unroll
      for (int rt = 0; rt < 2; ++rt) {
        xr[rt] = lds4f(L_XS + (32 * rw + 16 * rt + i16) * 4);
        xm[rt] = -lds4f(L_XS + 512 + (32 * rw + 16 * rt + i16) * 4);
      }
      const u32 so0 = zb0 + TILE(0, gn) * 65536;
#pragma unroll
      for (int j = 0; j < 3; ++j) {
        const f32x4 cc = lds16f(L_CS + 512 + tl[j] * 64 + g * 16);
        bfr[0][3 * j] = ld16_b(l16, sws, so0 ^ (3 * j * 1024));
        bfr[0][3 * j + 1] = ld16_b(l16, sws, so0 ^ ((3 * j + 1) * 1024));
        if (j < 2) bfr[0][3 * j + 2] = ld16_b(l16, sws, so0 ^ ((3 * j + 2) * 1024));
        u32x4 zz;
#pragma unroll
        for (int rt = 0; rt < 2; ++rt) {
          zz[2 * rt] = pk2v(gelu2(fma2(lo2(cc), bc2(xm[rt]), lo2(acc[rt][j])) * bc2(xr[rt])));
          zz[2 * rt + 1] = pk2v(gelu2(fma2(hi2(cc), bc2(xm[rt]), hi2(acc[rt][j])) * bc2(xr[rt])));
        }
        st16_sc1(zz, l16, sws, (tl[j] < z1n) ? WS_Z1 + ((4 * P + rw) * 43 + z1s + tl[j]) * 1024 : 0x7FFF0000u);
        SCHED();
      }
    }
    }
  } else {
    int vq = 2;
    const rsrc_t sw = __builtin_amdgcn_make_buffer_rsrc((void*)wqkv, 0, 512 * 3072, 0x00020000);
    const u32 vw = (8 * w + ((lane >> 2) & 7)) * 3072 + 128 * c + ((lane >> 5) * 4 + ((lane & 3) ^ ((w & 1) << 1))) * 16;
#define DMA_S(n, e)                                                                                                 \
  do {                                                                                                              \
    if ((e) < 2) DMA_X(((n) % 3) * 40960, n, (e) & 1);                                                              \
    else dma16(((n) % 3) * 40960 + 16384 + w * 3072 + ((e) - 2) * 1024, vw, sw, (((n) + rot) & 7) * (64 * 3072) + ((e) - 2) * 1024); \
  } while (0)
#define ISS_Q(u, base) do { VM(DMA_U(u, base, 0)); VM(DMA_U(u, base, 1)); mu[u] = vq; } while (0)
    int ms[8], mu[16];
    VM(DMA_S(0, 2)); VM(DMA_S(0, 3)); VM(DMA_S(0, 4)); ms[0] = vq;
    u32x4 xrow;
    VM(xrow = ld16(l16, sx, ((8 + c) * 8 + w) * 1024));
    const int mxr = vq;
#pragma unroll
    for (int e = 0; e < 5; ++e) { VM(DMA_S(1, e)); ms[1] = vq; }
    const int l32 = lane & 31, hh = lane >> 5;
    u32 xa = (32 * rw + l32) * 128 + ((hh ^ ((l32 >> 1) & 7)) * 16);
    u32 wa = 16384 + hh * 3072 + cw * 512 + ((lane >> 2) & 3) * 64 + (((((g & 1) << 1) | ((lane >> 1) & 1)) ^ (hh << 1)) * 16) + (lane & 1) * 8;
    u32 ca = 16384 + g * 3072 + min(w, 5) * 512 + ((lane >> 2) & 3) * 64 + ((((g & 1) << 1) | ((lane >> 1) & 1)) * 16) + (lane & 1) * 8;
    const f32x16 zero16 = {0, 0, 0, 0, 0, 0, 0, 0, 0, 0, 0, 0, 0, 0, 0, 0};
    f32x16 acc[3] = {zero16, zero16, zero16};
    f32x4 csa[2] = {zero4, zero4};
    u32x4 xg;
    int mxg = 0;
    WAITQ(ms[0]);
    BARRIER();
    USE_EPOCH()
    STAMP(7);
    {
      u32x4 xf[2], wf[2][4];
#pragma unroll
      for (int n = 0; n <= 8; ++n)
#pragma unroll
      for (int sq = 0; sq < 4; ++sq) {
        const int s = 4 * n + sq;
        if (s > 32) continue;
        if (s < 32 && sq == 0) {
          if (n > 0) {
            WAITQ(ms[n]);
            BARRIER();
          }
          const int d = n == 0 ? 0 : (n % 3 == 0 ? -81920 : 40960);
          xa = opq(xa + d); wa = opq(wa + d); ca = opq(ca + d);
        }
        const int q1 = (s + 1) & 1, q0 = s & 1;
#pragma unroll
        for (int m = 0; m < 4; ++m) {
          if (s > 0) {
            if (m < 3) acc[m] = mfmaL(wf[q1][m], xf[q1], acc[m]);
            else csa[q1] = mfma32(ones4, wf[q1][3], csa[q1]);
            SCHED();
          }
          if (s < 32) {
            if (m == 0) { xf[q0] = lds16(xa ^ (32 * sq)); wf[q0][0] = ldfrag(wa + sq * 6144, 256); }
            if (m == 1) { wf[q0][1] = ldfrag(wa + sq * 6144 + 1024, 256); wf[q0][2] = ldfrag(wa + sq * 6144 + 2048, 256); }
            if (m == 2) wf[q0][3] = ldfrag((ca ^ (32 * (sq & 1))) + (sq >> 1) * 12288, 256);
            if (m == 0 || m == 2) {
              const int h = 2 * sq + (m == 2);
              if (n <= 5 && h < 5) { VM(DMA_S(n + 2, h)); ms[n + 2] = vq; }
              if (n == 0 && h == 5) {
                rsrc_t sp = __builtin_amdgcn_make_buffer_rsrc((void*)wphi, 0, 16384, 0x00020000);
                VM(dma16(L_WPHI + (w & 1) * 1024, l16, sp, c * 2048 + (w & 1) * 1024));
              }
              if (n == 4 && h == 5) { VM(xg = ld16_sc1(i16 * 16, sws, WS_XG + (P * 128 + 16 * w) * 16)); mxg = vq; }
              if (n == 6) { VM(DMA_U(sq, (sq < 2 ? LQ_M : LQ_A) + (sq & 1) * 16384, h & 1)); mu[sq] = vq; }
              if (n == 7 && sq < 2) { VM(DMA_U(4 + sq, sq * 16384, h & 1)); mu[4 + sq] = vq; }
            }
          }
          SCHED();
        }
        if (s == 3) EARLY_STATS(mxr)
      }
    }
    GATHER_XS()
    sts4f(L_CS + w * 128 + i16 * 4, csa[0][0]);
    sts4f(L_CS + w * 128 + 64 + i16 * 4, csa[1][0]);
    BARRIER();
    STAMP(1);
    WAITQ(mu[3]);
    READ_U(0, LQ_M) READ_U(1, LQ_M + 16384) READ_U(2, LQ_A) READ_U(3, LQ_A + 16384)
    const float xr = lds4f(L_XS + (32 * rw + l32) * 4), xm = -lds4f(L_XS + 512 + (32 * rw + l32) * 4);
    u32x4 wp0, wp1;
    {
      const u32 a = L_WPHI + (2 * cw + (lane & 1)) * 512 + (4 * hh + ((lane >> 2) & 3)) * 32 + (2 * (g & 1) + ((lane >> 1) & 1)) * 8;
      const u32x4 f = ldfrag(a, 256);
      const bool own = (lane >> 2) & 1;
#pragma unroll
      for (int e = 0; e < 4; ++e) { wp0[e] = own ? 0u : f[e]; wp1[e] = own ? f[e] : 0u; }
    }
#define FEATURES(e_, dst)                                                                                           \
  {                                                                                                                 \
    u32x4 kp[2];                                                                                                    \
    _Pragma("unroll") for (int a = 0; a < 4; ++a) {                                                                \
      const f32x4 cc = lds16f(L_CS + (2 * (e_) + cw) * 128 + a * 32 + hh * 16);                                      \
      kp[a >> 1][2 * (a & 1)] = pk2v(fma2(lo2(cc), bc2(xm), (f32x2){acc[e_][4 * a], acc[e_][4 * a + 1]}));          \
      kp[a >> 1][2 * (a & 1) + 1] = pk2v(fma2(hi2(cc), bc2(xm), (f32x2){acc[e_][4 * a + 2], acc[e_][4 * a + 3]}));  \
    }                                                                                                               \
    f32x16 pl = mfmaL(wp0, kp[0], zero16);                                                                          \
    pl = mfmaL(wp1, kp[1], pl);                                                                                     \
    hedgehog16(pl, xr, dst);                                                                                        \
  }
    {
      const int s_ = 32 * rw + l32;
      const u32 key = (((s_ >> 1) & 3) ^ (((s_ >> 3) & 1) << 1)) * 16;
      {
        const u32 va = LQ_V + (s_ >> 3) * 1024 + cw * 512 + (s_ & 7) * 64 + hh * 8;
#pragma unroll
        for (int a = 0; a < 4; ++a) {
          const f32x4 cc = lds16f(L_CS + (4 + cw) * 128 + a * 32 + hh * 16);
          sts8(va + ((a * 16) ^ key), (u32x2){pk2v(fma2(lo2(cc), bc2(xm), (f32x2){acc[2][4 * a], acc[2][4 * a + 1]}) * bc2(xr)),
                                              pk2v(fma2(hi2(cc), bc2(xm), (f32x2){acc[2][4 * a + 2], acc[2][4 * a + 3]}) * bc2(xr))});
        }
      }
      WAITQ(mu[5]);
      READ_U(4, 0) READ_U(5, 16384)
      WAITL0();
      ISS_Q(6, LQ_M); ISS_Q(7, LQ_M + 16384); ISS_Q(8, LQ_A); ISS_Q(9, LQ_A + 16384); ISS_Q(10, 0); ISS_Q(11, 16384);
      u32x4 ph[4];
      FEATURES(1, ph)
      const u32 pa = LQ_PHI + (s_ >> 3) * 2048 + (2 * cw + hh) * 512 + (s_ & 7) * 64;
#pragma unroll
      for (int a = 0; a < 4; ++a) sts16(pa + ((a * 16) ^ key), ph[a]);
    }
    WAITQ(mu[9]);
    READ_U(6, LQ_M) READ_U(7, LQ_M + 16384) READ_U(8, LQ_A) READ_U(9, LQ_A + 16384)
    BARRIER();
    STAMP(2);
    {
      const u32 kx = (((lane >> 3) & 1) ^ ((g & 1) << 1)) * 16;
      const u32 pa = LQ_PHI + g * 2048 + (w >> 1) * 512 + ((lane >> 2) & 3) * 64 + (((2 * (w & 1) + ((lane >> 1) & 1)) * 16) ^ kx) + (lane & 1) * 8;
      const u32 va = LQ_V + g * 1024 + (w >> 2) * 512 + ((lane >> 2) & 3) * 64 + (((2 * ((w >> 1) & 1) + ((lane >> 1) & 1)) * 16) ^ kx) + (lane & 1) * 8;
      u32x4 pf[4], vf[4];
#pragma unroll
      for (int k = 0; k < 4; ++k) {
        pf[k] = cat(ldstr(pa + k * 8192), ldstr((pa ^ 32) + k * 8192 + 256));
        vf[k] = cat(ldstr(va + k * 4096), ldstr((va ^ 32) + k * 4096 + 256));
      }
      f32x4 kv = zero4, kz = zero4;
#pragma unroll
      for (int k = 0; k < 4; ++k) { kv = mfma32(pf[k], vf[k], kv); kz = mfma32(pf[k], ones4, kz); }
      const u32 so = WS_KP + (c * 16 + P) * KP_SZ;
      u32x4 g0 = {__float_as_uint(kv[0]), tlo, __float_as_uint(kv[1]), tlo}, g1 = {__float_as_uint(kv[2]), tlo, __float_as_uint(kv[3]), tlo};
      u32x4 g2 = {__float_as_uint((lane & 1) ? kz[2] : kz[0]), tlo, __float_as_uint((lane & 1) ? kz[3] : kz[1]), tlo};
      PIN(g0); PIN(g1); PIN(g2);
      asm volatile("s_nop 7" ::: "memory");
      VM(st16_sc1(g0, l16, sws, so + w * 2048));
      VM(st16_sc1(g1, l16, sws, so + w * 2048 + 1024));
      VM(st16_sc1(g2, g * 32 + (lane & 1) * 16 + ((i16 >> 1) << 26), sws, so + 16384 + w * 128));
    }
    STAMP(3);
    ISS_Q(12, LQ_M); ISS_Q(13, LQ_M + 16384); ISS_Q(14, LQ_A); ISS_Q(15, LQ_A + 16384);
    WAITQ(mu[11]);
    READ_U(10, 0) READ_U(11, 16384)
    BARRIER();
    {
      VWO();
#pragma unroll
      for (int e = 0; e < 7; ++e) DMA_WOUT(e)
    }
    u32x4 phq[4];
    FEATURES(0, phq)
    PIN(phq[0]); PIN(phq[1]); PIN(phq[2]); PIN(phq[3]);
    STAMP(4);
    if (w >= 5) {
      const int id = 2 * P + (w == 7 ? (lane & 1) : w - 5), wq = id >> 2, gq = id & 3;
      const u32 vi0 = (4 * g) * KP_SZ + (w == 7 ? 16384 + wq * 128 + gq * 32 : wq * 2048 + (gq * 16 + i16) * 16);
      const u32 vi1 = vi0 + (w == 7 ? 16 : 1024);
      const u32 vo = (w == 7 ? (WS_KZ - WS_KF) + c * 512 + id * 16 + ((i16 >> 1) << 25) : c * 8192 + id * 256 + i16 * 16) + (g << 28);
      float sa[4];
#pragma nounroll
      for (int it = 0; it < SPIN_CAP; ++it) {
        u32x4 a[4][2];
#pragma unroll
        for (int o = 0; o < 4; ++o) {
          a[o][0] = ld16_sc1(vi0, sws, WS_KP + (c * 16 + o) * KP_SZ);
          a[o][1] = ld16_sc1(vi1, sws, WS_KP + (c * 16 + o) * KP_SZ);
        }
        WAITV(0);
        bool ok = true;
#pragma unroll
        for (int o = 0; o < 4; ++o) { PIN(a[o][0]); PIN(a[o][1]); ok = ok && tag_ok(a[o][0], tlo) && tag_ok(a[o][1], tlo); }
#pragma unroll
        for (int e = 0; e < 4; ++e)
          sa[e] = (__uint_as_float(a[0][e >> 1][2 * (e & 1)]) + __uint_as_float(a[1][e >> 1][2 * (e & 1)])) +
                  (__uint_as_float(a[2][e >> 1][2 * (e & 1)]) + __uint_as_float(a[3][e >> 1][2 * (e & 1)]));
        if (__all(ok)) break;
        __builtin_amdgcn_s_sleep(POLL_SLEEP);
      }
      red2g(sa[0], sa[1]);
      red2g(sa[2], sa[3]);
      st16_sc1((u32x4){pk2(sa[0], sa[1]), tlo, pk2(sa[2], sa[3]), tlo}, vo, sws, WS_KF);
    }
    gf = ld8_sc1((lane >> 5) * 1024, sws, WS_FA + (16 * w + c2) * 128);
    WAITV(8);
    READ_U(12, LQ_M) READ_U(13, LQ_M + 16384) READ_U(14, LQ_A) READ_U(15, LQ_A + 16384)
    BARRIER();
#pragma unroll
    for (int a = 0; a < 4; ++a) sts16(L_WO1 + w * 4096 + a * 1024 + l16, phq[a]);
    WAITV(0);
    PIN(gf);
    if (!__all(gf[0] == tlo && gf[1] == thi)) poll_flags((lane >> 5) * 1024, true, sws, WS_FA + (16 * w + c2) * 128, tlo, thi);
    STAMP(6);
    { const u32 so_ = zb0 + TILE(0, gn) * 65536; LOAD_B(0, so_) }
  }

  tlo = __builtin_amdgcn_readfirstlane(tlo);
  thi = __builtin_amdgcn_readfirstlane(thi);
  int w2 = w, c22 = c2, p2 = p, cc2 = c;
  asm volatile("" : "+s"(w2), "+s"(tlo), "+s"(thi), "+s"(c22), "+s"(p2), "+s"(cc2));
  {
  const int w = w2, rw = w & 3, cw = w >> 2, c2 = c22, p = p2, c = cc2, P = p >> 1;
  RELANE();
  u32x4 kfg, kzg;
  u32 kfo = WS_KF + c * 8192 + w * 1024, kzo = WS_KZ + c * 512 + w * 64;
  asm volatile("" : "+s"(kfo), "+s"(kzo));
  if (t != 0) {
    kfg = ld16_sc1(l16, sws, kfo);
    kzg = ld16_sc1(g * 16, sws, kzo);
  }
  { const u32 so_ = zb0 + TILE(1, gn) * 65536; LOAD_B(1, so_) }
  STAMP(9);
  const u32 redw = opq(L_RED + w * 4096 + lane * 16), redr = opq(L_RED + (rw ^ (2 * ux0)) * 1024 + lane * 16);
  const u32 fd = WS_FD + (p * 16 + c2) * 128;
  const u32 f3s = WS_FB + (P * 8 + (c2 > 0 ? c2 - 1 : 0)) * 128;
  const u32 vw3 = ((lane >> 2) & 7) * 1024 + c2 * 128 + (lane >> 5) * 64;
  const rsrc_t swo = __builtin_amdgcn_make_buffer_rsrc((void*)wo, 0, 512 * 1024, 0x00020000);
#define DMA_WO(dst, half, e) dma16((dst) + w * 4096 + (e) * 1024, (vw3 + (lane & 3) * 16) ^ (((e) & 1) * 32), swo, ((w + 8 * (half)) * 4 + (e)) * 8192)
#define FV() ((lane & 32) ? (P * 8 + ((lane >> 2) & 7)) * 128 : (WS_FD - WS_FC) + (p * 16 + (((lane >> 1) & 14) | cw)) * 128)
  u32x2 z1r[6], hold;
  u32 bs;
  f32x4 sm0;
#define EMIT_H(jt, sm_)                                                                                             \
  {                                                                                                                 \
    const f32x2 bb = bc2(blo(bs));                                                                                  \
    const u32 h0 = pk2v((f32x2){blo(z1r[jt][0]), bhi(z1r[jt][0])} * (lo2(sm_) + bb));                               \
    const u32 h1 = pk2v((f32x2){blo(z1r[jt][1]), bhi(z1r[jt][1])} * (hi2(sm_) + bb));                               \
    const u32 hb_ = WS_HF + (((4 * p + rw) * 2 + ((jt) >> 1)) * 8 + c2) * 1024;                                     \
    if ((jt) == 0 || ((jt) == 2 && gn == 6)) hold = (u32x2){h0, h1};                                                \
    else if ((jt) == 1 || ((jt) == 3 && gn == 6)) st16_sc1((u32x4){hold[0], hold[1], h0, h1}, l16, sws, hb_);       \
    else if ((jt) == 2) st16_sc1((u32x4){h0, h1, 0u, 0u}, l16, sws, hb_);                                           \
    else st16_sc1((u32x4){h0, tlo, h1, tlo}, l16, sws, WS_HL + (((4 * p + rw) * 8 + c2) * 2 + (gn - 1 - (jt))) * 1024); \
  }
#pragma unroll
  for (int j = 0; j < 6; ++j) {
    if (j < 5 || gn == 6) {
      const int u = j & 1;
      if (j == 0) WAITV(8);
      if (j == 2) { WAITV(10); WAITV_IF(t, 0, 8); }
      if (j == 1) { WAITV(9); WAITV_IF(cw, 1, 8); }
      if (j == 3) { WAITV(9); WAITV_IF(cw, 1, 8); }
      if (j == 4) { WAITV(12); WAITV_IF(gn, 5, 4); }
      if (j == 5) { WAITV(10); WAITV_IF(cw, 1, 9); }
#pragma unroll
      for (int i = 0; i < 8; ++i) PIN(bfr[u][i]);
      {
        f32x4 a_[4];
#pragma unroll
        for (int rt = 0; rt < 4; ++rt) {
          a_[rt] = zero4;
#pragma unroll
          for (int i = 0; i < 8; ++i) a_[rt] = mfma32(bfr[u][i], af[rt][i], a_[rt]);
          SCHED();
          if (rt > 0) sts16f(redw + u * 32768 + (rt - 1) * 1024, a_[rt - 1]);
          SCHED();
        }
        if (j == 0 && t != 0) {
          PIN(kfg); PIN(kzg);
          poll_gran(kfg, l16, sws, kfo, tlo);
          poll_gran(kzg, g * 16, sws, kzo, tlo);
          sts8(L_KFS + (w >> 1) * 1024 + i16 * 64 + (w & 1) * 32 + g * 8, (u32x2){kfg[0], kfg[2]});
          sts8(L_KFS + 4096 + w * 32 + g * 8, (u32x2){kzg[0], kzg[2]});
          sts8(L_KFS + 4352 + (lane & 7) * 8, (u32x2){0u, 0u});
        }
        sts16f(redw + u * 32768 + 3072, a_[3]);
      }
      u32x4 of[2];
      if (j == 1 && t != 0) {
        const int l32 = lane & 31, hh = lane >> 5;
        const bool own = ((lane >> 2) & 1) == hh;
        const u32 ka = own ? L_KFS + (2 * cw + hh) * 1024 + ((l32 & 3) + 4 * (l32 >> 3)) * 64 : L_KFS + 4352;
        const u32 zaa = own ? L_KFS + 4096 + (2 * cw + hh) * 64 : L_KFS + 4352;
        const u32 qa = L_WO1 + w * 4096 + l16;
        const f32x16 zero16 = {0, 0, 0, 0, 0, 0, 0, 0, 0, 0, 0, 0, 0, 0, 0, 0};
        f32x16 den = zero16, num = zero16;
#pragma unroll
        for (int a = 0; a < 4; ++a) {
          const u32x4 pq_ = lds16(qa + a * 1024);
          den = mfmaL(lds16(zaa + a * 16), pq_, den);
          num = mfmaL(lds16(ka + a * 16), pq_, num);
        }
        const float inv = __builtin_amdgcn_rcpf(den[0] + 1e-6f);
#pragma unroll
        for (int a = 0; a < 2; ++a) {
          of[a] = (u32x4){pk2(num[8 * a] * inv, num[8 * a + 1] * inv), pk2(num[8 * a + 2] * inv, num[8 * a + 3] * inv),
                          pk2(num[8 * a + 4] * inv, num[8 * a + 5] * inv), pk2(num[8 * a + 6] * inv, num[8 * a + 7] * inv)};
          PIN(of[a]);
        }
        STAMP(5);
      }
      BAR_IF(cw, 1, 63);
      if (w == 4) {
        if (j == 0 && t == 0) SET_FLAG(WS_FB + (P * 8 + c) * 128);
        if (j == 3) SET_FLAG(fd);
        if (j == 3 && t != 0) SET_FLAG(WS_FC + (P * 8 + c) * 128);
        if ((j == 4 && gn == 5) || j == 5) SET_FLAG(fd + 1024);
      }
      if (j == 1) {
        if (cw == 0) {
          WAITV(0);
          PIN(gf);
          if (!__all(gf[0] == tlo && gf[1] == thi)) poll_flags((lane >> 5) * 128, true, sws, f3s, tlo, thi);
          rsrc_t sb = __builtin_amdgcn_make_buffer_rsrc((void*)bsp, 0, 4096, 0x00020000);
          bs = ld2(i16 * 2, sb, (64 * p + 16 * rw) * 2);
        }
      }
      if ((j == 1 || j == 2) && cw == 0) {
#pragma unroll
        for (int v = 3 * j - 3; v < 3 * j; ++v)
          z1r[v] = ld8_sc1(l16, sws, WS_Z1 + ((2 * p + (rw >> 1)) * 43 + gs + TILE(min(v, gn - 1), gn)) * 1024 + (rw & 1) * 8);
      }
      if ((j == 4 && gn == 5) || j == 5) gf = ld8_sc1(FV(), sws, WS_FC);
      if (j == 4 && t == 0) {
        VWO();
#pragma unroll
        for (int e = 2; e < 7; ++e) DMA_WOUT(e)
      }
      const u32 ra_ = redr + ((j + 1) & 1) * 32768;
      f32x4 sm, r_[2];
#pragma unroll
      for (int i = 0; i < 8; ++i) {
        if (j >= 1 && (i & 1) == 0 && cw == 0) {
          if (i == 2) sm = r_[0] + r_[1];
          if (i > 2) sm += r_[0] + r_[1];
          r_[0] = lds16f(ra_ + i * 4096); r_[1] = lds16f(ra_ + (i + 1) * 4096);
        }
        SCHED();
        if (j < 3) bfr[u][i] = ld16_b(l16, sws, (zb0 + TILE(j + 2, gn) * 65536) ^ (i * 1024));
        else if (j == 3) ld16_b_if6(bfr[1][i], gn, l16, sws, (zb0 + TILE(5, gn) * 65536) ^ (i * 1024));
        SCHED();
      }
      if (j == 0 && cw == 0) gf = ld8_sc1((lane >> 5) * 128, sws, f3s);
      if (j == 1 && t != 0) {
        const u32 vo = ((lane >> 4) & 1) * 16384 + (lane >> 5) * 512 + i16 * 16;
        st16_sc1(of[0], vo, sws, WS_OF + ((8 * P + 2 * rw) * 16 + 2 * c + cw) * 1024);
        st16_sc1(of[1], vo, sws, WS_OF + ((8 * P + 2 * rw) * 16 + 2 * c + cw) * 1024 + 256);
      }
      if (j == 2) { PIN(bs); _Pragma("unroll") for (int v = 0; v < 3; ++v) PIN(z1r[v]); }
      if (j == 3) { _Pragma("unroll") for (int v = 3; v < 6; ++v) PIN(z1r[v]); }
      if (j >= 1 && cw == 0) {
        sm += r_[0] + r_[1];
        if (j == 1) sm0 = sm;
        if (j == 2) EMIT_H(0, sm0)
        if (j >= 2) EMIT_H(j >= 2 ? j - 1 : 0, sm)
      }
      if (j == 3) {
#pragma unroll
        for (int e = 0; e < 4; ++e) DMA_WO(L_WO1, 0, e);
      }
      if (j == 3 && cw == 0) { WAITV(12); WAITV_IF(gn, 5, 5); }
      if (j == 5) WAITV(2);
      BAR_IF(cw, 0, 63);
    }
  }
  {
    STAMP(10);
    const u32 bufA = L_RED + (gn & 1) * 32768, bufB = L_RED + 32768 - (gn & 1) * 32768;
    const u32 fv = FV();
    if (cw == 0) {
      const u32 ra_ = redr + (bufB - L_RED);
      const f32x4 sm = ((lds16f(ra_) + lds16f(ra_ + 4096)) + (lds16f(ra_ + 8192) + lds16f(ra_ + 12288))) +
           ((lds16f(ra_ + 16384) + lds16f(ra_ + 20480)) + (lds16f(ra_ + 24576) + lds16f(ra_ + 28672)));
      if (gn == 6) EMIT_H(5, sm) else EMIT_H(4, sm)
    }
    WAITV(2); WAITV_IF(cw, 1, 0);
    PIN(gf);
    {
      const bool n0 = (lane & 48) != 16;
      if (!__all(!n0 || (gf[0] == tlo && gf[1] == thi))) poll_flags(fv, n0, sws, WS_FC, tlo, thi);
    }
    RELANE();
    u32x4 hf[8], of[8];
    const u32 ofb = WS_OF + ((4 * p + rw) * 16 + cw) * 1024, hfb = WS_HF + ((4 * p + rw) * 16 + cw) * 1024;
#pragma unroll
    for (int e = 0; e < 4; ++e) of[e] = ld16_b(l16, sws, ofb + e * 2048);
#pragma unroll
    for (int e = 0; e < 4; ++e) hf[e] = ld16_b(l16, sws, hfb + e * 2048);
    BARRIER();
    const u32 qb = ((lane >> 2) & 3) * 64 + ((((g & 1) << 1) | ((lane >> 1) & 1)) * 16) + (lane & 1) * 8;
    f32x4 a3[4];
#pragma unroll
    for (int ct = 0; ct < 4; ++ct) a3[ct] = zero4;
    u32x4 wf[3][4], hl[4][2];
#define RDW2(dst, lo0, hi0, offl, offh) _Pragma("unroll") for (int ct = 0; ct < 4; ++ct) dst[ct] = cat(ldstr(((lo0) ^ ((ct & 1) * 32)) + (offl) + (ct >> 1) * 512), ldstr(((hi0) ^ ((ct & 1) * 32)) + (offh) + (ct >> 1) * 512))
#define MM3(src, frag) _Pragma("unroll") for (int ct = 0; ct < 4; ++ct) a3[ct] = mfma32(src[ct], frag, a3[ct])
    const u32 lb = WS_HL + ((4 * p + rw) * 8 + cw) * 2048;
    const rsrc_t sxo = __builtin_amdgcn_make_buffer_rsrc((void*)(x + (size_t)p * 65536), 0, 65536, 0x00020000);
    const u32 w0 = opq(L_WOUT + qb + (g >> 1) * 1024 + (g & 1) * 256);
    const u32 wa = w0 + cw * 4096, wb = w0 + cw * 2048;
    const u32 oq = qb + g * 1024 + cw * 4096;
    const u32 oa = opq(L_WO1 + oq), ob = opq(bufA + oq);
    u32x4 xres;
    VWO();
#pragma unroll
    for (int e = 0; e < 4; ++e) DMA_WO(bufA, 1, e);
    STAMP(11);
#define RDK(dst, k)                                                                                                 \
  if ((k) < 4) { RDW2(dst, oa, oa, (k) * 8192, (k) * 8192 + 256); }                                                 \
  else if ((k) < 8) { RDW2(dst, wa, wa, ((k) - 4) * 8192, ((k) - 4) * 8192 + 2048); }                               \
  else if ((k) < 12) { RDW2(dst, ob, ob, ((k) - 8) * 8192, ((k) - 8) * 8192 + 256); }                               \
  else { RDW2(dst, wb, w0, 32768 + ((k) - 12) * 4096, 49152 + ((k) - 12) * 2048); }
    RDK(wf[0], 0)
    RDK(wf[1], 1)
#pragma unroll
    for (int k = 0; k < 16; ++k) {
      if (k == 0) WAITV(8);
      if (k == 4) WAITV(13);
      if (k == 6) {
        WAITV(0);
        PIN(gf);
        if (!__all(gf[0] == tlo && gf[1] == thi)) poll_flags(fv, true, sws, WS_FC, tlo, thi);
#pragma unroll
        for (int e = 4; e < 8; ++e) hf[e] = ld16_b(l16, sws, hfb + e * 2048);
        BARRIER();
        STAMP(12);
      }
      if (k == 12) WAITV(9);
      if (k + 2 < 16) { RDK(wf[(k + 2) % 3], k + 2) }
      SCHED();
      if (k < 4) { PIN(of[k]); MM3(wf[k % 3], of[k]); }
      else if (k < 8) { PIN(hf[k - 4]); MM3(wf[k % 3], hf[k - 4]); }
      else if (k < 12) { PIN(of[k - 4]); MM3(wf[k % 3], of[k - 4]); }
      else { PIN(hf[k - 8]); MM3(wf[k % 3], hf[k - 8]); }
      SCHED();
      if (k < 4) {
        of[4 + k] = ld16_b(l16, sws, ofb + (4 + k) * 2048);
        DMA_WOUT_AT(bufB + w * 4096, 7 + k)
        if (k == 3) gf = ld8_sc1(fv, sws, WS_FC);
      } else if (k >= 6 && k < 10) {
        hl[k - 6][0] = ld16_sc1(l16, sws, lb + (k - 6) * 4096);
        hl[k - 6][1] = ld16_sc1(l16, sws, lb + (k - 6) * 4096 + 1024);
      } else if (k == 10) {
        xres = ld16((tid >> 3) * 1024 + (tid & 7) * 16, sxo, c2 * 128);
      }
    }
    STAMP(13);
    {
      const u32 wl_ = opq(bufB + (w0 - L_WOUT) + cw * 4096);
      u32x4 wg[4][4];
#pragma unroll
      for (int e = 0; e < 4; ++e) RDW2(wg[e], wl_, wl_, e * 8192, e * 8192 + 2048);
      WAITV(0);
      PIN(xres);
#pragma unroll
      for (int e = 0; e < 4; ++e) { PIN(hl[e][0]); PIN(hl[e][1]); }
#pragma nounroll
      for (int it = 0; it < SPIN_CAP; ++it) {
        u32 bad = 0;
#pragma unroll
        for (int e = 0; e < 8; ++e) bad |= (hl[e >> 1][e & 1][1] ^ tlo) | (hl[e >> 1][e & 1][3] ^ tlo);
        if (__all(bad == 0)) break;
        __builtin_amdgcn_s_sleep(2);
        asm volatile(
            "buffer_load_dwordx4 %0, %8, %9, %10 offen sc1\n\tbuffer_load_dwordx4 %1, %8, %9, %10 offen offset:1024 sc1\n\t"
            "buffer_load_dwordx4 %2, %8, %9, %11 offen sc1\n\tbuffer_load_dwordx4 %3, %8, %9, %11 offen offset:1024 sc1\n\t"
            "buffer_load_dwordx4 %4, %8, %9, %12 offen sc1\n\tbuffer_load_dwordx4 %5, %8, %9, %12 offen offset:1024 sc1\n\t"
            "buffer_load_dwordx4 %6, %8, %9, %13 offen sc1\n\tbuffer_load_dwordx4 %7, %8, %9, %13 offen offset:1024 sc1\n\t"
            "s_waitcnt vmcnt(0)"
            : "=&v"(hl[0][0]), "=&v"(hl[0][1]), "=&v"(hl[1][0]), "=&v"(hl[1][1]), "=&v"(hl[2][0]), "=&v"(hl[2][1]), "=&v"(hl[3][0]), "=&v"(hl[3][1])
            : "v"(l16), "s"(sws), "s"(lb), "s"(lb + 4096), "s"(lb + 8192), "s"(lb + 12288)
            : "memory");
      }
#pragma unroll
      for (int e = 0; e < 4; ++e) MM3(wg[e], ((u32x4){hl[e][0][0], hl[e][0][2], hl[e][1][0], hl[e][1][2]}));
    }
    STAMP(14);
    const u32 fin = L_WO1 - 2048;
#pragma unroll
    for (int ct = 0; ct < 4; ++ct) sts16f(fin + 2048 + cw * 16384 + (16 * rw + i16) * 256 + (((4 * ct + g) ^ i16) * 16), a3[ct]);
    BARRIER();
    {
      const int row = tid >> 3;
      const u32 a = fin + 2048 + row * 256 + (((2 * (tid & 7)) ^ (row & 15)) * 16);
      const f32x4 s0 = lds16f(a) + lds16f(a + 16384), s1 = lds16f(a ^ 16) + lds16f((a ^ 16) + 16384);
      u32x4 o = {pk2(blo(xres[0]) + s0[0], bhi(xres[0]) + s0[1]), pk2(blo(xres[1]) + s0[2], bhi(xres[1]) + s0[3]),
                 pk2(blo(xres[2]) + s1[0], bhi(xres[2]) + s1[1]), pk2(blo(xres[3]) + s1[2], bhi(xres[3]) + s1[3])};
      st16_sc1(o, row * 1024 + (tid & 7) * 16, __builtin_amdgcn_make_buffer_rsrc((void*)(out + (size_t)p * 65536), 0, 65536, 0x00020000), c2 * 128);
    }
  }
  }
  if (b == 0 && w == 0 && l16 == 0) *(u64*)(ws + WS_EP) = ((u64)thi << 32) | tlo;
  STAMP(15);
  STAMPC(31);
}

extern "C" void kernel_launch(void* const* d_in, const int* in_sizes, int n_in, void* d_out, int out_size, void* d_ws, size_t ws_size,
                              hipStream_t stream) {
  fused_kernel<<<256, 512, 0, stream>>>((const char*)d_in[0], (const char*)d_in[1], (const char*)d_in[2], (const char*)d_in[3],
                                        (const char*)d_in[5], (const char*)d_in[6], (const char*)d_in[7], (const char*)d_in[8], (char*)d_out,
                                        (char*)d_ws);
}
```

```cpp
#include <hip/hip_runtime.h>
#include <hip/hip_bf16.h>

typedef unsigned int u32;
typedef unsigned long long u64;
typedef short s16x4 __attribute__((ext_vector_type(4)));
typedef __bf16 bf16x2 __attribute__((ext_vector_type(2)));
typedef __bf16 bf16x8 __attribute__((ext_vector_type(8)));
typedef float f32x16 __attribute__((ext_vector_type(16)));
typedef float f32x4 __attribute__((ext_vector_type(4)));
typedef float f32x2 __attribute__((ext_vector_type(2)));
typedef u32 u32x4 __attribute__((ext_vector_type(4)));
typedef u32 u32x2 __attribute__((ext_vector_type(2)));
typedef __amdgpu_buffer_rsrc_t rsrc_t;
#define LDSP __attribute__((address_space(3)))
#define DI __device__ __forceinline__

#define STAMPS 0

constexpr u32 WS_EP = 0;
constexpr u32 WS_FA = 8192;
constexpr u32 WS_FB = WS_FA + 16384;
constexpr u32 WS_FC = WS_FB + 16384;
constexpr u32 WS_FD = WS_FC + 16384;
constexpr u32 WS_XG = WS_FD + 65536;
constexpr u32 WS_SG = WS_XG + 32768;
constexpr u32 WS_ZB = WS_SG + 524288;
constexpr u32 WS_Z1 = WS_ZB + 43 * 65536;
constexpr u32 KP_SZ = 17408;
constexpr u32 WS_KP = WS_Z1 + 64 * 43 * 1024;
constexpr u32 WS_KF = WS_KP + 128 * KP_SZ;
constexpr u32 WS_KZ = WS_KF + 65536;
constexpr u32 WS_OF = WS_KZ + 4096;
constexpr u32 WS_HF = WS_OF + 128 * 16384;
constexpr u32 WS_HL = WS_HF + 128 * 16 * 1024;
constexpr u32 WS_ST = WS_HL + 128 * 16 * 1024;
constexpr u32 WS_END = WS_ST + 256 * 8 * 32 * 8;

constexpr u32 LZ_W = 65536;
constexpr u32 LZ_U = 139264;
constexpr u32 LQ_M = 122880;
constexpr u32 LQ_A = 81920;
constexpr u32 LQ_PHI = 49152;
constexpr u32 LQ_V = 32768;
constexpr u32 L_TAB = 155648;
constexpr u32 L_XS = L_TAB;
constexpr u32 L_CS = L_TAB + 1024;
constexpr u32 L_ZST = L_TAB + 3072;
constexpr u32 L_WPHI = L_TAB + 3072;
constexpr u32 L_KFS = L_TAB + 2048;
constexpr u32 L_JUNK = L_TAB + 7168;
constexpr u32 L_WOUT = 0;
constexpr u32 L_RED = 57344;
constexpr u32 L_WO1 = 122880;

constexpr u64 Z2S = 0x26211B16110B0500ull;
constexpr u64 Z1S = 0x26211C16110C0600ull;
DI int grp_start(u64 tab, int c) { return c >= 8 ? 43 : (int)((tab >> (8 * c)) & 0xFF); }

#define SCHED() __builtin_amdgcn_sched_barrier(0)
#define TIC()
#define TOC(acc)
#define WAITV(n) do { TIC(); asm volatile("s_waitcnt vmcnt(%0)" ::"n"(n) : "memory"); TOC(acv); SCHED(); } while (0)
#define BARRIER() do { SCHED(); TIC(); asm volatile("s_waitcnt lgkmcnt(0)\n\ts_barrier" ::: "memory"); TOC(acb); SCHED(); } while (0)
#define WAITV_IF(g_, v, n) do { TIC(); asm volatile("s_cmp_lg_u32 %0, %1\n\ts_cbranch_scc1 1f\n\ts_waitcnt vmcnt(%2)\n1:" ::"s"(g_), "n"(v), "n"(n) : "scc", "memory"); TOC(acv); SCHED(); } while (0)
#define BAR_IF(g_, v, n) do { SCHED(); asm volatile("s_cmp_lg_u32 %0, %1\n\ts_cbranch_scc1 1f\n\ts_waitcnt vmcnt(%2) lgkmcnt(0)\n\ts_barrier\n1:" ::"s"(g_), "n"(v), "n"(n) : "scc", "memory"); SCHED(); } while (0)
#define WAITL0() asm volatile("s_waitcnt lgkmcnt(0)" ::: "memory")
#define PIN(x) asm volatile("" : "+v"(x))

DI void dma16(u32 lds, u32 voff, rsrc_t srd, u32 soff) {
  asm volatile("s_mov_b32 m0, %0\n\ts_nop 0\n\tbuffer_load_dwordx4 %1, %2, %3 offen lds" ::"s"(lds), "v"(voff), "s"(srd), "s"(soff) : "memory");
}
#define DMA_C(wb, CST, voff, srd, soff) asm volatile("s_add_u32 m0, %0, %1\n\ts_nop 0\n\tbuffer_load_dwordx4 %2, %3, %4 offen lds" ::"s"(wb), "n"(CST), "v"(voff), "s"(srd), "s"(soff) : "scc", "memory")
#define DMA_CC(wb, CST, voff, srd, OP, sb, SC) do { u32 so__; asm volatile("s_add_u32 m0, %1, %2\n\t" OP " %0, %5, %6\n\tbuffer_load_dwordx4 %3, %4, %0 offen lds" : "=&s"(so__) : "s"(wb), "n"(CST), "v"(voff), "s"(srd), "s"(sb), "n"(SC) : "scc", "memory"); } while (0)
DI void touch(u32 lds, u32 voff, rsrc_t srd, u32 soff) {
  asm volatile("s_mov_b32 m0, %0\n\ts_nop 0\n\tbuffer_load_dword %1, %2, %3 offen lds" ::"s"(lds), "v"(voff), "s"(srd), "s"(soff) : "memory");
}
DI u32x4 ld16_sc1(u32 voff, rsrc_t srd, u32 soff) {
  u32x4 r;
  asm volatile("buffer_load_dwordx4 %0, %1, %2, %3 offen sc1" : "=v"(r) : "v"(voff), "s"(srd), "s"(soff) : "memory");
  return r;
}
template <int OFF> DI u32x4 ld16_sc1o(u32 voff, rsrc_t srd, u32 soff) {
  u32x4 r;
  asm volatile("buffer_load_dwordx4 %0, %1, %2, %3 offen offset:%4 sc1" : "=v"(r) : "v"(voff), "s"(srd), "s"(soff), "n"(OFF) : "memory");
  return r;
}
#define BPOL " sc1"
DI u32x4 ld16_b(u32 voff, rsrc_t srd, u32 soff) {
  u32x4 r;
  asm volatile("buffer_load_dwordx4 %0, %1, %2, %3 offen" BPOL : "=v"(r) : "v"(voff), "s"(srd), "s"(soff) : "memory");
  return r;
}
template <int OFF> DI u32x4 ld16_bo(u32 voff, rsrc_t srd, u32 soff) {
  u32x4 r;
  asm volatile("buffer_load_dwordx4 %0, %1, %2, %3 offen offset:%4" BPOL : "=v"(r) : "v"(voff), "s"(srd), "s"(soff), "n"(OFF) : "memory");
  return r;
}
DI void ld16_b_if6(u32x4& r, int gn, u32 voff, rsrc_t srd, u32 soff) {
  asm volatile("s_cmp_lg_u32 %4, 6\n\ts_cbranch_scc1 1f\n\tbuffer_load_dwordx4 %0, %1, %2, %3 offen" BPOL "\n1:" : "+v"(r) : "v"(voff), "s"(srd), "s"(soff), "s"(gn) : "scc", "memory");
}
DI u32x4 ld16(u32 voff, rsrc_t srd, u32 soff) {
  u32x4 r;
  asm volatile("buffer_load_dwordx4 %0, %1, %2, %3 offen" : "=v"(r) : "v"(voff), "s"(srd), "s"(soff) : "memory");
  return r;
}
DI u32x2 ld8_sc1(u32 voff, rsrc_t srd, u32 soff) {
  u32x2 r;
  asm volatile("buffer_load_dwordx2 %0, %1, %2, %3 offen sc1" : "=v"(r) : "v"(voff), "s"(srd), "s"(soff) : "memory");
  return r;
}
DI u32 ld2(u32 voff, rsrc_t srd, u32 soff) {
  u32 r;
  asm volatile("buffer_load_ushort %0, %1, %2, %3 offen" : "=v"(r) : "v"(voff), "s"(srd), "s"(soff) : "memory");
  return r;
}
DI void st16_sc1(u32x4 v, u32 voff, rsrc_t srd, u32 soff) {
  asm volatile("buffer_store_dwordx4 %0, %1, %2, %3 offen sc1\n\ts_nop 1" ::"v"(v), "v"(voff), "s"(srd), "s"(soff) : "memory");
}
DI void st8_sc1(u32x2 v, u32 voff, rsrc_t srd, u32 soff) {
  asm volatile("buffer_store_dwordx2 %0, %1, %2, %3 offen sc1" ::"v"(v), "v"(voff), "s"(srd), "s"(soff) : "memory");
}

DI u32x4 lds16(u32 a) { return *(const LDSP u32x4*)(size_t)a; }
DI u32x2 lds8(u32 a) { return *(const volatile LDSP u32x2*)(size_t)a; }
DI float lds4f(u32 a) { return *(const LDSP float*)(size_t)a; }
DI f32x4 lds16f(u32 a) { return *(const LDSP f32x4*)(size_t)a; }
DI void sts16f(u32 a, f32x4 v) { *(LDSP f32x4*)(size_t)a = v; }
DI void sts16(u32 a, u32x4 v) { *(LDSP u32x4*)(size_t)a = v; }
DI void sts8(u32 a, u32x2 v) { *(volatile LDSP u32x2*)(size_t)a = v; }
DI void sts4f(u32 a, float v) { *(LDSP float*)(size_t)a = v; }
DI u32x2 ldstr(u32 a) {
  return __builtin_bit_cast(u32x2, __builtin_amdgcn_ds_read_tr16_b64_v4i16((LDSP s16x4*)(size_t)a)); }
DI u32x4 cat(u32x2 lo, u32x2 hi) { return (u32x4){lo[0], lo[1], hi[0], hi[1]}; }
DI u32x4 ldfrag(u32 a, u32 d) { u32x2 lo = ldstr(a), hi = ldstr(a + d); return cat(lo, hi); }

DI f32x4 mfma32(u32x4 a, u32x4 b, f32x4 c) {
  return __builtin_amdgcn_mfma_f32_16x16x32_bf16(__builtin_bit_cast(bf16x8, a), __builtin_bit_cast(bf16x8, b), c, 0, 0, 0);
}
DI f32x16 mfmaL(u32x4 a, u32x4 b, f32x16 c) {
  return __builtin_amdgcn_mfma_f32_32x32x16_bf16(__builtin_bit_cast(bf16x8, a), __builtin_bit_cast(bf16x8, b), c, 0, 0, 0);
}
DI u32 pk2(float a, float b) { bf16x2 v = {(__bf16)a, (__bf16)b}; return __builtin_bit_cast(u32, v); }
DI float blo(u32 u) { return __uint_as_float(u << 16); }
DI float bhi(u32 u) { return __uint_as_float(u & 0xffff0000u); }
DI float rbf(float a) { return blo(pk2(a, a)); }
DI float dot2(u32 a, u32 b, float c) {
  return __builtin_amdgcn_fdot2_f32_bf16(__builtin_bit_cast(bf16x2, a), __builtin_bit_cast(bf16x2, b), c, false);
}
DI f32x2 fma2(f32x2 a, f32x2 b, f32x2 c) { return __builtin_elementwise_fma(a, b, c); }
DI f32x2 bc2(float a) { return (f32x2){a, a}; }
DI f32x2 lo2(f32x4 v) { return (f32x2){v[0], v[1]}; }
DI f32x2 hi2(f32x4 v) { return (f32x2){v[2], v[3]}; }
DI u32 pk2v(f32x2 v) { return pk2(v[0], v[1]); }
DI f32x2 gelu2(f32x2 x) {
  f32x2 u = fma2(x * x, bc2(-0.10294324f), bc2(-2.3022082f)) * x;
  f32x2 e = {__builtin_amdgcn_exp2f(u[0]), __builtin_amdgcn_exp2f(u[1])};
  e = e + bc2(1.0f);
  f32x2 r = {__builtin_amdgcn_rcpf(e[0]), __builtin_amdgcn_rcpf(e[1])};
  return x * r;
}
DI void red2g(float& a, float& b) {
  auto s = __builtin_amdgcn_permlane32_swap(__float_as_uint(a), __float_as_uint(b), false, false);
  float c = __uint_as_float(s[0]) + __uint_as_float(s[1]);
  auto s2 = __builtin_amdgcn_permlane16_swap(__float_as_uint(c), __float_as_uint(c), false, false);
  float d = __uint_as_float(s2[0]) + __uint_as_float(s2[1]);
  auto s3 = __builtin_amdgcn_permlane32_swap(__float_as_uint(d), __float_as_uint(d), false, false);
  a = __uint_as_float(s3[0]);
  b = __uint_as_float(s3[1]);
}
template <int CTRL> DI float dpp(float v) {
  return __uint_as_float(__builtin_amdgcn_update_dpp(0, __float_as_uint(v), CTRL, 0xF, 0xF, true));
}
template <class F> DI void hedgehog16(const f32x16& p, float s, u32x4 (&o)[4], F hook) {
  float mx = fmaxf(p[0], p[1]), mn = fminf(p[0], p[1]);
#pragma unroll
  for (int i = 2; i < 16; i += 2) { mx = fmaxf(fmaxf(mx, p[i]), p[i + 1]); mn = fminf(fminf(mn, p[i]), p[i + 1]); }
  const float c = s * 1.44269504f;
  const float a = -mx * c, b = mn * c;
  f32x2 ep[8], en[8];
#pragma unroll
  for (int i = 0; i < 8; ++i) {
    hook(i);
    const f32x2 v = {p[2 * i], p[2 * i + 1]};
    const f32x2 u = fma2(v, bc2(c), bc2(a)), w = fma2(v, bc2(-c), bc2(b));
    ep[i] = (f32x2){__builtin_amdgcn_exp2f(u[0]), __builtin_amdgcn_exp2f(u[1])};
    en[i] = (f32x2){__builtin_amdgcn_exp2f(w[0]), __builtin_amdgcn_exp2f(w[1])};
  }
  hook(8);
  const f32x2 sp = ((ep[0] + ep[1]) + (ep[2] + ep[3])) + ((ep[4] + ep[5]) + (ep[6] + ep[7]));
  const f32x2 sn = ((en[0] + en[1]) + (en[2] + en[3])) + ((en[4] + en[5]) + (en[6] + en[7]));
  const float ip = __builtin_amdgcn_rcpf(sp[0] + sp[1]), in = __builtin_amdgcn_rcpf(sn[0] + sn[1]);
#pragma unroll
  for (int i = 0; i < 8; ++i) {
    if (i == 0 || i == 3 || i == 6) hook(9 + i / 3);
    o[i >> 2][i & 3] = pk2v(ep[i] * bc2(ip));
    o[2 + (i >> 2)][i & 3] = pk2v(en[i] * bc2(in));
  }
}
DI void ln_fin(float s, float q, float invn, float& r, float& m) {
  m = s * invn;
  float v = fmaxf(q * invn - m * m, 0.0f);
  float sd = rbf(__builtin_amdgcn_sqrtf(rbf(rbf(v) + 1e-5f)));
  r = __builtin_amdgcn_rcpf(sd);
  m = rbf(m);
}

#define POLL_SLEEP 6
constexpr int SPIN_CAP = 1 << 17;
DI void poll_flags(u32 voff, bool act, rsrc_t srd, u32 soff, u32 tlo, u32 thi) {
#pragma nounroll
  for (int it = 0; it < SPIN_CAP; ++it) {
    u32x2 v;
    asm volatile("buffer_load_dwordx2 %0, %1, %2, %3 offen sc1\n\ts_waitcnt vmcnt(0)" : "=&v"(v) : "v"(voff), "s"(srd), "s"(soff) : "memory");
    bool ok = !act || (v[0] == tlo && v[1] == thi);
    if (__all(ok)) break;
    __builtin_amdgcn_s_sleep(POLL_SLEEP);
  }
}
DI bool tag_ok(u32x4 q, u32 t) { return q[1] == t && q[3] == t; }
DI void poll_gran(u32x4& q, u32 voff, rsrc_t srd, u32 soff, u32 tlo) {
#pragma nounroll
  for (int it = 0; it < SPIN_CAP; ++it) {
    if (__all(tag_ok(q, tlo))) break;
    __builtin_amdgcn_s_sleep(POLL_SLEEP);
    asm volatile("buffer_load_dwordx4 %0, %1, %2, %3 offen sc1\n\ts_waitcnt vmcnt(0)" : "=&v"(q) : "v"(voff), "s"(srd), "s"(soff) : "memory");
  }
}
DI u32 opq(u32 v) { asm volatile("" : "+v"(v)); return v; }

#define STAMP(i)
#define STAMPC(i)

#define GQ_N 3
#define PREFETCH 0
#define CODE_PF 56000
#define OUT_SC1 1
#define PRIO 0
#define UROT 1
static_assert(WS_ZB % 8192 == 0, "");
#define TLB_PF 0
#define VM(x) do { x; ++vq; } while (0)
#define WAITQ(mark) WAITV(vq - (mark))

template <int t>
DI void run(const char* __restrict__ x, const char* __restrict__ wqkv, const char* __restrict__ wphi, const char* __restrict__ wo,
            const char* __restrict__ win, const char* __restrict__ wsp, const char* __restrict__ bsp, const char* __restrict__ wout,
            char* __restrict__ out, char* ws, u64 ep) {
  const int tid = threadIdx.x, lane = tid & 63;
  const int w = __builtin_amdgcn_readfirstlane(tid >> 6);
  const int g = lane >> 4;
  const int b = blockIdx.x, xcd = b & 7, r = b >> 3;
  const int Pl = r >> 3, cl = r & 3;
  const int P = (xcd >> 1) * 4 + Pl;
  const int rot = 2 * ((Pl + cl) & 3);
  u32 acv = 0, acb = 0; (void)acv; (void)acb;
  const rsrc_t sx = __builtin_amdgcn_make_buffer_rsrc((void*)(x + (size_t)P * 131072), 0, 131072, 0x00020000);
  const u32 vx0 = (16 * w + (lane >> 3)) * 1024 + (((lane & 7) ^ g) * 16);
  const u32 vx1 = (vx0 + 8192) ^ 64;
  const u32 w2k = w * 2048;
#define DMA_X(dst, n, e) DMA_C(w2k, (dst) + (e) * 1024, (e) ? vx1 : vx0, sx, (((n) + rot) & 7) * 128)
  const int rw = w & 3, cw = w >> 2, i16 = lane & 15;
  const int c = (xcd & 1) * 4 + cl;
  const int p = 2 * P + (cl & 1);
  const int c2 = (xcd & 1) * 4 + (t ? 3 * (cl >> 1) : 1 + (cl >> 1));
  const u32 l16 = opq(lane * 16);
  const rsrc_t sa = __builtin_amdgcn_make_buffer_rsrc((void*)(wsp + (size_t)p * 262144), 0, 262144, 0x00020000);
  const rsrc_t sws = __builtin_amdgcn_make_buffer_rsrc((void*)ws, 0, WS_END, 0x00020000);
  const u32 vA0 = (lane >> 3) * 4096 + (((lane & 7) ^ g) * 16);
  const u32 vA1 = (vA0 + 32768) ^ 64;
  const int ux0 = UROT ? (cl >> 1) : 0, ux1 = UROT ? t : 0;
  const u32 uxs = w * 512 + ux1 * 256 + ux0 * 131072;
#define DMA_U(u, base, e) DMA_CC(w2k, (base) + (e) * 1024, (e) ? vA1 : vA0, sa, "s_xor_b32", uxs, (((u) >> 1) & 3) * 128 + (((u) >> 3) * 32 + ((u) & 1) * 16) * 4096)
  const f32x4 zero4 = {0, 0, 0, 0};
  u32x4 ones4 = {0x3F803F80u, 0x3F803F80u, 0x3F803F80u, 0x3F803F80u};
  u32 tlo, thi;
  u32x4 af[4][8];
  u32x4 bfr[2][8];
  u32x2 gf;
  const u32 ard = w * 2048 + i16 * 128 + (((g >> 1) ^ (i16 >> 1)) * 16) + (g & 1) * 8;
#define READ_U(u, base)                                                                                             \
  {                                                                                                                 \
    const u32 ab_ = opq(ard + (base));                                                                              \
    _Pragma("unroll") for (int kk = 0; kk < 2; ++kk)                                                               \
      af[2 * ((u) >> 3) + ((u) & 1)][((u) & 6) + kk] = cat(lds8(ab_ ^ (64 * kk)), lds8(ab_ ^ (64 * kk + 32)));      \
  }
#define USE_EPOCH() { asm volatile("" : "+s"(ep)); ep += 1; tlo = (u32)ep; thi = (u32)(ep >> 32); PIN(ones4); }
#define EARLY_STATS(mark)                                                                                           \
  {                                                                                                                 \
    WAITQ(mark); PIN(xrow);                                                                                         \
    float s_ = 0, q_ = 0;                                                                                           \
    _Pragma("unroll") for (int e = 0; e < 4; ++e) { s_ = dot2(xrow[e], ones4[0], s_); q_ = dot2(xrow[e], xrow[e], q_); } \
    s_ += dpp<0x128>(s_); q_ += dpp<0x128>(q_);                                                                     \
    s_ += dpp<0x124>(s_); q_ += dpp<0x124>(q_);                                                                     \
    s_ += dpp<0x4E>(s_); q_ += dpp<0x4E>(q_);                                                                       \
    s_ += dpp<0xB1>(s_); q_ += dpp<0xB1>(q_);                                                                       \
    red2g(s_, q_);                                                                                                  \
    float r_, m_;                                                                                                   \
    ln_fin(s_, q_, 1.0f / 512, r_, m_);                                                                             \
    VM(st16_sc1((u32x4){__float_as_uint(r_), tlo, __float_as_uint(m_), tlo}, l16 << 22, sws, WS_XG + (P * 128 + (t * 8 + c) * 8 + w) * 16)); \
  }
#define GATHER_XS()                                                                                                 \
  WAITQ(mxg); PIN(xg);                                                                                              \
  poll_gran(xg, i16 * 16, sws, WS_XG + (P * 128 + 16 * w) * 16, tlo);                                               \
  sts4f(L_XS + (16 * w + i16) * 4, __uint_as_float(xg[0]));                                                         \
  sts4f(L_XS + 512 + (16 * w + i16) * 4, __uint_as_float(xg[2]));
#define RELANE() const int lane = opq(l16) >> 4, i16 = lane & 15, g = lane >> 4, tid = w * 64 + lane; (void)tid; (void)g; (void)i16
#define SET_FLAG(off) st8_sc1((u32x2){tlo, thi}, l16 << 22, sws, (off))
  const int gs = grp_start(Z2S, c2), gn = t ? 5 : grp_start(Z2S, c2 + 1) - gs;
  const int trot = (p & 7) >= 5 ? (p & 7) - 5 : (p & 7);
#define TILE(j, n_) (((j) + trot) >= (n_) ? (j) + trot - (n_) : (j) + trot)
  const u32 zb0 = WS_ZB + (gs * 64 + 8 * w) * 1024 + ux1 * 4096;
#define DMA_WOUT(e)                                                                                                 \
  {                                                                                                                 \
    const rsrc_t so_ = __builtin_amdgcn_make_buffer_rsrc((void*)wout, 0, 688 * 1024, 0x00020000);                   \
    const int ws_ = grp_start(Z2S, w), wn_ = grp_start(Z2S, w + 1) - ws_;                                           \
    if ((e) < 6) dma16(L_WOUT + ((e) < 4 ? 2 * w + ((e) >> 1) : 16 + w) * 2048 + ((e) & 1) * 1024, vwo, so_, ((ws_ + TILE((e) >> 1, wn_)) * 2 + ((e) & 1)) * 8192); \
    else if ((e) == 6) dma16(L_WOUT + 24 * 2048 + w * 1024, vwo, so_, ((((w >> 1) == 0 ? 5 : ((w >> 1) == 1 ? 11 : ((w >> 1) == 2 ? 27 : 64))) + TILE(3, 6)) * 2 + (w & 1)) * 8192); \
  }
#define DMA_WOUT_AT(dst, e)                                                                                         \
  {                                                                                                                 \
    const rsrc_t so_ = __builtin_amdgcn_make_buffer_rsrc((void*)wout, 0, 688 * 1024, 0x00020000);                   \
    const int ws_ = grp_start(Z2S, w), wn_ = grp_start(Z2S, w + 1) - ws_;                                           \
    dma16((dst) + ((e) - 7) * 1024, vwo, so_, ((ws_ + TILE(wn_ - 1 - (((e) - 7) >> 1), wn_)) * 2 + (((e) - 7) & 1)) * 8192); \
  }
#define VWO() const u32 vwo = ((lane >> 2) & 7) * 1024 + c2 * 128 + (lane >> 5) * 64 + (((lane & 3) ^ (((lane >> 4) & 1) << 1)) * 16)
#define LOAD_B(u, so_)                                                                                              \
  bfr[u][0] = ld16_bo<0>(l16, sws, so_); bfr[u][1] = ld16_bo<1024>(l16, sws, so_);                                  \
  bfr[u][2] = ld16_bo<2048>(l16, sws, so_); bfr[u][3] = ld16_bo<3072>(l16, sws, so_);                               \
  bfr[u][4] = ld16_bo<0>(l16, sws, (so_) ^ 4096); bfr[u][5] = ld16_bo<1024>(l16, sws, (so_) ^ 4096);                \
  bfr[u][6] = ld16_bo<2048>(l16, sws, (so_) ^ 4096); bfr[u][7] = ld16_bo<3072>(l16, sws, (so_) ^ 4096);

  if constexpr (t == 0) {
    int vq = 2;
    const rsrc_t sw = __builtin_amdgcn_make_buffer_rsrc((void*)win, 0, 512 * 2752, 0x00020000);
    const int z2s = grp_start(Z2S, c), z2n = grp_start(Z2S, c + 1) - z2s;
    const int z1s = grp_start(Z1S, c), z1n = grp_start(Z1S, c + 1) - z1s;
    u32 vw[3];
    int wch[3];
#pragma unroll
    for (int e = 0; e < 3; ++e) {
      const int sg = 2 * e + (lane >> 5), hi = sg >= 3;
      wch[e] = (sg - 3 * hi) * 4 + ((lane & 3) ^ (hi << 1));
      vw[e] = wch[e] < 2 * z2n ? (16 * w + 8 * hi + ((lane >> 2) & 7)) * 2752 + 1376 + 32 * z2s + 16 * wch[e] : 0x7FFF0000u;
    }
    const u32 w3k = w * 3072;
#define DMA_W(bk, e) DMA_C(w3k, LZ_W + ((bk) % 3) * 24576 + (e) * 1024, vw[e], sw, ((2 * (bk) + rot) & 7) * (64 * 2752))
#define UBASE(u) ((u) % 5 == 0 ? LZ_U : ((u) % 5 - 1) * 16384)
#define ISS_X(n) do { VM(DMA_X(((n) & 3) * 16384, n, 0)); VM(DMA_X(((n) & 3) * 16384, n, 1)); mx[n] = vq; } while (0)
#define ISS_U(u) do { VM(DMA_U(u, UBASE(u), 0)); VM(DMA_U(u, UBASE(u), 1)); mu[u] = vq; } while (0)
#define ISS_W(bk, e0, e1) do { _Pragma("unroll") for (int e_ = e0; e_ < e1; ++e_) VM(DMA_W(bk, e_)); mw[bk] = vq; } while (0)
    int mx[8], mw[8], mu[16];
    mx[0] = vq;
    ISS_W(0, 0, 3);
    u32x4 xrow;
    VM(xrow = ld16(l16, sx, (c * 8 + w) * 1024));
    const int mxr = vq;
    ISS_X(1);
    const u32 xa0 = opq((32 * rw + i16) * 128 + ((g ^ (i16 >> 1)) * 16)), xa1 = xa0 ^ 64;
    u32 ta[3];
    int tl[3];
    {
      const u32 trb = LZ_W + g * 1536 + ((lane >> 2) & 3) * 64 + ((((g & 1) << 1) | ((lane >> 1) & 1)) * 16) + (lane & 1) * 8;
#pragma unroll
      for (int j = 0; j < 3; ++j) {
        tl[j] = 3 * cw + (j + rw) % 3;
        ta[j] = (trb + (tl[j] >> 1) * 512) ^ ((tl[j] & 1) * 32);
      }
    }
#define NEXT_BLOCK(bk) _Pragma("unroll") for (int j = 0; j < 3; ++j) ta[j] = opq(ta[j] + ((bk) == 0 ? 0 : ((bk) % 3 == 0 ? -49152 : 24576)))
    f32x4 acc[2][3], csa = zero4;
#pragma unroll
    for (int j = 0; j < 3; ++j) acc[0][j] = acc[1][j] = zero4;
    u32x4 xf[16][2], wf[2][3];
    u32x4 xg;
    int mxg = 0;
    WAITQ(mw[0]);
    BARRIER();
    USE_EPOCH()
    STAMP(7);
#define P1_MEM(n, h)                                                                                                \
  {                                                                                                                 \
    if ((n) == 0) { if ((h) == 0) ISS_X(2); if ((h) == 1) ISS_W(1, 0, 2); if ((h) == 2) ISS_W(1, 2, 3); if ((h) == 3) ISS_X(3); } \
    if ((n) == 1) { if ((h) == 0) ISS_W(2, 0, 2); if ((h) == 1) ISS_W(2, 2, 3); if ((h) == 2) VM(DMA_X(0, 4, 0)); if ((h) == 3) { VM(DMA_X(0, 4, 1)); mx[4] = vq; } } \
    if ((n) == 2 || (n) == 4) { if ((h) < 2) { VM(DMA_X((((n) + 3) & 3) * 16384, (n) + 3, (h) & 1)); mx[(n) + 3] = vq; } else ISS_W((n) / 2 + 2, (h) - 2, (h) - 1); } \
    if ((n) == 3) { if ((h) == 0) ISS_W(3, 2, 3); else if ((h) < 3) { VM(DMA_X(32768, 6, (h) - 1)); mx[6] = vq; } else ISS_U(0); } \
    if ((n) == 5) { if ((h) == 0) ISS_W(4, 2, 3); if ((h) == 1) { VM(xg = ld16_sc1(i16 * 16, sws, WS_XG + (P * 128 + 16 * w) * 16)); mxg = vq; } \
                    if ((h) >= 2) { VM(DMA_U(1, UBASE(1), (h) - 2)); mu[1] = vq; } }                                 \
    if ((n) == 6) { if ((h) < 3) ISS_W(5, h, (h) + 1); else ISS_U(2); }                                             \
    if ((n) == 7) { if ((h) < 2) { VM(DMA_U(3, UBASE(3), h)); mu[3] = vq; } }                                       \
  }
#pragma unroll
    for (int ks = 0; ks <= 16; ++ks) {
      const int n = ks >> 1, kk = ks & 1;
      if (ks > 0 && ks < 16 && kk == 0) {
        { const int mk = (n & 1) ? mx[n] : (mx[n] > mw[n >> 1] ? mx[n] : mw[n >> 1]); WAITQ(mk); }
        BARRIER();
      }
      if (ks < 16 && (ks & 3) == 0) { NEXT_BLOCK(ks >> 2); }
      if (ks == 8) {
#pragma unroll
        for (int e = 0; e < 3; ++e)
          vw[e] = wch[e] < 2 * z1n ? (16 * w + 8 * (2 * e + (lane >> 5) >= 3) + ((lane >> 2) & 7)) * 2752 + 32 * z1s + 16 * wch[e] : 0x7FFF0000u;
      }
      const int k1 = ks > 0 ? ks - 1 : 0, q1 = k1 & 1;
#pragma unroll
      for (int m = 0; m < 7; ++m) {
        if (ks > 0) {
          if (m < 6) acc[m & 1][m >> 1] = mfma32(xf[k1][m & 1], wf[q1][m >> 1], acc[m & 1][m >> 1]);
          else csa = mfma32(ones4, wf[q1][0], csa);
          SCHED();
        }
        if (ks < 16) {
          if (m == 0) { P1_MEM(n, 2 * kk) xf[ks & 15][0] = lds16((kk ? xa1 : xa0) + (n & 3) * 16384); xf[ks & 15][1] = lds16((kk ? xa1 : xa0) + (n & 3) * 16384 + 2048); }
          else if (m < 4) wf[kk][m - 1] = ldfrag(ta[m - 1] + (ks & 3) * 6144, 256);
          if (m == 4) P1_MEM(n, 2 * kk + 1)
        }
        SCHED();
      }
#pragma unroll
      for (int j = 0; j < 3; ++j) { PIN(acc[0][j]); PIN(acc[1][j]); }
      PIN(csa);
      if (ks == 2) EARLY_STATS(mxr)
    }
    GATHER_XS()
    sts4f(L_CS + tl[0] * 64 + i16 * 4, csa[0]);
    WAITQ(mu[1] > mw[4] ? mu[1] : mw[4]);
    READ_U(0, UBASE(0)) READ_U(1, UBASE(1))
    BARRIER();
    STAMP(1);
    u32x4 zp[3];
    ISS_W(6, 0, 3);
    {
      f32x2 ps[4], pq[4];
      float cs[3];
#pragma unroll
      for (int j = 0; j < 3; ++j) cs[j] = -lds4f(L_CS + tl[j] * 64 + i16 * 4);
#pragma unroll
      for (int rt = 0; rt < 2; ++rt) {
        const f32x4 rr = lds16f(L_XS + (32 * rw + 16 * rt + 4 * g) * 4), mm = lds16f(L_XS + 512 + (32 * rw + 16 * rt + 4 * g) * 4);
        f32x2 z0[3], z1[3];
#pragma unroll
        for (int j = 0; j < 3; ++j) {
          z0[j] = gelu2(fma2(lo2(mm), bc2(cs[j]), lo2(acc[rt][j])) * lo2(rr));
          z1[j] = gelu2(fma2(hi2(mm), bc2(cs[j]), hi2(acc[rt][j])) * hi2(rr));
          zp[j][2 * rt] = pk2v(z0[j]); zp[j][2 * rt + 1] = pk2v(z1[j]);
        }
        ps[2 * rt] = z0[0] + z0[1] + z0[2]; ps[2 * rt + 1] = z1[0] + z1[1] + z1[2];
        pq[2 * rt] = fma2(z0[2], z0[2], fma2(z0[1], z0[1], z0[0] * z0[0]));
        pq[2 * rt + 1] = fma2(z1[2], z1[2], fma2(z1[1], z1[1], z1[0] * z1[0]));
        SCHED();
        if (rt == 0) { ISS_U(4); ISS_U(5); }
        else ISS_U(6);
        SCHED();
      }
      const bool b3 = lane & 8, b2 = lane & 4, b1 = lane & 2;
#pragma unroll
      for (int m = 0; m < 2; ++m)
#pragma unroll
        for (int e = 0; e < 2; ++e) {
          ps[m][e] = (b3 ? ps[m + 2][e] : ps[m][e]) + dpp<0x128>(b3 ? ps[m][e] : ps[m + 2][e]);
          pq[m][e] = (b3 ? pq[m + 2][e] : pq[m][e]) + dpp<0x128>(b3 ? pq[m][e] : pq[m + 2][e]);
        }
#pragma unroll
      for (int e = 0; e < 2; ++e) {
        ps[0][e] = (b2 ? ps[1][e] : ps[0][e]) + dpp<0x141>(b2 ? ps[0][e] : ps[1][e]);
        pq[0][e] = (b2 ? pq[1][e] : pq[0][e]) + dpp<0x141>(b2 ? pq[0][e] : pq[1][e]);
      }
      float s_ = (b1 ? ps[0][1] : ps[0][0]) + dpp<0x4E>(b1 ? ps[0][0] : ps[0][1]);
      float q_ = (b1 ? pq[0][1] : pq[0][0]) + dpp<0x4E>(b1 ? pq[0][0] : pq[0][1]);
      s_ += dpp<0xB1>(s_);
      q_ += dpp<0xB1>(q_);
      const int row = 32 * rw + 16 * (i16 >> 3) + 4 * g + ((i16 >> 1) & 3);
      VM(st16_sc1((u32x4){__float_as_uint(s_), tlo, __float_as_uint(q_), tlo}, row * 16 + ((lane & 1) << 30), sws, WS_SG + ((P * 8 + c) * 2 + cw) * 2048));
      PIN(zp[0]); PIN(zp[1]); PIN(zp[2]);
    }
    STAMP(2);
    {
    RELANE();
#pragma unroll
    for (int j = 0; j < 3; ++j) acc[0][j] = acc[1][j] = zero4;
    csa = zero4;
    u32x4 q0, q1, q2, q3;
    int mq = 0, mz = 0;
    const u32 gvo = (2 * g) * 4096 + (32 * rw + 16 * cw + i16) * 16;
    const u32 gso = WS_SG + P * 32768;
#pragma unroll
    for (int ks = 0; ks <= 16; ++ks) {
      if (ks == 8) {
        WAITQ(mq); PIN(q0); PIN(q1); PIN(q2); PIN(q3);
#pragma nounroll
        for (int it = 0; it < SPIN_CAP; ++it) {
          if (__all(tag_ok(q0, tlo) && tag_ok(q1, tlo) && tag_ok(q2, tlo) && tag_ok(q3, tlo))) break;
          __builtin_amdgcn_s_sleep(POLL_SLEEP);
          asm volatile(
              "buffer_load_dwordx4 %0, %4, %5, %6 offen sc1\n\t"
              "buffer_load_dwordx4 %1, %4, %5, %6 offen offset:2048 sc1\n\t"
              "buffer_load_dwordx4 %2, %4, %5, %7 offen sc1\n\t"
              "buffer_load_dwordx4 %3, %4, %5, %7 offen offset:2048 sc1\n\t"
              "s_waitcnt vmcnt(0)"
              : "=&v"(q0), "=&v"(q1), "=&v"(q2), "=&v"(q3)
              : "v"(gvo), "s"(sws), "s"(gso), "s"(gso + 4096)
              : "memory");
        }
        float s = (__uint_as_float(q0[0]) + __uint_as_float(q1[0])) + (__uint_as_float(q2[0]) + __uint_as_float(q3[0]));
        float q = (__uint_as_float(q0[2]) + __uint_as_float(q1[2])) + (__uint_as_float(q2[2]) + __uint_as_float(q3[2]));
        red2g(s, q);
        float r2, m2;
        ln_fin(s, q, 1.0f / 688, r2, m2);
        sts4f(L_ZST + (32 * rw + 16 * cw + i16) * 4, r2);
        sts4f(L_ZST + 512 + (32 * rw + 16 * cw + i16) * 4, -m2 * r2);
        STAMP(3);
      }
      if (ks < 16 && (ks & 3) == 0) {
        if (ks > 0) { WAITQ(ks == 12 ? mz : mw[4 + (ks >> 2)]); BARRIER(); }
        if (ks == 12) { if (w == 0) SET_FLAG(WS_FA + (P * 8 + c) * 128); STAMP(5); }
        NEXT_BLOCK(4 + (ks >> 2));
      }
      if (ks == 8) {
#pragma unroll
        for (int rt = 0; rt < 2; ++rt) {
          const u32 ra = L_ZST + (32 * rw + 16 * rt + 4 * g) * 4;
          const f32x4 r4 = lds16f(ra), m4 = lds16f(ra + 512);
#pragma unroll
          for (int j = 0; j < 3; ++j) {
            const u32 u0 = zp[j][2 * rt], u1 = zp[j][2 * rt + 1];
            zp[j][2 * rt] = pk2v(fma2((f32x2){blo(u0), bhi(u0)}, lo2(r4), lo2(m4)));
            zp[j][2 * rt + 1] = pk2v(fma2((f32x2){blo(u1), bhi(u1)}, hi2(r4), hi2(m4)));
          }
        }
#pragma unroll
        for (int j = 0; j < 3; ++j)
          VM(st16_sc1(zp[j], l16, sws, (tl[j] < z2n) ? WS_ZB + ((z2s + tl[j]) * 64 + 4 * P + rw) * 1024 : 0x7FFF0000u));
        mz = vq;
        STAMP(4);
        SCHED();
      }
      if (ks >= 5 && ks <= 9) { WAITQ(mu[ks - 3]); READ_U(ks - 3, UBASE(ks - 3)) }
      if (ks >= 9 && ks <= 13) { WAITQ(mu[ks - 2]); READ_U(ks - 2, UBASE(ks - 2)) }
      if (ks >= 13) { WAITQ(mu[ks - 1]); READ_U(ks - 1, UBASE(ks - 1)) }
      const int k1 = ks > 0 ? ks - 1 : 0, q1_ = k1 & 1;
#pragma unroll
      for (int m = 0; m < 7; ++m) {
        if (ks > 0) {
          if (m < 6) acc[m & 1][m >> 1] = mfma32(wf[q1_][m >> 1], xf[k1][m & 1], acc[m & 1][m >> 1]);
          else csa = mfma32(ones4, wf[q1_][0], csa);
          SCHED();
        }
        if (ks < 16 && m >= 1 && m < 4) wf[ks & 1][m - 1] = ldfrag(ta[m - 1] + (ks & 3) * 6144, 256);
        if (m == 0 || m == 4) {
          const int h = m == 4;
          if (h == 0 && ks >= 6 && ks <= 13) asm volatile("s_waitcnt lgkmcnt(10)" ::: "memory");
          if (ks >= 6 && ks <= 10) { VM(DMA_U(ks + 1, UBASE(ks + 1), h)); mu[ks + 1] = vq; }
          if (ks >= 10 && ks <= 13) { VM(DMA_U(ks + 2, UBASE(ks + 2), h)); mu[ks + 2] = vq; }
        }
        if (m == 6) {
          if (ks >= 4 && ks <= 6) ISS_W(7, ks - 4, ks - 3);
        }
        if (m == 2) {
          if (ks == 5) { VM(q0 = ld16_sc1o<0>(gvo, sws, gso)); VM(q1 = ld16_sc1o<2048>(gvo, sws, gso)); }
          if (ks == 6) { VM(q2 = ld16_sc1o<0>(gvo, sws, gso + 4096)); VM(q3 = ld16_sc1o<2048>(gvo, sws, gso + 4096)); mq = vq; }
        }
        SCHED();
      }
#pragma unroll
      for (int j = 0; j < 3; ++j) { PIN(acc[0][j]); PIN(acc[1][j]); }
      PIN(csa);
    }
    sts4f(L_CS + 512 + tl[0] * 64 + i16 * 4, csa[0]);
    gf = ld8_sc1((lane >> 5) * 1024, sws, WS_FA + (16 * w + c2) * 128);
    BARRIER();
    VWO();
    DMA_WOUT(0) DMA_WOUT(1)
    WAITV(2);
    PIN(gf);
    if (!__all(gf[0] == tlo && gf[1] == thi)) poll_flags((lane >> 5) * 1024, true, sws, WS_FA + (16 * w + c2) * 128, tlo, thi);
    STAMP(6);
    {
      float xr[2], xm[2];
#pragma unroll
      for (int rt = 0; rt < 2; ++rt) {
        xr[rt] = lds4f(L_XS + (32 * rw + 16 * rt + i16) * 4);
        xm[rt] = -lds4f(L_XS + 512 + (32 * rw + 16 * rt + i16) * 4);
      }
      const u32 so0 = zb0 + TILE(0, gn) * 65536;
#pragma unroll
      for (int j = 0; j < 3; ++j) {
        const f32x4 cc = lds16f(L_CS + 512 + tl[j] * 64 + g * 16);
        bfr[0][3 * j] = ld16_b(l16, sws, so0 ^ (3 * j * 1024));
        bfr[0][3 * j + 1] = ld16_b(l16, sws, so0 ^ ((3 * j + 1) * 1024));
        if (j < 2) bfr[0][3 * j + 2] = ld16_b(l16, sws, so0 ^ ((3 * j + 2) * 1024));
        u32x4 zz;
#pragma unroll
        for (int rt = 0; rt < 2; ++rt) {
          zz[2 * rt] = pk2v(gelu2(fma2(lo2(cc), bc2(xm[rt]), lo2(acc[rt][j])) * bc2(xr[rt])));
          zz[2 * rt + 1] = pk2v(gelu2(fma2(hi2(cc), bc2(xm[rt]), hi2(acc[rt][j])) * bc2(xr[rt])));
        }
        st16_sc1(zz, l16, sws, (tl[j] < z1n) ? WS_Z1 + ((4 * P + rw) * 43 + z1s + tl[j]) * 1024 : 0x7FFF0000u);
        SCHED();
      }
    }
    }
  } else {
    int vq = 2;
    const rsrc_t sw = __builtin_amdgcn_make_buffer_rsrc((void*)wqkv, 0, 512 * 3072, 0x00020000);
    const u32 vw = (8 * w + ((lane >> 2) & 7)) * 3072 + 128 * c + ((lane >> 5) * 4 + ((lane & 3) ^ ((w & 1) << 1))) * 16;
    const u32 w3k = w * 3072;
#define DMA_S(n, e)                                                                                                 \
  do {                                                                                                              \
    if ((e) < 2) DMA_X(((n) % 3) * 40960, n, (e) & 1);                                                              \
    else DMA_CC(w3k, ((n) % 3) * 40960 + 16384 + ((e) - 2) * 1024, vw, sw, "s_add_u32", (((n) + rot) & 7) * (64 * 3072), ((e) - 2) * 1024); \
  } while (0)
#define ISS_Q(u, base) do { VM(DMA_U(u, base, 0)); VM(DMA_U(u, base, 1)); mu[u] = vq; } while (0)
    int ms[8], mu[16];
    VM(DMA_S(0, 2)); VM(DMA_S(0, 3)); VM(DMA_S(0, 4)); ms[0] = vq;
    u32x4 xrow;
    VM(xrow = ld16(l16, sx, ((8 + c) * 8 + w) * 1024));
    const int mxr = vq;
#pragma unroll
    for (int e = 0; e < 5; ++e) { VM(DMA_S(1, e)); ms[1] = vq; }
    const int l32 = lane & 31, hh = lane >> 5;
    u32 xa = (32 * rw + l32) * 128 + ((hh ^ ((l32 >> 1) & 7)) * 16);
    u32 wa = 16384 + hh * 3072 + cw * 512 + ((lane >> 2) & 3) * 64 + (((((g & 1) << 1) | ((lane >> 1) & 1)) ^ (hh << 1)) * 16) + (lane & 1) * 8;
    u32 ca = 16384 + g * 3072 + min(w, 5) * 512 + ((lane >> 2) & 3) * 64 + ((((g & 1) << 1) | ((lane >> 1) & 1)) * 16) + (lane & 1) * 8;
    const f32x16 zero16 = {0, 0, 0, 0, 0, 0, 0, 0, 0, 0, 0, 0, 0, 0, 0, 0};
    f32x16 acc[3] = {zero16, zero16, zero16};
    f32x4 csa[2] = {zero4, zero4};
    u32x4 xg;
    int mxg = 0;
    WAITQ(ms[0]);
    BARRIER();
    USE_EPOCH()
    STAMP(7);
    {
      u32x4 xf[2], wf[2][4];
#pragma unroll
      for (int n = 0; n <= 8; ++n)
#pragma unroll
      for (int sq = 0; sq < 4; ++sq) {
        const int s = 4 * n + sq;
        if (s > 32) continue;
        if (s < 32 && sq == 0) {
          if (n > 0) {
            WAITQ(ms[n]);
            BARRIER();
          }
          const int d = n == 0 ? 0 : (n % 3 == 0 ? -81920 : 40960);
          xa = opq(xa + d); wa = opq(wa + d); ca = opq(ca + d);
        }
        const int q1 = (s + 1) & 1, q0 = s & 1;
#pragma unroll
        for (int m = 0; m < 4; ++m) {
          if (s > 0) {
            if (m < 3) acc[m] = mfmaL(wf[q1][m], xf[q1], acc[m]);
            else csa[q1] = mfma32(ones4, wf[q1][3], csa[q1]);
            SCHED();
          }
          if (s < 32) {
            if (m == 0) { xf[q0] = lds16(xa ^ (32 * sq)); wf[q0][0] = ldfrag(wa + sq * 6144, 256); }
            if (m == 1) { wf[q0][1] = ldfrag(wa + sq * 6144 + 1024, 256); wf[q0][2] = ldfrag(wa + sq * 6144 + 2048, 256); }
            if (m == 2) wf[q0][3] = ldfrag((ca ^ (32 * (sq & 1))) + (sq >> 1) * 12288, 256);
            if (m == 0 || m == 2) {
              const int h = 2 * sq + (m == 2);
              if (n <= 5 && h < 5) { VM(DMA_S(n + 2, h)); ms[n + 2] = vq; }
              if (n == 0 && h == 5) {
                rsrc_t sp = __builtin_amdgcn_make_buffer_rsrc((void*)wphi, 0, 16384, 0x00020000);
                VM(dma16(L_WPHI + (w & 1) * 1024, l16, sp, c * 2048 + (w & 1) * 1024));
              }
              if (n == 4 && h == 5) { VM(xg = ld16_sc1(i16 * 16, sws, WS_XG + (P * 128 + 16 * w) * 16)); mxg = vq; }
              if (n == 6) { VM(DMA_U(sq, (sq < 2 ? LQ_M : LQ_A) + (sq & 1) * 16384, h & 1)); mu[sq] = vq; }
              if (n == 7 && sq < 2) { VM(DMA_U(4 + sq, sq * 16384, h & 1)); mu[4 + sq] = vq; }
            }
          }
          SCHED();
        }
        PIN(acc[0]); PIN(acc[1]); PIN(acc[2]); PIN(csa[0]); PIN(csa[1]);
        if (s == 3) EARLY_STATS(mxr)
      }
    }
    GATHER_XS()
    sts4f(L_CS + w * 128 + i16 * 4, csa[0][0]);
    sts4f(L_CS + w * 128 + 64 + i16 * 4, csa[1][0]);
    BARRIER();
    STAMP(1);
    WAITQ(mu[3]);
    READ_U(0, LQ_M) READ_U(1, LQ_M + 16384) READ_U(2, LQ_A) READ_U(3, LQ_A + 16384)
    const float xr = lds4f(L_XS + (32 * rw + l32) * 4), xm = -lds4f(L_XS + 512 + (32 * rw + l32) * 4);
    u32x4 wp0, wp1;
    {
      const u32 a = L_WPHI + (2 * cw + (lane & 1)) * 512 + (4 * hh + ((lane >> 2) & 3)) * 32 + (2 * (g & 1) + ((lane >> 1) & 1)) * 8;
      const u32x4 f = ldfrag(a, 256);
      const bool own = (lane >> 2) & 1;
#pragma unroll
      for (int e = 0; e < 4; ++e) { wp0[e] = own ? 0u : f[e]; wp1[e] = own ? f[e] : 0u; }
    }
#define FEATURES(e_, dst, hook)                                                                                         \
  {                                                                                                                 \
    u32x4 kp[2];                                                                                                    \
    _Pragma("unroll") for (int a = 0; a < 4; ++a) {                                                                \
      const f32x4 cc = lds16f(L_CS + (2 * (e_) + cw) * 128 + a * 32 + hh * 16);                                      \
      kp[a >> 1][2 * (a & 1)] = pk2v(fma2(lo2(cc), bc2(xm), (f32x2){acc[e_][4 * a], acc[e_][4 * a + 1]}));          \
      kp[a >> 1][2 * (a & 1) + 1] = pk2v(fma2(hi2(cc), bc2(xm), (f32x2){acc[e_][4 * a + 2], acc[e_][4 * a + 3]}));  \
    }                                                                                                               \
    f32x16 pl = mfmaL(wp0, kp[0], zero16);                                                                          \
    pl = mfmaL(wp1, kp[1], pl);                                                                                     \
    hedgehog16(pl, xr, dst, hook);                                                                                  \
  }
    {
      const int s_ = 32 * rw + l32;
      const u32 key = (((s_ >> 1) & 3) ^ (((s_ >> 3) & 1) << 1)) * 16;
      {
        const u32 va = LQ_V + (s_ >> 3) * 1024 + cw * 512 + (s_ & 7) * 64 + hh * 8;
#pragma unroll
        for (int a = 0; a < 4; ++a) {
          const f32x4 cc = lds16f(L_CS + (4 + cw) * 128 + a * 32 + hh * 16);
          sts8(va + ((a * 16) ^ key), (u32x2){pk2v(fma2(lo2(cc), bc2(xm), (f32x2){acc[2][4 * a], acc[2][4 * a + 1]}) * bc2(xr)),
                                              pk2v(fma2(hi2(cc), bc2(xm), (f32x2){acc[2][4 * a + 2], acc[2][4 * a + 3]}) * bc2(xr))});
        }
      }
      WAITQ(mu[5]);
      READ_U(4, 0) READ_U(5, 16384)
      WAITL0();
      u32x4 ph[4];
      auto hk = [&](int i) __attribute__((always_inline)) {
        const int u = 6 + (i >> 1);
        SCHED();
        VM(DMA_U(u, (u < 8 ? LQ_M : (u < 10 ? LQ_A : 0)) + (u & 1) * 16384, i & 1));
        mu[u] = vq;
        SCHED();
      };
      FEATURES(1, ph, hk)
      const u32 pa = LQ_PHI + (s_ >> 3) * 2048 + (2 * cw + hh) * 512 + (s_ & 7) * 64;
#pragma unroll
      for (int a = 0; a < 4; ++a) sts16(pa + ((a * 16) ^ key), ph[a]);
    }
    BARRIER();
    STAMP(2);
    {
      const u32 kx = (((lane >> 3) & 1) ^ ((g & 1) << 1)) * 16;
      const u32 pa = LQ_PHI + g * 2048 + (w >> 1) * 512 + ((lane >> 2) & 3) * 64 + (((2 * (w & 1) + ((lane >> 1) & 1)) * 16) ^ kx) + (lane & 1) * 8;
      const u32 va = LQ_V + g * 1024 + (w >> 2) * 512 + ((lane >> 2) & 3) * 64 + (((2 * ((w >> 1) & 1) + ((lane >> 1) & 1)) * 16) ^ kx) + (lane & 1) * 8;
      u32x4 pf[4], vf[4];
#pragma unroll
      for (int k = 0; k < 4; ++k) {
        pf[k] = cat(ldstr(pa + k * 8192), ldstr((pa ^ 32) + k * 8192 + 256));
        vf[k] = cat(ldstr(va + k * 4096), ldstr((va ^ 32) + k * 4096 + 256));
      }
      f32x4 kv = zero4, kz = zero4;
#pragma unroll
      for (int k = 0; k < 4; ++k) { kv = mfma32(pf[k], vf[k], kv); kz = mfma32(pf[k], ones4, kz); }
      const u32 so = WS_KP + (c * 16 + P) * KP_SZ;
      u32x4 g0 = {__float_as_uint(kv[0]), tlo, __float_as_uint(kv[1]), tlo}, g1 = {__float_as_uint(kv[2]), tlo, __float_as_uint(kv[3]), tlo};
      u32x4 g2 = {__float_as_uint((lane & 1) ? kz[2] : kz[0]), tlo, __float_as_uint((lane & 1) ? kz[3] : kz[1]), tlo};
      PIN(g0); PIN(g1); PIN(g2);
      asm volatile("s_nop 7" ::: "memory");
      VM(st16_sc1(g0, l16, sws, so + w * 2048));
      VM(st16_sc1(g1, l16, sws, so + w * 2048 + 1024));
      VM(st16_sc1(g2, g * 32 + (lane & 1) * 16 + ((i16 >> 1) << 26), sws, so + 16384 + w * 128));
    }
    STAMP(3);
    WAITQ(mu[11]);
    READ_U(6, LQ_M) READ_U(7, LQ_M + 16384) READ_U(8, LQ_A) READ_U(9, LQ_A + 16384) READ_U(10, 0) READ_U(11, 16384)
    BARRIER();
    ISS_Q(12, LQ_M); VM(DMA_U(13, LQ_M + 16384, 0));
    u32x4 phq[4];
    {
      VWO();
      auto hk = [&](int i) __attribute__((always_inline)) {
        SCHED();
        if (i == 0) DMA_U(13, LQ_M + 16384, 1);
        else if (i < 5) DMA_U(14 + ((i - 1) >> 1), LQ_A + ((i - 1) >> 1) * 16384, (i - 1) & 1);
        else DMA_WOUT(i - 5)
        SCHED();
      };
      FEATURES(0, phq, hk)
    }
    PIN(phq[0]); PIN(phq[1]); PIN(phq[2]); PIN(phq[3]);
    STAMP(4);
    if (w >= 5) {
      const int id = 2 * P + (w == 7 ? (lane & 1) : w - 5), wq = id >> 2, gq = id & 3;
      const u32 vi0 = (4 * g) * KP_SZ + (w == 7 ? 16384 + wq * 128 + gq * 32 : wq * 2048 + (gq * 16 + i16) * 16);
      const u32 vi1 = vi0 + (w == 7 ? 16 : 1024);
      const u32 vo = (w == 7 ? (WS_KZ - WS_KF) + c * 512 + id * 16 + ((i16 >> 1) << 25) : c * 8192 + id * 256 + i16 * 16) + (g << 28);
      float sa[4];
#pragma nounroll
      for (int it = 0; it < SPIN_CAP; ++it) {
        u32x4 a[4][2];
#pragma unroll
        for (int o = 0; o < 4; ++o) {
          a[o][0] = ld16_sc1(vi0, sws, WS_KP + (c * 16 + o) * KP_SZ);
          a[o][1] = ld16_sc1(vi1, sws, WS_KP + (c * 16 + o) * KP_SZ);
        }
        WAITV(0);
        bool ok = true;
#pragma unroll
        for (int o = 0; o < 4; ++o) { PIN(a[o][0]); PIN(a[o][1]); ok = ok && tag_ok(a[o][0], tlo) && tag_ok(a[o][1], tlo); }
#pragma unroll
        for (int e = 0; e < 4; ++e)
          sa[e] = (__uint_as_float(a[0][e >> 1][2 * (e & 1)]) + __uint_as_float(a[1][e >> 1][2 * (e & 1)])) +
                  (__uint_as_float(a[2][e >> 1][2 * (e & 1)]) + __uint_as_float(a[3][e >> 1][2 * (e & 1)]));
        if (__all(ok)) break;
        __builtin_amdgcn_s_sleep(POLL_SLEEP);
      }
      red2g(sa[0], sa[1]);
      red2g(sa[2], sa[3]);
      st16_sc1((u32x4){pk2(sa[0], sa[1]), tlo, pk2(sa[2], sa[3]), tlo}, vo, sws, WS_KF);
    }
    gf = ld8_sc1((lane >> 5) * 1024, sws, WS_FA + (16 * w + c2) * 128);
    WAITV(8);
    READ_U(12, LQ_M) READ_U(13, LQ_M + 16384) READ_U(14, LQ_A) READ_U(15, LQ_A + 16384)
    BARRIER();
#pragma unroll
    for (int a = 0; a < 4; ++a) sts16(L_WO1 + w * 4096 + a * 1024 + l16, phq[a]);
    WAITV(0);
    PIN(gf);
    if (!__all(gf[0] == tlo && gf[1] == thi)) poll_flags((lane >> 5) * 1024, true, sws, WS_FA + (16 * w + c2) * 128, tlo, thi);
    STAMP(6);
    { const u32 so_ = zb0 + TILE(0, gn) * 65536; LOAD_B(0, so_) }
  }

  {
  RELANE();
  u32x4 kfg, kzg;
  const u32 kfo = WS_KF + c * 8192 + w * 1024, kzo = WS_KZ + c * 512 + w * 64;
  if (t != 0) {
    kfg = ld16_sc1(l16, sws, kfo);
    kzg = ld16_sc1(g * 16, sws, kzo);
  }
  { const u32 so_ = zb0 + TILE(1, gn) * 65536; LOAD_B(1, so_) }
  STAMP(9);
  const u32 redw = opq(L_RED + w * 4096 + lane * 16), redr = opq(L_RED + (rw ^ (2 * ux0)) * 1024 + lane * 16);
  const u32 fd = WS_FD + (p * 16 + c2) * 128;
  const u32 f3s = __builtin_amdgcn_readfirstlane(WS_FB + (P * 8 + (c2 > 0 ? c2 - 1 : 0)) * 128);
  const u32 vw3 = ((lane >> 2) & 7) * 1024 + c2 * 128 + (lane >> 5) * 64;
  const rsrc_t swo = __builtin_amdgcn_make_buffer_rsrc((void*)wo, 0, 512 * 1024, 0x00020000);
#define DMA_WO(dst, half, e) dma16((dst) + w * 4096 + (e) * 1024, (vw3 + (lane & 3) * 16) ^ (((e) & 1) * 32), swo, ((w + 8 * (half)) * 4 + (e)) * 8192)
#define FV() ((lane & 32) ? (P * 8 + ((lane >> 2) & 7)) * 128 : (WS_FD - WS_FC) + (p * 16 + (((lane >> 1) & 14) | cw)) * 128)
  u32x2 z1r[6], hold;
  u32 bs;
  f32x4 sm0;
#define EMIT_H(jt, sm_)                                                                                             \
  {                                                                                                                 \
    const f32x2 bb = bc2(blo(bs));                                                                                  \
    const u32 h0 = pk2v((f32x2){blo(z1r[jt][0]), bhi(z1r[jt][0])} * (lo2(sm_) + bb));                               \
    const u32 h1 = pk2v((f32x2){blo(z1r[jt][1]), bhi(z1r[jt][1])} * (hi2(sm_) + bb));                               \
    const u32 hb_ = WS_HF + (((4 * p + rw) * 2 + ((jt) >> 1)) * 8 + c2) * 1024;                                     \
    if ((jt) == 0 || ((jt) == 2 && gn == 6)) hold = (u32x2){h0, h1};                                                \
    else if ((jt) == 1 || ((jt) == 3 && gn == 6)) st16_sc1((u32x4){hold[0], hold[1], h0, h1}, l16, sws, hb_);       \
    else if ((jt) == 2) st16_sc1((u32x4){h0, h1, 0u, 0u}, l16, sws, hb_);                                           \
    else st16_sc1((u32x4){h0, tlo, h1, tlo}, l16, sws, WS_HL + (((4 * p + rw) * 8 + c2) * 2 + (gn - 1 - (jt))) * 1024); \
  }
#pragma unroll
  for (int j = 0; j < 6; ++j) {
    if (j < 5 || gn == 6) {
      const int u = j & 1;
      if (j == 0) WAITV(8);
      if (j == 2) WAITV(t ? 10 : 8);
      if (j == 1) { WAITV(9); WAITV_IF(cw, 1, 8); }
      if (j == 3) { WAITV(9); WAITV_IF(cw, 1, 8); }
      if (j == 4) { if (t) WAITV(4); else { WAITV(12); WAITV_IF(gn, 5, 4); } }
      if (j == 5) { WAITV(10); WAITV_IF(cw, 1, 9); }
#pragma unroll
      for (int i = 0; i < 8; ++i) PIN(bfr[u][i]);
      {
        f32x4 a_[4];
#pragma unroll
        for (int rt = 0; rt < 4; ++rt) {
          a_[rt] = zero4;
#pragma unroll
          for (int i = 0; i < 8; ++i) a_[rt] = mfma32(bfr[u][i], af[rt][i], a_[rt]);
          SCHED();
          if (rt > 0) sts16f(redw + u * 32768 + (rt - 1) * 1024, a_[rt - 1]);
          SCHED();
        }
        if (j == 0 && t != 0) {
          PIN(kfg); PIN(kzg);
          poll_gran(kfg, l16, sws, kfo, tlo);
          poll_gran(kzg, g * 16, sws, kzo, tlo);
          sts8(L_KFS + (w >> 1) * 1024 + i16 * 64 + (w & 1) * 32 + g * 8, (u32x2){kfg[0], kfg[2]});
          sts8(L_KFS + 4096 + w * 32 + g * 8, (u32x2){kzg[0], kzg[2]});
          sts8(L_KFS + 4352 + (lane & 7) * 8, (u32x2){0u, 0u});
        }
        sts16f(redw + u * 32768 + 3072, a_[3]);
      }
      u32x4 of[2];
      if (j == 1 && t != 0) {
        const int l32 = lane & 31, hh = lane >> 5;
        const bool own = ((lane >> 2) & 1) == hh;
        const u32 ka = own ? L_KFS + (2 * cw + hh) * 1024 + ((l32 & 3) + 4 * (l32 >> 3)) * 64 : L_KFS + 4352;
        const u32 zaa = own ? L_KFS + 4096 + (2 * cw + hh) * 64 : L_KFS + 4352;
        const u32 qa = L_WO1 + w * 4096 + l16;
        const f32x16 zero16 = {0, 0, 0, 0, 0, 0, 0, 0, 0, 0, 0, 0, 0, 0, 0, 0};
        f32x16 den = zero16, num = zero16;
#pragma unroll
        for (int a = 0; a < 4; ++a) {
          const u32x4 pq_ = lds16(qa + a * 1024);
          den = mfmaL(lds16(zaa + a * 16), pq_, den);
          num = mfmaL(lds16(ka + a * 16), pq_, num);
        }
        const float inv = __builtin_amdgcn_rcpf(den[0] + 1e-6f);
#pragma unroll
        for (int a = 0; a < 2; ++a) {
          of[a] = (u32x4){pk2(num[8 * a] * inv, num[8 * a + 1] * inv), pk2(num[8 * a + 2] * inv, num[8 * a + 3] * inv),
                          pk2(num[8 * a + 4] * inv, num[8 * a + 5] * inv), pk2(num[8 * a + 6] * inv, num[8 * a + 7] * inv)};
          PIN(of[a]);
        }
        STAMP(5);
      }
      BAR_IF(cw, 1, 63);
      if (w == 4) {
        if (j == 0 && t == 0) SET_FLAG(WS_FB + (P * 8 + c) * 128);
        if (j == 3) SET_FLAG(fd);
        if (j == 3 && t != 0) SET_FLAG(WS_FC + (P * 8 + c) * 128);
        if ((j == 4 && gn == 5) || j == 5) SET_FLAG(fd + 1024);
      }
      if (j == 1) {
        if (cw == 0) {
          WAITV(0);
          PIN(gf);
          if (!__all(gf[0] == tlo && gf[1] == thi)) poll_flags((lane >> 5) * 128, true, sws, f3s, tlo, thi);
          rsrc_t sb = __builtin_amdgcn_make_buffer_rsrc((void*)bsp, 0, 4096, 0x00020000);
          bs = ld2(i16 * 2, sb, (64 * p + 16 * rw) * 2);
        }
      }
      if ((j == 1 || j == 2) && cw == 0) {
#pragma unroll
        for (int v = 3 * j - 3; v < 3 * j; ++v)
          z1r[v] = ld8_sc1(l16, sws, WS_Z1 + ((2 * p + (rw >> 1)) * 43 + gs + TILE(min(v, gn - 1), gn)) * 1024 + (rw & 1) * 8);
      }
      if ((j == 4 && gn == 5) || j == 5) gf = ld8_sc1(FV(), sws, WS_FC);
      if (j == 4 && t == 0) {
        VWO();
#pragma unroll
        for (int e = 2; e < 7; ++e) DMA_WOUT(e)
      }
      const u32 ra_ = redr + ((j + 1) & 1) * 32768;
      f32x4 sm, r_[2];
#pragma unroll
      for (int i = 0; i < 8; ++i) {
        if (j >= 1 && j < (t ? 3 : 4) && (i & 1) == 0 && cw == 0) {
          if (i == 2) sm = r_[0] + r_[1];
          if (i > 2) sm += r_[0] + r_[1];
          r_[0] = lds16f(ra_ + i * 4096); r_[1] = lds16f(ra_ + (i + 1) * 4096);
        }
        SCHED();
        if (j < 3) bfr[u][i] = ld16_b(l16, sws, (zb0 + TILE(j + 2, gn) * 65536) ^ (i * 1024));
        else if (j == 3 && t == 0) ld16_b_if6(bfr[1][i], gn, l16, sws, (zb0 + TILE(5, gn) * 65536) ^ (i * 1024));
        SCHED();
      }
      if (j == 0 && cw == 0) gf = ld8_sc1((lane >> 5) * 128, sws, f3s);
      if (j == 1 && t != 0) {
        const u32 vo = ((lane >> 4) & 1) * 16384 + (lane >> 5) * 512 + i16 * 16;
        st16_sc1(of[0], vo, sws, WS_OF + ((8 * P + 2 * rw) * 16 + 2 * c + cw) * 1024);
        st16_sc1(of[1], vo, sws, WS_OF + ((8 * P + 2 * rw) * 16 + 2 * c + cw) * 1024 + 256);
      }
      if (j == 2) { PIN(bs); _Pragma("unroll") for (int v = 0; v < 3; ++v) PIN(z1r[v]); }
      if (j == 3) { _Pragma("unroll") for (int v = 3; v < 6; ++v) PIN(z1r[v]); }
      if (j >= 1 && cw == 0) {
        if (j < (t ? 3 : 4)) sm += r_[0] + r_[1];
        else {
          f32x4 q_[8];
#pragma unroll
          for (int o = 0; o < 8; ++o) q_[o] = lds16f(ra_ + o * 4096);
          SCHED();
          sm = ((q_[0] + q_[1]) + (q_[2] + q_[3])) + ((q_[4] + q_[5]) + (q_[6] + q_[7]));
        }
        if (j == 1) sm0 = sm;
        if (j == 2) EMIT_H(0, sm0)
        if (j >= 2) EMIT_H(j >= 2 ? j - 1 : 0, sm)
      }
      if (j == 3) {
#pragma unroll
        for (int e = 0; e < 4; ++e) DMA_WO(L_WO1, 0, e);
      }
      if (j == 3 && cw == 0) { if (t) WAITV(5); else { WAITV(12); WAITV_IF(gn, 5, 5); } }
      if (j == 5) WAITV(2);
      BAR_IF(cw, 0, 63);
    }
  }
  {
    STAMP(10);
    const u32 bufA = L_RED + (gn & 1) * 32768, bufB = L_RED + 32768 - (gn & 1) * 32768;
    const u32 fv = FV();
    if (cw == 0) {
      const u32 ra_ = redr + (bufB - L_RED);
      f32x4 q_[8];
#pragma unroll
      for (int o = 0; o < 8; ++o) q_[o] = lds16f(ra_ + o * 4096);
      SCHED();
      const f32x4 sm = ((q_[0] + q_[1]) + (q_[2] + q_[3])) + ((q_[4] + q_[5]) + (q_[6] + q_[7]));
      if (gn == 6) EMIT_H(5, sm) else EMIT_H(4, sm)
    }
    WAITV(2); WAITV_IF(cw, 1, 0);
    PIN(gf);
    {
      const bool n0 = (lane & 48) != 16;
      if (!__all(!n0 || (gf[0] == tlo && gf[1] == thi))) poll_flags(fv, n0, sws, WS_FC, tlo, thi);
    }
    RELANE();
    u32x4 hf[8], of[8];
    const u32 ofb = WS_OF + ((4 * p + rw) * 16 + cw) * 1024, hfb = WS_HF + ((4 * p + rw) * 16 + cw) * 1024;
#pragma unroll
    for (int e = 0; e < 4; ++e) of[e] = ld16_b(l16, sws, ofb + e * 2048);
#pragma unroll
    for (int e = 0; e < 4; ++e) hf[e] = ld16_b(l16, sws, hfb + e * 2048);
    BARRIER();
    const u32 qb = ((lane >> 2) & 3) * 64 + ((((g & 1) << 1) | ((lane >> 1) & 1)) * 16) + (lane & 1) * 8;
    f32x4 a3[4];
#pragma unroll
    for (int ct = 0; ct < 4; ++ct) a3[ct] = zero4;
    u32x4 wf[3][4], hl[4][2];
#define RDW2(dst, lo0, hi0, offl, offh) _Pragma("unroll") for (int ct = 0; ct < 4; ++ct) dst[ct] = cat(ldstr(((lo0) ^ ((ct & 1) * 32)) + (offl) + (ct >> 1) * 512), ldstr(((hi0) ^ ((ct & 1) * 32)) + (offh) + (ct >> 1) * 512))
#define MM3(src, frag) _Pragma("unroll") for (int ct = 0; ct < 4; ++ct) a3[ct] = mfma32(src[ct], frag, a3[ct])
    const u32 lb = WS_HL + ((4 * p + rw) * 8 + cw) * 2048;
    const rsrc_t sxo = __builtin_amdgcn_make_buffer_rsrc((void*)(x + (size_t)p * 65536), 0, 65536, 0x00020000);
    const u32 w0 = opq(L_WOUT + qb + (g >> 1) * 1024 + (g & 1) * 256);
    const u32 wa = w0 + cw * 4096, wb = w0 + cw * 2048;
    const u32 oq = qb + g * 1024 + cw * 4096;
    const u32 oa = opq(L_WO1 + oq), ob = opq(bufA + oq);
    u32x4 xres;
    VWO();
#pragma unroll
    for (int e = 0; e < 4; ++e) DMA_WO(bufA, 1, e);
    STAMP(11);
#define RDK(dst, k)                                                                                                 \
  if ((k) < 4) { RDW2(dst, oa, oa, (k) * 8192, (k) * 8192 + 256); }                                                 \
  else if ((k) < 8) { RDW2(dst, wa, wa, ((k) - 4) * 8192, ((k) - 4) * 8192 + 2048); }                               \
  else if ((k) < 12) { RDW2(dst, ob, ob, ((k) - 8) * 8192, ((k) - 8) * 8192 + 256); }                               \
  else { RDW2(dst, wb, w0, 32768 + ((k) - 12) * 4096, 49152 + ((k) - 12) * 2048); }
    RDK(wf[0], 0)
    RDK(wf[1], 1)
#pragma unroll
    for (int k = 0; k < 16; ++k) {
      if (k == 0) WAITV(8);
      if (k == 4) WAITV(13);
      if (k == 6) {
        WAITV(0);
        PIN(gf);
        if (!__all(gf[0] == tlo && gf[1] == thi)) poll_flags(fv, true, sws, WS_FC, tlo, thi);
#pragma unroll
        for (int e = 4; e < 8; ++e) hf[e] = ld16_b(l16, sws, hfb + e * 2048);
        BARRIER();
        STAMP(12);
      }
      if (k == 12) WAITV(9);
      if (k + 2 < 16) { RDK(wf[(k + 2) % 3], k + 2) }
      SCHED();
      if (k < 4) { PIN(of[k]); MM3(wf[k % 3], of[k]); }
      else if (k < 8) { PIN(hf[k - 4]); MM3(wf[k % 3], hf[k - 4]); }
      else if (k < 12) { PIN(of[k - 4]); MM3(wf[k % 3], of[k - 4]); }
      else { PIN(hf[k - 8]); MM3(wf[k % 3], hf[k - 8]); }
      SCHED();
      if (k < 4) {
        of[4 + k] = ld16_b(l16, sws, ofb + (4 + k) * 2048);
        DMA_WOUT_AT(bufB + w * 4096, 7 + k)
        if (k == 3) gf = ld8_sc1(fv, sws, WS_FC);
      } else if (k >= 6 && k < 10) {
        hl[k - 6][0] = ld16_sc1(l16, sws, lb + (k - 6) * 4096);
        hl[k - 6][1] = ld16_sc1(l16, sws, lb + (k - 6) * 4096 + 1024);
      } else if (k == 10) {
        xres = ld16((tid >> 3) * 1024 + (tid & 7) * 16, sxo, c2 * 128);
      }
    }
    STAMP(13);
    {
      const u32 wl_ = opq(bufB + (w0 - L_WOUT) + cw * 4096);
      u32x4 wg[4][4];
#pragma unroll
      for (int e = 0; e < 4; ++e) RDW2(wg[e], wl_, wl_, e * 8192, e * 8192 + 2048);
      WAITV(0);
      PIN(xres);
#pragma unroll
      for (int e = 0; e < 4; ++e) { PIN(hl[e][0]); PIN(hl[e][1]); }
#pragma nounroll
      for (int it = 0; it < SPIN_CAP; ++it) {
        u32 bad = 0;
#pragma unroll
        for (int e = 0; e < 8; ++e) bad |= (hl[e >> 1][e & 1][1] ^ tlo) | (hl[e >> 1][e & 1][3] ^ tlo);
        if (__all(bad == 0)) break;
        __builtin_amdgcn_s_sleep(2);
        asm volatile(
            "buffer_load_dwordx4 %0, %8, %9, %10 offen sc1\n\tbuffer_load_dwordx4 %1, %8, %9, %10 offen offset:1024 sc1\n\t"
            "buffer_load_dwordx4 %2, %8, %9, %11 offen sc1\n\tbuffer_load_dwordx4 %3, %8, %9, %11 offen offset:1024 sc1\n\t"
            "buffer_load_dwordx4 %4, %8, %9, %12 offen sc1\n\tbuffer_load_dwordx4 %5, %8, %9, %12 offen offset:1024 sc1\n\t"
            "buffer_load_dwordx4 %6, %8, %9, %13 offen sc1\n\tbuffer_load_dwordx4 %7, %8, %9, %13 offen offset:1024 sc1\n\t"
            "s_waitcnt vmcnt(0)"
            : "=&v"(hl[0][0]), "=&v"(hl[0][1]), "=&v"(hl[1][0]), "=&v"(hl[1][1]), "=&v"(hl[2][0]), "=&v"(hl[2][1]), "=&v"(hl[3][0]), "=&v"(hl[3][1])
            : "v"(l16), "s"(sws), "s"(lb), "s"(lb + 4096), "s"(lb + 8192), "s"(lb + 12288)
            : "memory");
      }
#pragma unroll
      for (int e = 0; e < 4; ++e) MM3(wg[e], ((u32x4){hl[e][0][0], hl[e][0][2], hl[e][1][0], hl[e][1][2]}));
    }
    STAMP(14);
    const u32 fin = L_WO1 - 2048;
#pragma unroll
    for (int ct = 0; ct < 4; ++ct) sts16f(fin + 2048 + cw * 16384 + (16 * rw + i16) * 256 + (((4 * ct + g) ^ i16) * 16), a3[ct]);
    BARRIER();
    {
      const int row = tid >> 3;
      const u32 a = fin + 2048 + row * 256 + (((2 * (tid & 7)) ^ (row & 15)) * 16);
      const f32x4 s0 = lds16f(a) + lds16f(a + 16384), s1 = lds16f(a ^ 16) + lds16f((a ^ 16) + 16384);
      u32x4 o = {pk2(blo(xres[0]) + s0[0], bhi(xres[0]) + s0[1]), pk2(blo(xres[1]) + s0[2], bhi(xres[1]) + s0[3]),
                 pk2(blo(xres[2]) + s1[0], bhi(xres[2]) + s1[1]), pk2(blo(xres[3]) + s1[2], bhi(xres[3]) + s1[3])};
      st16_sc1(o, row * 1024 + (tid & 7) * 16, __builtin_amdgcn_make_buffer_rsrc((void*)(out + (size_t)p * 65536), 0, 65536, 0x00020000), c2 * 128);
    }
  }
  }
  if (b == 0 && w == 0 && l16 == 0) *(u64*)(ws + WS_EP) = ((u64)thi << 32) | tlo;
  STAMP(15);
  STAMPC(31);
}

__global__ __launch_bounds__(512, 2) void fused_kernel(const char* __restrict__ x, const char* __restrict__ wqkv, const char* __restrict__ wphi,
                                                        const char* __restrict__ wo, const char* __restrict__ win, const char* __restrict__ wsp,
                                                        const char* __restrict__ bsp, const char* __restrict__ wout, char* __restrict__ out, char* ws) {
  __shared__ __attribute__((aligned(1024))) char smem[163840];
  asm volatile("" ::"v"((u32)(size_t)(LDSP char*)smem));
  asm volatile("" ::"s"(x), "s"(wqkv), "s"(win), "s"(ws), "s"(wsp));
  u64 pc, ep;
  asm volatile("s_getpc_b64 %0" : "=s"(pc));
  {
    const int lane = threadIdx.x & 63, g = lane >> 4;
    const int w = __builtin_amdgcn_readfirstlane(threadIdx.x >> 6);
    const int r = blockIdx.x >> 3, P = ((blockIdx.x & 7) >> 1) * 4 + (r >> 3);
    const int rot = 2 * (((r >> 3) + (r & 3)) & 3);
    u32 acv = 0, acb = 0; (void)acv; (void)acb;
    STAMP(0);
    STAMPC(30);
    if (CODE_PF && w == 7)
      touch(L_JUNK, lane * 128, __builtin_amdgcn_make_buffer_rsrc((void*)(pc & ~255ull), 0, CODE_PF, 0x00020000), (r & 7) * 8192);
    const rsrc_t sx = __builtin_amdgcn_make_buffer_rsrc((void*)(x + (size_t)P * 131072), 0, 131072, 0x00020000);
    const u32 vx0 = (16 * w + (lane >> 3)) * 1024 + (((lane & 7) ^ g) * 16);
    const u32 vx1 = (vx0 + 8192) ^ 64;
    const u32 w2k = w * 2048;
    DMA_X(0, 0, 0);
    DMA_X(0, 0, 1);
    asm volatile("s_load_dwordx2 %0, %1, 0x0" : "=s"(ep) : "s"(ws) : "memory");
    SCHED();
  }
  if (__builtin_expect(((blockIdx.x >> 5) & 1) == 0, 1)) run<0>(x, wqkv, wphi, wo, win, wsp, bsp, wout, out, ws, ep);
  else run<1>(x, wqkv, wphi, wo, win, wsp, bsp, wout, out, ws, ep);
}

extern "C" void kernel_launch(void* const* d_in, const int* in_sizes, int n_in, void* d_out, int out_size, void* d_ws, size_t ws_size,
                              hipStream_t stream) {
  fused_kernel<<<256, 512, 0, stream>>>((const char*)d_in[0], (const char*)d_in[1], (const char*)d_in[2], (const char*)d_in[3],
                                        (const char*)d_in[5], (const char*)d_in[6], (const char*)d_in[7], (const char*)d_in[8], (char*)d_out,
                                        (char*)d_ws);
}
```

```cpp
#include <hip/hip_runtime.h>
#include <hip/hip_bf16.h>

typedef unsigned int u32;
typedef unsigned long long u64;
typedef short s16x4 __attribute__((ext_vector_type(4)));
typedef __bf16 bf16x2 __attribute__((ext_vector_type(2)));
typedef __bf16 bf16x8 __attribute__((ext_vector_type(8)));
typedef float f32x16 __attribute__((ext_vector_type(16)));
typedef float f32x4 __attribute__((ext_vector_type(4)));
typedef float f32x2 __attribute__((ext_vector_type(2)));
typedef u32 u32x4 __attribute__((ext_vector_type(4)));
typedef u32 u32x2 __attribute__((ext_vector_type(2)));
typedef __amdgpu_buffer_rsrc_t rsrc_t;
#define LDSP __attribute__((address_space(3)))
#define DI __device__ __forceinline__

#define STAMPS 0

constexpr u32 WS_EP = 0;
constexpr u32 WS_FA = 8192;
constexpr u32 WS_FB = WS_FA + 16384;
constexpr u32 WS_FC = WS_FB + 16384;
constexpr u32 WS_FD = WS_FC + 16384;
constexpr u32 WS_XG = WS_FD + 262144;
constexpr u32 WS_SG = WS_XG + 32768;
constexpr u32 WS_ZB = WS_SG + 524288;
constexpr u32 WS_Z1 = WS_ZB + 43 * 65536;
constexpr u32 KP_SZ = 17408;
constexpr u32 WS_KP = WS_Z1 + 64 * 43 * 1024;
constexpr u32 WS_KF = WS_KP + 128 * KP_SZ;
constexpr u32 WS_KZ = WS_KF + 65536;
constexpr u32 WS_OF = WS_KZ + 4096;
constexpr u32 WS_HF = WS_OF + 128 * 16384;
constexpr u32 WS_HL = WS_HF + 128 * 16 * 1024;
constexpr u32 WS_ST = WS_HL + 128 * 16 * 1024;
constexpr u32 WS_END = WS_ST + 256 * 8 * 32 * 8;

constexpr u32 LZ_W = 65536;
constexpr u32 LZ_U = 139264;
constexpr u32 LQ_M = 122880;
constexpr u32 LQ_A = 81920;
constexpr u32 LQ_PHI = 49152;
constexpr u32 LQ_V = 32768;
constexpr u32 L_TAB = 155648;
constexpr u32 L_XS = L_TAB;
constexpr u32 L_CS = L_TAB + 1024;
constexpr u32 L_ZST = L_TAB + 3072;
constexpr u32 L_WPHI = L_TAB + 3072;
constexpr u32 L_KFS = L_TAB + 2048;
constexpr u32 L_JUNK = L_TAB + 7168;
constexpr u32 L_WOUT = 0;
constexpr u32 L_RED = 57344;
constexpr u32 L_WO1 = 122880;

constexpr u64 Z2S = 0x26211B16110B0500ull;
constexpr u64 Z1S = 0x26211C16110C0600ull;
DI int grp_start(u64 tab, int c) { return c >= 8 ? 43 : (int)((tab >> (8 * c)) & 0xFF); }

#define SCHED() __builtin_amdgcn_sched_barrier(0)
#define TIC()
#define TOC(acc)
#define WAITV(n) do { TIC(); asm volatile("s_waitcnt vmcnt(%0)" ::"n"(n) : "memory"); TOC(acv); SCHED(); } while (0)
#define BARRIER() do { SCHED(); TIC(); asm volatile("s_waitcnt lgkmcnt(0)\n\ts_barrier" ::: "memory"); TOC(acb); SCHED(); } while (0)
#define WAITV_IF(g_, v, n) do { TIC(); asm volatile("s_cmp_lg_u32 %0, %1\n\ts_cbranch_scc1 1f\n\ts_waitcnt vmcnt(%2)\n1:" ::"s"(g_), "n"(v), "n"(n) : "scc", "memory"); TOC(acv); SCHED(); } while (0)
#define BAR_IF(g_, v, n) do { SCHED(); asm volatile("s_cmp_lg_u32 %0, %1\n\ts_cbranch_scc1 1f\n\ts_waitcnt vmcnt(%2) lgkmcnt(0)\n\ts_barrier\n1:" ::"s"(g_), "n"(v), "n"(n) : "scc", "memory"); SCHED(); } while (0)
#define WAITL0() asm volatile("s_waitcnt lgkmcnt(0)" ::: "memory")
#define PIN(x) asm volatile("" : "+v"(x))

DI void dma16(u32 lds, u32 voff, rsrc_t srd, u32 soff) {
  asm volatile("s_mov_b32 m0, %0\n\ts_nop 0\n\tbuffer_load_dwordx4 %1, %2, %3 offen lds" ::"s"(lds), "v"(voff), "s"(srd), "s"(soff) : "memory");
}
#define DMA_C(wb, CST, voff, srd, soff) asm volatile("s_add_u32 m0, %0, %1\n\ts_nop 0\n\tbuffer_load_dwordx4 %2, %3, %4 offen lds" ::"s"(wb), "n"(CST), "v"(voff), "s"(srd), "s"(soff) : "scc", "memory")
#define DMA_CC(wb, CST, voff, srd, OP, sb, SC) do { u32 so__; asm volatile("s_add_u32 m0, %1, %2\n\t" OP " %0, %5, %6\n\tbuffer_load_dwordx4 %3, %4, %0 offen lds" : "=&s"(so__) : "s"(wb), "n"(CST), "v"(voff), "s"(srd), "s"(sb), "n"(SC) : "scc", "memory"); } while (0)
DI void touch(u32 lds, u32 voff, rsrc_t srd, u32 soff) {
  asm volatile("s_mov_b32 m0, %0\n\ts_nop 0\n\tbuffer_load_dword %1, %2, %3 offen lds" ::"s"(lds), "v"(voff), "s"(srd), "s"(soff) : "memory");
}
DI u32x4 ld16_sc1(u32 voff, rsrc_t srd, u32 soff) {
  u32x4 r;
  asm volatile("buffer_load_dwordx4 %0, %1, %2, %3 offen sc1" : "=v"(r) : "v"(voff), "s"(srd), "s"(soff) : "memory");
  return r;
}
template <int OFF> DI u32x4 ld16_sc1o(u32 voff, rsrc_t srd, u32 soff) {
  u32x4 r;
  asm volatile("buffer_load_dwordx4 %0, %1, %2, %3 offen offset:%4 sc1" : "=v"(r) : "v"(voff), "s"(srd), "s"(soff), "n"(OFF) : "memory");
  return r;
}
#define BPOL " sc1"
DI u32x4 ld16_b(u32 voff, rsrc_t srd, u32 soff) {
  u32x4 r;
  asm volatile("buffer_load_dwordx4 %0, %1, %2, %3 offen" BPOL : "=v"(r) : "v"(voff), "s"(srd), "s"(soff) : "memory");
  return r;
}
template <int OFF> DI u32x4 ld16_bo(u32 voff, rsrc_t srd, u32 soff) {
  u32x4 r;
  asm volatile("buffer_load_dwordx4 %0, %1, %2, %3 offen offset:%4" BPOL : "=v"(r) : "v"(voff), "s"(srd), "s"(soff), "n"(OFF) : "memory");
  return r;
}
DI void ld16_b_if6(u32x4& r, int gn, u32 voff, rsrc_t srd, u32 soff) {
  asm volatile("s_cmp_lg_u32 %4, 6\n\ts_cbranch_scc1 1f\n\tbuffer_load_dwordx4 %0, %1, %2, %3 offen" BPOL "\n1:" : "+v"(r) : "v"(voff), "s"(srd), "s"(soff), "s"(gn) : "scc", "memory");
}
DI u32x4 ld16(u32 voff, rsrc_t srd, u32 soff) {
  u32x4 r;
  asm volatile("buffer_load_dwordx4 %0, %1, %2, %3 offen" : "=v"(r) : "v"(voff), "s"(srd), "s"(soff) : "memory");
  return r;
}
DI u32x2 ld8_sc1(u32 voff, rsrc_t srd, u32 soff) {
  u32x2 r;
  asm volatile("buffer_load_dwordx2 %0, %1, %2, %3 offen sc1" : "=v"(r) : "v"(voff), "s"(srd), "s"(soff) : "memory");
  return r;
}
DI u32 ld2(u32 voff, rsrc_t srd, u32 soff) {
  u32 r;
  asm volatile("buffer_load_ushort %0, %1, %2, %3 offen" : "=v"(r) : "v"(voff), "s"(srd), "s"(soff) : "memory");
  return r;
}
DI void st16_sc1(u32x4 v, u32 voff, rsrc_t srd, u32 soff) {
  asm volatile("buffer_store_dwordx4 %0, %1, %2, %3 offen sc1\n\ts_nop 1" ::"v"(v), "v"(voff), "s"(srd), "s"(soff) : "memory");
}
DI void st8_sc1(u32x2 v, u32 voff, rsrc_t srd, u32 soff) {
  asm volatile("buffer_store_dwordx2 %0, %1, %2, %3 offen sc1" ::"v"(v), "v"(voff), "s"(srd), "s"(soff) : "memory");
}

DI u32x4 lds16(u32 a) { return *(const LDSP u32x4*)(size_t)a; }
DI u32x2 lds8(u32 a) { return *(const volatile LDSP u32x2*)(size_t)a; }
DI float lds4f(u32 a) { return *(const LDSP float*)(size_t)a; }
DI f32x4 lds16f(u32 a) { return *(const LDSP f32x4*)(size_t)a; }
DI void sts16f(u32 a, f32x4 v) { *(LDSP f32x4*)(size_t)a = v; }
DI void sts16(u32 a, u32x4 v) { *(LDSP u32x4*)(size_t)a = v; }
DI void sts8(u32 a, u32x2 v) { *(volatile LDSP u32x2*)(size_t)a = v; }
DI void sts4f(u32 a, float v) { *(LDSP float*)(size_t)a = v; }
DI u32x2 ldstr(u32 a) {
  return __builtin_bit_cast(u32x2, __builtin_amdgcn_ds_read_tr16_b64_v4i16((LDSP s16x4*)(size_t)a)); }
DI u32x4 cat(u32x2 lo, u32x2 hi) { return (u32x4){lo[0], lo[1], hi[0], hi[1]}; }
DI u32x4 ldfrag(u32 a, u32 d) { u32x2 lo = ldstr(a), hi = ldstr(a + d); return cat(lo, hi); }

DI f32x4 mfma32(u32x4 a, u32x4 b, f32x4 c) {
  return __builtin_amdgcn_mfma_f32_16x16x32_bf16(__builtin_bit_cast(bf16x8, a), __builtin_bit_cast(bf16x8, b), c, 0, 0, 0);
}
DI f32x16 mfmaL(u32x4 a, u32x4 b, f32x16 c) {
  return __builtin_amdgcn_mfma_f32_32x32x16_bf16(__builtin_bit_cast(bf16x8, a), __builtin_bit_cast(bf16x8, b), c, 0, 0, 0);
}
DI u32 pk2(float a, float b) { bf16x2 v = {(__bf16)a, (__bf16)b}; return __builtin_bit_cast(u32, v); }
DI float blo(u32 u) { return __uint_as_float(u << 16); }
DI float bhi(u32 u) { return __uint_as_float(u & 0xffff0000u); }
DI float rbf(float a) { return blo(pk2(a, a)); }
DI float dot2(u32 a, u32 b, float c) {
  return __builtin_amdgcn_fdot2_f32_bf16(__builtin_bit_cast(bf16x2, a), __builtin_bit_cast(bf16x2, b), c, false);
}
DI f32x2 fma2(f32x2 a, f32x2 b, f32x2 c) { return __builtin_elementwise_fma(a, b, c); }
DI f32x2 bc2(float a) { return (f32x2){a, a}; }
DI f32x2 lo2(f32x4 v) { return (f32x2){v[0], v[1]}; }
DI f32x2 hi2(f32x4 v) { return (f32x2){v[2], v[3]}; }
DI u32 pk2v(f32x2 v) { return pk2(v[0], v[1]); }
DI f32x2 gelu2(f32x2 x) {
  f32x2 u = fma2(x * x, bc2(-0.10294324f), bc2(-2.3022082f)) * x;
  f32x2 e = {__builtin_amdgcn_exp2f(u[0]), __builtin_amdgcn_exp2f(u[1])};
  e = e + bc2(1.0f);
  f32x2 r = {__builtin_amdgcn_rcpf(e[0]), __builtin_amdgcn_rcpf(e[1])};
  return x * r;
}
DI void red2g(float& a, float& b) {
  auto s = __builtin_amdgcn_permlane32_swap(__float_as_uint(a), __float_as_uint(b), false, false);
  float c = __uint_as_float(s[0]) + __uint_as_float(s[1]);
  auto s2 = __builtin_amdgcn_permlane16_swap(__float_as_uint(c), __float_as_uint(c), false, false);
  float d = __uint_as_float(s2[0]) + __uint_as_float(s2[1]);
  auto s3 = __builtin_amdgcn_permlane32_swap(__float_as_uint(d), __float_as_uint(d), false, false);
  a = __uint_as_float(s3[0]);
  b = __uint_as_float(s3[1]);
}
template <int CTRL> DI float dpp(float v) {
  return __uint_as_float(__builtin_amdgcn_update_dpp(0, __float_as_uint(v), CTRL, 0xF, 0xF, true));
}
template <class F> DI void hedgehog16(const f32x16& p, float s, u32x4 (&o)[4], F hook) {
  float mx = fmaxf(p[0], p[1]), mn = fminf(p[0], p[1]);
#pragma unroll
  for (int i = 2; i < 16; i += 2) { mx = fmaxf(fmaxf(mx, p[i]), p[i + 1]); mn = fminf(fminf(mn, p[i]), p[i + 1]); }
  const float c = s * 1.44269504f;
  const float a = -mx * c, b = mn * c;
  f32x2 ep[8], en[8];
#pragma unroll
  for (int i = 0; i < 8; ++i) {
    hook(i);
    const f32x2 v = {p[2 * i], p[2 * i + 1]};
    const f32x2 u = fma2(v, bc2(c), bc2(a)), w = fma2(v, bc2(-c), bc2(b));
    ep[i] = (f32x2){__builtin_amdgcn_exp2f(u[0]), __builtin_amdgcn_exp2f(u[1])};
    en[i] = (f32x2){__builtin_amdgcn_exp2f(w[0]), __builtin_amdgcn_exp2f(w[1])};
  }
  hook(8);
  const f32x2 sp = ((ep[0] + ep[1]) + (ep[2] + ep[3])) + ((ep[4] + ep[5]) + (ep[6] + ep[7]));
  const f32x2 sn = ((en[0] + en[1]) + (en[2] + en[3])) + ((en[4] + en[5]) + (en[6] + en[7]));
  const float ip = __builtin_amdgcn_rcpf(sp[0] + sp[1]), in = __builtin_amdgcn_rcpf(sn[0] + sn[1]);
#pragma unroll
  for (int i = 0; i < 8; ++i) {
    if (i == 0 || i == 3 || i == 6) hook(9 + i / 3);
    o[i >> 2][i & 3] = pk2v(ep[i] * bc2(ip));
    o[2 + (i >> 2)][i & 3] = pk2v(en[i] * bc2(in));
  }
}
DI void ln_fin(float s, float q, float invn, float& r, float& m) {
  m = s * invn;
  float v = fmaxf(q * invn - m * m, 0.0f);
  float sd = rbf(__builtin_amdgcn_sqrtf(rbf(rbf(v) + 1e-5f)));
  r = __builtin_amdgcn_rcpf(sd);
  m = rbf(m);
}

#define POLL_SLEEP 6
constexpr int SPIN_CAP = 1 << 17;
DI void poll_flags(u32 voff, bool act, rsrc_t srd, u32 soff, u32 tlo, u32 thi) {
#pragma nounroll
  for (int it = 0; it < SPIN_CAP; ++it) {
    u32x2 v;
    asm volatile("buffer_load_dwordx2 %0, %1, %2, %3 offen sc1\n\ts_waitcnt vmcnt(0)" : "=&v"(v) : "v"(voff), "s"(srd), "s"(soff) : "memory");
    bool ok = !act || (v[0] == tlo && v[1] == thi);
    if (__all(ok)) break;
    __builtin_amdgcn_s_sleep(POLL_SLEEP);
  }
}
DI bool tag_ok(u32x4 q, u32 t) { return q[1] == t && q[3] == t; }
DI void poll_gran(u32x4& q, u32 voff, rsrc_t srd, u32 soff, u32 tlo) {
#pragma nounroll
  for (int it = 0; it < SPIN_CAP; ++it) {
    if (__all(tag_ok(q, tlo))) break;
    __builtin_amdgcn_s_sleep(POLL_SLEEP);
    asm volatile("buffer_load_dwordx4 %0, %1, %2, %3 offen sc1\n\ts_waitcnt vmcnt(0)" : "=&v"(q) : "v"(voff), "s"(srd), "s"(soff) : "memory");
  }
}
DI u32 opq(u32 v) { asm volatile("" : "+v"(v)); return v; }

#define STAMP(i)
#define STAMPC(i)

#define GQ_N 3
#define PREFETCH 0
#define CODE_PF 56000
#define OUT_SC1 1
#define PRIO 0
#define UROT 1
static_assert(WS_ZB % 8192 == 0, "");
#define TLB_PF 0
#define WSP_PF 0
#define HL_K 10
#define VM(x) do { x; ++vq; } while (0)
#define WAITQ(mark) WAITV(vq - (mark))

template <int t>
DI void run(const char* __restrict__ x, const char* __restrict__ wqkv, const char* __restrict__ wphi, const char* __restrict__ wo,
            const char* __restrict__ win, const char* __restrict__ wsp, const char* __restrict__ bsp, const char* __restrict__ wout,
            char* __restrict__ out, char* ws, u64 ep) {
  const int tid = threadIdx.x, lane = tid & 63;
  const int w = __builtin_amdgcn_readfirstlane(tid >> 6);
  const int g = lane >> 4;
  const int b = blockIdx.x, xcd = b & 7, r = b >> 3;
  const int Pl = r >> 3, cl = r & 3;
  const int P = (xcd >> 1) * 4 + Pl;
  const int rot = 2 * ((Pl + cl) & 3);
  u32 acv = 0, acb = 0; (void)acv; (void)acb;
  const rsrc_t sx = __builtin_amdgcn_make_buffer_rsrc((void*)(x + (size_t)P * 131072), 0, 131072, 0x00020000);
  const u32 vx0 = (16 * w + (lane >> 3)) * 1024 + (((lane & 7) ^ g) * 16);
  const u32 vx1 = (vx0 + 8192) ^ 64;
  const u32 w2k = w * 2048;
#define DMA_X(dst, n, e) DMA_C(w2k, (dst) + (e) * 1024, (e) ? vx1 : vx0, sx, (((n) + rot) & 7) * 128)
  const int rw = w & 3, cw = w >> 2, i16 = lane & 15;
  const int c = (xcd & 1) * 4 + cl;
  const int p = 2 * P + (cl & 1);
  const int c2 = (xcd & 1) * 4 + (t ? 3 * (cl >> 1) : 1 + (cl >> 1));
  const u32 l16 = opq(lane * 16);
  const rsrc_t sa = __builtin_amdgcn_make_buffer_rsrc((void*)(wsp + (size_t)p * 262144), 0, 262144, 0x00020000);
  const rsrc_t sws = __builtin_amdgcn_make_buffer_rsrc((void*)ws, 0, WS_END, 0x00020000);
  const u32 vA0 = (lane >> 3) * 4096 + (((lane & 7) ^ g) * 16);
  const u32 vA1 = (vA0 + 32768) ^ 64;
  const int ux0 = UROT ? (cl >> 1) : 0, ux1 = UROT ? t : 0;
  const u32 uxs = w * 512 + ux1 * 256 + ux0 * 131072;
#define DMA_U(u, base, e) DMA_CC(w2k, (base) + (e) * 1024, (e) ? vA1 : vA0, sa, "s_xor_b32", uxs, (((u) >> 1) & 3) * 128 + (((u) >> 3) * 32 + ((u) & 1) * 16) * 4096)
  const f32x4 zero4 = {0, 0, 0, 0};
  u32x4 ones4 = {0x3F803F80u, 0x3F803F80u, 0x3F803F80u, 0x3F803F80u};
  u32 tlo, thi;
  u32x4 af[4][8];
  u32x4 bfr[2][8];
  u32x2 gf;
  const u32 ard = w * 2048 + i16 * 128 + (((g >> 1) ^ (i16 >> 1)) * 16) + (g & 1) * 8;
#define READ_U(u, base)                                                                                             \
  {                                                                                                                 \
    const u32 ab_ = opq(ard + (base));                                                                              \
    _Pragma("unroll") for (int kk = 0; kk < 2; ++kk)                                                               \
      af[2 * ((u) >> 3) + ((u) & 1)][((u) & 6) + kk] = cat(lds8(ab_ ^ (64 * kk)), lds8(ab_ ^ (64 * kk + 32)));      \
  }
#define USE_EPOCH() { asm volatile("" : "+s"(ep)); ep += 1; tlo = (u32)ep; thi = (u32)(ep >> 32); PIN(ones4); }
#define EARLY_STATS(mark)                                                                                           \
  {                                                                                                                 \
    WAITQ(mark); PIN(xrow);                                                                                         \
    float s_ = 0, q_ = 0;                                                                                           \
    _Pragma("unroll") for (int e = 0; e < 4; ++e) { s_ = dot2(xrow[e], ones4[0], s_); q_ = dot2(xrow[e], xrow[e], q_); } \
    s_ += dpp<0x128>(s_); q_ += dpp<0x128>(q_);                                                                     \
    s_ += dpp<0x124>(s_); q_ += dpp<0x124>(q_);                                                                     \
    s_ += dpp<0x4E>(s_); q_ += dpp<0x4E>(q_);                                                                       \
    s_ += dpp<0xB1>(s_); q_ += dpp<0xB1>(q_);                                                                       \
    red2g(s_, q_);                                                                                                  \
    float r_, m_;                                                                                                   \
    ln_fin(s_, q_, 1.0f / 512, r_, m_);                                                                             \
    VM(st16_sc1((u32x4){__float_as_uint(r_), tlo, __float_as_uint(m_), tlo}, l16 << 22, sws, WS_XG + (P * 128 + (t * 8 + c) * 8 + w) * 16)); \
  }
#define GATHER_XS()                                                                                                 \
  WAITQ(mxg); PIN(xg);                                                                                              \
  poll_gran(xg, i16 * 16, sws, WS_XG + (P * 128 + 16 * w) * 16, tlo);                                               \
  sts4f(L_XS + (16 * w + i16) * 4, __uint_as_float(xg[0]));                                                         \
  sts4f(L_XS + 512 + (16 * w + i16) * 4, __uint_as_float(xg[2]));
#define RELANE() const int lane = opq(l16) >> 4, i16 = lane & 15, g = lane >> 4, tid = w * 64 + lane; (void)tid; (void)g; (void)i16
#define SET_FLAG(off) st8_sc1((u32x2){tlo, thi}, l16 << 22, sws, (off))
  const int gs = grp_start(Z2S, c2), gn = t ? 5 : grp_start(Z2S, c2 + 1) - gs;
  const int trot = (p & 7) >= 5 ? (p & 7) - 5 : (p & 7);
#define TILE(j, n_) (((j) + trot) >= (n_) ? (j) + trot - (n_) : (j) + trot)
  const u32 zb0 = WS_ZB + (gs * 64 + 8 * w) * 1024 + ux1 * 4096;
#define DMA_WOUT(e)                                                                                                 \
  {                                                                                                                 \
    const rsrc_t so_ = __builtin_amdgcn_make_buffer_rsrc((void*)wout, 0, 688 * 1024, 0x00020000);                   \
    const int ws_ = grp_start(Z2S, w), wn_ = grp_start(Z2S, w + 1) - ws_;                                           \
    if ((e) < 6) dma16(L_WOUT + ((e) < 4 ? 2 * w + ((e) >> 1) : 16 + w) * 2048 + ((e) & 1) * 1024, vwo, so_, ((ws_ + TILE((e) >> 1, wn_)) * 2 + ((e) & 1)) * 8192); \
    else if ((e) == 6) dma16(L_WOUT + 24 * 2048 + w * 1024, vwo, so_, ((((w >> 1) == 0 ? 5 : ((w >> 1) == 1 ? 11 : ((w >> 1) == 2 ? 27 : 64))) + TILE(3, 6)) * 2 + (w & 1)) * 8192); \
  }
#define DMA_WOUT_AT(dst, e)                                                                                         \
  {                                                                                                                 \
    const rsrc_t so_ = __builtin_amdgcn_make_buffer_rsrc((void*)wout, 0, 688 * 1024, 0x00020000);                   \
    const int ws_ = grp_start(Z2S, w), wn_ = grp_start(Z2S, w + 1) - ws_;                                           \
    dma16((dst) + ((e) - 7) * 1024, vwo, so_, ((ws_ + TILE(wn_ - 1 - (((e) - 7) >> 1), wn_)) * 2 + (((e) - 7) & 1)) * 8192); \
  }
#define VWO() const u32 vwo = ((lane >> 2) & 7) * 1024 + c2 * 128 + (lane >> 5) * 64 + (((lane & 3) ^ (((lane >> 4) & 1) << 1)) * 16)
#define LOAD_B(u, so_)                                                                                              \
  bfr[u][0] = ld16_bo<0>(l16, sws, so_); bfr[u][1] = ld16_bo<1024>(l16, sws, so_);                                  \
  bfr[u][2] = ld16_bo<2048>(l16, sws, so_); bfr[u][3] = ld16_bo<3072>(l16, sws, so_);                               \
  bfr[u][4] = ld16_bo<0>(l16, sws, (so_) ^ 4096); bfr[u][5] = ld16_bo<1024>(l16, sws, (so_) ^ 4096);                \
  bfr[u][6] = ld16_bo<2048>(l16, sws, (so_) ^ 4096); bfr[u][7] = ld16_bo<3072>(l16, sws, (so_) ^ 4096);

  if constexpr (t == 0) {
    int vq = 2;
    const rsrc_t sw = __builtin_amdgcn_make_buffer_rsrc((void*)win, 0, 512 * 2752, 0x00020000);
    const int z2s = grp_start(Z2S, c), z2n = grp_start(Z2S, c + 1) - z2s;
    const int z1s = grp_start(Z1S, c), z1n = grp_start(Z1S, c + 1) - z1s;
    u32 vw[3];
    int wch[3];
#pragma unroll
    for (int e = 0; e < 3; ++e) {
      const int sg = 2 * e + (lane >> 5), hi = sg >= 3;
      wch[e] = (sg - 3 * hi) * 4 + ((lane & 3) ^ (hi << 1));
      vw[e] = wch[e] < 2 * z2n ? (16 * w + 8 * hi + ((lane >> 2) & 7)) * 2752 + 1376 + 32 * z2s + 16 * wch[e] : 0x7FFF0000u;
    }
    const u32 w3k = w * 3072;
#define DMA_W(bk, e) DMA_C(w3k, LZ_W + ((bk) % 3) * 24576 + (e) * 1024, vw[e], sw, ((2 * (bk) + rot) & 7) * (64 * 2752))
#define UBASE(u) ((u) % 5 == 0 ? LZ_U : ((u) % 5 - 1) * 16384)
#define ISS_X(n) do { VM(DMA_X(((n) & 3) * 16384, n, 0)); VM(DMA_X(((n) & 3) * 16384, n, 1)); mx[n] = vq; } while (0)
#define ISS_U(u) do { VM(DMA_U(u, UBASE(u), 0)); VM(DMA_U(u, UBASE(u), 1)); mu[u] = vq; } while (0)
#define ISS_W(bk, e0, e1) do { _Pragma("unroll") for (int e_ = e0; e_ < e1; ++e_) VM(DMA_W(bk, e_)); mw[bk] = vq; } while (0)
    int mx[8], mw[8], mu[16];
    mx[0] = vq;
    ISS_W(0, 0, 3);
    u32x4 xrow;
    VM(xrow = ld16(l16, sx, (c * 8 + w) * 1024));
    const int mxr = vq;
    ISS_X(1);
    const u32 xa0 = opq((32 * rw + i16) * 128 + ((g ^ (i16 >> 1)) * 16)), xa1 = xa0 ^ 64;
    u32 ta[3];
    int tl[3];
    {
      const u32 trb = LZ_W + g * 1536 + ((lane >> 2) & 3) * 64 + ((((g & 1) << 1) | ((lane >> 1) & 1)) * 16) + (lane & 1) * 8;
#pragma unroll
      for (int j = 0; j < 3; ++j) {
        tl[j] = 3 * cw + (j + rw) % 3;
        ta[j] = (trb + (tl[j] >> 1) * 512) ^ ((tl[j] & 1) * 32);
      }
    }
#define NEXT_BLOCK(bk) _Pragma("unroll") for (int j = 0; j < 3; ++j) ta[j] = opq(ta[j] + ((bk) == 0 ? 0 : ((bk) % 3 == 0 ? -49152 : 24576)))
    f32x4 acc[2][3], csa = zero4;
#pragma unroll
    for (int j = 0; j < 3; ++j) acc[0][j] = acc[1][j] = zero4;
    u32x4 xf[16][2], wf[2][3];
    u32x4 xg;
    int mxg = 0;
    WAITQ(mw[0]);
    BARRIER();
    USE_EPOCH()
    STAMP(7);
#define P1_MEM(n, h)                                                                                                \
  {                                                                                                                 \
    if ((n) == 0) { if ((h) == 0) ISS_X(2); if ((h) == 1) ISS_W(1, 0, 2); if ((h) == 2) ISS_W(1, 2, 3); if ((h) == 3) ISS_X(3); } \
    if ((n) == 1) { if ((h) == 0) ISS_W(2, 0, 2); if ((h) == 1) ISS_W(2, 2, 3); if ((h) == 2) VM(DMA_X(0, 4, 0)); if ((h) == 3) { VM(DMA_X(0, 4, 1)); mx[4] = vq; } } \
    if ((n) == 2 || (n) == 4) { if ((h) < 2) { VM(DMA_X((((n) + 3) & 3) * 16384, (n) + 3, (h) & 1)); mx[(n) + 3] = vq; } else ISS_W((n) / 2 + 2, (h) - 2, (h) - 1); } \
    if ((n) == 3) { if ((h) == 0) ISS_W(3, 2, 3); else if ((h) < 3) { VM(DMA_X(32768, 6, (h) - 1)); mx[6] = vq; } else ISS_U(0); } \
    if ((n) == 5) { if ((h) == 0) ISS_W(4, 2, 3); if ((h) == 1) { VM(xg = ld16_sc1(i16 * 16, sws, WS_XG + (P * 128 + 16 * w) * 16)); mxg = vq; } \
                    if ((h) >= 2) { VM(DMA_U(1, UBASE(1), (h) - 2)); mu[1] = vq; } }                                 \
    if ((n) == 6) { if ((h) < 3) ISS_W(5, h, (h) + 1); else ISS_U(2); }                                             \
    if ((n) == 7) { if ((h) < 2) { VM(DMA_U(3, UBASE(3), h)); mu[3] = vq; } }                                       \
  }
#pragma unroll
    for (int ks = 0; ks <= 16; ++ks) {
      const int n = ks >> 1, kk = ks & 1;
      if (ks > 0 && ks < 16 && kk == 0) {
        { const int mk = (n & 1) ? mx[n] : (mx[n] > mw[n >> 1] ? mx[n] : mw[n >> 1]); WAITQ(mk); }
        BARRIER();
      }
      if (ks < 16 && (ks & 3) == 0) { NEXT_BLOCK(ks >> 2); }
      if (ks == 8) {
#pragma unroll
        for (int e = 0; e < 3; ++e)
          vw[e] = wch[e] < 2 * z1n ? (16 * w + 8 * (2 * e + (lane >> 5) >= 3) + ((lane >> 2) & 7)) * 2752 + 32 * z1s + 16 * wch[e] : 0x7FFF0000u;
      }
      const int k1 = ks > 0 ? ks - 1 : 0, q1 = k1 & 1;
#pragma unroll
      for (int m = 0; m < 7; ++m) {
        if (ks > 0) {
          if (m < 6) acc[m & 1][m >> 1] = mfma32(xf[k1][m & 1], wf[q1][m >> 1], acc[m & 1][m >> 1]);
          else csa = mfma32(ones4, wf[q1][0], csa);
          SCHED();
        }
        if (ks < 16) {
          if (m == 0) { P1_MEM(n, 2 * kk) xf[ks & 15][0] = lds16((kk ? xa1 : xa0) + (n & 3) * 16384); xf[ks & 15][1] = lds16((kk ? xa1 : xa0) + (n & 3) * 16384 + 2048); }
          else if (m < 4) wf[kk][m - 1] = ldfrag(ta[m - 1] + (ks & 3) * 6144, 256);
          if (m == 4) P1_MEM(n, 2 * kk + 1)
        }
        SCHED();
      }
#pragma unroll
      for (int j = 0; j < 3; ++j) { PIN(acc[0][j]); PIN(acc[1][j]); }
      PIN(csa);
      if (ks == 2) EARLY_STATS(mxr)
    }
    GATHER_XS()
    sts4f(L_CS + tl[0] * 64 + i16 * 4, csa[0]);
    WAITQ(mu[1] > mw[4] ? mu[1] : mw[4]);
    READ_U(0, UBASE(0)) READ_U(1, UBASE(1))
    BARRIER();
    STAMP(1);
    u32x4 zp[3];
    ISS_W(6, 0, 3);
    {
      f32x2 ps[4], pq[4];
      float cs[3];
#pragma unroll
      for (int j = 0; j < 3; ++j) cs[j] = -lds4f(L_CS + tl[j] * 64 + i16 * 4);
#pragma unroll
      for (int rt = 0; rt < 2; ++rt) {
        const f32x4 rr = lds16f(L_XS + (32 * rw + 16 * rt + 4 * g) * 4), mm = lds16f(L_XS + 512 + (32 * rw + 16 * rt + 4 * g) * 4);
        f32x2 z0[3], z1[3];
#pragma unroll
        for (int j = 0; j < 3; ++j) {
          z0[j] = gelu2(fma2(lo2(mm), bc2(cs[j]), lo2(acc[rt][j])) * lo2(rr));
          z1[j] = gelu2(fma2(hi2(mm), bc2(cs[j]), hi2(acc[rt][j])) * hi2(rr));
          zp[j][2 * rt] = pk2v(z0[j]); zp[j][2 * rt + 1] = pk2v(z1[j]);
        }
        ps[2 * rt] = z0[0] + z0[1] + z0[2]; ps[2 * rt + 1] = z1[0] + z1[1] + z1[2];
        pq[2 * rt] = fma2(z0[2], z0[2], fma2(z0[1], z0[1], z0[0] * z0[0]));
        pq[2 * rt + 1] = fma2(z1[2], z1[2], fma2(z1[1], z1[1], z1[0] * z1[0]));
        SCHED();
        if (rt == 0) { ISS_U(4); ISS_U(5); }
        else ISS_U(6);
        SCHED();
      }
      const bool b3 = lane & 8, b2 = lane & 4, b1 = lane & 2;
#pragma unroll
      for (int m = 0; m < 2; ++m)
#pragma unroll
        for (int e = 0; e < 2; ++e) {
          ps[m][e] = (b3 ? ps[m + 2][e] : ps[m][e]) + dpp<0x128>(b3 ? ps[m][e] : ps[m + 2][e]);
          pq[m][e] = (b3 ? pq[m + 2][e] : pq[m][e]) + dpp<0x128>(b3 ? pq[m][e] : pq[m + 2][e]);
        }
#pragma unroll
      for (int e = 0; e < 2; ++e) {
        ps[0][e] = (b2 ? ps[1][e] : ps[0][e]) + dpp<0x141>(b2 ? ps[0][e] : ps[1][e]);
        pq[0][e] = (b2 ? pq[1][e] : pq[0][e]) + dpp<0x141>(b2 ? pq[0][e] : pq[1][e]);
      }
      float s_ = (b1 ? ps[0][1] : ps[0][0]) + dpp<0x4E>(b1 ? ps[0][0] : ps[0][1]);
      float q_ = (b1 ? pq[0][1] : pq[0][0]) + dpp<0x4E>(b1 ? pq[0][0] : pq[0][1]);
      s_ += dpp<0xB1>(s_);
      q_ += dpp<0xB1>(q_);
      const int row = 32 * rw + 16 * (i16 >> 3) + 4 * g + ((i16 >> 1) & 3);
      VM(st16_sc1((u32x4){__float_as_uint(s_), tlo, __float_as_uint(q_), tlo}, row * 16 + ((lane & 1) << 30), sws, WS_SG + ((P * 8 + c) * 2 + cw) * 2048));
      PIN(zp[0]); PIN(zp[1]); PIN(zp[2]);
    }
    STAMP(2);
    {
    RELANE();
#pragma unroll
    for (int j = 0; j < 3; ++j) acc[0][j] = acc[1][j] = zero4;
    csa = zero4;
    u32x4 q0, q1, q2, q3;
    int mq = 0, mz = 0;
    const u32 gvo = (2 * g) * 4096 + (32 * rw + 16 * cw + i16) * 16;
    const u32 gso = WS_SG + P * 32768;
#pragma unroll
    for (int ks = 0; ks <= 16; ++ks) {
      if (ks == 8) {
        WAITQ(mq); PIN(q0); PIN(q1); PIN(q2); PIN(q3);
#pragma nounroll
        for (int it = 0; it < SPIN_CAP; ++it) {
          if (__all(tag_ok(q0, tlo) && tag_ok(q1, tlo) && tag_ok(q2, tlo) && tag_ok(q3, tlo))) break;
          __builtin_amdgcn_s_sleep(POLL_SLEEP);
          asm volatile(
              "buffer_load_dwordx4 %0, %4, %5, %6 offen sc1\n\t"
              "buffer_load_dwordx4 %1, %4, %5, %6 offen offset:2048 sc1\n\t"
              "buffer_load_dwordx4 %2, %4, %5, %7 offen sc1\n\t"
              "buffer_load_dwordx4 %3, %4, %5, %7 offen offset:2048 sc1\n\t"
              "s_waitcnt vmcnt(0)"
              : "=&v"(q0), "=&v"(q1), "=&v"(q2), "=&v"(q3)
              : "v"(gvo), "s"(sws), "s"(gso), "s"(gso + 4096)
              : "memory");
        }
        float s = (__uint_as_float(q0[0]) + __uint_as_float(q1[0])) + (__uint_as_float(q2[0]) + __uint_as_float(q3[0]));
        float q = (__uint_as_float(q0[2]) + __uint_as_float(q1[2])) + (__uint_as_float(q2[2]) + __uint_as_float(q3[2]));
        red2g(s, q);
        float r2, m2;
        ln_fin(s, q, 1.0f / 688, r2, m2);
        sts4f(L_ZST + (32 * rw + 16 * cw + i16) * 4, r2);
        sts4f(L_ZST + 512 + (32 * rw + 16 * cw + i16) * 4, -m2 * r2);
        STAMP(3);
      }
      if (ks < 16 && (ks & 3) == 0) {
        if (ks > 0) { WAITQ(ks == 12 ? mz : mw[4 + (ks >> 2)]); BARRIER(); }
        if (ks == 12) { if (w == 0) SET_FLAG(WS_FA + (P * 8 + c) * 128); STAMP(5); }
        NEXT_BLOCK(4 + (ks >> 2));
      }
      if (ks == 8) {
#pragma unroll
        for (int rt = 0; rt < 2; ++rt) {
          const u32 ra = L_ZST + (32 * rw + 16 * rt + 4 * g) * 4;
          const f32x4 r4 = lds16f(ra), m4 = lds16f(ra + 512);
#pragma unroll
          for (int j = 0; j < 3; ++j) {
            const u32 u0 = zp[j][2 * rt], u1 = zp[j][2 * rt + 1];
            zp[j][2 * rt] = pk2v(fma2((f32x2){blo(u0), bhi(u0)}, lo2(r4), lo2(m4)));
            zp[j][2 * rt + 1] = pk2v(fma2((f32x2){blo(u1), bhi(u1)}, hi2(r4), hi2(m4)));
          }
        }
#pragma unroll
        for (int j = 0; j < 3; ++j)
          VM(st16_sc1(zp[j], l16, sws, (tl[j] < z2n) ? WS_ZB + ((z2s + tl[j]) * 64 + 4 * P + rw) * 1024 : 0x7FFF0000u));
        mz = vq;
        STAMP(4);
        SCHED();
      }
      if (ks >= 5 && ks <= 9) { WAITQ(mu[ks - 3]); READ_U(ks - 3, UBASE(ks - 3)) }
      if (ks >= 9 && ks <= 13) { WAITQ(mu[ks - 2]); READ_U(ks - 2, UBASE(ks - 2)) }
      if (ks >= 13) { WAITQ(mu[ks - 1]); READ_U(ks - 1, UBASE(ks - 1)) }
      const int k1 = ks > 0 ? ks - 1 : 0, q1_ = k1 & 1;
#pragma unroll
      for (int m = 0; m < 7; ++m) {
        if (ks > 0) {
          if (m < 6) acc[m & 1][m >> 1] = mfma32(wf[q1_][m >> 1], xf[k1][m & 1], acc[m & 1][m >> 1]);
          else csa = mfma32(ones4, wf[q1_][0], csa);
          SCHED();
        }
        if (ks < 16 && m >= 1 && m < 4) wf[ks & 1][m - 1] = ldfrag(ta[m - 1] + (ks & 3) * 6144, 256);
        if (m == 0 || m == 4) {
          const int h = m == 4;
          if (h == 0 && ks >= 6 && ks <= 13) asm volatile("s_waitcnt lgkmcnt(10)" ::: "memory");
          if (ks >= 6 && ks <= 10) { VM(DMA_U(ks + 1, UBASE(ks + 1), h)); mu[ks + 1] = vq; }
          if (ks >= 10 && ks <= 13) { VM(DMA_U(ks + 2, UBASE(ks + 2), h)); mu[ks + 2] = vq; }
        }
        if (m == 6) {
          if (ks >= 4 && ks <= 6) ISS_W(7, ks - 4, ks - 3);
        }
        if (m == 2) {
          if (ks == 5) { VM(q0 = ld16_sc1o<0>(gvo, sws, gso)); VM(q1 = ld16_sc1o<2048>(gvo, sws, gso)); }
          if (ks == 6) { VM(q2 = ld16_sc1o<0>(gvo, sws, gso + 4096)); VM(q3 = ld16_sc1o<2048>(gvo, sws, gso + 4096)); mq = vq; }
        }
        SCHED();
      }
#pragma unroll
      for (int j = 0; j < 3; ++j) { PIN(acc[0][j]); PIN(acc[1][j]); }
      PIN(csa);
    }
    sts4f(L_CS + 512 + tl[0] * 64 + i16 * 4, csa[0]);
    gf = ld8_sc1((lane >> 5) * 1024, sws, WS_FA + (16 * w + c2) * 128);
    BARRIER();
    VWO();
    DMA_WOUT(0) DMA_WOUT(1)
    WAITV(2);
    PIN(gf);
    if (!__all(gf[0] == tlo && gf[1] == thi)) poll_flags((lane >> 5) * 1024, true, sws, WS_FA + (16 * w + c2) * 128, tlo, thi);
    STAMP(6);
    {
      float xr[2], xm[2];
#pragma unroll
      for (int rt = 0; rt < 2; ++rt) {
        xr[rt] = lds4f(L_XS + (32 * rw + 16 * rt + i16) * 4);
        xm[rt] = -lds4f(L_XS + 512 + (32 * rw + 16 * rt + i16) * 4);
      }
      const u32 so0 = zb0 + TILE(0, gn) * 65536;
#pragma unroll
      for (int j = 0; j < 3; ++j) {
        const f32x4 cc = lds16f(L_CS + 512 + tl[j] * 64 + g * 16);
        bfr[0][3 * j] = ld16_b(l16, sws, so0 ^ (3 * j * 1024));
        bfr[0][3 * j + 1] = ld16_b(l16, sws, so0 ^ ((3 * j + 1) * 1024));
        if (j < 2) bfr[0][3 * j + 2] = ld16_b(l16, sws, so0 ^ ((3 * j + 2) * 1024));
        u32x4 zz;
#pragma unroll
        for (int rt = 0; rt < 2; ++rt) {
          zz[2 * rt] = pk2v(gelu2(fma2(lo2(cc), bc2(xm[rt]), lo2(acc[rt][j])) * bc2(xr[rt])));
          zz[2 * rt + 1] = pk2v(gelu2(fma2(hi2(cc), bc2(xm[rt]), hi2(acc[rt][j])) * bc2(xr[rt])));
        }
        st16_sc1(zz, l16, sws, (tl[j] < z1n) ? WS_Z1 + ((4 * P + rw) * 43 + z1s + tl[j]) * 1024 : 0x7FFF0000u);
        SCHED();
      }
    }
    }
  } else {
    int vq = 2;
    const rsrc_t sw = __builtin_amdgcn_make_buffer_rsrc((void*)wqkv, 0, 512 * 3072, 0x00020000);
    const u32 vw = (8 * w + ((lane >> 2) & 7)) * 3072 + 128 * c + ((lane >> 5) * 4 + ((lane & 3) ^ ((w & 1) << 1))) * 16;
    const u32 w3k = w * 3072;
#define DMA_S(n, e)                                                                                                 \
  do {                                                                                                              \
    if ((e) < 2) DMA_X(((n) % 3) * 40960, n, (e) & 1);                                                              \
    else DMA_CC(w3k, ((n) % 3) * 40960 + 16384 + ((e) - 2) * 1024, vw, sw, "s_add_u32", (((n) + rot) & 7) * (64 * 3072), ((e) - 2) * 1024); \
  } while (0)
#define ISS_Q(u, base) do { VM(DMA_U(u, base, 0)); VM(DMA_U(u, base, 1)); mu[u] = vq; } while (0)
    int ms[8], mu[16];
    VM(DMA_S(0, 2)); VM(DMA_S(0, 3)); VM(DMA_S(0, 4)); ms[0] = vq;
    u32x4 xrow;
    VM(xrow = ld16(l16, sx, ((8 + c) * 8 + w) * 1024));
    const int mxr = vq;
#pragma unroll
    for (int e = 0; e < 5; ++e) { VM(DMA_S(1, e)); ms[1] = vq; }
    const int l32 = lane & 31, hh = lane >> 5;
    u32 xa = (32 * rw + l32) * 128 + ((hh ^ ((l32 >> 1) & 7)) * 16);
    u32 wa = 16384 + hh * 3072 + cw * 512 + ((lane >> 2) & 3) * 64 + (((((g & 1) << 1) | ((lane >> 1) & 1)) ^ (hh << 1)) * 16) + (lane & 1) * 8;
    u32 ca = 16384 + g * 3072 + min(w, 5) * 512 + ((lane >> 2) & 3) * 64 + ((((g & 1) << 1) | ((lane >> 1) & 1)) * 16) + (lane & 1) * 8;
    const f32x16 zero16 = {0, 0, 0, 0, 0, 0, 0, 0, 0, 0, 0, 0, 0, 0, 0, 0};
    f32x16 acc[3] = {zero16, zero16, zero16};
    f32x4 csa[2] = {zero4, zero4};
    u32x4 xg;
    int mxg = 0;
    WAITQ(ms[0]);
    BARRIER();
    USE_EPOCH()
    STAMP(7);
    {
      u32x4 xf[2], wf[2][4];
#pragma unroll
      for (int n = 0; n <= 8; ++n)
#pragma unroll
      for (int sq = 0; sq < 4; ++sq) {
        const int s = 4 * n + sq;
        if (s > 32) continue;
        if (s < 32 && sq == 0) {
          if (n > 0) {
            WAITQ(ms[n]);
            BARRIER();
          }
          const int d = n == 0 ? 0 : (n % 3 == 0 ? -81920 : 40960);
          xa = opq(xa + d); wa = opq(wa + d); ca = opq(ca + d);
        }
        const int q1 = (s + 1) & 1, q0 = s & 1;
#pragma unroll
        for (int m = 0; m < 4; ++m) {
          if (s > 0) {
            if (m < 3) acc[m] = mfmaL(wf[q1][m], xf[q1], acc[m]);
            else csa[q1] = mfma32(ones4, wf[q1][3], csa[q1]);
            SCHED();
          }
          if (s < 32) {
            if (m == 0) { xf[q0] = lds16(xa ^ (32 * sq)); wf[q0][0] = ldfrag(wa + sq * 6144, 256); }
            if (m == 1) { wf[q0][1] = ldfrag(wa + sq * 6144 + 1024, 256); wf[q0][2] = ldfrag(wa + sq * 6144 + 2048, 256); }
            if (m == 2) wf[q0][3] = ldfrag((ca ^ (32 * (sq & 1))) + (sq >> 1) * 12288, 256);
            if (m == 0 || m == 2) {
              const int h = 2 * sq + (m == 2);
              if (n <= 5 && h < 5) { VM(DMA_S(n + 2, h)); ms[n + 2] = vq; }
              if (n == 0 && h == 5) {
                rsrc_t sp = __builtin_amdgcn_make_buffer_rsrc((void*)wphi, 0, 16384, 0x00020000);
                VM(dma16(L_WPHI + (w & 1) * 1024, l16, sp, c * 2048 + (w & 1) * 1024));
              }
              if (WSP_PF && h == 6 && (n == WSP_PF || n == WSP_PF + 2))
                VM(touch(L_JUNK, l16 * 8, sa, (((cl >> 1) * 2 + (n != WSP_PF)) * 8 + w) * 8192));
              if (n == 4 && h == 5) { VM(xg = ld16_sc1(i16 * 16, sws, WS_XG + (P * 128 + 16 * w) * 16)); mxg = vq; }
              if (n == 6) { VM(DMA_U(sq, (sq < 2 ? LQ_M : LQ_A) + (sq & 1) * 16384, h & 1)); mu[sq] = vq; }
              if (n == 7 && sq < 2) { VM(DMA_U(4 + sq, sq * 16384, h & 1)); mu[4 + sq] = vq; }
            }
          }
          SCHED();
        }
        PIN(acc[0]); PIN(acc[1]); PIN(acc[2]); PIN(csa[0]); PIN(csa[1]);
        if (s == 3) EARLY_STATS(mxr)
      }
    }
    GATHER_XS()
    sts4f(L_CS + w * 128 + i16 * 4, csa[0][0]);
    sts4f(L_CS + w * 128 + 64 + i16 * 4, csa[1][0]);
    BARRIER();
    STAMP(1);
    WAITQ(mu[3]);
    READ_U(0, LQ_M) READ_U(1, LQ_M + 16384) READ_U(2, LQ_A) READ_U(3, LQ_A + 16384)
    const float xr = lds4f(L_XS + (32 * rw + l32) * 4), xm = -lds4f(L_XS + 512 + (32 * rw + l32) * 4);
    u32x4 wp0, wp1;
    {
      const u32 a = L_WPHI + (2 * cw + (lane & 1)) * 512 + (4 * hh + ((lane >> 2) & 3)) * 32 + (2 * (g & 1) + ((lane >> 1) & 1)) * 8;
      const u32x4 f = ldfrag(a, 256);
      const bool own = (lane >> 2) & 1;
#pragma unroll
      for (int e = 0; e < 4; ++e) { wp0[e] = own ? 0u : f[e]; wp1[e] = own ? f[e] : 0u; }
    }
#define FEATURES(e_, dst, hook)                                                                                         \
  {                                                                                                                 \
    u32x4 kp[2];                                                                                                    \
    _Pragma("unroll") for (int a = 0; a < 4; ++a) {                                                                \
      const f32x4 cc = lds16f(L_CS + (2 * (e_) + cw) * 128 + a * 32 + hh * 16);                                      \
      kp[a >> 1][2 * (a & 1)] = pk2v(fma2(lo2(cc), bc2(xm), (f32x2){acc[e_][4 * a], acc[e_][4 * a + 1]}));          \
      kp[a >> 1][2 * (a & 1) + 1] = pk2v(fma2(hi2(cc), bc2(xm), (f32x2){acc[e_][4 * a + 2], acc[e_][4 * a + 3]}));  \
    }                                                                                                               \
    f32x16 pl = mfmaL(wp0, kp[0], zero16);                                                                          \
    pl = mfmaL(wp1, kp[1], pl);                                                                                     \
    hedgehog16(pl, xr, dst, hook);                                                                                  \
  }
    {
      const int s_ = 32 * rw + l32;
      const u32 key = (((s_ >> 1) & 3) ^ (((s_ >> 3) & 1) << 1)) * 16;
      {
        const u32 va = LQ_V + (s_ >> 3) * 1024 + cw * 512 + (s_ & 7) * 64 + hh * 8;
#pragma unroll
        for (int a = 0; a < 4; ++a) {
          const f32x4 cc = lds16f(L_CS + (4 + cw) * 128 + a * 32 + hh * 16);
          sts8(va + ((a * 16) ^ key), (u32x2){pk2v(fma2(lo2(cc), bc2(xm), (f32x2){acc[2][4 * a], acc[2][4 * a + 1]}) * bc2(xr)),
                                              pk2v(fma2(hi2(cc), bc2(xm), (f32x2){acc[2][4 * a + 2], acc[2][4 * a + 3]}) * bc2(xr))});
        }
      }
      WAITQ(mu[5]);
      READ_U(4, 0) READ_U(5, 16384)
      WAITL0();
      u32x4 ph[4];
      auto hk = [&](int i) __attribute__((always_inline)) {
        const int u = 6 + (i >> 1);
        SCHED();
        VM(DMA_U(u, (u < 8 ? LQ_M : (u < 10 ? LQ_A : 0)) + (u & 1) * 16384, i & 1));
        mu[u] = vq;
        SCHED();
      };
      FEATURES(1, ph, hk)
      const u32 pa = LQ_PHI + (s_ >> 3) * 2048 + (2 * cw + hh) * 512 + (s_ & 7) * 64;
#pragma unroll
      for (int a = 0; a < 4; ++a) sts16(pa + ((a * 16) ^ key), ph[a]);
    }
    BARRIER();
    STAMP(2);
    {
      const u32 kx = (((lane >> 3) & 1) ^ ((g & 1) << 1)) * 16;
      const u32 pa = LQ_PHI + g * 2048 + (w >> 1) * 512 + ((lane >> 2) & 3) * 64 + (((2 * (w & 1) + ((lane >> 1) & 1)) * 16) ^ kx) + (lane & 1) * 8;
      const u32 va = LQ_V + g * 1024 + (w >> 2) * 512 + ((lane >> 2) & 3) * 64 + (((2 * ((w >> 1) & 1) + ((lane >> 1) & 1)) * 16) ^ kx) + (lane & 1) * 8;
      u32x4 pf[4], vf[4];
#pragma unroll
      for (int k = 0; k < 4; ++k) {
        pf[k] = cat(ldstr(pa + k * 8192), ldstr((pa ^ 32) + k * 8192 + 256));
        vf[k] = cat(ldstr(va + k * 4096), ldstr((va ^ 32) + k * 4096 + 256));
      }
      f32x4 kv = zero4, kz = zero4;
#pragma unroll
      for (int k = 0; k < 4; ++k) { kv = mfma32(pf[k], vf[k], kv); kz = mfma32(pf[k], ones4, kz); }
      const u32 so = WS_KP + (c * 16 + P) * KP_SZ;
      u32x4 g0 = {__float_as_uint(kv[0]), tlo, __float_as_uint(kv[1]), tlo}, g1 = {__float_as_uint(kv[2]), tlo, __float_as_uint(kv[3]), tlo};
      u32x4 g2 = {__float_as_uint((lane & 1) ? kz[2] : kz[0]), tlo, __float_as_uint((lane & 1) ? kz[3] : kz[1]), tlo};
      PIN(g0); PIN(g1); PIN(g2);
      asm volatile("s_nop 7" ::: "memory");
      VM(st16_sc1(g0, l16, sws, so + w * 2048));
      VM(st16_sc1(g1, l16, sws, so + w * 2048 + 1024));
      VM(st16_sc1(g2, g * 32 + (lane & 1) * 16 + ((i16 >> 1) << 26), sws, so + 16384 + w * 128));
    }
    STAMP(3);
    WAITQ(mu[11]);
    READ_U(6, LQ_M) READ_U(7, LQ_M + 16384) READ_U(8, LQ_A) READ_U(9, LQ_A + 16384) READ_U(10, 0) READ_U(11, 16384)
    BARRIER();
    ISS_Q(12, LQ_M); VM(DMA_U(13, LQ_M + 16384, 0));
    u32x4 phq[4];
    {
      VWO();
      auto hk = [&](int i) __attribute__((always_inline)) {
        SCHED();
        if (i == 0) DMA_U(13, LQ_M + 16384, 1);
        else if (i < 5) DMA_U(14 + ((i - 1) >> 1), LQ_A + ((i - 1) >> 1) * 16384, (i - 1) & 1);
        else DMA_WOUT(i - 5)
        SCHED();
      };
      FEATURES(0, phq, hk)
    }
    PIN(phq[0]); PIN(phq[1]); PIN(phq[2]); PIN(phq[3]);
    STAMP(4);
    if (w >= 5) {
      const int id = 2 * P + (w == 7 ? (lane & 1) : w - 5), wq = id >> 2, gq = id & 3;
      const u32 vi0 = (4 * g) * KP_SZ + (w == 7 ? 16384 + wq * 128 + gq * 32 : wq * 2048 + (gq * 16 + i16) * 16);
      const u32 vi1 = vi0 + (w == 7 ? 16 : 1024);
      const u32 vo = (w == 7 ? (WS_KZ - WS_KF) + c * 512 + id * 16 + ((i16 >> 1) << 25) : c * 8192 + id * 256 + i16 * 16) + (g << 28);
      float sa[4];
#pragma nounroll
      for (int it = 0; it < SPIN_CAP; ++it) {
        u32x4 a[4][2];
#pragma unroll
        for (int o = 0; o < 4; ++o) {
          a[o][0] = ld16_sc1(vi0, sws, WS_KP + (c * 16 + o) * KP_SZ);
          a[o][1] = ld16_sc1(vi1, sws, WS_KP + (c * 16 + o) * KP_SZ);
        }
        WAITV(0);
        bool ok = true;
#pragma unroll
        for (int o = 0; o < 4; ++o) { PIN(a[o][0]); PIN(a[o][1]); ok = ok && tag_ok(a[o][0], tlo) && tag_ok(a[o][1], tlo); }
#pragma unroll
        for (int e = 0; e < 4; ++e)
          sa[e] = (__uint_as_float(a[0][e >> 1][2 * (e & 1)]) + __uint_as_float(a[1][e >> 1][2 * (e & 1)])) +
                  (__uint_as_float(a[2][e >> 1][2 * (e & 1)]) + __uint_as_float(a[3][e >> 1][2 * (e & 1)]));
        if (__all(ok)) break;
        __builtin_amdgcn_s_sleep(POLL_SLEEP);
      }
      red2g(sa[0], sa[1]);
      red2g(sa[2], sa[3]);
      st16_sc1((u32x4){pk2(sa[0], sa[1]), tlo, pk2(sa[2], sa[3]), tlo}, vo, sws, WS_KF);
    }
    gf = ld8_sc1((lane >> 5) * 1024, sws, WS_FA + (16 * w + c2) * 128);
    WAITV(8);
    READ_U(12, LQ_M) READ_U(13, LQ_M + 16384) READ_U(14, LQ_A) READ_U(15, LQ_A + 16384)
    BARRIER();
#pragma unroll
    for (int a = 0; a < 4; ++a) sts16(L_WO1 + w * 4096 + a * 1024 + l16, phq[a]);
    WAITV(0);
    PIN(gf);
    if (!__all(gf[0] == tlo && gf[1] == thi)) poll_flags((lane >> 5) * 1024, true, sws, WS_FA + (16 * w + c2) * 128, tlo, thi);
    STAMP(6);
    { const u32 so_ = zb0 + TILE(0, gn) * 65536; LOAD_B(0, so_) }
  }

  {
  RELANE();
  u32x4 kfg, kzg;
  const u32 kfo = WS_KF + c * 8192 + w * 1024, kzo = WS_KZ + c * 512 + w * 64;
  if (t != 0) {
    kfg = ld16_sc1(l16, sws, kfo);
    kzg = ld16_sc1(g * 16, sws, kzo);
  }
  { const u32 so_ = zb0 + TILE(1, gn) * 65536; LOAD_B(1, so_) }
  STAMP(9);
  const u32 redw = opq(L_RED + w * 4096 + lane * 16), redr = opq(L_RED + (rw ^ (2 * ux0)) * 1024 + lane * 16);
  const u32 fd = WS_FD + ((4 * p + rw) * 16 + c2) * 128;
  const u32 f3s = __builtin_amdgcn_readfirstlane(WS_FB + (P * 8 + (c2 > 0 ? c2 - 1 : 0)) * 128);
  const u32 vw3 = ((lane >> 2) & 7) * 1024 + c2 * 128 + (lane >> 5) * 64;
  const rsrc_t swo = __builtin_amdgcn_make_buffer_rsrc((void*)wo, 0, 512 * 1024, 0x00020000);
#define DMA_WO(dst, half, e) dma16((dst) + w * 4096 + (e) * 1024, (vw3 + (lane & 3) * 16) ^ (((e) & 1) * 32), swo, ((w + 8 * (half)) * 4 + (e)) * 8192)
#define FV() ((lane & 32) ? (P * 8 + ((lane >> 2) & 7)) * 128 : (WS_FD - WS_FC) + ((4 * p + rw) * 16 + (((lane >> 1) & 14) | cw)) * 128)
  u32x2 z1r[6], hold;
  u32 bs;
  f32x4 sm0;
#define EMIT_H(jt, sm_)                                                                                             \
  {                                                                                                                 \
    const f32x2 bb = bc2(blo(bs));                                                                                  \
    const u32 h0 = pk2v((f32x2){blo(z1r[jt][0]), bhi(z1r[jt][0])} * (lo2(sm_) + bb));                               \
    const u32 h1 = pk2v((f32x2){blo(z1r[jt][1]), bhi(z1r[jt][1])} * (hi2(sm_) + bb));                               \
    const u32 hb_ = WS_HF + (((4 * p + rw) * 2 + ((jt) >> 1)) * 8 + c2) * 1024;                                     \
    if ((jt) == 0 || ((jt) == 2 && gn == 6)) hold = (u32x2){h0, h1};                                                \
    else if ((jt) == 1 || ((jt) == 3 && gn == 6)) st16_sc1((u32x4){hold[0], hold[1], h0, h1}, l16, sws, hb_);       \
    else if ((jt) == 2) st16_sc1((u32x4){h0, h1, 0u, 0u}, l16, sws, hb_);                                           \
    else st16_sc1((u32x4){h0, tlo, h1, tlo}, l16, sws, WS_HL + (((4 * p + rw) * 8 + c2) * 2 + (gn - 1 - (jt))) * 1024); \
  }
#pragma unroll
  for (int j = 0; j < 6; ++j) {
    if (j < 5 || gn == 6) {
      const int u = j & 1;
      if (j == 0) WAITV(8);
      if (j == 2) WAITV(t ? 10 : 8);
      if (j == 1) { WAITV(9); WAITV_IF(cw, 1, 8); }
      if (j == 3) { WAITV(9); WAITV_IF(cw, 1, 8); }
      if (j == 4) { if (t) WAITV(4); else { WAITV(12); WAITV_IF(gn, 5, 4); } }
      if (j == 5) { WAITV(5); WAITV_IF(cw, 1, 4); }
#pragma unroll
      for (int i = 0; i < 8; ++i) PIN(bfr[u][i]);
      {
        f32x4 a_[4];
#pragma unroll
        for (int rt = 0; rt < 4; ++rt) {
          a_[rt] = zero4;
#pragma unroll
          for (int i = 0; i < 8; ++i) a_[rt] = mfma32(bfr[u][i], af[rt][i], a_[rt]);
          SCHED();
          if (rt > 0) sts16f(redw + u * 32768 + (rt - 1) * 1024, a_[rt - 1]);
          SCHED();
        }
        if (j == 0 && t != 0) {
          PIN(kfg); PIN(kzg);
          poll_gran(kfg, l16, sws, kfo, tlo);
          poll_gran(kzg, g * 16, sws, kzo, tlo);
          sts8(L_KFS + (w >> 1) * 1024 + i16 * 64 + (w & 1) * 32 + g * 8, (u32x2){kfg[0], kfg[2]});
          sts8(L_KFS + 4096 + w * 32 + g * 8, (u32x2){kzg[0], kzg[2]});
          sts8(L_KFS + 4352 + (lane & 7) * 8, (u32x2){0u, 0u});
        }
        sts16f(redw + u * 32768 + 3072, a_[3]);
      }
      u32x4 of[2];
      if (j == 1 && t != 0) {
        const int l32 = lane & 31, hh = lane >> 5;
        const bool own = ((lane >> 2) & 1) == hh;
        const u32 ka = own ? L_KFS + (2 * cw + hh) * 1024 + ((l32 & 3) + 4 * (l32 >> 3)) * 64 : L_KFS + 4352;
        const u32 zaa = own ? L_KFS + 4096 + (2 * cw + hh) * 64 : L_KFS + 4352;
        const u32 qa = L_WO1 + w * 4096 + l16;
        const f32x16 zero16 = {0, 0, 0, 0, 0, 0, 0, 0, 0, 0, 0, 0, 0, 0, 0, 0};
        f32x16 den = zero16, num = zero16;
#pragma unroll
        for (int a = 0; a < 4; ++a) {
          const u32x4 pq_ = lds16(qa + a * 1024);
          den = mfmaL(lds16(zaa + a * 16), pq_, den);
          num = mfmaL(lds16(ka + a * 16), pq_, num);
        }
        const float inv = __builtin_amdgcn_rcpf(den[0] + 1e-6f);
#pragma unroll
        for (int a = 0; a < 2; ++a) {
          of[a] = (u32x4){pk2(num[8 * a] * inv, num[8 * a + 1] * inv), pk2(num[8 * a + 2] * inv, num[8 * a + 3] * inv),
                          pk2(num[8 * a + 4] * inv, num[8 * a + 5] * inv), pk2(num[8 * a + 6] * inv, num[8 * a + 7] * inv)};
          PIN(of[a]);
        }
        STAMP(5);
      }
      BAR_IF(cw, 1, j >= 4 ? 0 : 63);
      if (w == 4) {
        if (j == 0 && t == 0) SET_FLAG(WS_FB + (P * 8 + c) * 128);
        if (j == 3 && t != 0) SET_FLAG(WS_FC + (P * 8 + c) * 128);
      }
      if (j == 1) {
        if (cw == 0) {
          WAITV(0);
          PIN(gf);
          if (!__all(gf[0] == tlo && gf[1] == thi)) poll_flags((lane >> 5) * 128, true, sws, f3s, tlo, thi);
          rsrc_t sb = __builtin_amdgcn_make_buffer_rsrc((void*)bsp, 0, 4096, 0x00020000);
          bs = ld2(i16 * 2, sb, (64 * p + 16 * rw) * 2);
        }
      }
      if ((j == 1 || j == 2) && cw == 0) {
#pragma unroll
        for (int v = 3 * j - 3; v < 3 * j; ++v)
          z1r[v] = ld8_sc1(l16, sws, WS_Z1 + ((2 * p + (rw >> 1)) * 43 + gs + TILE(min(v, gn - 1), gn)) * 1024 + (rw & 1) * 8);
      }
      if (j == 4 && gn == 5 && cw == 0) SET_FLAG(fd + 1024);
      if (j == 5 && cw == 0) { WAITV(0); SET_FLAG(fd + 1024); }
      if ((j == 4 && gn == 5) || j == 5) gf = ld8_sc1(FV(), sws, WS_FC);
      if (j == 2 && t == 0) {
        VWO();
#pragma unroll
        for (int e = 2; e < 7; ++e) DMA_WOUT(e)
      }
      const u32 ra_ = redr + ((j + 1) & 1) * 32768;
      f32x4 sm, r_[2];
#pragma unroll
      for (int i = 0; i < 8; ++i) {
        if (j >= 1 && j < (t ? 3 : 4) && (i & 1) == 0 && cw == 0) {
          if (i == 2) sm = r_[0] + r_[1];
          if (i > 2) sm += r_[0] + r_[1];
          r_[0] = lds16f(ra_ + i * 4096); r_[1] = lds16f(ra_ + (i + 1) * 4096);
        }
        SCHED();
        if (j < 3) bfr[u][i] = ld16_b(l16, sws, (zb0 + TILE(j + 2, gn) * 65536) ^ (i * 1024));
        else if (j == 3 && t == 0) ld16_b_if6(bfr[1][i], gn, l16, sws, (zb0 + TILE(5, gn) * 65536) ^ (i * 1024));
        SCHED();
      }
      if (j == 0 && cw == 0) gf = ld8_sc1((lane >> 5) * 128, sws, f3s);
      if (j == 1 && t != 0) {
        const u32 vo = ((lane >> 4) & 1) * 16384 + (lane >> 5) * 512 + i16 * 16;
        st16_sc1(of[0], vo, sws, WS_OF + ((8 * P + 2 * rw) * 16 + 2 * c + cw) * 1024);
        st16_sc1(of[1], vo, sws, WS_OF + ((8 * P + 2 * rw) * 16 + 2 * c + cw) * 1024 + 256);
      }
      if (j == 2) { PIN(bs); _Pragma("unroll") for (int v = 0; v < 3; ++v) PIN(z1r[v]); }
      if (j == 3) { _Pragma("unroll") for (int v = 3; v < 6; ++v) PIN(z1r[v]); }
      if (j >= 1 && cw == 0) {
        if (j < (t ? 3 : 4)) sm += r_[0] + r_[1];
        else {
          f32x4 q_[8];
#pragma unroll
          for (int o = 0; o < 8; ++o) q_[o] = lds16f(ra_ + o * 4096);
          SCHED();
          sm = ((q_[0] + q_[1]) + (q_[2] + q_[3])) + ((q_[4] + q_[5]) + (q_[6] + q_[7]));
        }
        if (j == 1) sm0 = sm;
        if (j == 2) EMIT_H(0, sm0)
        if (j >= 2) EMIT_H(j >= 2 ? j - 1 : 0, sm)
      }
      if (j == 3) {
#pragma unroll
        for (int e = 0; e < 4; ++e) DMA_WO(L_WO1, 0, e);
      }
      if (j == 3 && cw == 0) { if (t) WAITV(5); else { WAITV(12); WAITV_IF(gn, 5, 5); } SET_FLAG(fd); }
      if (j < 4) BAR_IF(cw, 0, 63);
      if (j == 4 && t == 0) BAR_IF(cw + 6 - gn, 0, 63);
    }
  }
  {
    STAMP(10);
    const u32 bufA = L_RED + (gn & 1) * 32768, bufB = L_RED + 32768 - (gn & 1) * 32768;
    const u32 fv = FV();
    WAITV(1); WAITV_IF(cw, 1, 0);
    PIN(gf);
    {
      const bool n0 = (lane & 48) != 16;
      if (!__all(!n0 || (gf[0] == tlo && gf[1] == thi))) poll_flags(fv, n0, sws, WS_FC, tlo, thi);
    }
    u32x4 hf[8], of[8];
    const u32 ofb = WS_OF + ((4 * p + rw) * 16 + cw) * 1024, hfb = WS_HF + ((4 * p + rw) * 16 + cw) * 1024;
#pragma unroll
    for (int e = 0; e < 4; ++e) of[e] = ld16_b(l16, sws, ofb + e * 2048);
#pragma unroll
    for (int e = 0; e < 4; ++e) hf[e] = ld16_b(l16, sws, hfb + e * 2048);
    BAR_IF(cw, 0, 63);
    if (cw == 0) {
      const u32 ra_ = redr + (bufB - L_RED);
      f32x4 q_[8];
#pragma unroll
      for (int o = 0; o < 8; ++o) q_[o] = lds16f(ra_ + o * 4096);
      SCHED();
      const f32x4 sm = ((q_[0] + q_[1]) + (q_[2] + q_[3])) + ((q_[4] + q_[5]) + (q_[6] + q_[7]));
      if (gn == 6) EMIT_H(5, sm) else EMIT_H(4, sm)
    }
    RELANE();
    const u32 qb = ((lane >> 2) & 3) * 64 + ((((g & 1) << 1) | ((lane >> 1) & 1)) * 16) + (lane & 1) * 8;
    f32x4 a3[4];
#pragma unroll
    for (int ct = 0; ct < 4; ++ct) a3[ct] = zero4;
    u32x4 wf[3][4], hl[4][2];
#define RDW2(dst, lo0, hi0, offl, offh) _Pragma("unroll") for (int ct = 0; ct < 4; ++ct) dst[ct] = cat(ldstr(((lo0) ^ ((ct & 1) * 32)) + (offl) + (ct >> 1) * 512), ldstr(((hi0) ^ ((ct & 1) * 32)) + (offh) + (ct >> 1) * 512))
#define MM3(src, frag) _Pragma("unroll") for (int ct = 0; ct < 4; ++ct) a3[ct] = mfma32(src[ct], frag, a3[ct])
    const u32 lb = WS_HL + ((4 * p + rw) * 8 + cw) * 2048;
    const rsrc_t sxo = __builtin_amdgcn_make_buffer_rsrc((void*)(x + (size_t)p * 65536), 0, 65536, 0x00020000);
    const u32 w0 = opq(L_WOUT + qb + (g >> 1) * 1024 + (g & 1) * 256);
    const u32 wa = w0 + cw * 4096, wb = w0 + cw * 2048;
    const u32 oq = qb + g * 1024 + cw * 4096;
    const u32 oa = opq(L_WO1 + oq), ob = opq(bufA + oq);
    u32x4 xres;
    VWO();
#pragma unroll
    for (int e = 0; e < 4; ++e) DMA_WO(bufA, 1, e);
    STAMP(11);
#define RDK(dst, k)                                                                                                 \
  if ((k) < 4) { RDW2(dst, oa, oa, (k) * 8192, (k) * 8192 + 256); }                                                 \
  else if ((k) < 8) { RDW2(dst, wa, wa, ((k) - 4) * 8192, ((k) - 4) * 8192 + 2048); }                               \
  else if ((k) < 12) { RDW2(dst, ob, ob, ((k) - 8) * 8192, ((k) - 8) * 8192 + 256); }                               \
  else { RDW2(dst, wb, w0, 32768 + ((k) - 12) * 4096, 49152 + ((k) - 12) * 2048); }
    RDK(wf[0], 0)
    RDK(wf[1], 1)
#pragma unroll
    for (int k = 0; k < 16; ++k) {
      if (k == 0) WAITV(8);
      if (k == 2) BARRIER();
      if (k == 4) WAITV(13);
      if (k == 6) {
        WAITV(0);
        PIN(gf);
        if (!__all(gf[0] == tlo && gf[1] == thi)) poll_flags(fv, true, sws, WS_FC, tlo, thi);
#pragma unroll
        for (int e = 4; e < 8; ++e) hf[e] = ld16_b(l16, sws, hfb + e * 2048);
        BARRIER();
        STAMP(12);
      }
      if (k == 12) WAITV(1 + 2 * (12 - HL_K));
      if (k + 2 < 16) { RDK(wf[(k + 2) % 3], k + 2) }
      SCHED();
      if (k < 4) { PIN(of[k]); MM3(wf[k % 3], of[k]); }
      else if (k < 8) { PIN(hf[k - 4]); MM3(wf[k % 3], hf[k - 4]); }
      else if (k < 12) { PIN(of[k - 4]); MM3(wf[k % 3], of[k - 4]); }
      else { PIN(hf[k - 8]); MM3(wf[k % 3], hf[k - 8]); }
      SCHED();
      if (k < 4) {
        if (k < 2) { of[4 + 2 * k] = ld16_b(l16, sws, ofb + (4 + 2 * k) * 2048); of[5 + 2 * k] = ld16_b(l16, sws, ofb + (5 + 2 * k) * 2048); }
        else { DMA_WOUT_AT(bufB + w * 4096, 3 + 2 * k) DMA_WOUT_AT(bufB + w * 4096, 4 + 2 * k) }
        if (k == 3) gf = ld8_sc1(fv, sws, WS_FC);
      } else if (k >= HL_K && k < HL_K + 4) {
        hl[k - HL_K][0] = ld16_sc1(l16, sws, lb + (k - HL_K) * 4096);
        hl[k - HL_K][1] = ld16_sc1(l16, sws, lb + (k - HL_K) * 4096 + 1024);
      } else if (k == 6) {
        xres = ld16((tid >> 3) * 1024 + (tid & 7) * 16, sxo, c2 * 128);
      }
    }
    STAMP(13);
    {
      const u32 wl_ = opq(bufB + (w0 - L_WOUT) + cw * 4096);
      u32x4 wg[4][4];
#pragma unroll
      for (int e = 0; e < 4; ++e) RDW2(wg[e], wl_, wl_, e * 8192, e * 8192 + 2048);
      WAITV(0);
      PIN(xres);
#pragma unroll
      for (int e = 0; e < 4; ++e) { PIN(hl[e][0]); PIN(hl[e][1]); }
#pragma nounroll
      for (int it = 0; it < SPIN_CAP; ++it) {
        u32 bad = 0;
#pragma unroll
        for (int e = 0; e < 8; ++e) bad |= (hl[e >> 1][e & 1][1] ^ tlo) | (hl[e >> 1][e & 1][3] ^ tlo);
        if (__all(bad == 0)) break;
        __builtin_amdgcn_s_sleep(2);
        asm volatile(
            "buffer_load_dwordx4 %0, %8, %9, %10 offen sc1\n\tbuffer_load_dwordx4 %1, %8, %9, %10 offen offset:1024 sc1\n\t"
            "buffer_load_dwordx4 %2, %8, %9, %11 offen sc1\n\tbuffer_load_dwordx4 %3, %8, %9, %11 offen offset:1024 sc1\n\t"
            "buffer_load_dwordx4 %4, %8, %9, %12 offen sc1\n\tbuffer_load_dwordx4 %5, %8, %9, %12 offen offset:1024 sc1\n\t"
            "buffer_load_dwordx4 %6, %8, %9, %13 offen sc1\n\tbuffer_load_dwordx4 %7, %8, %9, %13 offen offset:1024 sc1\n\t"
            "s_waitcnt vmcnt(0)"
            : "=&v"(hl[0][0]), "=&v"(hl[0][1]), "=&v"(hl[1][0]), "=&v"(hl[1][1]), "=&v"(hl[2][0]), "=&v"(hl[2][1]), "=&v"(hl[3][0]), "=&v"(hl[3][1])
            : "v"(l16), "s"(sws), "s"(lb), "s"(lb + 4096), "s"(lb + 8192), "s"(lb + 12288)
            : "memory");
      }
#pragma unroll
      for (int e = 0; e < 4; ++e) MM3(wg[e], ((u32x4){hl[e][0][0], hl[e][0][2], hl[e][1][0], hl[e][1][2]}));
    }
    STAMP(14);
    const u32 fin = L_WO1 - 2048;
#pragma unroll
    for (int ct = 0; ct < 4; ++ct) sts16f(fin + 2048 + cw * 16384 + (16 * rw + i16) * 256 + (((4 * ct + g) ^ i16) * 16), a3[ct]);
    BARRIER();
    {
      const int row = tid >> 3;
      const u32 a = fin + 2048 + row * 256 + (((2 * (tid & 7)) ^ (row & 15)) * 16);
      const f32x4 s0 = lds16f(a) + lds16f(a + 16384), s1 = lds16f(a ^ 16) + lds16f((a ^ 16) + 16384);
      u32x4 o = {pk2(blo(xres[0]) + s0[0], bhi(xres[0]) + s0[1]), pk2(blo(xres[1]) + s0[2], bhi(xres[1]) + s0[3]),
                 pk2(blo(xres[2]) + s1[0], bhi(xres[2]) + s1[1]), pk2(blo(xres[3]) + s1[2], bhi(xres[3]) + s1[3])};
      st16_sc1(o, row * 1024 + (tid & 7) * 16, __builtin_amdgcn_make_buffer_rsrc((void*)(out + (size_t)p * 65536), 0, 65536, 0x00020000), c2 * 128);
    }
  }
  }
  if (b == 0 && w == 0 && l16 == 0) *(u64*)(ws + WS_EP) = ((u64)thi << 32) | tlo;
  STAMP(15);
  STAMPC(31);
}

__global__ __launch_bounds__(512, 2) void fused_kernel(const char* __restrict__ x, const char* __restrict__ wqkv, const char* __restrict__ wphi,
                                                        const char* __restrict__ wo, const char* __restrict__ win, const char* __restrict__ wsp,
                                                        const char* __restrict__ bsp, const char* __restrict__ wout, char* __restrict__ out, char* ws) {
  __shared__ __attribute__((aligned(1024))) char smem[163840];
  asm volatile("" ::"v"((u32)(size_t)(LDSP char*)smem));
  asm volatile("" ::"s"(x), "s"(wqkv), "s"(win), "s"(ws), "s"(wsp));
  u64 pc, ep;
  asm volatile("s_getpc_b64 %0" : "=s"(pc));
  {
    const int lane = threadIdx.x & 63, g = lane >> 4;
    const int w = __builtin_amdgcn_readfirstlane(threadIdx.x >> 6);
    const int r = blockIdx.x >> 3, P = ((blockIdx.x & 7) >> 1) * 4 + (r >> 3);
    const int rot = 2 * (((r >> 3) + (r & 3)) & 3);
    u32 acv = 0, acb = 0; (void)acv; (void)acb;
    STAMP(0);
    STAMPC(30);
    if (CODE_PF && w == 7)
      touch(L_JUNK, lane * 128, __builtin_amdgcn_make_buffer_rsrc((void*)(pc & ~255ull), 0, CODE_PF, 0x00020000), (r & 7) * 8192);
    const rsrc_t sx = __builtin_amdgcn_make_buffer_rsrc((void*)(x + (size_t)P * 131072), 0, 131072, 0x00020000);
    const u32 vx0 = (16 * w + (lane >> 3)) * 1024 + (((lane & 7) ^ g) * 16);
    const u32 vx1 = (vx0 + 8192) ^ 64;
    const u32 w2k = w * 2048;
    DMA_X(0, 0, 0);
    DMA_X(0, 0, 1);
    asm volatile("s_load_dwordx2 %0, %1, 0x0" : "=s"(ep) : "s"(ws) : "memory");
    SCHED();
  }
  if (__builtin_expect(((blockIdx.x >> 5) & 1) == 0, 1)) run<0>(x, wqkv, wphi, wo, win, wsp, bsp, wout, out, ws, ep);
  else run<1>(x, wqkv, wphi, wo, win, wsp, bsp, wout, out, ws, ep);
}

extern "C" void kernel_launch(void* const* d_in, const int* in_sizes, int n_in, void* d_out, int out_size, void* d_ws, size_t ws_size,
                              hipStream_t stream) {
  fused_kernel<<<256, 512, 0, stream>>>((const char*)d_in[0], (const char*)d_in[1], (const char*)d_in[2], (const char*)d_in[3],
                                        (const char*)d_in[5], (const char*)d_in[6], (const char*)d_in[7], (const char*)d_in[8], (char*)d_out,
                                        (char*)d_ws);
}
```

```cpp
#include <hip/hip_runtime.h>
#include <hip/hip_bf16.h>

typedef unsigned int u32;
typedef unsigned long long u64;
typedef short s16x4 __attribute__((ext_vector_type(4)));
typedef __bf16 bf16x2 __attribute__((ext_vector_type(2)));
typedef __bf16 bf16x8 __attribute__((ext_vector_type(8)));
typedef float f32x16 __attribute__((ext_vector_type(16)));
typedef float f32x4 __attribute__((ext_vector_type(4)));
typedef float f32x2 __attribute__((ext_vector_type(2)));
typedef u32 u32x4 __attribute__((ext_vector_type(4)));
typedef u32 u32x2 __attribute__((ext_vector_type(2)));
typedef __amdgpu_buffer_rsrc_t rsrc_t;
#define LDSP __attribute__((address_space(3)))
#define DI __device__ __forceinline__

#define STAMPS 0

constexpr u32 WS_EP = 0;
constexpr u32 WS_FA = 8192;
constexpr u32 WS_FB = WS_FA + 16384;
constexpr u32 WS_FC = WS_FB + 16384;
constexpr u32 WS_FD = WS_FC + 16384;
constexpr u32 WS_XG = WS_FD + 65536;
constexpr u32 WS_SG = WS_XG + 32768;
constexpr u32 WS_ZB = WS_SG + 524288;
constexpr u32 WS_Z1 = WS_ZB + 43 * 65536;
constexpr u32 KP_SZ = 17408;
constexpr u32 WS_KP = WS_Z1 + 64 * 43 * 1024;
constexpr u32 WS_KF = WS_KP + 128 * KP_SZ;
constexpr u32 WS_KZ = WS_KF + 65536;
constexpr u32 WS_OF = WS_KZ + 4096;
constexpr u32 WS_HF = WS_OF + 128 * 16384;
constexpr u32 WS_HL = WS_HF + 128 * 16 * 1024;
constexpr u32 WS_ST = WS_HL + 128 * 16 * 1024;
constexpr u32 WS_END = WS_ST + 256 * 8 * 32 * 8;

constexpr u32 LZ_W = 65536;
constexpr u32 LZ_U = 139264;
constexpr u32 LQ_M = 122880;
constexpr u32 LQ_A = 81920;
constexpr u32 LQ_PHI = 49152;
constexpr u32 LQ_V = 32768;
constexpr u32 L_TAB = 155648;
constexpr u32 L_XS = L_TAB;
constexpr u32 L_CS = L_TAB + 1024;
constexpr u32 L_ZST = L_TAB + 3072;
constexpr u32 L_WPHI = L_TAB + 3072;
constexpr u32 L_KFS = L_TAB + 2048;
constexpr u32 L_JUNK = L_TAB + 7168;
constexpr u32 L_WOUT = 0;
constexpr u32 L_RED = 57344;
constexpr u32 L_WO1 = 122880;

constexpr u64 Z2S = 0x26211B16110B0500ull;
constexpr u64 Z1S = 0x26211C16110C0600ull;
DI int grp_start(u64 tab, int c) { return c >= 8 ? 43 : (int)((tab >> (8 * c)) & 0xFF); }

#define SCHED() __builtin_amdgcn_sched_barrier(0)
#define TIC()
#define TOC(acc)
#define WAITV(n) do { TIC(); asm volatile("s_waitcnt vmcnt(%0)" ::"n"(n) : "memory"); TOC(acv); SCHED(); } while (0)
#define BARRIER() do { SCHED(); TIC(); asm volatile("s_waitcnt lgkmcnt(0)\n\ts_barrier" ::: "memory"); TOC(acb); SCHED(); } while (0)
#define WAITV_IF(g_, v, n) do { TIC(); asm volatile("s_cmp_lg_u32 %0, %1\n\ts_cbranch_scc1 1f\n\ts_waitcnt vmcnt(%2)\n1:" ::"s"(g_), "n"(v), "n"(n) : "scc", "memory"); TOC(acv); SCHED(); } while (0)
#define BAR_IF(g_, v, n) do { SCHED(); asm volatile("s_cmp_lg_u32 %0, %1\n\ts_cbranch_scc1 1f\n\ts_waitcnt vmcnt(%2) lgkmcnt(0)\n\ts_barrier\n1:" ::"s"(g_), "n"(v), "n"(n) : "scc", "memory"); SCHED(); } while (0)
#define WAITL0() asm volatile("s_waitcnt lgkmcnt(0)" ::: "memory")
#define PIN(x) asm volatile("" : "+v"(x))

DI void dma16(u32 lds, u32 voff, rsrc_t srd, u32 soff) {
  asm volatile("s_mov_b32 m0, %0\n\ts_nop 0\n\tbuffer_load_dwordx4 %1, %2, %3 offen lds" ::"s"(lds), "v"(voff), "s"(srd), "s"(soff) : "memory");
}
#define DMA_C(wb, CST, voff, srd, soff) asm volatile("s_add_u32 m0, %0, %1\n\ts_nop 0\n\tbuffer_load_dwordx4 %2, %3, %4 offen lds" ::"s"(wb), "n"(CST), "v"(voff), "s"(srd), "s"(soff) : "scc", "memory")
#define DMA_CC(wb, CST, voff, srd, OP, sb, SC) do { u32 so__; asm volatile("s_add_u32 m0, %1, %2\n\t" OP " %0, %5, %6\n\tbuffer_load_dwordx4 %3, %4, %0 offen lds" : "=&s"(so__) : "s"(wb), "n"(CST), "v"(voff), "s"(srd), "s"(sb), "n"(SC) : "scc", "memory"); } while (0)
DI void touch(u32 lds, u32 voff, rsrc_t srd, u32 soff) {
  asm volatile("s_mov_b32 m0, %0\n\ts_nop 0\n\tbuffer_load_dword %1, %2, %3 offen lds" ::"s"(lds), "v"(voff), "s"(srd), "s"(soff) : "memory");
}
DI u32x4 ld16_sc1(u32 voff, rsrc_t srd, u32 soff) {
  u32x4 r;
  asm volatile("buffer_load_dwordx4 %0, %1, %2, %3 offen sc1" : "=v"(r) : "v"(voff), "s"(srd), "s"(soff) : "memory");
  return r;
}
template <int OFF> DI u32x4 ld16_sc1o(u32 voff, rsrc_t srd, u32 soff) {
  u32x4 r;
  asm volatile("buffer_load_dwordx4 %0, %1, %2, %3 offen offset:%4 sc1" : "=v"(r) : "v"(voff), "s"(srd), "s"(soff), "n"(OFF) : "memory");
  return r;
}
#define BPOL " sc1"
DI u32x4 ld16_b(u32 voff, rsrc_t srd, u32 soff) {
  u32x4 r;
  asm volatile("buffer_load_dwordx4 %0, %1, %2, %3 offen" BPOL : "=v"(r) : "v"(voff), "s"(srd), "s"(soff) : "memory");
  return r;
}
template <int OFF> DI u32x4 ld16_bo(u32 voff, rsrc_t srd, u32 soff) {
  u32x4 r;
  asm volatile("buffer_load_dwordx4 %0, %1, %2, %3 offen offset:%4" BPOL : "=v"(r) : "v"(voff), "s"(srd), "s"(soff), "n"(OFF) : "memory");
  return r;
}
DI void ld16_b_if6(u32x4& r, int gn, u32 voff, rsrc_t srd, u32 soff) {
  asm volatile("s_cmp_lg_u32 %4, 6\n\ts_cbranch_scc1 1f\n\tbuffer_load_dwordx4 %0, %1, %2, %3 offen" BPOL "\n1:" : "+v"(r) : "v"(voff), "s"(srd), "s"(soff), "s"(gn) : "scc", "memory");
}
DI u32x4 ld16(u32 voff, rsrc_t srd, u32 soff) {
  u32x4 r;
  asm volatile("buffer_load_dwordx4 %0, %1, %2, %3 offen" : "=v"(r) : "v"(voff), "s"(srd), "s"(soff) : "memory");
  return r;
}
DI u32x2 ld8_sc1(u32 voff, rsrc_t srd, u32 soff) {
  u32x2 r;
  asm volatile("buffer_load_dwordx2 %0, %1, %2, %3 offen sc1" : "=v"(r) : "v"(voff), "s"(srd), "s"(soff) : "memory");
  return r;
}
DI u32 ld2(u32 voff, rsrc_t srd, u32 soff) {
  u32 r;
  asm volatile("buffer_load_ushort %0, %1, %2, %3 offen" : "=v"(r) : "v"(voff), "s"(srd), "s"(soff) : "memory");
  return r;
}
DI void st16_sc1(u32x4 v, u32 voff, rsrc_t srd, u32 soff) {
  asm volatile("buffer_store_dwordx4 %0, %1, %2, %3 offen sc1\n\ts_nop 1" ::"v"(v), "v"(voff), "s"(srd), "s"(soff) : "memory");
}
DI void st8_sc1(u32x2 v, u32 voff, rsrc_t srd, u32 soff) {
  asm volatile("buffer_store_dwordx2 %0, %1, %2, %3 offen sc1" ::"v"(v), "v"(voff), "s"(srd), "s"(soff) : "memory");
}

DI u32x4 lds16(u32 a) { return *(const LDSP u32x4*)(size_t)a; }
DI u32x2 lds8(u32 a) { return *(const volatile LDSP u32x2*)(size_t)a; }
DI float lds4f(u32 a) { return *(const LDSP float*)(size_t)a; }
DI f32x4 lds16f(u32 a) { return *(const LDSP f32x4*)(size_t)a; }
DI void sts16f(u32 a, f32x4 v) { *(LDSP f32x4*)(size_t)a = v; }
DI void sts16(u32 a, u32x4 v) { *(LDSP u32x4*)(size_t)a = v; }
DI void sts8(u32 a, u32x2 v) { *(volatile LDSP u32x2*)(size_t)a = v; }
DI void sts4f(u32 a, float v) { *(LDSP float*)(size_t)a = v; }
DI u32x2 ldstr(u32 a) {
  return __builtin_bit_cast(u32x2, __builtin_amdgcn_ds_read_tr16_b64_v4i16((LDSP s16x4*)(size_t)a)); }
DI u32x4 cat(u32x2 lo, u32x2 hi) { return (u32x4){lo[0], lo[1], hi[0], hi[1]}; }
DI u32x4 ldfrag(u32 a, u32 d) { u32x2 lo = ldstr(a), hi = ldstr(a + d); return cat(lo, hi); }

DI f32x4 mfma32(u32x4 a, u32x4 b, f32x4 c) {
  return __builtin_amdgcn_mfma_f32_16x16x32_bf16(__builtin_bit_cast(bf16x8, a), __builtin_bit_cast(bf16x8, b), c, 0, 0, 0);
}
DI f32x16 mfmaL(u32x4 a, u32x4 b, f32x16 c) {
  return __builtin_amdgcn_mfma_f32_32x32x16_bf16(__builtin_bit_cast(bf16x8, a), __builtin_bit_cast(bf16x8, b), c, 0, 0, 0);
}
DI u32 pk2(float a, float b) { bf16x2 v = {(__bf16)a, (__bf16)b}; return __builtin_bit_cast(u32, v); }
DI float blo(u32 u) { return __uint_as_float(u << 16); }
DI float bhi(u32 u) { return __uint_as_float(u & 0xffff0000u); }
DI float rbf(float a) { return blo(pk2(a, a)); }
DI float dot2(u32 a, u32 b, float c) {
  return __builtin_amdgcn_fdot2_f32_bf16(__builtin_bit_cast(bf16x2, a), __builtin_bit_cast(bf16x2, b), c, false);
}
DI f32x2 fma2(f32x2 a, f32x2 b, f32x2 c) { return __builtin_elementwise_fma(a, b, c); }
DI f32x2 bc2(float a) { return (f32x2){a, a}; }
DI f32x2 lo2(f32x4 v) { return (f32x2){v[0], v[1]}; }
DI f32x2 hi2(f32x4 v) { return (f32x2){v[2], v[3]}; }
DI u32 pk2v(f32x2 v) { return pk2(v[0], v[1]); }
DI f32x2 gelu2(f32x2 x) {
  f32x2 u = fma2(x * x, bc2(-0.10294324f), bc2(-2.3022082f)) * x;
  f32x2 e = {__builtin_amdgcn_exp2f(u[0]), __builtin_amdgcn_exp2f(u[1])};
  e = e + bc2(1.0f);
  f32x2 r = {__builtin_amdgcn_rcpf(e[0]), __builtin_amdgcn_rcpf(e[1])};
  return x * r;
}
DI void red2g(float& a, float& b) {
  auto s = __builtin_amdgcn_permlane32_swap(__float_as_uint(a), __float_as_uint(b), false, false);
  float c = __uint_as_float(s[0]) + __uint_as_float(s[1]);
  auto s2 = __builtin_amdgcn_permlane16_swap(__float_as_uint(c), __float_as_uint(c), false, false);
  float d = __uint_as_float(s2[0]) + __uint_as_float(s2[1]);
  auto s3 = __builtin_amdgcn_permlane32_swap(__float_as_uint(d), __float_as_uint(d), false, false);
  a = __uint_as_float(s3[0]);
  b = __uint_as_float(s3[1]);
}
template <int CTRL> DI float dpp(float v) {
  return __uint_as_float(__builtin_amdgcn_update_dpp(0, __float_as_uint(v), CTRL, 0xF, 0xF, true));
}
template <class F> DI void hedgehog16(const f32x16& p, float s, u32x4 (&o)[4], F hook) {
  float mx = fmaxf(p[0], p[1]), mn = fminf(p[0], p[1]);
#pragma unroll
  for (int i = 2; i < 16; i += 2) { mx = fmaxf(fmaxf(mx, p[i]), p[i + 1]); mn = fminf(fminf(mn, p[i]), p[i + 1]); }
  const float c = s * 1.44269504f;
  const float a = -mx * c, b = mn * c;
  f32x2 ep[8], en[8];
#pragma unroll
  for (int i = 0; i < 8; ++i) {
    hook(i);
    const f32x2 v = {p[2 * i], p[2 * i + 1]};
    const f32x2 u = fma2(v, bc2(c), bc2(a)), w = fma2(v, bc2(-c), bc2(b));
    ep[i] = (f32x2){__builtin_amdgcn_exp2f(u[0]), __builtin_amdgcn_exp2f(u[1])};
    en[i] = (f32x2){__builtin_amdgcn_exp2f(w[0]), __builtin_amdgcn_exp2f(w[1])};
  }
  hook(8);
  const f32x2 sp = ((ep[0] + ep[1]) + (ep[2] + ep[3])) + ((ep[4] + ep[5]) + (ep[6] + ep[7]));
  const f32x2 sn = ((en[0] + en[1]) + (en[2] + en[3])) + ((en[4] + en[5]) + (en[6] + en[7]));
  const float ip = __builtin_amdgcn_rcpf(sp[0] + sp[1]), in = __builtin_amdgcn_rcpf(sn[0] + sn[1]);
#pragma unroll
  for (int i = 0; i < 8; ++i) {
    if (i == 0 || i == 3 || i == 6) hook(9 + i / 3);
    o[i >> 2][i & 3] = pk2v(ep[i] * bc2(ip));
    o[2 + (i >> 2)][i & 3] = pk2v(en[i] * bc2(in));
  }
}
DI void ln_fin(float s, float q, float invn, float& r, float& m) {
  m = s * invn;
  float v = fmaxf(q * invn - m * m, 0.0f);
  float sd = rbf(__builtin_amdgcn_sqrtf(rbf(rbf(v) + 1e-5f)));
  r = __builtin_amdgcn_rcpf(sd);
  m = rbf(m);
}

#define POLL_SLEEP 6
constexpr int SPIN_CAP = 1 << 17;
DI void poll_flags(u32 voff, bool act, rsrc_t srd, u32 soff, u32 tlo, u32 thi) {
#pragma nounroll
  for (int it = 0; it < SPIN_CAP; ++it) {
    u32x2 v;
    asm volatile("buffer_load_dwordx2 %0, %1, %2, %3 offen sc1\n\ts_waitcnt vmcnt(0)" : "=&v"(v) : "v"(voff), "s"(srd), "s"(soff) : "memory");
    bool ok = !act || (v[0] == tlo && v[1] == thi);
    if (__all(ok)) break;
    __builtin_amdgcn_s_sleep(POLL_SLEEP);
  }
}
DI bool tag_ok(u32x4 q, u32 t) { return q[1] == t && q[3] == t; }
DI void poll_gran(u32x4& q, u32 voff, rsrc_t srd, u32 soff, u32 tlo) {
#pragma nounroll
  for (int it = 0; it < SPIN_CAP; ++it) {
    if (__all(tag_ok(q, tlo))) break;
    __builtin_amdgcn_s_sleep(POLL_SLEEP);
    asm volatile("buffer_load_dwordx4 %0, %1, %2, %3 offen sc1\n\ts_waitcnt vmcnt(0)" : "=&v"(q) : "v"(voff), "s"(srd), "s"(soff) : "memory");
  }
}
DI u32 opq(u32 v) { asm volatile("" : "+v"(v)); return v; }

#define STAMP(i)
#define STAMPC(i)

#define GQ_N 3
#define PREFETCH 0
#define CODE_PF 56000
#define OUT_SC1 1
#define PRIO 0
#define UROT 1
static_assert(WS_ZB % 8192 == 0, "");
#define TLB_PF 0
#define VM(x) do { x; ++vq; } while (0)
#define WAITQ(mark) WAITV(vq - (mark))

template <int t>
DI void run(const char* __restrict__ x, const char* __restrict__ wqkv, const char* __restrict__ wphi, const char* __restrict__ wo,
            const char* __restrict__ win, const char* __restrict__ wsp, const char* __restrict__ bsp, const char* __restrict__ wout,
            char* __restrict__ out, char* ws, u64 ep) {
  const int tid = threadIdx.x, lane = tid & 63;
  const int w = __builtin_amdgcn_readfirstlane(tid >> 6);
  const int g = lane >> 4;
  const int b = blockIdx.x, xcd = b & 7, r = b >> 3;
  const int Pl = r >> 3, cl = r & 3;
  const int P = (xcd >> 1) * 4 + Pl;
  const int rot = 2 * ((Pl + cl) & 3);
  u32 acv = 0, acb = 0; (void)acv; (void)acb;
  const rsrc_t sx = __builtin_amdgcn_make_buffer_rsrc((void*)(x + (size_t)P * 131072), 0, 131072, 0x00020000);
  const u32 vx0 = (16 * w + (lane >> 3)) * 1024 + (((lane & 7) ^ g) * 16);
  const u32 vx1 = (vx0 + 8192) ^ 64;
  const u32 w2k = w * 2048;
#define DMA_X(dst, n, e) DMA_C(w2k, (dst) + (e) * 1024, (e) ? vx1 : vx0, sx, (((n) + rot) & 7) * 128)
  const int rw = w & 3, cw = w >> 2, i16 = lane & 15;
  const int c = (xcd & 1) * 4 + cl;
  const int p = 2 * P + (cl & 1);
  const int c2 = (xcd & 1) * 4 + (t ? 3 * (cl >> 1) : 1 + (cl >> 1));
  const u32 l16 = opq(lane * 16);
  const rsrc_t sa = __builtin_amdgcn_make_buffer_rsrc((void*)(wsp + (size_t)p * 262144), 0, 262144, 0x00020000);
  const rsrc_t sws = __builtin_amdgcn_make_buffer_rsrc((void*)ws, 0, WS_END, 0x00020000);
  const u32 vA0 = (lane >> 3) * 4096 + (((lane & 7) ^ g) * 16);
  const u32 vA1 = (vA0 + 32768) ^ 64;
  const int ux0 = UROT ? (cl >> 1) : 0, ux1 = UROT ? t : 0;
  const u32 uxs = w * 512 + ux1 * 256 + ux0 * 131072;
#define DMA_U(u, base, e) DMA_CC(w2k, (base) + (e) * 1024, (e) ? vA1 : vA0, sa, "s_xor_b32", uxs, (((u) >> 1) & 3) * 128 + (((u) >> 3) * 32 + ((u) & 1) * 16) * 4096)
  const f32x4 zero4 = {0, 0, 0, 0};
  u32x4 ones4 = {0x3F803F80u, 0x3F803F80u, 0x3F803F80u, 0x3F803F80u};
  u32 tlo, thi;
  u32x4 af[4][8];
  u32x4 bfr[2][8];
  u32x2 gf;
  const u32 ard = w * 2048 + i16 * 128 + (((g >> 1) ^ (i16 >> 1)) * 16) + (g & 1) * 8;
#define READ_U(u, base)                                                                                             \
  {                                                                                                                 \
    const u32 ab_ = opq(ard + (base));                                                                              \
    _Pragma("unroll") for (int kk = 0; kk < 2; ++kk)                                                               \
      af[2 * ((u) >> 3) + ((u) & 1)][((u) & 6) + kk] = cat(lds8(ab_ ^ (64 * kk)), lds8(ab_ ^ (64 * kk + 32)));      \
  }
#define USE_EPOCH() { asm volatile("" : "+s"(ep)); ep += 1; tlo = (u32)ep; thi = (u32)(ep >> 32); PIN(ones4); }
#define EARLY_STATS(mark)                                                                                           \
  {                                                                                                                 \
    WAITQ(mark); PIN(xrow);                                                                                         \
    float s_ = 0, q_ = 0;                                                                                           \
    _Pragma("unroll") for (int e = 0; e < 4; ++e) { s_ = dot2(xrow[e], ones4[0], s_); q_ = dot2(xrow[e], xrow[e], q_); } \
    s_ += dpp<0x128>(s_); q_ += dpp<0x128>(q_);                                                                     \
    s_ += dpp<0x124>(s_); q_ += dpp<0x124>(q_);                                                                     \
    s_ += dpp<0x4E>(s_); q_ += dpp<0x4E>(q_);                                                                       \
    s_ += dpp<0xB1>(s_); q_ += dpp<0xB1>(q_);                                                                       \
    red2g(s_, q_);                                                                                                  \
    float r_, m_;                                                                                                   \
    ln_fin(s_, q_, 1.0f / 512, r_, m_);                                                                             \
    VM(st16_sc1((u32x4){__float_as_uint(r_), tlo, __float_as_uint(m_), tlo}, l16 << 22, sws, WS_XG + (P * 128 + (t * 8 + c) * 8 + w) * 16)); \
  }
#define GATHER_XS()                                                                                                 \
  WAITQ(mxg); PIN(xg);                                                                                              \
  poll_gran(xg, i16 * 16, sws, WS_XG + (P * 128 + 16 * w) * 16, tlo);                                               \
  sts4f(L_XS + (16 * w + i16) * 4, __uint_as_float(xg[0]));                                                         \
  sts4f(L_XS + 512 + (16 * w + i16) * 4, __uint_as_float(xg[2]));
#define RELANE() const int lane = opq(l16) >> 4, i16 = lane & 15, g = lane >> 4, tid = w * 64 + lane; (void)tid; (void)g; (void)i16
#define SET_FLAG(off) st8_sc1((u32x2){tlo, thi}, l16 << 22, sws, (off))
  const int gs = grp_start(Z2S, c2), gn = t ? 5 : grp_start(Z2S, c2 + 1) - gs;
  const int trot = (p & 7) >= 5 ? (p & 7) - 5 : (p & 7);
#define TILE(j, n_) (((j) + trot) >= (n_) ? (j) + trot - (n_) : (j) + trot)
  const u32 zb0 = WS_ZB + (gs * 64 + 8 * w) * 1024 + ux1 * 4096;
#define DMA_WOUT(e)                                                                                                 \
  {                                                                                                                 \
    const rsrc_t so_ = __builtin_amdgcn_make_buffer_rsrc((void*)wout, 0, 688 * 1024, 0x00020000);                   \
    const int ws_ = grp_start(Z2S, w), wn_ = grp_start(Z2S, w + 1) - ws_;                                           \
    if ((e) < 6) dma16(L_WOUT + ((e) < 4 ? 2 * w + ((e) >> 1) : 16 + w) * 2048 + ((e) & 1) * 1024, vwo, so_, ((ws_ + TILE((e) >> 1, wn_)) * 2 + ((e) & 1)) * 8192); \
    else if ((e) == 6) dma16(L_WOUT + 24 * 2048 + w * 1024, vwo, so_, ((((w >> 1) == 0 ? 5 : ((w >> 1) == 1 ? 11 : ((w >> 1) == 2 ? 27 : 64))) + TILE(3, 6)) * 2 + (w & 1)) * 8192); \
  }
#define DMA_WOUT_AT(dst, e)                                                                                         \
  {                                                                                                                 \
    const rsrc_t so_ = __builtin_amdgcn_make_buffer_rsrc((void*)wout, 0, 688 * 1024, 0x00020000);                   \
    const int ws_ = grp_start(Z2S, w), wn_ = grp_start(Z2S, w + 1) - ws_;                                           \
    dma16((dst) + ((e) - 7) * 1024, vwo, so_, ((ws_ + TILE(wn_ - 1 - (((e) - 7) >> 1), wn_)) * 2 + (((e) - 7) & 1)) * 8192); \
  }
#define VWO() const u32 vwo = ((lane >> 2) & 7) * 1024 + c2 * 128 + (lane >> 5) * 64 + (((lane & 3) ^ (((lane >> 4) & 1) << 1)) * 16)
#define LOAD_B(u, so_)                                                                                              \
  bfr[u][0] = ld16_bo<0>(l16, sws, so_); bfr[u][1] = ld16_bo<1024>(l16, sws, so_);                                  \
  bfr[u][2] = ld16_bo<2048>(l16, sws, so_); bfr[u][3] = ld16_bo<3072>(l16, sws, so_);                               \
  bfr[u][4] = ld16_bo<0>(l16, sws, (so_) ^ 4096); bfr[u][5] = ld16_bo<1024>(l16, sws, (so_) ^ 4096);                \
  bfr[u][6] = ld16_bo<2048>(l16, sws, (so_) ^ 4096); bfr[u][7] = ld16_bo<3072>(l16, sws, (so_) ^ 4096);

  if constexpr (t == 0) {
    int vq = 2;
    const rsrc_t sw = __builtin_amdgcn_make_buffer_rsrc((void*)win, 0, 512 * 2752, 0x00020000);
    const int z2s = grp_start(Z2S, c), z2n = grp_start(Z2S, c + 1) - z2s;
    const int z1s = grp_start(Z1S, c), z1n = grp_start(Z1S, c + 1) - z1s;
    u32 vw[3];
    int wch[3];
#pragma unroll
    for (int e = 0; e < 3; ++e) {
      const int sg = 2 * e + (lane >> 5), hi = sg >= 3;
      wch[e] = (sg - 3 * hi) * 4 + ((lane & 3) ^ (hi << 1));
      vw[e] = wch[e] < 2 * z2n ? (16 * w + 8 * hi + ((lane >> 2) & 7)) * 2752 + 1376 + 32 * z2s + 16 * wch[e] : 0x7FFF0000u;
    }
    const u32 w3k = w * 3072;
#define DMA_W(bk, e) DMA_C(w3k, LZ_W + ((bk) % 3) * 24576 + (e) * 1024, vw[e], sw, ((2 * (bk) + rot) & 7) * (64 * 2752))
#define UBASE(u) ((u) % 5 == 0 ? LZ_U : ((u) % 5 - 1) * 16384)
#define ISS_X(n) do { VM(DMA_X(((n) & 3) * 16384, n, 0)); VM(DMA_X(((n) & 3) * 16384, n, 1)); mx[n] = vq; } while (0)
#define ISS_U(u) do { VM(DMA_U(u, UBASE(u), 0)); VM(DMA_U(u, UBASE(u), 1)); mu[u] = vq; } while (0)
#define ISS_W(bk, e0, e1) do { _Pragma("unroll") for (int e_ = e0; e_ < e1; ++e_) VM(DMA_W(bk, e_)); mw[bk] = vq; } while (0)
    int mx[8], mw[8], mu[16];
    mx[0] = vq;
    ISS_W(0, 0, 3);
    u32x4 xrow;
    VM(xrow = ld16(l16, sx, (c * 8 + w) * 1024));
    const int mxr = vq;
    ISS_X(1);
    const u32 xa0 = opq((32 * rw + i16) * 128 + ((g ^ (i16 >> 1)) * 16)), xa1 = xa0 ^ 64;
    u32 ta[3];
    int tl[3];
    {
      const u32 trb = LZ_W + g * 1536 + ((lane >> 2) & 3) * 64 + ((((g & 1) << 1) | ((lane >> 1) & 1)) * 16) + (lane & 1) * 8;
#pragma unroll
      for (int j = 0; j < 3; ++j) {
        tl[j] = 3 * cw + (j + rw) % 3;
        ta[j] = (trb + (tl[j] >> 1) * 512) ^ ((tl[j] & 1) * 32);
      }
    }
#define NEXT_BLOCK(bk) _Pragma("unroll") for (int j = 0; j < 3; ++j) ta[j] = opq(ta[j] + ((bk) == 0 ? 0 : ((bk) % 3 == 0 ? -49152 : 24576)))
    f32x4 acc[2][3], csa = zero4;
#pragma unroll
    for (int j = 0; j < 3; ++j) acc[0][j] = acc[1][j] = zero4;
    u32x4 xf[16][2], wf[2][3];
    u32x4 xg;
    int mxg = 0;
    WAITQ(mw[0]);
    BARRIER();
    USE_EPOCH()
    STAMP(7);
#define P1_MEM(n, h)                                                                                                \
  {                                                                                                                 \
    if ((n) == 0) { if ((h) == 0) ISS_X(2); if ((h) == 1) ISS_W(1, 0, 2); if ((h) == 2) ISS_W(1, 2, 3); if ((h) == 3) ISS_X(3); } \
    if ((n) == 1) { if ((h) == 0) ISS_W(2, 0, 2); if ((h) == 1) ISS_W(2, 2, 3); if ((h) == 2) VM(DMA_X(0, 4, 0)); if ((h) == 3) { VM(DMA_X(0, 4, 1)); mx[4] = vq; } } \
    if ((n) == 2 || (n) == 4) { if ((h) < 2) { VM(DMA_X((((n) + 3) & 3) * 16384, (n) + 3, (h) & 1)); mx[(n) + 3] = vq; } else ISS_W((n) / 2 + 2, (h) - 2, (h) - 1); } \
    if ((n) == 3) { if ((h) == 0) ISS_W(3, 2, 3); else if ((h) < 3) { VM(DMA_X(32768, 6, (h) - 1)); mx[6] = vq; } else ISS_U(0); } \
    if ((n) == 5) { if ((h) == 0) ISS_W(4, 2, 3); if ((h) == 1) { VM(xg = ld16_sc1(i16 * 16, sws, WS_XG + (P * 128 + 16 * w) * 16)); mxg = vq; } \
                    if ((h) >= 2) { VM(DMA_U(1, UBASE(1), (h) - 2)); mu[1] = vq; } }                                 \
    if ((n) == 6) { if ((h) < 3) ISS_W(5, h, (h) + 1); else ISS_U(2); }                                             \
    if ((n) == 7) { if ((h) < 2) { VM(DMA_U(3, UBASE(3), h)); mu[3] = vq; } }                                       \
  }
#pragma unroll
    for (int ks = 0; ks <= 16; ++ks) {
      const int n = ks >> 1, kk = ks & 1;
      if (ks > 0 && ks < 16 && kk == 0) {
        { const int mk = (n & 1) ? mx[n] : (mx[n] > mw[n >> 1] ? mx[n] : mw[n >> 1]); WAITQ(mk); }
        BARRIER();
      }
      if (ks < 16 && (ks & 3) == 0) { NEXT_BLOCK(ks >> 2); }
      if (ks == 8) {
#pragma unroll
        for (int e = 0; e < 3; ++e)
          vw[e] = wch[e] < 2 * z1n ? (16 * w + 8 * (2 * e + (lane >> 5) >= 3) + ((lane >> 2) & 7)) * 2752 + 32 * z1s + 16 * wch[e] : 0x7FFF0000u;
      }
      const int k1 = ks > 0 ? ks - 1 : 0, q1 = k1 & 1;
#pragma unroll
      for (int m = 0; m < 7; ++m) {
        if (ks > 0) {
          if (m < 6) acc[m & 1][m >> 1] = mfma32(xf[k1][m & 1], wf[q1][m >> 1], acc[m & 1][m >> 1]);
          else csa = mfma32(ones4, wf[q1][0], csa);
          SCHED();
        }
        if (ks < 16) {
          if (m == 0) { P1_MEM(n, 2 * kk) xf[ks & 15][0] = lds16((kk ? xa1 : xa0) + (n & 3) * 16384); xf[ks & 15][1] = lds16((kk ? xa1 : xa0) + (n & 3) * 16384 + 2048); }
          else if (m < 4) wf[kk][m - 1] = ldfrag(ta[m - 1] + (ks & 3) * 6144, 256);
          if (m == 4) P1_MEM(n, 2 * kk + 1)
        }
        SCHED();
      }
#pragma unroll
      for (int j = 0; j < 3; ++j) { PIN(acc[0][j]); PIN(acc[1][j]); }
      PIN(csa);
      if (ks == 2) EARLY_STATS(mxr)
    }
    GATHER_XS()
    sts4f(L_CS + tl[0] * 64 + i16 * 4, csa[0]);
    WAITQ(mu[1] > mw[4] ? mu[1] : mw[4]);
    READ_U(0, UBASE(0)) READ_U(1, UBASE(1))
    BARRIER();
    STAMP(1);
    u32x4 zp[3];
    ISS_W(6, 0, 3);
    {
      f32x2 ps[4], pq[4];
      float cs[3];
#pragma unroll
      for (int j = 0; j < 3; ++j) cs[j] = -lds4f(L_CS + tl[j] * 64 + i16 * 4);
#pragma unroll
      for (int rt = 0; rt < 2; ++rt) {
        const f32x4 rr = lds16f(L_XS + (32 * rw + 16 * rt + 4 * g) * 4), mm = lds16f(L_XS + 512 + (32 * rw + 16 * rt + 4 * g) * 4);
        f32x2 z0[3], z1[3];
#pragma unroll
        for (int j = 0; j < 3; ++j) {
          z0[j] = gelu2(fma2(lo2(mm), bc2(cs[j]), lo2(acc[rt][j])) * lo2(rr));
          z1[j] = gelu2(fma2(hi2(mm), bc2(cs[j]), hi2(acc[rt][j])) * hi2(rr));
          zp[j][2 * rt] = pk2v(z0[j]); zp[j][2 * rt + 1] = pk2v(z1[j]);
        }
        ps[2 * rt] = z0[0] + z0[1] + z0[2]; ps[2 * rt + 1] = z1[0] + z1[1] + z1[2];
        pq[2 * rt] = fma2(z0[2], z0[2], fma2(z0[1], z0[1], z0[0] * z0[0]));
        pq[2 * rt + 1] = fma2(z1[2], z1[2], fma2(z1[1], z1[1], z1[0] * z1[0]));
        SCHED();
        if (rt == 0) { ISS_U(4); ISS_U(5); }
        else ISS_U(6);
        SCHED();
      }
      const bool b3 = lane & 8, b2 = lane & 4, b1 = lane & 2;
#pragma unroll
      for (int m = 0; m < 2; ++m)
#pragma unroll
        for (int e = 0; e < 2; ++e) {
          ps[m][e] = (b3 ? ps[m + 2][e] : ps[m][e]) + dpp<0x128>(b3 ? ps[m][e] : ps[m + 2][e]);
          pq[m][e] = (b3 ? pq[m + 2][e] : pq[m][e]) + dpp<0x128>(b3 ? pq[m][e] : pq[m + 2][e]);
        }
#pragma unroll
      for (int e = 0; e < 2; ++e) {
        ps[0][e] = (b2 ? ps[1][e] : ps[0][e]) + dpp<0x141>(b2 ? ps[0][e] : ps[1][e]);
        pq[0][e] = (b2 ? pq[1][e] : pq[0][e]) + dpp<0x141>(b2 ? pq[0][e] : pq[1][e]);
      }
      float s_ = (b1 ? ps[0][1] : ps[0][0]) + dpp<0x4E>(b1 ? ps[0][0] : ps[0][1]);
      float q_ = (b1 ? pq[0][1] : pq[0][0]) + dpp<0x4E>(b1 ? pq[0][0] : pq[0][1]);
      s_ += dpp<0xB1>(s_);
      q_ += dpp<0xB1>(q_);
      const int row = 32 * rw + 16 * (i16 >> 3) + 4 * g + ((i16 >> 1) & 3);
      VM(st16_sc1((u32x4){__float_as_uint(s_), tlo, __float_as_uint(q_), tlo}, row * 16 + ((lane & 1) << 30), sws, WS_SG + ((P * 8 + c) * 2 + cw) * 2048));
      PIN(zp[0]); PIN(zp[1]); PIN(zp[2]);
    }
    STAMP(2);
    {
    RELANE();
#pragma unroll
    for (int j = 0; j < 3; ++j) acc[0][j] = acc[1][j] = zero4;
    csa = zero4;
    u32x4 q0, q1, q2, q3;
    int mq = 0, mz = 0;
    const u32 gvo = (2 * g) * 4096 + (32 * rw + 16 * cw + i16) * 16;
    const u32 gso = WS_SG + P * 32768;
#pragma unroll
    for (int ks = 0; ks <= 16; ++ks) {
      if (ks == 8) {
        WAITQ(mq); PIN(q0); PIN(q1); PIN(q2); PIN(q3);
#pragma nounroll
        for (int it = 0; it < SPIN_CAP; ++it) {
          if (__all(tag_ok(q0, tlo) && tag_ok(q1, tlo) && tag_ok(q2, tlo) && tag_ok(q3, tlo))) break;
          __builtin_amdgcn_s_sleep(POLL_SLEEP);
          asm volatile(
              "buffer_load_dwordx4 %0, %4, %5, %6 offen sc1\n\t"
              "buffer_load_dwordx4 %1, %4, %5, %6 offen offset:2048 sc1\n\t"
              "buffer_load_dwordx4 %2, %4, %5, %7 offen sc1\n\t"
              "buffer_load_dwordx4 %3, %4, %5, %7 offen offset:2048 sc1\n\t"
              "s_waitcnt vmcnt(0)"
              : "=&v"(q0), "=&v"(q1), "=&v"(q2), "=&v"(q3)
              : "v"(gvo), "s"(sws), "s"(gso), "s"(gso + 4096)
              : "memory");
        }
        float s = (__uint_as_float(q0[0]) + __uint_as_float(q1[0])) + (__uint_as_float(q2[0]) + __uint_as_float(q3[0]));
        float q = (__uint_as_float(q0[2]) + __uint_as_float(q1[2])) + (__uint_as_float(q2[2]) + __uint_as_float(q3[2]));
        red2g(s, q);
        float r2, m2;
        ln_fin(s, q, 1.0f / 688, r2, m2);
        sts4f(L_ZST + (32 * rw + 16 * cw + i16) * 4, r2);
        sts4f(L_ZST + 512 + (32 * rw + 16 * cw + i16) * 4, -m2 * r2);
        STAMP(3);
      }
      if (ks < 16 && (ks & 3) == 0) {
        if (ks > 0) { WAITQ(ks == 12 ? mz : mw[4 + (ks >> 2)]); BARRIER(); }
        if (ks == 12) { if (w == 0) SET_FLAG(WS_FA + (P * 8 + c) * 128); STAMP(5); }
        NEXT_BLOCK(4 + (ks >> 2));
      }
      if (ks == 8) {
#pragma unroll
        for (int rt = 0; rt < 2; ++rt) {
          const u32 ra = L_ZST + (32 * rw + 16 * rt + 4 * g) * 4;
          const f32x4 r4 = lds16f(ra), m4 = lds16f(ra + 512);
#pragma unroll
          for (int j = 0; j < 3; ++j) {
            const u32 u0 = zp[j][2 * rt], u1 = zp[j][2 * rt + 1];
            zp[j][2 * rt] = pk2v(fma2((f32x2){blo(u0), bhi(u0)}, lo2(r4), lo2(m4)));
            zp[j][2 * rt + 1] = pk2v(fma2((f32x2){blo(u1), bhi(u1)}, hi2(r4), hi2(m4)));
          }
        }
#pragma unroll
        for (int j = 0; j < 3; ++j)
          VM(st16_sc1(zp[j], l16, sws, (tl[j] < z2n) ? WS_ZB + ((z2s + tl[j]) * 64 + 4 * P + rw) * 1024 : 0x7FFF0000u));
        mz = vq;
        STAMP(4);
        SCHED();
      }
      if (ks >= 5 && ks <= 9) { WAITQ(mu[ks - 3]); READ_U(ks - 3, UBASE(ks - 3)) }
      if (ks >= 9 && ks <= 13) { WAITQ(mu[ks - 2]); READ_U(ks - 2, UBASE(ks - 2)) }
      if (ks >= 13) { WAITQ(mu[ks - 1]); READ_U(ks - 1, UBASE(ks - 1)) }
      const int k1 = ks > 0 ? ks - 1 : 0, q1_ = k1 & 1;
#pragma unroll
      for (int m = 0; m < 7; ++m) {
        if (ks > 0) {
          if (m < 6) acc[m & 1][m >> 1] = mfma32(wf[q1_][m >> 1], xf[k1][m & 1], acc[m & 1][m >> 1]);
          else csa = mfma32(ones4, wf[q1_][0], csa);
          SCHED();
        }
        if (ks < 16 && m >= 1 && m < 4) wf[ks & 1][m - 1] = ldfrag(ta[m - 1] + (ks & 3) * 6144, 256);
        if (m == 0 || m == 4) {
          const int h = m == 4;
          if (h == 0 && ks >= 6 && ks <= 13) asm volatile("s_waitcnt lgkmcnt(10)" ::: "memory");
          if (ks >= 6 && ks <= 10) { VM(DMA_U(ks + 1, UBASE(ks + 1), h)); mu[ks + 1] = vq; }
          if (ks >= 10 && ks <= 13) { VM(DMA_U(ks + 2, UBASE(ks + 2), h)); mu[ks + 2] = vq; }
        }
        if (m == 6) {
          if (ks >= 4 && ks <= 6) ISS_W(7, ks - 4, ks - 3);
        }
        if (m == 2) {
          if (ks == 5) { VM(q0 = ld16_sc1o<0>(gvo, sws, gso)); VM(q1 = ld16_sc1o<2048>(gvo, sws, gso)); }
          if (ks == 6) { VM(q2 = ld16_sc1o<0>(gvo, sws, gso + 4096)); VM(q3 = ld16_sc1o<2048>(gvo, sws, gso + 4096)); mq = vq; }
        }
        SCHED();
      }
#pragma unroll
      for (int j = 0; j < 3; ++j) { PIN(acc[0][j]); PIN(acc[1][j]); }
      PIN(csa);
    }
    sts4f(L_CS + 512 + tl[0] * 64 + i16 * 4, csa[0]);
    gf = ld8_sc1((lane >> 5) * 1024, sws, WS_FA + (16 * w + c2) * 128);
    BARRIER();
    VWO();
    DMA_WOUT(0) DMA_WOUT(1)
    WAITV(2);
    PIN(gf);
    if (!__all(gf[0] == tlo && gf[1] == thi)) poll_flags((lane >> 5) * 1024, true, sws, WS_FA + (16 * w + c2) * 128, tlo, thi);
    STAMP(6);
    {
      float xr[2], xm[2];
#pragma unroll
      for (int rt = 0; rt < 2; ++rt) {
        xr[rt] = lds4f(L_XS + (32 * rw + 16 * rt + i16) * 4);
        xm[rt] = -lds4f(L_XS + 512 + (32 * rw + 16 * rt + i16) * 4);
      }
      const u32 so0 = zb0 + TILE(0, gn) * 65536;
#pragma unroll
      for (int j = 0; j < 3; ++j) {
        const f32x4 cc = lds16f(L_CS + 512 + tl[j] * 64 + g * 16);
        bfr[0][3 * j] = ld16_b(l16, sws, so0 ^ (3 * j * 1024));
        bfr[0][3 * j + 1] = ld16_b(l16, sws, so0 ^ ((3 * j + 1) * 1024));
        if (j < 2) bfr[0][3 * j + 2] = ld16_b(l16, sws, so0 ^ ((3 * j + 2) * 1024));
        u32x4 zz;
#pragma unroll
        for (int rt = 0; rt < 2; ++rt) {
          zz[2 * rt] = pk2v(gelu2(fma2(lo2(cc), bc2(xm[rt]), lo2(acc[rt][j])) * bc2(xr[rt])));
          zz[2 * rt + 1] = pk2v(gelu2(fma2(hi2(cc), bc2(xm[rt]), hi2(acc[rt][j])) * bc2(xr[rt])));
        }
        st16_sc1(zz, l16, sws, (tl[j] < z1n) ? WS_Z1 + ((4 * P + rw) * 43 + z1s + tl[j]) * 1024 : 0x7FFF0000u);
        SCHED();
      }
    }
    }
  } else {
    int vq = 2;
    const rsrc_t sw = __builtin_amdgcn_make_buffer_rsrc((void*)wqkv, 0, 512 * 3072, 0x00020000);
    const u32 vw = (8 * w + ((lane >> 2) & 7)) * 3072 + 128 * c + ((lane >> 5) * 4 + ((lane & 3) ^ ((w & 1) << 1))) * 16;
    const u32 w3k = w * 3072;
#define DMA_S(n, e)                                                                                                 \
  do {                                                                                                              \
    if ((e) < 2) DMA_X(((n) % 3) * 40960, n, (e) & 1);                                                              \
    else DMA_CC(w3k, ((n) % 3) * 40960 + 16384 + ((e) - 2) * 1024, vw, sw, "s_add_u32", (((n) + rot) & 7) * (64 * 3072), ((e) - 2) * 1024); \
  } while (0)
#define ISS_Q(u, base) do { VM(DMA_U(u, base, 0)); VM(DMA_U(u, base, 1)); mu[u] = vq; } while (0)
    int ms[8], mu[16];
    VM(DMA_S(0, 2)); VM(DMA_S(0, 3)); VM(DMA_S(0, 4)); ms[0] = vq;
    u32x4 xrow;
    VM(xrow = ld16(l16, sx, ((8 + c) * 8 + w) * 1024));
    const int mxr = vq;
#pragma unroll
    for (int e = 0; e < 5; ++e) { VM(DMA_S(1, e)); ms[1] = vq; }
    const int l32 = lane & 31, hh = lane >> 5;
    u32 xa = (32 * rw + l32) * 128 + ((hh ^ ((l32 >> 1) & 7)) * 16);
    u32 wa = 16384 + hh * 3072 + cw * 512 + ((lane >> 2) & 3) * 64 + (((((g & 1) << 1) | ((lane >> 1) & 1)) ^ (hh << 1)) * 16) + (lane & 1) * 8;
    u32 ca = 16384 + g * 3072 + min(w, 5) * 512 + ((lane >> 2) & 3) * 64 + ((((g & 1) << 1) | ((lane >> 1) & 1)) * 16) + (lane & 1) * 8;
    const f32x16 zero16 = {0, 0, 0, 0, 0, 0, 0, 0, 0, 0, 0, 0, 0, 0, 0, 0};
    f32x16 acc[3] = {zero16, zero16, zero16};
    f32x4 csa[2] = {zero4, zero4};
    u32x4 xg;
    int mxg = 0;
    WAITQ(ms[0]);
    BARRIER();
    USE_EPOCH()
    STAMP(7);
    {
      u32x4 xf[2], wf[2][4];
#pragma unroll
      for (int n = 0; n <= 8; ++n)
#pragma unroll
      for (int sq = 0; sq < 4; ++sq) {
        const int s = 4 * n + sq;
        if (s > 32) continue;
        if (s < 32 && sq == 0) {
          if (n > 0) {
            WAITQ(ms[n]);
            BARRIER();
          }
          const int d = n == 0 ? 0 : (n % 3 == 0 ? -81920 : 40960);
          xa = opq(xa + d); wa = opq(wa + d); ca = opq(ca + d);
        }
        const int q1 = (s + 1) & 1, q0 = s & 1;
#pragma unroll
        for (int m = 0; m < 4; ++m) {
          if (s > 0) {
            if (m < 3) acc[m] = mfmaL(wf[q1][m], xf[q1], acc[m]);
            else csa[q1] = mfma32(ones4, wf[q1][3], csa[q1]);
            SCHED();
          }
          if (s < 32) {
            if (m == 0) { xf[q0] = lds16(xa ^ (32 * sq)); wf[q0][0] = ldfrag(wa + sq * 6144, 256); }
            if (m == 1) { wf[q0][1] = ldfrag(wa + sq * 6144 + 1024, 256); wf[q0][2] = ldfrag(wa + sq * 6144 + 2048, 256); }
            if (m == 2) wf[q0][3] = ldfrag((ca ^ (32 * (sq & 1))) + (sq >> 1) * 12288, 256);
            if (m == 0 || m == 2) {
              const int h = 2 * sq + (m == 2);
              if (n <= 5 && h < 5) { VM(DMA_S(n + 2, h)); ms[n + 2] = vq; }
              if (n == 0 && h == 5) {
                rsrc_t sp = __builtin_amdgcn_make_buffer_rsrc((void*)wphi, 0, 16384, 0x00020000);
                VM(dma16(L_WPHI + (w & 1) * 1024, l16, sp, c * 2048 + (w & 1) * 1024));
              }
              if (n == 4 && h == 5) { VM(xg = ld16_sc1(i16 * 16, sws, WS_XG + (P * 128 + 16 * w) * 16)); mxg = vq; }
              if (n == 6) { VM(DMA_U(sq, (sq < 2 ? LQ_M : LQ_A) + (sq & 1) * 16384, h & 1)); mu[sq] = vq; }
              if (n == 7 && sq < 2) { VM(DMA_U(4 + sq, sq * 16384, h & 1)); mu[4 + sq] = vq; }
            }
          }
          SCHED();
        }
        PIN(acc[0]); PIN(acc[1]); PIN(acc[2]); PIN(csa[0]); PIN(csa[1]);
        if (s == 3) EARLY_STATS(mxr)
      }
    }
    GATHER_XS()
    sts4f(L_CS + w * 128 + i16 * 4, csa[0][0]);
    sts4f(L_CS + w * 128 + 64 + i16 * 4, csa[1][0]);
    BARRIER();
    STAMP(1);
    WAITQ(mu[3]);
    READ_U(0, LQ_M) READ_U(1, LQ_M + 16384) READ_U(2, LQ_A) READ_U(3, LQ_A + 16384)
    const float xr = lds4f(L_XS + (32 * rw + l32) * 4), xm = -lds4f(L_XS + 512 + (32 * rw + l32) * 4);
    u32x4 wp0, wp1;
    {
      const u32 a = L_WPHI + (2 * cw + (lane & 1)) * 512 + (4 * hh + ((lane >> 2) & 3)) * 32 + (2 * (g & 1) + ((lane >> 1) & 1)) * 8;
      const u32x4 f = ldfrag(a, 256);
      const bool own = (lane >> 2) & 1;
#pragma unroll
      for (int e = 0; e < 4; ++e) { wp0[e] = own ? 0u : f[e]; wp1[e] = own ? f[e] : 0u; }
    }
#define FEATURES(e_, dst, hook)                                                                                         \
  {                                                                                                                 \
    u32x4 kp[2];                                                                                                    \
    _Pragma("unroll") for (int a = 0; a < 4; ++a) {                                                                \
      const f32x4 cc = lds16f(L_CS + (2 * (e_) + cw) * 128 + a * 32 + hh * 16);                                      \
      kp[a >> 1][2 * (a & 1)] = pk2v(fma2(lo2(cc), bc2(xm), (f32x2){acc[e_][4 * a], acc[e_][4 * a + 1]}));          \
      kp[a >> 1][2 * (a & 1) + 1] = pk2v(fma2(hi2(cc), bc2(xm), (f32x2){acc[e_][4 * a + 2], acc[e_][4 * a + 3]}));  \
    }                                                                                                               \
    f32x16 pl = mfmaL(wp0, kp[0], zero16);                                                                          \
    pl = mfmaL(wp1, kp[1], pl);                                                                                     \
    hedgehog16(pl, xr, dst, hook);                                                                                  \
  }
    {
      const int s_ = 32 * rw + l32;
      const u32 key = (((s_ >> 1) & 3) ^ (((s_ >> 3) & 1) << 1)) * 16;
      {
        const u32 va = LQ_V + (s_ >> 3) * 1024 + cw * 512 + (s_ & 7) * 64 + hh * 8;
#pragma unroll
        for (int a = 0; a < 4; ++a) {
          const f32x4 cc = lds16f(L_CS + (4 + cw) * 128 + a * 32 + hh * 16);
          sts8(va + ((a * 16) ^ key), (u32x2){pk2v(fma2(lo2(cc), bc2(xm), (f32x2){acc[2][4 * a], acc[2][4 * a + 1]}) * bc2(xr)),
                                              pk2v(fma2(hi2(cc), bc2(xm), (f32x2){acc[2][4 * a + 2], acc[2][4 * a + 3]}) * bc2(xr))});
        }
      }
      WAITQ(mu[5]);
      READ_U(4, 0) READ_U(5, 16384)
      WAITL0();
      u32x4 ph[4];
      auto hk = [&](int i) __attribute__((always_inline)) {
        const int u = 6 + (i >> 1);
        SCHED();
        VM(DMA_U(u, (u < 8 ? LQ_M : (u < 10 ? LQ_A : 0)) + (u & 1) * 16384, i & 1));
        mu[u] = vq;
        SCHED();
      };
      FEATURES(1, ph, hk)
      const u32 pa = LQ_PHI + (s_ >> 3) * 2048 + (2 * cw + hh) * 512 + (s_ & 7) * 64;
#pragma unroll
      for (int a = 0; a < 4; ++a) sts16(pa + ((a * 16) ^ key), ph[a]);
    }
    BARRIER();
    STAMP(2);
    {
      const u32 kx = (((lane >> 3) & 1) ^ ((g & 1) << 1)) * 16;
      const u32 pa = LQ_PHI + g * 2048 + (w >> 1) * 512 + ((lane >> 2) & 3) * 64 + (((2 * (w & 1) + ((lane >> 1) & 1)) * 16) ^ kx) + (lane & 1) * 8;
      const u32 va = LQ_V + g * 1024 + (w >> 2) * 512 + ((lane >> 2) & 3) * 64 + (((2 * ((w >> 1) & 1) + ((lane >> 1) & 1)) * 16) ^ kx) + (lane & 1) * 8;
      u32x4 pf[4], vf[4];
#pragma unroll
      for (int k = 0; k < 4; ++k) {
        pf[k] = cat(ldstr(pa + k * 8192), ldstr((pa ^ 32) + k * 8192 + 256));
        vf[k] = cat(ldstr(va + k * 4096), ldstr((va ^ 32) + k * 4096 + 256));
      }
      f32x4 kv = zero4, kz = zero4;
#pragma unroll
      for (int k = 0; k < 4; ++k) { kv = mfma32(pf[k], vf[k], kv); kz = mfma32(pf[k], ones4, kz); }
      const u32 so = WS_KP + (c * 16 + P) * KP_SZ;
      u32x4 g0 = {__float_as_uint(kv[0]), tlo, __float_as_uint(kv[1]), tlo}, g1 = {__float_as_uint(kv[2]), tlo, __float_as_uint(kv[3]), tlo};
      u32x4 g2 = {__float_as_uint((lane & 1) ? kz[2] : kz[0]), tlo, __float_as_uint((lane & 1) ? kz[3] : kz[1]), tlo};
      PIN(g0); PIN(g1); PIN(g2);
      asm volatile("s_nop 7" ::: "memory");
      VM(st16_sc1(g0, l16, sws, so + w * 2048));
      VM(st16_sc1(g1, l16, sws, so + w * 2048 + 1024));
      VM(st16_sc1(g2, g * 32 + (lane & 1) * 16 + ((i16 >> 1) << 26), sws, so + 16384 + w * 128));
    }
    STAMP(3);
    WAITQ(mu[11]);
    READ_U(6, LQ_M) READ_U(7, LQ_M + 16384) READ_U(8, LQ_A) READ_U(9, LQ_A + 16384) READ_U(10, 0) READ_U(11, 16384)
    BARRIER();
    ISS_Q(12, LQ_M); VM(DMA_U(13, LQ_M + 16384, 0));
    u32x4 phq[4];
    {
      VWO();
      auto hk = [&](int i) __attribute__((always_inline)) {
        SCHED();
        if (i == 0) DMA_U(13, LQ_M + 16384, 1);
        else if (i < 5) DMA_U(14 + ((i - 1) >> 1), LQ_A + ((i - 1) >> 1) * 16384, (i - 1) & 1);
        else DMA_WOUT(i - 5)
        SCHED();
      };
      FEATURES(0, phq, hk)
    }
    PIN(phq[0]); PIN(phq[1]); PIN(phq[2]); PIN(phq[3]);
    STAMP(4);
    if (w >= 5) {
      const int id = 2 * P + (w == 7 ? (lane & 1) : w - 5), wq = id >> 2, gq = id & 3;
      const u32 vi0 = (4 * g) * KP_SZ + (w == 7 ? 16384 + wq * 128 + gq * 32 : wq * 2048 + (gq * 16 + i16) * 16);
      const u32 vi1 = vi0 + (w == 7 ? 16 : 1024);
      const u32 vo = (w == 7 ? (WS_KZ - WS_KF) + c * 512 + id * 16 + ((i16 >> 1) << 25) : c * 8192 + id * 256 + i16 * 16) + (g << 28);
      float sa[4];
#pragma nounroll
      for (int it = 0; it < SPIN_CAP; ++it) {
        u32x4 a[4][2];
#pragma unroll
        for (int o = 0; o < 4; ++o) {
          a[o][0] = ld16_sc1(vi0, sws, WS_KP + (c * 16 + o) * KP_SZ);
          a[o][1] = ld16_sc1(vi1, sws, WS_KP + (c * 16 + o) * KP_SZ);
        }
        WAITV(0);
        bool ok = true;
#pragma unroll
        for (int o = 0; o < 4; ++o) { PIN(a[o][0]); PIN(a[o][1]); ok = ok && tag_ok(a[o][0], tlo) && tag_ok(a[o][1], tlo); }
#pragma unroll
        for (int e = 0; e < 4; ++e)
          sa[e] = (__uint_as_float(a[0][e >> 1][2 * (e & 1)]) + __uint_as_float(a[1][e >> 1][2 * (e & 1)])) +
                  (__uint_as_float(a[2][e >> 1][2 * (e & 1)]) + __uint_as_float(a[3][e >> 1][2 * (e & 1)]));
        if (__all(ok)) break;
        __builtin_amdgcn_s_sleep(POLL_SLEEP);
      }
      red2g(sa[0], sa[1]);
      red2g(sa[2], sa[3]);
      st16_sc1((u32x4){pk2(sa[0], sa[1]), tlo, pk2(sa[2], sa[3]), tlo}, vo, sws, WS_KF);
    }
    gf = ld8_sc1((lane >> 5) * 1024, sws, WS_FA + (16 * w + c2) * 128);
    WAITV(8);
    READ_U(12, LQ_M) READ_U(13, LQ_M + 16384) READ_U(14, LQ_A) READ_U(15, LQ_A + 16384)
    BARRIER();
#pragma unroll
    for (int a = 0; a < 4; ++a) sts16(L_WO1 + w * 4096 + a * 1024 + l16, phq[a]);
    WAITV(0);
    PIN(gf);
    if (!__all(gf[0] == tlo && gf[1] == thi)) poll_flags((lane >> 5) * 1024, true, sws, WS_FA + (16 * w + c2) * 128, tlo, thi);
    STAMP(6);
    { const u32 so_ = zb0 + TILE(0, gn) * 65536; LOAD_B(0, so_) }
  }

  {
  RELANE();
  u32x4 kfg, kzg;
  const u32 kfo = WS_KF + c * 8192 + w * 1024, kzo = WS_KZ + c * 512 + w * 64;
  if (t != 0) {
    kfg = ld16_sc1(l16, sws, kfo);
    kzg = ld16_sc1(g * 16, sws, kzo);
  }
  { const u32 so_ = zb0 + TILE(1, gn) * 65536; LOAD_B(1, so_) }
  STAMP(9);
  const u32 redw = opq(L_RED + w * 4096 + lane * 16), redr = opq(L_RED + (rw ^ (2 * ux0)) * 1024 + lane * 16);
  const u32 fd = WS_FD + (p * 16 + c2) * 128;
  const u32 f3s = __builtin_amdgcn_readfirstlane(WS_FB + (P * 8 + (c2 > 0 ? c2 - 1 : 0)) * 128);
  const u32 vw3 = ((lane >> 2) & 7) * 1024 + c2 * 128 + (lane >> 5) * 64;
  const rsrc_t swo = __builtin_amdgcn_make_buffer_rsrc((void*)wo, 0, 512 * 1024, 0x00020000);
#define DMA_WO(dst, half, e) dma16((dst) + w * 4096 + (e) * 1024, (vw3 + (lane & 3) * 16) ^ (((e) & 1) * 32), swo, ((w + 8 * (half)) * 4 + (e)) * 8192)
#define FV() ((lane & 32) ? (P * 8 + ((lane >> 2) & 7)) * 128 : (WS_FD - WS_FC) + (p * 16 + (((lane >> 1) & 14) | cw)) * 128)
  u32x2 z1r[6], hold;
  u32 bs;
  f32x4 sm0;
#define EMIT_H(jt, sm_)                                                                                             \
  {                                                                                                                 \
    const f32x2 bb = bc2(blo(bs));                                                                                  \
    const u32 h0 = pk2v((f32x2){blo(z1r[jt][0]), bhi(z1r[jt][0])} * (lo2(sm_) + bb));                               \
    const u32 h1 = pk2v((f32x2){blo(z1r[jt][1]), bhi(z1r[jt][1])} * (hi2(sm_) + bb));                               \
    const u32 hb_ = WS_HF + (((4 * p + rw) * 2 + ((jt) >> 1)) * 8 + c2) * 1024;                                     \
    if ((jt) == 0 || ((jt) == 2 && gn == 6)) hold = (u32x2){h0, h1};                                                \
    else if ((jt) == 1 || ((jt) == 3 && gn == 6)) st16_sc1((u32x4){hold[0], hold[1], h0, h1}, l16, sws, hb_);       \
    else if ((jt) == 2) st16_sc1((u32x4){h0, h1, 0u, 0u}, l16, sws, hb_);                                           \
    else st16_sc1((u32x4){h0, tlo, h1, tlo}, l16, sws, WS_HL + (((4 * p + rw) * 8 + c2) * 2 + (gn - 1 - (jt))) * 1024); \
  }
#pragma unroll
  for (int j = 0; j < 6; ++j) {
    if (j < 5 || gn == 6) {
      const int u = j & 1;
      if (j == 0) WAITV(8);
      if (j == 2) WAITV(t ? 10 : 8);
      if (j == 1) { WAITV(9); WAITV_IF(cw, 1, 8); }
      if (j == 3) { WAITV(9); WAITV_IF(cw, 1, 8); }
      if (j == 4) { if (t) WAITV(4); else { WAITV(12); WAITV_IF(gn, 5, 4); } }
      if (j == 5) { WAITV(10); WAITV_IF(cw, 1, 9); }
#pragma unroll
      for (int i = 0; i < 8; ++i) PIN(bfr[u][i]);
      {
        f32x4 a_[4];
#pragma unroll
        for (int rt = 0; rt < 4; ++rt) {
          a_[rt] = zero4;
#pragma unroll
          for (int i = 0; i < 8; ++i) a_[rt] = mfma32(bfr[u][i], af[rt][i], a_[rt]);
          SCHED();
          if (rt > 0) sts16f(redw + u * 32768 + (rt - 1) * 1024, a_[rt - 1]);
          SCHED();
        }
        if (j == 0 && t != 0) {
          PIN(kfg); PIN(kzg);
          poll_gran(kfg, l16, sws, kfo, tlo);
          poll_gran(kzg, g * 16, sws, kzo, tlo);
          sts8(L_KFS + (w >> 1) * 1024 + i16 * 64 + (w & 1) * 32 + g * 8, (u32x2){kfg[0], kfg[2]});
          sts8(L_KFS + 4096 + w * 32 + g * 8, (u32x2){kzg[0], kzg[2]});
          sts8(L_KFS + 4352 + (lane & 7) * 8, (u32x2){0u, 0u});
        }
        sts16f(redw + u * 32768 + 3072, a_[3]);
      }
      u32x4 of[2];
      if (j == 1 && t != 0) {
        const int l32 = lane & 31, hh = lane >> 5;
        const bool own = ((lane >> 2) & 1) == hh;
        const u32 ka = own ? L_KFS + (2 * cw + hh) * 1024 + ((l32 & 3) + 4 * (l32 >> 3)) * 64 : L_KFS + 4352;
        const u32 zaa = own ? L_KFS + 4096 + (2 * cw + hh) * 64 : L_KFS + 4352;
        const u32 qa = L_WO1 + w * 4096 + l16;
        const f32x16 zero16 = {0, 0, 0, 0, 0, 0, 0, 0, 0, 0, 0, 0, 0, 0, 0, 0};
        f32x16 den = zero16, num = zero16;
#pragma unroll
        for (int a = 0; a < 4; ++a) {
          const u32x4 pq_ = lds16(qa + a * 1024);
          den = mfmaL(lds16(zaa + a * 16), pq_, den);
          num = mfmaL(lds16(ka + a * 16), pq_, num);
        }
        const float inv = __builtin_amdgcn_rcpf(den[0] + 1e-6f);
#pragma unroll
        for (int a = 0; a < 2; ++a) {
          of[a] = (u32x4){pk2(num[8 * a] * inv, num[8 * a + 1] * inv), pk2(num[8 * a + 2] * inv, num[8 * a + 3] * inv),
                          pk2(num[8 * a + 4] * inv, num[8 * a + 5] * inv), pk2(num[8 * a + 6] * inv, num[8 * a + 7] * inv)};
          PIN(of[a]);
        }
        STAMP(5);
      }
      BAR_IF(cw, 1, 63);
      if (w == 4) {
        if (j == 0 && t == 0) SET_FLAG(WS_FB + (P * 8 + c) * 128);
        if (j == 3) SET_FLAG(fd);
        if (j == 3 && t != 0) SET_FLAG(WS_FC + (P * 8 + c) * 128);
        if ((j == 4 && gn == 5) || j == 5) SET_FLAG(fd + 1024);
      }
      if (j == 1) {
        if (cw == 0) {
          WAITV(0);
          PIN(gf);
          if (!__all(gf[0] == tlo && gf[1] == thi)) poll_flags((lane >> 5) * 128, true, sws, f3s, tlo, thi);
          rsrc_t sb = __builtin_amdgcn_make_buffer_rsrc((void*)bsp, 0, 4096, 0x00020000);
          bs = ld2(i16 * 2, sb, (64 * p + 16 * rw) * 2);
        }
      }
      if ((j == 1 || j == 2) && cw == 0) {
#pragma unroll
        for (int v = 3 * j - 3; v < 3 * j; ++v)
          z1r[v] = ld8_sc1(l16, sws, WS_Z1 + ((2 * p + (rw >> 1)) * 43 + gs + TILE(min(v, gn - 1), gn)) * 1024 + (rw & 1) * 8);
      }
      if ((j == 4 && gn == 5) || j == 5) gf = ld8_sc1(FV(), sws, WS_FC);
      if (j == 4 && t == 0) {
        VWO();
#pragma unroll
        for (int e = 2; e < 7; ++e) DMA_WOUT(e)
      }
      const u32 ra_ = redr + ((j + 1) & 1) * 32768;
      f32x4 sm, r_[2];
#pragma unroll
      for (int i = 0; i < 8; ++i) {
        if (j >= 1 && j < (t ? 3 : 4) && (i & 1) == 0 && cw == 0) {
          if (i == 2) sm = r_[0] + r_[1];
          if (i > 2) sm += r_[0] + r_[1];
          r_[0] = lds16f(ra_ + i * 4096); r_[1] = lds16f(ra_ + (i + 1) * 4096);
        }
        SCHED();
        if (j < 3) bfr[u][i] = ld16_b(l16, sws, (zb0 + TILE(j + 2, gn) * 65536) ^ (i * 1024));
        else if (j == 3 && t == 0) ld16_b_if6(bfr[1][i], gn, l16, sws, (zb0 + TILE(5, gn) * 65536) ^ (i * 1024));
        SCHED();
      }
      if (j == 0 && cw == 0) gf = ld8_sc1((lane >> 5) * 128, sws, f3s);
      if (j == 1 && t != 0) {
        const u32 vo = ((lane >> 4) & 1) * 16384 + (lane >> 5) * 512 + i16 * 16;
        st16_sc1(of[0], vo, sws, WS_OF + ((8 * P + 2 * rw) * 16 + 2 * c + cw) * 1024);
        st16_sc1(of[1], vo, sws, WS_OF + ((8 * P + 2 * rw) * 16 + 2 * c + cw) * 1024 + 256);
      }
      if (j == 2) { PIN(bs); _Pragma("unroll") for (int v = 0; v < 3; ++v) PIN(z1r[v]); }
      if (j == 3) { _Pragma("unroll") for (int v = 3; v < 6; ++v) PIN(z1r[v]); }
      if (j >= 1 && cw == 0) {
        if (j < (t ? 3 : 4)) sm += r_[0] + r_[1];
        else {
          f32x4 q_[8];
#pragma unroll
          for (int o = 0; o < 8; ++o) q_[o] = lds16f(ra_ + o * 4096);
          SCHED();
          sm = ((q_[0] + q_[1]) + (q_[2] + q_[3])) + ((q_[4] + q_[5]) + (q_[6] + q_[7]));
        }
        if (j == 1) sm0 = sm;
        if (j == 2) EMIT_H(0, sm0)
        if (j >= 2) EMIT_H(j >= 2 ? j - 1 : 0, sm)
      }
      if (j == 3) {
#pragma unroll
        for (int e = 0; e < 4; ++e) DMA_WO(L_WO1, 0, e);
      }
      if (j == 3 && cw == 0) { if (t) WAITV(5); else { WAITV(12); WAITV_IF(gn, 5, 5); } }
      if (j == 5) WAITV(2);
      BAR_IF(cw, 0, 63);
    }
  }
  {
    STAMP(10);
    const u32 bufA = L_RED + (gn & 1) * 32768, bufB = L_RED + 32768 - (gn & 1) * 32768;
    const u32 fv = FV();
    if (cw == 0) {
      const u32 ra_ = redr + (bufB - L_RED);
      f32x4 q_[8];
#pragma unroll
      for (int o = 0; o < 8; ++o) q_[o] = lds16f(ra_ + o * 4096);
      SCHED();
      const f32x4 sm = ((q_[0] + q_[1]) + (q_[2] + q_[3])) + ((q_[4] + q_[5]) + (q_[6] + q_[7]));
      if (gn == 6) EMIT_H(5, sm) else EMIT_H(4, sm)
    }
    WAITV(2); WAITV_IF(cw, 1, 0);
    PIN(gf);
    {
      const bool n0 = (lane & 48) != 16;
      if (!__all(!n0 || (gf[0] == tlo && gf[1] == thi))) poll_flags(fv, n0, sws, WS_FC, tlo, thi);
    }
    RELANE();
    u32x4 hf[8], of[8];
    const u32 ofb = WS_OF + ((4 * p + rw) * 16 + cw) * 1024, hfb = WS_HF + ((4 * p + rw) * 16 + cw) * 1024;
#pragma unroll
    for (int e = 0; e < 4; ++e) of[e] = ld16_b(l16, sws, ofb + e * 2048);
#pragma unroll
    for (int e = 0; e < 4; ++e) hf[e] = ld16_b(l16, sws, hfb + e * 2048);
    BARRIER();
    const u32 qb = ((lane >> 2) & 3) * 64 + ((((g & 1) << 1) | ((lane >> 1) & 1)) * 16) + (lane & 1) * 8;
    f32x4 a3[4];
#pragma unroll
    for (int ct = 0; ct < 4; ++ct) a3[ct] = zero4;
    u32x4 wf[3][4], hl[4][2];
#define RDW2(dst, lo0, hi0, offl, offh) _Pragma("unroll") for (int ct = 0; ct < 4; ++ct) dst[ct] = cat(ldstr(((lo0) ^ ((ct & 1) * 32)) + (offl) + (ct >> 1) * 512), ldstr(((hi0) ^ ((ct & 1) * 32)) + (offh) + (ct >> 1) * 512))
#define MM3(src, frag) _Pragma("unroll") for (int ct = 0; ct < 4; ++ct) a3[ct] = mfma32(src[ct], frag, a3[ct])
    const u32 lb = WS_HL + ((4 * p + rw) * 8 + cw) * 2048;
    const rsrc_t sxo = __builtin_amdgcn_make_buffer_rsrc((void*)(x + (size_t)p * 65536), 0, 65536, 0x00020000);
    const u32 w0 = opq(L_WOUT + qb + (g >> 1) * 1024 + (g & 1) * 256);
    const u32 wa = w0 + cw * 4096, wb = w0 + cw * 2048;
    const u32 oq = qb + g * 1024 + cw * 4096;
    const u32 oa = opq(L_WO1 + oq), ob = opq(bufA + oq);
    u32x4 xres;
    VWO();
#pragma unroll
    for (int e = 0; e < 4; ++e) DMA_WO(bufA, 1, e);
    STAMP(11);
#define RDK(dst, k)                                                                                                 \
  if ((k) < 4) { RDW2(dst, oa, oa, (k) * 8192, (k) * 8192 + 256); }                                                 \
  else if ((k) < 8) { RDW2(dst, wa, wa, ((k) - 4) * 8192, ((k) - 4) * 8192 + 2048); }                               \
  else if ((k) < 12) { RDW2(dst, ob, ob, ((k) - 8) * 8192, ((k) - 8) * 8192 + 256); }                               \
  else { RDW2(dst, wb, w0, 32768 + ((k) - 12) * 4096, 49152 + ((k) - 12) * 2048); }
    RDK(wf[0], 0)
    RDK(wf[1], 1)
#pragma unroll
    for (int k = 0; k < 16; ++k) {
      if (k == 0) WAITV(8);
      if (k == 4) WAITV(13);
      if (k == 6) {
        WAITV(0);
        PIN(gf);
        if (!__all(gf[0] == tlo && gf[1] == thi)) poll_flags(fv, true, sws, WS_FC, tlo, thi);
#pragma unroll
        for (int e = 4; e < 8; ++e) hf[e] = ld16_b(l16, sws, hfb + e * 2048);
        BARRIER();
        STAMP(12);
      }
      if (k == 12) WAITV(9);
      if (k + 2 < 16) { RDK(wf[(k + 2) % 3], k + 2) }
      SCHED();
      if (k < 4) { PIN(of[k]); MM3(wf[k % 3], of[k]); }
      else if (k < 8) { PIN(hf[k - 4]); MM3(wf[k % 3], hf[k - 4]); }
      else if (k < 12) { PIN(of[k - 4]); MM3(wf[k % 3], of[k - 4]); }
      else { PIN(hf[k - 8]); MM3(wf[k % 3], hf[k - 8]); }
      SCHED();
      if (k < 4) {
        of[4 + k] = ld16_b(l16, sws, ofb + (4 + k) * 2048);
        DMA_WOUT_AT(bufB + w * 4096, 7 + k)
        if (k == 3) gf = ld8_sc1(fv, sws, WS_FC);
      } else if (k >= 6 && k < 10) {
        hl[k - 6][0] = ld16_sc1(l16, sws, lb + (k - 6) * 4096);
        hl[k - 6][1] = ld16_sc1(l16, sws, lb + (k - 6) * 4096 + 1024);
      } else if (k == 10) {
        xres = ld16((tid >> 3) * 1024 + (tid & 7) * 16, sxo, c2 * 128);
      }
    }
    STAMP(13);
    {
      const u32 wl_ = opq(bufB + (w0 - L_WOUT) + cw * 4096);
      u32x4 wg[4][4];
#pragma unroll
      for (int e = 0; e < 4; ++e) RDW2(wg[e], wl_, wl_, e * 8192, e * 8192 + 2048);
      WAITV(0);
      PIN(xres);
#pragma unroll
      for (int e = 0; e < 4; ++e) { PIN(hl[e][0]); PIN(hl[e][1]); }
#pragma nounroll
      for (int it = 0; it < SPIN_CAP; ++it) {
        u32 bad = 0;
#pragma unroll
        for (int e = 0; e < 8; ++e) bad |= (hl[e >> 1][e & 1][1] ^ tlo) | (hl[e >> 1][e & 1][3] ^ tlo);
        if (__all(bad == 0)) break;
        __builtin_amdgcn_s_sleep(2);
        asm volatile(
            "buffer_load_dwordx4 %0, %8, %9, %10 offen sc1\n\tbuffer_load_dwordx4 %1, %8, %9, %10 offen offset:1024 sc1\n\t"
            "buffer_load_dwordx4 %2, %8, %9, %11 offen sc1\n\tbuffer_load_dwordx4 %3, %8, %9, %11 offen offset:1024 sc1\n\t"
            "buffer_load_dwordx4 %4, %8, %9, %12 offen sc1\n\tbuffer_load_dwordx4 %5, %8, %9, %12 offen offset:1024 sc1\n\t"
            "buffer_load_dwordx4 %6, %8, %9, %13 offen sc1\n\tbuffer_load_dwordx4 %7, %8, %9, %13 offen offset:1024 sc1\n\t"
            "s_waitcnt vmcnt(0)"
            : "=&v"(hl[0][0]), "=&v"(hl[0][1]), "=&v"(hl[1][0]), "=&v"(hl[1][1]), "=&v"(hl[2][0]), "=&v"(hl[2][1]), "=&v"(hl[3][0]), "=&v"(hl[3][1])
            : "v"(l16), "s"(sws), "s"(lb), "s"(lb + 4096), "s"(lb + 8192), "s"(lb + 12288)
            : "memory");
      }
#pragma unroll
      for (int e = 0; e < 4; ++e) MM3(wg[e], ((u32x4){hl[e][0][0], hl[e][0][2], hl[e][1][0], hl[e][1][2]}));
    }
    STAMP(14);
    const u32 fin = L_WO1 - 2048;
#pragma unroll
    for (int ct = 0; ct < 4; ++ct) sts16f(fin + 2048 + cw * 16384 + (16 * rw + i16) * 256 + (((4 * ct + g) ^ i16) * 16), a3[ct]);
    BARRIER();
    {
      const int row = tid >> 3;
      const u32 a = fin + 2048 + row * 256 + (((2 * (tid & 7)) ^ (row & 15)) * 16);
      const f32x4 s0 = lds16f(a) + lds16f(a + 16384), s1 = lds16f(a ^ 16) + lds16f((a ^ 16) + 16384);
      u32x4 o = {pk2(blo(xres[0]) + s0[0], bhi(xres[0]) + s0[1]), pk2(blo(xres[1]) + s0[2], bhi(xres[1]) + s0[3]),
                 pk2(blo(xres[2]) + s1[0], bhi(xres[2]) + s1[1]), pk2(blo(xres[3]) + s1[2], bhi(xres[3]) + s1[3])};
      st16_sc1(o, row * 1024 + (tid & 7) * 16, __builtin_amdgcn_make_buffer_rsrc((void*)(out + (size_t)p * 65536), 0, 65536, 0x00020000), c2 * 128);
    }
  }
  }
  if (b == 0 && w == 0 && l16 == 0) *(u64*)(ws + WS_EP) = ((u64)thi << 32) | tlo;
  STAMP(15);
  STAMPC(31);
}

__global__ __launch_bounds__(512, 2) void fused_kernel(const char* __restrict__ x, const char* __restrict__ wqkv, const char* __restrict__ wphi,
                                                        const char* __restrict__ wo, const char* __restrict__ win, const char* __restrict__ wsp,
                                                        const char* __restrict__ bsp, const char* __restrict__ wout, char* __restrict__ out, char* ws) {
  __shared__ __attribute__((aligned(1024))) char smem[163840];
  asm volatile("" ::"v"((u32)(size_t)(LDSP char*)smem));
  asm volatile("" ::"s"(x), "s"(wqkv), "s"(win), "s"(ws), "s"(wsp));
  u64 pc, ep;
  const u64 tstart = __builtin_amdgcn_s_memrealtime();
  asm volatile("s_getpc_b64 %0" : "=s"(pc));
  {
    const int lane = threadIdx.x & 63, g = lane >> 4;
    const int w = __builtin_amdgcn_readfirstlane(threadIdx.x >> 6);
    const int r = blockIdx.x >> 3, P = ((blockIdx.x & 7) >> 1) * 4 + (r >> 3);
    const int rot = 2 * (((r >> 3) + (r & 3)) & 3);
    u32 acv = 0, acb = 0; (void)acv; (void)acb;
    STAMP(0);
    STAMPC(30);
    if (CODE_PF && w == 7)
      touch(L_JUNK, lane * 128, __builtin_amdgcn_make_buffer_rsrc((void*)(pc & ~255ull), 0, CODE_PF, 0x00020000), (r & 7) * 8192);
    const rsrc_t sx = __builtin_amdgcn_make_buffer_rsrc((void*)(x + (size_t)P * 131072), 0, 131072, 0x00020000);
    const u32 vx0 = (16 * w + (lane >> 3)) * 1024 + (((lane & 7) ^ g) * 16);
    const u32 vx1 = (vx0 + 8192) ^ 64;
    const u32 w2k = w * 2048;
    DMA_X(0, 0, 0);
    DMA_X(0, 0, 1);
    asm volatile("s_load_dwordx2 %0, %1, 0x0" : "=s"(ep) : "s"(ws) : "memory");
    SCHED();
  }
  if (__builtin_expect(((blockIdx.x >> 5) & 1) == 0, 1)) run<0>(x, wqkv, wphi, wo, win, wsp, bsp, wout, out, ws, ep);
  else run<1>(x, wqkv, wphi, wo, win, wsp, bsp, wout, out, ws, ep);
  if (blockIdx.x == 255) for (int it = 0; it < 100000; ++it) { if (__builtin_amdgcn_s_memrealtime() - tstart >= 5000) break; __builtin_amdgcn_s_sleep(1); }
}

extern "C" void kernel_launch(void* const* d_in, const int* in_sizes, int n_in, void* d_out, int out_size, void* d_ws, size_t ws_size,
                              hipStream_t stream) {
  fused_kernel<<<256, 512, 0, stream>>>((const char*)d_in[0], (const char*)d_in[1], (const char*)d_in[2], (const char*)d_in[3],
                                        (const char*)d_in[5], (const char*)d_in[6], (const char*)d_in[7], (const char*)d_in[8], (char*)d_out,
                                        (char*)d_ws);
}
```

```cpp
#include <hip/hip_runtime.h>
#include <hip/hip_bf16.h>

typedef unsigned int u32;
typedef unsigned long long u64;
typedef short s16x4 __attribute__((ext_vector_type(4)));
typedef __bf16 bf16x2 __attribute__((ext_vector_type(2)));
typedef __bf16 bf16x8 __attribute__((ext_vector_type(8)));
typedef float f32x16 __attribute__((ext_vector_type(16)));
typedef float f32x4 __attribute__((ext_vector_type(4)));
typedef float f32x2 __attribute__((ext_vector_type(2)));
typedef u32 u32x4 __attribute__((ext_vector_type(4)));
typedef u32 u32x2 __attribute__((ext_vector_type(2)));
typedef __amdgpu_buffer_rsrc_t rsrc_t;
#define LDSP __attribute__((address_space(3)))
#define DI __device__ __forceinline__

#define STAMPS 0

constexpr u32 WS_EP = 0;
constexpr u32 WS_FA = 8192;
constexpr u32 WS_FB = WS_FA + 16384;
constexpr u32 WS_FC = WS_FB + 16384;
constexpr u32 WS_FD = WS_FC + 16384;
constexpr u32 WS_XG = WS_FD + 65536;
constexpr u32 WS_SG = WS_XG + 32768;
constexpr u32 WS_ZB = WS_SG + 524288;
constexpr u32 WS_Z1 = WS_ZB + 43 * 65536;
constexpr u32 KP_SZ = 17408;
constexpr u32 WS_KP = WS_Z1 + 64 * 43 * 1024;
constexpr u32 WS_KF = WS_KP + 128 * KP_SZ;
constexpr u32 WS_KZ = WS_KF + 65536;
constexpr u32 WS_OF = WS_KZ + 4096;
constexpr u32 WS_HF = WS_OF + 128 * 16384;
constexpr u32 WS_HL = WS_HF + 128 * 16 * 1024;
constexpr u32 WS_ST = WS_HL + 128 * 16 * 1024;
constexpr u32 WS_END = WS_ST + 256 * 8 * 32 * 8;

constexpr u32 LZ_W = 65536;
constexpr u32 LZ_U = 139264;
constexpr u32 LQ_M = 122880;
constexpr u32 LQ_A = 81920;
constexpr u32 LQ_PHI = 49152;
constexpr u32 LQ_V = 32768;
constexpr u32 L_TAB = 155648;
constexpr u32 L_XS = L_TAB;
constexpr u32 L_CS = L_TAB + 1024;
constexpr u32 L_ZST = L_TAB + 3072;
constexpr u32 L_WPHI = L_TAB + 3072;
constexpr u32 L_KFS = L_TAB + 2048;
constexpr u32 L_JUNK = L_TAB + 7168;
constexpr u32 L_WOUT = 0;
constexpr u32 L_RED = 57344;
constexpr u32 L_WO1 = 122880;

constexpr u64 Z2S = 0x26211B16110B0500ull;
constexpr u64 Z1S = 0x26211C16110C0600ull;
DI int grp_start(u64 tab, int c) { return c >= 8 ? 43 : (int)((tab >> (8 * c)) & 0xFF); }

#define SCHED() __builtin_amdgcn_sched_barrier(0)
#define TIC()
#define TOC(acc)
#define WAITV(n) do { TIC(); asm volatile("s_waitcnt vmcnt(%0)" ::"n"(n) : "memory"); TOC(acv); SCHED(); } while (0)
#define BARRIER() do { SCHED(); TIC(); asm volatile("s_waitcnt lgkmcnt(0)\n\ts_barrier" ::: "memory"); TOC(acb); SCHED(); } while (0)
#define WAITV_IF(g_, v, n) do { TIC(); asm volatile("s_cmp_lg_u32 %0, %1\n\ts_cbranch_scc1 1f\n\ts_waitcnt vmcnt(%2)\n1:" ::"s"(g_), "n"(v), "n"(n) : "scc", "memory"); TOC(acv); SCHED(); } while (0)
#define BAR_IF(g_, v, n) do { SCHED(); asm volatile("s_cmp_lg_u32 %0, %1\n\ts_cbranch_scc1 1f\n\ts_waitcnt vmcnt(%2) lgkmcnt(0)\n\ts_barrier\n1:" ::"s"(g_), "n"(v), "n"(n) : "scc", "memory"); SCHED(); } while (0)
#define WAITL0() asm volatile("s_waitcnt lgkmcnt(0)" ::: "memory")
#define PIN(x) asm volatile("" : "+v"(x))

DI void dma16(u32 lds, u32 voff, rsrc_t srd, u32 soff) {
  asm volatile("s_mov_b32 m0, %0\n\ts_nop 0\n\tbuffer_load_dwordx4 %1, %2, %3 offen lds" ::"s"(lds), "v"(voff), "s"(srd), "s"(soff) : "memory");
}
#define DMA_C(wb, CST, voff, srd, soff) asm volatile("s_add_u32 m0, %0, %1\n\ts_nop 0\n\tbuffer_load_dwordx4 %2, %3, %4 offen lds" ::"s"(wb), "n"(CST), "v"(voff), "s"(srd), "s"(soff) : "scc", "memory")
#define DMA_CC(wb, CST, voff, srd, OP, sb, SC) do { u32 so__; asm volatile("s_add_u32 m0, %1, %2\n\t" OP " %0, %5, %6\n\tbuffer_load_dwordx4 %3, %4, %0 offen lds" : "=&s"(so__) : "s"(wb), "n"(CST), "v"(voff), "s"(srd), "s"(sb), "n"(SC) : "scc", "memory"); } while (0)
DI void touch(u32 lds, u32 voff, rsrc_t srd, u32 soff) {
  asm volatile("s_mov_b32 m0, %0\n\ts_nop 0\n\tbuffer_load_dword %1, %2, %3 offen lds" ::"s"(lds), "v"(voff), "s"(srd), "s"(soff) : "memory");
}
DI u32x4 ld16_sc1(u32 voff, rsrc_t srd, u32 soff) {
  u32x4 r;
  asm volatile("buffer_load_dwordx4 %0, %1, %2, %3 offen sc1" : "=v"(r) : "v"(voff), "s"(srd), "s"(soff) : "memory");
  return r;
}
template <int OFF> DI u32x4 ld16_sc1o(u32 voff, rsrc_t srd, u32 soff) {
  u32x4 r;
  asm volatile("buffer_load_dwordx4 %0, %1, %2, %3 offen offset:%4 sc1" : "=v"(r) : "v"(voff), "s"(srd), "s"(soff), "n"(OFF) : "memory");
  return r;
}
#define BPOL " sc1"
DI u32x4 ld16_b(u32 voff, rsrc_t srd, u32 soff) {
  u32x4 r;
  asm volatile("buffer_load_dwordx4 %0, %1, %2, %3 offen" BPOL : "=v"(r) : "v"(voff), "s"(srd), "s"(soff) : "memory");
  return r;
}
template <int OFF> DI u32x4 ld16_bo(u32 voff, rsrc_t srd, u32 soff) {
  u32x4 r;
  asm volatile("buffer_load_dwordx4 %0, %1, %2, %3 offen offset:%4" BPOL : "=v"(r) : "v"(voff), "s"(srd), "s"(soff), "n"(OFF) : "memory");
  return r;
}
DI void ld16_b_if6(u32x4& r, int gn, u32 voff, rsrc_t srd, u32 soff) {
  asm volatile("s_cmp_lg_u32 %4, 6\n\ts_cbranch_scc1 1f\n\tbuffer_load_dwordx4 %0, %1, %2, %3 offen" BPOL "\n1:" : "+v"(r) : "v"(voff), "s"(srd), "s"(soff), "s"(gn) : "scc", "memory");
}
DI u32x4 ld16(u32 voff, rsrc_t srd, u32 soff) {
  u32x4 r;
  asm volatile("buffer_load_dwordx4 %0, %1, %2, %3 offen" : "=v"(r) : "v"(voff), "s"(srd), "s"(soff) : "memory");
  return r;
}
DI u32x2 ld8_sc1(u32 voff, rsrc_t srd, u32 soff) {
  u32x2 r;
  asm volatile("buffer_load_dwordx2 %0, %1, %2, %3 offen sc1" : "=v"(r) : "v"(voff), "s"(srd), "s"(soff) : "memory");
  return r;
}
DI u32 ld2(u32 voff, rsrc_t srd, u32 soff) {
  u32 r;
  asm volatile("buffer_load_ushort %0, %1, %2, %3 offen" : "=v"(r) : "v"(voff), "s"(srd), "s"(soff) : "memory");
  return r;
}
DI void st16_sc1(u32x4 v, u32 voff, rsrc_t srd, u32 soff) {
  asm volatile("buffer_store_dwordx4 %0, %1, %2, %3 offen sc1\n\ts_nop 1" ::"v"(v), "v"(voff), "s"(srd), "s"(soff) : "memory");
}
DI void st8_sc1(u32x2 v, u32 voff, rsrc_t srd, u32 soff) {
  asm volatile("buffer_store_dwordx2 %0, %1, %2, %3 offen sc1" ::"v"(v), "v"(voff), "s"(srd), "s"(soff) : "memory");
}

DI u32x4 lds16(u32 a) { return *(const LDSP u32x4*)(size_t)a; }
DI u32x2 lds8(u32 a) { return *(const volatile LDSP u32x2*)(size_t)a; }
DI float lds4f(u32 a) { return *(const LDSP float*)(size_t)a; }
DI f32x4 lds16f(u32 a) { return *(const LDSP f32x4*)(size_t)a; }
DI void sts16f(u32 a, f32x4 v) { *(LDSP f32x4*)(size_t)a = v; }
DI void sts16(u32 a, u32x4 v) { *(LDSP u32x4*)(size_t)a = v; }
DI void sts8(u32 a, u32x2 v) { *(volatile LDSP u32x2*)(size_t)a = v; }
DI void sts4f(u32 a, float v) { *(LDSP float*)(size_t)a = v; }
DI u32x2 ldstr(u32 a) {
  return __builtin_bit_cast(u32x2, __builtin_amdgcn_ds_read_tr16_b64_v4i16((LDSP s16x4*)(size_t)a)); }
DI u32x4 cat(u32x2 lo, u32x2 hi) { return (u32x4){lo[0], lo[1], hi[0], hi[1]}; }
DI u32x4 ldfrag(u32 a, u32 d) { u32x2 lo = ldstr(a), hi = ldstr(a + d); return cat(lo, hi); }

DI f32x4 mfma32(u32x4 a, u32x4 b, f32x4 c) {
  return __builtin_amdgcn_mfma_f32_16x16x32_bf16(__builtin_bit_cast(bf16x8, a), __builtin_bit_cast(bf16x8, b), c, 0, 0, 0);
}
DI f32x16 mfmaL(u32x4 a, u32x4 b, f32x16 c) {
  return __builtin_amdgcn_mfma_f32_32x32x16_bf16(__builtin_bit_cast(bf16x8, a), __builtin_bit_cast(bf16x8, b), c, 0, 0, 0);
}
DI u32 pk2(float a, float b) { bf16x2 v = {(__bf16)a, (__bf16)b}; return __builtin_bit_cast(u32, v); }
DI float blo(u32 u) { return __uint_as_float(u << 16); }
DI float bhi(u32 u) { return __uint_as_float(u & 0xffff0000u); }
DI float rbf(float a) { return blo(pk2(a, a)); }
DI float dot2(u32 a, u32 b, float c) {
  return __builtin_amdgcn_fdot2_f32_bf16(__builtin_bit_cast(bf16x2, a), __builtin_bit_cast(bf16x2, b), c, false);
}
DI f32x2 fma2(f32x2 a, f32x2 b, f32x2 c) { return __builtin_elementwise_fma(a, b, c); }
DI f32x2 bc2(float a) { return (f32x2){a, a}; }
DI f32x2 lo2(f32x4 v) { return (f32x2){v[0], v[1]}; }
DI f32x2 hi2(f32x4 v) { return (f32x2){v[2], v[3]}; }
DI u32 pk2v(f32x2 v) { return pk2(v[0], v[1]); }
DI f32x2 gelu2(f32x2 x) {
  f32x2 u = fma2(x * x, bc2(-0.10294324f), bc2(-2.3022082f)) * x;
  f32x2 e = {__builtin_amdgcn_exp2f(u[0]), __builtin_amdgcn_exp2f(u[1])};
  e = e + bc2(1.0f);
  f32x2 r = {__builtin_amdgcn_rcpf(e[0]), __builtin_amdgcn_rcpf(e[1])};
  return x * r;
}
DI void red2g(float& a, float& b) {
  auto s = __builtin_amdgcn_permlane32_swap(__float_as_uint(a), __float_as_uint(b), false, false);
  float c = __uint_as_float(s[0]) + __uint_as_float(s[1]);
  auto s2 = __builtin_amdgcn_permlane16_swap(__float_as_uint(c), __float_as_uint(c), false, false);
  float d = __uint_as_float(s2[0]) + __uint_as_float(s2[1]);
  auto s3 = __builtin_amdgcn_permlane32_swap(__float_as_uint(d), __float_as_uint(d), false, false);
  a = __uint_as_float(s3[0]);
  b = __uint_as_float(s3[1]);
}
template <int CTRL> DI float dpp(float v) {
  return __uint_as_float(__builtin_amdgcn_update_dpp(0, __float_as_uint(v), CTRL, 0xF, 0xF, true));
}
template <class F> DI void hedgehog16(const f32x16& p, float s, u32x4 (&o)[4], F hook) {
  float mx = fmaxf(p[0], p[1]), mn = fminf(p[0], p[1]);
#pragma unroll
  for (int i = 2; i < 16; i += 2) { mx = fmaxf(fmaxf(mx, p[i]), p[i + 1]); mn = fminf(fminf(mn, p[i]), p[i + 1]); }
  const float c = s * 1.44269504f;
  const float a = -mx * c, b = mn * c;
  f32x2 ep[8], en[8];
#pragma unroll
  for (int i = 0; i < 8; ++i) {
    hook(i);
    const f32x2 v = {p[2 * i], p[2 * i + 1]};
    const f32x2 u = fma2(v, bc2(c), bc2(a)), w = fma2(v, bc2(-c), bc2(b));
    ep[i] = (f32x2){__builtin_amdgcn_exp2f(u[0]), __builtin_amdgcn_exp2f(u[1])};
    en[i] = (f32x2){__builtin_amdgcn_exp2f(w[0]), __builtin_amdgcn_exp2f(w[1])};
  }
  hook(8);
  const f32x2 sp = ((ep[0] + ep[1]) + (ep[2] + ep[3])) + ((ep[4] + ep[5]) + (ep[6] + ep[7]));
  const f32x2 sn = ((en[0] + en[1]) + (en[2] + en[3])) + ((en[4] + en[5]) + (en[6] + en[7]));
  const float ip = __builtin_amdgcn_rcpf(sp[0] + sp[1]), in = __builtin_amdgcn_rcpf(sn[0] + sn[1]);
#pragma unroll
  for (int i = 0; i < 8; ++i) {
    if (i == 0 || i == 3 || i == 6) hook(9 + i / 3);
    o[i >> 2][i & 3] = pk2v(ep[i] * bc2(ip));
    o[2 + (i >> 2)][i & 3] = pk2v(en[i] * bc2(in));
  }
}
DI void ln_fin(float s, float q, float invn, float& r, float& m) {
  m = s * invn;
  float v = fmaxf(q * invn - m * m, 0.0f);
  float sd = rbf(__builtin_amdgcn_sqrtf(rbf(rbf(v) + 1e-5f)));
  r = __builtin_amdgcn_rcpf(sd);
  m = rbf(m);
}

#define POLL_SLEEP 6
constexpr int SPIN_CAP = 1 << 17;
DI void poll_flags(u32 voff, bool act, rsrc_t srd, u32 soff, u32 tlo, u32 thi) {
#pragma nounroll
  for (int it = 0; it < SPIN_CAP; ++it) {
    u32x2 v;
    asm volatile("buffer_load_dwordx2 %0, %1, %2, %3 offen sc1\n\ts_waitcnt vmcnt(0)" : "=&v"(v) : "v"(voff), "s"(srd), "s"(soff) : "memory");
    bool ok = !act || (v[0] == tlo && v[1] == thi);
    if (__all(ok)) break;
    __builtin_amdgcn_s_sleep(POLL_SLEEP);
  }
}
DI bool tag_ok(u32x4 q, u32 t) { return q[1] == t && q[3] == t; }
DI void poll_gran(u32x4& q, u32 voff, rsrc_t srd, u32 soff, u32 tlo) {
#pragma nounroll
  for (int it = 0; it < SPIN_CAP; ++it) {
    if (__all(tag_ok(q, tlo))) break;
    __builtin_amdgcn_s_sleep(POLL_SLEEP);
    asm volatile("buffer_load_dwordx4 %0, %1, %2, %3 offen sc1\n\ts_waitcnt vmcnt(0)" : "=&v"(q) : "v"(voff), "s"(srd), "s"(soff) : "memory");
  }
}
DI u32 opq(u32 v) { asm volatile("" : "+v"(v)); return v; }

#define STAMP(i)
#define STAMPC(i)

#define GQ_N 3
#define PREFETCH 0
#define CODE_PF 56000
#define OUT_SC1 1
#define PRIO 0
#define UROT 1
static_assert(WS_ZB % 8192 == 0, "");
#define TLB_PF 0
#define VM(x) do { x; ++vq; } while (0)
#define WAITQ(mark) WAITV(vq - (mark))

template <int t>
DI void run(const char* __restrict__ x, const char* __restrict__ wqkv, const char* __restrict__ wphi, const char* __restrict__ wo,
            const char* __restrict__ win, const char* __restrict__ wsp, const char* __restrict__ bsp, const char* __restrict__ wout,
            char* __restrict__ out, char* ws, u64 ep) {
  const int tid = threadIdx.x, lane = tid & 63;
  const int w = __builtin_amdgcn_readfirstlane(tid >> 6);
  const int g = lane >> 4;
  const int b = blockIdx.x, xcd = b & 7, r = b >> 3;
  const int Pl = r >> 3, cl = r & 3;
  const int P = (xcd >> 1) * 4 + Pl;
  const int rot = 2 * ((Pl + cl) & 3);
  u32 acv = 0, acb = 0; (void)acv; (void)acb;
  const rsrc_t sx = __builtin_amdgcn_make_buffer_rsrc((void*)(x + (size_t)P * 131072), 0, 131072, 0x00020000);
  const u32 vx0 = (16 * w + (lane >> 3)) * 1024 + (((lane & 7) ^ g) * 16);
  const u32 vx1 = (vx0 + 8192) ^ 64;
  const u32 w2k = w * 2048;
#define DMA_X(dst, n, e) DMA_C(w2k, (dst) + (e) * 1024, (e) ? vx1 : vx0, sx, (((n) + rot) & 7) * 128)
  const int rw = w & 3, cw = w >> 2, i16 = lane & 15;
  const int c = (xcd & 1) * 4 + cl;
  const int p = 2 * P + (cl & 1);
  const int c2 = (xcd & 1) * 4 + (t ? 3 * (cl >> 1) : 1 + (cl >> 1));
  const u32 l16 = opq(lane * 16);
  const rsrc_t sa = __builtin_amdgcn_make_buffer_rsrc((void*)(wsp + (size_t)p * 262144), 0, 262144, 0x00020000);
  const rsrc_t sws = __builtin_amdgcn_make_buffer_rsrc((void*)ws, 0, WS_END, 0x00020000);
  const u32 vA0 = (lane >> 3) * 4096 + (((lane & 7) ^ g) * 16);
  const u32 vA1 = (vA0 + 32768) ^ 64;
  const int ux0 = UROT ? (cl >> 1) : 0, ux1 = UROT ? t : 0;
  const u32 uxs = w * 512 + ux1 * 256 + ux0 * 131072;
#define DMA_U(u, base, e) DMA_CC(w2k, (base) + (e) * 1024, (e) ? vA1 : vA0, sa, "s_xor_b32", uxs, (((u) >> 1) & 3) * 128 + (((u) >> 3) * 32 + ((u) & 1) * 16) * 4096)
  const f32x4 zero4 = {0, 0, 0, 0};
  u32x4 ones4 = {0x3F803F80u, 0x3F803F80u, 0x3F803F80u, 0x3F803F80u};
  u32 tlo, thi;
  u32x4 af[4][8];
  u32x4 bfr[2][8];
  u32x2 gf;
  const u32 ard = w * 2048 + i16 * 128 + (((g >> 1) ^ (i16 >> 1)) * 16) + (g & 1) * 8;
#define READ_U(u, base)                                                                                             \
  {                                                                                                                 \
    const u32 ab_ = opq(ard + (base));                                                                              \
    _Pragma("unroll") for (int kk = 0; kk < 2; ++kk)                                                               \
      af[2 * ((u) >> 3) + ((u) & 1)][((u) & 6) + kk] = cat(lds8(ab_ ^ (64 * kk)), lds8(ab_ ^ (64 * kk + 32)));      \
  }
#define USE_EPOCH() { asm volatile("" : "+s"(ep)); ep += 1; tlo = (u32)ep; thi = (u32)(ep >> 32); PIN(ones4); }
#define EARLY_STATS(mark)                                                                                           \
  {                                                                                                                 \
    WAITQ(mark); PIN(xrow);                                                                                         \
    float s_ = 0, q_ = 0;                                                                                           \
    _Pragma("unroll") for (int e = 0; e < 4; ++e) { s_ = dot2(xrow[e], ones4[0], s_); q_ = dot2(xrow[e], xrow[e], q_); } \
    s_ += dpp<0x128>(s_); q_ += dpp<0x128>(q_);                                                                     \
    s_ += dpp<0x124>(s_); q_ += dpp<0x124>(q_);                                                                     \
    s_ += dpp<0x4E>(s_); q_ += dpp<0x4E>(q_);                                                                       \
    s_ += dpp<0xB1>(s_); q_ += dpp<0xB1>(q_);                                                                       \
    red2g(s_, q_);                                                                                                  \
    float r_, m_;                                                                                                   \
    ln_fin(s_, q_, 1.0f / 512, r_, m_);                                                                             \
    VM(st16_sc1((u32x4){__float_as_uint(r_), tlo, __float_as_uint(m_), tlo}, l16 << 22, sws, WS_XG + (P * 128 + (t * 8 + c) * 8 + w) * 16)); \
  }
#define GATHER_XS()                                                                                                 \
  WAITQ(mxg); PIN(xg);                                                                                              \
  poll_gran(xg, i16 * 16, sws, WS_XG + (P * 128 + 16 * w) * 16, tlo);                                               \
  sts4f(L_XS + (16 * w + i16) * 4, __uint_as_float(xg[0]));                                                         \
  sts4f(L_XS + 512 + (16 * w + i16) * 4, __uint_as_float(xg[2]));
#define RELANE() const int lane = opq(l16) >> 4, i16 = lane & 15, g = lane >> 4, tid = w * 64 + lane; (void)tid; (void)g; (void)i16
#define SET_FLAG(off) st8_sc1((u32x2){tlo, thi}, l16 << 22, sws, (off))
  const int gs = grp_start(Z2S, c2), gn = t ? 5 : grp_start(Z2S, c2 + 1) - gs;
  const int trot = (p & 7) >= 5 ? (p & 7) - 5 : (p & 7);
#define TILE(j, n_) (((j) + trot) >= (n_) ? (j) + trot - (n_) : (j) + trot)
  const u32 zb0 = WS_ZB + (gs * 64 + 8 * w) * 1024 + ux1 * 4096;
#define DMA_WOUT(e)                                                                                                 \
  {                                                                                                                 \
    const rsrc_t so_ = __builtin_amdgcn_make_buffer_rsrc((void*)wout, 0, 688 * 1024, 0x00020000);                   \
    const int ws_ = grp_start(Z2S, w), wn_ = grp_start(Z2S, w + 1) - ws_;                                           \
    if ((e) < 6) dma16(L_WOUT + ((e) < 4 ? 2 * w + ((e) >> 1) : 16 + w) * 2048 + ((e) & 1) * 1024, vwo, so_, ((ws_ + TILE((e) >> 1, wn_)) * 2 + ((e) & 1)) * 8192); \
    else if ((e) == 6) dma16(L_WOUT + 24 * 2048 + w * 1024, vwo, so_, ((((w >> 1) == 0 ? 5 : ((w >> 1) == 1 ? 11 : ((w >> 1) == 2 ? 27 : 64))) + TILE(3, 6)) * 2 + (w & 1)) * 8192); \
  }
#define DMA_WOUT_AT(dst, e)                                                                                         \
  {                                                                                                                 \
    const rsrc_t so_ = __builtin_amdgcn_make_buffer_rsrc((void*)wout, 0, 688 * 1024, 0x00020000);                   \
    const int ws_ = grp_start(Z2S, w), wn_ = grp_start(Z2S, w + 1) - ws_;                                           \
    dma16((dst) + ((e) - 7) * 1024, vwo, so_, ((ws_ + TILE(wn_ - 1 - (((e) - 7) >> 1), wn_)) * 2 + (((e) - 7) & 1)) * 8192); \
  }
#define VWO() const u32 vwo = ((lane >> 2) & 7) * 1024 + c2 * 128 + (lane >> 5) * 64 + (((lane & 3) ^ (((lane >> 4) & 1) << 1)) * 16)
#define LOAD_B(u, so_)                                                                                              \
  bfr[u][0] = ld16_bo<0>(l16, sws, so_); bfr[u][1] = ld16_bo<1024>(l16, sws, so_);                                  \
  bfr[u][2] = ld16_bo<2048>(l16, sws, so_); bfr[u][3] = ld16_bo<3072>(l16, sws, so_);                               \
  bfr[u][4] = ld16_bo<0>(l16, sws, (so_) ^ 4096); bfr[u][5] = ld16_bo<1024>(l16, sws, (so_) ^ 4096);                \
  bfr[u][6] = ld16_bo<2048>(l16, sws, (so_) ^ 4096); bfr[u][7] = ld16_bo<3072>(l16, sws, (so_) ^ 4096);

  if constexpr (t == 0) {
    int vq = 2;
    const rsrc_t sw = __builtin_amdgcn_make_buffer_rsrc((void*)win, 0, 512 * 2752, 0x00020000);
    const int z2s = grp_start(Z2S, c), z2n = grp_start(Z2S, c + 1) - z2s;
    const int z1s = grp_start(Z1S, c), z1n = grp_start(Z1S, c + 1) - z1s;
    u32 vw[3];
    int wch[3];
#pragma unroll
    for (int e = 0; e < 3; ++e) {
      const int sg = 2 * e + (lane >> 5), hi = sg >= 3;
      wch[e] = (sg - 3 * hi) * 4 + ((lane & 3) ^ (hi << 1));
      vw[e] = wch[e] < 2 * z2n ? (16 * w + 8 * hi + ((lane >> 2) & 7)) * 2752 + 1376 + 32 * z2s + 16 * wch[e] : 0x7FFF0000u;
    }
    const u32 w3k = w * 3072;
#define DMA_W(bk, e) DMA_C(w3k, LZ_W + ((bk) % 3) * 24576 + (e) * 1024, vw[e], sw, ((2 * (bk) + rot) & 7) * (64 * 2752))
#define UBASE(u) ((u) % 5 == 0 ? LZ_U : ((u) % 5 - 1) * 16384)
#define ISS_X(n) do { VM(DMA_X(((n) & 3) * 16384, n, 0)); VM(DMA_X(((n) & 3) * 16384, n, 1)); mx[n] = vq; } while (0)
#define ISS_U(u) do { VM(DMA_U(u, UBASE(u), 0)); VM(DMA_U(u, UBASE(u), 1)); mu[u] = vq; } while (0)
#define ISS_W(bk, e0, e1) do { _Pragma("unroll") for (int e_ = e0; e_ < e1; ++e_) VM(DMA_W(bk, e_)); mw[bk] = vq; } while (0)
    int mx[8], mw[8], mu[16];
    mx[0] = vq;
    ISS_W(0, 0, 3);
    u32x4 xrow;
    VM(xrow = ld16(l16, sx, (c * 8 + w) * 1024));
    const int mxr = vq;
    ISS_X(1);
    const u32 xa0 = opq((32 * rw + i16) * 128 + ((g ^ (i16 >> 1)) * 16)), xa1 = xa0 ^ 64;
    u32 ta[3];
    int tl[3];
    {
      const u32 trb = LZ_W + g * 1536 + ((lane >> 2) & 3) * 64 + ((((g & 1) << 1) | ((lane >> 1) & 1)) * 16) + (lane & 1) * 8;
#pragma unroll
      for (int j = 0; j < 3; ++j) {
        tl[j] = 3 * cw + (j + rw) % 3;
        ta[j] = (trb + (tl[j] >> 1) * 512) ^ ((tl[j] & 1) * 32);
      }
    }
#define NEXT_BLOCK(bk) _Pragma("unroll") for (int j = 0; j < 3; ++j) ta[j] = opq(ta[j] + ((bk) == 0 ? 0 : ((bk) % 3 == 0 ? -49152 : 24576)))
    f32x4 acc[2][3], csa = zero4;
#pragma unroll
    for (int j = 0; j < 3; ++j) acc[0][j] = acc[1][j] = zero4;
    u32x4 xf[16][2], wf[2][3];
    u32x4 xg;
    int mxg = 0;
    WAITQ(mw[0]);
    BARRIER();
    USE_EPOCH()
    STAMP(7);
#define P1_MEM(n, h)                                                                                                \
  {                                                                                                                 \
    if ((n) == 0) { if ((h) == 0) ISS_X(2); if ((h) == 1) ISS_W(1, 0, 2); if ((h) == 2) ISS_W(1, 2, 3); if ((h) == 3) ISS_X(3); } \
    if ((n) == 1) { if ((h) == 0) ISS_W(2, 0, 2); if ((h) == 1) ISS_W(2, 2, 3); if ((h) == 2) VM(DMA_X(0, 4, 0)); if ((h) == 3) { VM(DMA_X(0, 4, 1)); mx[4] = vq; } } \
    if ((n) == 2 || (n) == 4) { if ((h) < 2) { VM(DMA_X((((n) + 3) & 3) * 16384, (n) + 3, (h) & 1)); mx[(n) + 3] = vq; } else ISS_W((n) / 2 + 2, (h) - 2, (h) - 1); } \
    if ((n) == 3) { if ((h) == 0) ISS_W(3, 2, 3); else if ((h) < 3) { VM(DMA_X(32768, 6, (h) - 1)); mx[6] = vq; } else ISS_U(0); } \
    if ((n) == 5) { if ((h) == 0) ISS_W(4, 2, 3); if ((h) == 1) { VM(xg = ld16_sc1(i16 * 16, sws, WS_XG + (P * 128 + 16 * w) * 16)); mxg = vq; } \
                    if ((h) >= 2) { VM(DMA_U(1, UBASE(1), (h) - 2)); mu[1] = vq; } }                                 \
    if ((n) == 6) { if ((h) < 3) ISS_W(5, h, (h) + 1); else ISS_U(2); }                                             \
    if ((n) == 7) { if ((h) < 2) { VM(DMA_U(3, UBASE(3), h)); mu[3] = vq; } }                                       \
  }
#pragma unroll
    for (int ks = 0; ks <= 16; ++ks) {
      const int n = ks >> 1, kk = ks & 1;
      if (ks > 0 && ks < 16 && kk == 0) {
        { const int mk = (n & 1) ? mx[n] : (mx[n] > mw[n >> 1] ? mx[n] : mw[n >> 1]); WAITQ(mk); }
        BARRIER();
      }
      if (ks < 16 && (ks & 3) == 0) { NEXT_BLOCK(ks >> 2); }
      if (ks == 8) {
#pragma unroll
        for (int e = 0; e < 3; ++e)
          vw[e] = wch[e] < 2 * z1n ? (16 * w + 8 * (2 * e + (lane >> 5) >= 3) + ((lane >> 2) & 7)) * 2752 + 32 * z1s + 16 * wch[e] : 0x7FFF0000u;
      }
      const int k1 = ks > 0 ? ks - 1 : 0, q1 = k1 & 1;
#pragma unroll
      for (int m = 0; m < 7; ++m) {
        if (ks > 0) {
          if (m < 6) acc[m & 1][m >> 1] = mfma32(xf[k1][m & 1], wf[q1][m >> 1], acc[m & 1][m >> 1]);
          else csa = mfma32(ones4, wf[q1][0], csa);
          SCHED();
        }
        if (ks < 16) {
          if (m == 0) { P1_MEM(n, 2 * kk) xf[ks & 15][0] = lds16((kk ? xa1 : xa0) + (n & 3) * 16384); xf[ks & 15][1] = lds16((kk ? xa1 : xa0) + (n & 3) * 16384 + 2048); }
          else if (m < 4) wf[kk][m - 1] = ldfrag(ta[m - 1] + (ks & 3) * 6144, 256);
          if (m == 4) P1_MEM(n, 2 * kk + 1)
        }
        SCHED();
      }
#pragma unroll
      for (int j = 0; j < 3; ++j) { PIN(acc[0][j]); PIN(acc[1][j]); }
      PIN(csa);
      if (ks == 2) EARLY_STATS(mxr)
    }
    GATHER_XS()
    sts4f(L_CS + tl[0] * 64 + i16 * 4, csa[0]);
    WAITQ(mu[1] > mw[4] ? mu[1] : mw[4]);
    READ_U(0, UBASE(0)) READ_U(1, UBASE(1))
    BARRIER();
    STAMP(1);
    u32x4 zp[3];
    ISS_W(6, 0, 3);
    {
      f32x2 ps[4], pq[4];
      float cs[3];
#pragma unroll
      for (int j = 0; j < 3; ++j) cs[j] = -lds4f(L_CS + tl[j] * 64 + i16 * 4);
#pragma unroll
      for (int rt = 0; rt < 2; ++rt) {
        const f32x4 rr = lds16f(L_XS + (32 * rw + 16 * rt + 4 * g) * 4), mm = lds16f(L_XS + 512 + (32 * rw + 16 * rt + 4 * g) * 4);
        f32x2 z0[3], z1[3];
#pragma unroll
        for (int j = 0; j < 3; ++j) {
          z0[j] = gelu2(fma2(lo2(mm), bc2(cs[j]), lo2(acc[rt][j])) * lo2(rr));
          z1[j] = gelu2(fma2(hi2(mm), bc2(cs[j]), hi2(acc[rt][j])) * hi2(rr));
          zp[j][2 * rt] = pk2v(z0[j]); zp[j][2 * rt + 1] = pk2v(z1[j]);
        }
        ps[2 * rt] = z0[0] + z0[1] + z0[2]; ps[2 * rt + 1] = z1[0] + z1[1] + z1[2];
        pq[2 * rt] = fma2(z0[2], z0[2], fma2(z0[1], z0[1], z0[0] * z0[0]));
        pq[2 * rt + 1] = fma2(z1[2], z1[2], fma2(z1[1], z1[1], z1[0] * z1[0]));
        SCHED();
        if (rt == 0) { ISS_U(4); ISS_U(5); }
        else ISS_U(6);
        SCHED();
      }
      const bool b3 = lane & 8, b2 = lane & 4, b1 = lane & 2;
#pragma unroll
      for (int m = 0; m < 2; ++m)
#pragma unroll
        for (int e = 0; e < 2; ++e) {
          ps[m][e] = (b3 ? ps[m + 2][e] : ps[m][e]) + dpp<0x128>(b3 ? ps[m][e] : ps[m + 2][e]);
          pq[m][e] = (b3 ? pq[m + 2][e] : pq[m][e]) + dpp<0x128>(b3 ? pq[m][e] : pq[m + 2][e]);
        }
#pragma unroll
      for (int e = 0; e < 2; ++e) {
        ps[0][e] = (b2 ? ps[1][e] : ps[0][e]) + dpp<0x141>(b2 ? ps[0][e] : ps[1][e]);
        pq[0][e] = (b2 ? pq[1][e] : pq[0][e]) + dpp<0x141>(b2 ? pq[0][e] : pq[1][e]);
      }
      float s_ = (b1 ? ps[0][1] : ps[0][0]) + dpp<0x4E>(b1 ? ps[0][0] : ps[0][1]);
      float q_ = (b1 ? pq[0][1] : pq[0][0]) + dpp<0x4E>(b1 ? pq[0][0] : pq[0][1]);
      s_ += dpp<0xB1>(s_);
      q_ += dpp<0xB1>(q_);
      const int row = 32 * rw + 16 * (i16 >> 3) + 4 * g + ((i16 >> 1) & 3);
      VM(st16_sc1((u32x4){__float_as_uint(s_), tlo, __float_as_uint(q_), tlo}, row * 16 + ((lane & 1) << 30), sws, WS_SG + ((P * 8 + c) * 2 + cw) * 2048));
      PIN(zp[0]); PIN(zp[1]); PIN(zp[2]);
    }
    STAMP(2);
    {
    RELANE();
#pragma unroll
    for (int j = 0; j < 3; ++j) acc[0][j] = acc[1][j] = zero4;
    csa = zero4;
    u32x4 q0, q1, q2, q3;
    int mq = 0, mz = 0;
    const u32 gvo = (2 * g) * 4096 + (32 * rw + 16 * cw + i16) * 16;
    const u32 gso = WS_SG + P * 32768;
#pragma unroll
    for (int ks = 0; ks <= 16; ++ks) {
      if (ks == 8) {
        WAITQ(mq); PIN(q0); PIN(q1); PIN(q2); PIN(q3);
#pragma nounroll
        for (int it = 0; it < SPIN_CAP; ++it) {
          if (__all(tag_ok(q0, tlo) && tag_ok(q1, tlo) && tag_ok(q2, tlo) && tag_ok(q3, tlo))) break;
          __builtin_amdgcn_s_sleep(POLL_SLEEP);
          asm volatile(
              "buffer_load_dwordx4 %0, %4, %5, %6 offen sc1\n\t"
              "buffer_load_dwordx4 %1, %4, %5, %6 offen offset:2048 sc1\n\t"
              "buffer_load_dwordx4 %2, %4, %5, %7 offen sc1\n\t"
              "buffer_load_dwordx4 %3, %4, %5, %7 offen offset:2048 sc1\n\t"
              "s_waitcnt vmcnt(0)"
              : "=&v"(q0), "=&v"(q1), "=&v"(q2), "=&v"(q3)
              : "v"(gvo), "s"(sws), "s"(gso), "s"(gso + 4096)
              : "memory");
        }
        float s = (__uint_as_float(q0[0]) + __uint_as_float(q1[0])) + (__uint_as_float(q2[0]) + __uint_as_float(q3[0]));
        float q = (__uint_as_float(q0[2]) + __uint_as_float(q1[2])) + (__uint_as_float(q2[2]) + __uint_as_float(q3[2]));
        red2g(s, q);
        float r2, m2;
        ln_fin(s, q, 1.0f / 688, r2, m2);
        sts4f(L_ZST + (32 * rw + 16 * cw + i16) * 4, r2);
        sts4f(L_ZST + 512 + (32 * rw + 16 * cw + i16) * 4, -m2 * r2);
        STAMP(3);
      }
      if (ks < 16 && (ks & 3) == 0) {
        if (ks > 0) { WAITQ(ks == 12 ? mz : mw[4 + (ks >> 2)]); BARRIER(); }
        if (ks == 12) { if (w == 0) SET_FLAG(WS_FA + (P * 8 + c) * 128); STAMP(5); }
        NEXT_BLOCK(4 + (ks >> 2));
      }
      if (ks == 8) {
#pragma unroll
        for (int rt = 0; rt < 2; ++rt) {
          const u32 ra = L_ZST + (32 * rw + 16 * rt + 4 * g) * 4;
          const f32x4 r4 = lds16f(ra), m4 = lds16f(ra + 512);
#pragma unroll
          for (int j = 0; j < 3; ++j) {
            const u32 u0 = zp[j][2 * rt], u1 = zp[j][2 * rt + 1];
            zp[j][2 * rt] = pk2v(fma2((f32x2){blo(u0), bhi(u0)}, lo2(r4), lo2(m4)));
            zp[j][2 * rt + 1] = pk2v(fma2((f32x2){blo(u1), bhi(u1)}, hi2(r4), hi2(m4)));
          }
        }
#pragma unroll
        for (int j = 0; j < 3; ++j)
          VM(st16_sc1(zp[j], l16, sws, (tl[j] < z2n) ? WS_ZB + ((z2s + tl[j]) * 64 + 4 * P + rw) * 1024 : 0x7FFF0000u));
        mz = vq;
        STAMP(4);
        SCHED();
      }
      if (ks >= 5 && ks <= 9) { WAITQ(mu[ks - 3]); READ_U(ks - 3, UBASE(ks - 3)) }
      if (ks >= 9 && ks <= 13) { WAITQ(mu[ks - 2]); READ_U(ks - 2, UBASE(ks - 2)) }
      if (ks >= 13) { WAITQ(mu[ks - 1]); READ_U(ks - 1, UBASE(ks - 1)) }
      const int k1 = ks > 0 ? ks - 1 : 0, q1_ = k1 & 1;
#pragma unroll
      for (int m = 0; m < 7; ++m) {
        if (ks > 0) {
          if (m < 6) acc[m & 1][m >> 1] = mfma32(wf[q1_][m >> 1], xf[k1][m & 1], acc[m & 1][m >> 1]);
          else csa = mfma32(ones4, wf[q1_][0], csa);
          SCHED();
        }
        if (ks < 16 && m >= 1 && m < 4) wf[ks & 1][m - 1] = ldfrag(ta[m - 1] + (ks & 3) * 6144, 256);
        if (m == 0 || m == 4) {
          const int h = m == 4;
          if (h == 0 && ks >= 6 && ks <= 13) asm volatile("s_waitcnt lgkmcnt(10)" ::: "memory");
          if (ks >= 6 && ks <= 10) { VM(DMA_U(ks + 1, UBASE(ks + 1), h)); mu[ks + 1] = vq; }
          if (ks >= 10 && ks <= 13) { VM(DMA_U(ks + 2, UBASE(ks + 2), h)); mu[ks + 2] = vq; }
        }
        if (m == 6) {
          if (ks >= 4 && ks <= 6) ISS_W(7, ks - 4, ks - 3);
        }
        if (m == 2) {
          if (ks == 5) { VM(q0 = ld16_sc1o<0>(gvo, sws, gso)); VM(q1 = ld16_sc1o<2048>(gvo, sws, gso)); }
          if (ks == 6) { VM(q2 = ld16_sc1o<0>(gvo, sws, gso + 4096)); VM(q3 = ld16_sc1o<2048>(gvo, sws, gso + 4096)); mq = vq; }
        }
        SCHED();
      }
#pragma unroll
      for (int j = 0; j < 3; ++j) { PIN(acc[0][j]); PIN(acc[1][j]); }
      PIN(csa);
    }
    sts4f(L_CS + 512 + tl[0] * 64 + i16 * 4, csa[0]);
    gf = ld8_sc1((lane >> 5) * 1024, sws, WS_FA + (16 * w + c2) * 128);
    BARRIER();
    VWO();
    DMA_WOUT(0) DMA_WOUT(1)
    WAITV(2);
    PIN(gf);
    if (!__all(gf[0] == tlo && gf[1] == thi)) poll_flags((lane >> 5) * 1024, true, sws, WS_FA + (16 * w + c2) * 128, tlo, thi);
    STAMP(6);
    {
      float xr[2], xm[2];
#pragma unroll
      for (int rt = 0; rt < 2; ++rt) {
        xr[rt] = lds4f(L_XS + (32 * rw + 16 * rt + i16) * 4);
        xm[rt] = -lds4f(L_XS + 512 + (32 * rw + 16 * rt + i16) * 4);
      }
      const u32 so0 = zb0 + TILE(0, gn) * 65536;
#pragma unroll
      for (int j = 0; j < 3; ++j) {
        const f32x4 cc = lds16f(L_CS + 512 + tl[j] * 64 + g * 16);
        bfr[0][3 * j] = ld16_b(l16, sws, so0 ^ (3 * j * 1024));
        bfr[0][3 * j + 1] = ld16_b(l16, sws, so0 ^ ((3 * j + 1) * 1024));
        if (j < 2) bfr[0][3 * j + 2] = ld16_b(l16, sws, so0 ^ ((3 * j + 2) * 1024));
        u32x4 zz;
#pragma unroll
        for (int rt = 0; rt < 2; ++rt) {
          zz[2 * rt] = pk2v(gelu2(fma2(lo2(cc), bc2(xm[rt]), lo2(acc[rt][j])) * bc2(xr[rt])));
          zz[2 * rt + 1] = pk2v(gelu2(fma2(hi2(cc), bc2(xm[rt]), hi2(acc[rt][j])) * bc2(xr[rt])));
        }
        st16_sc1(zz, l16, sws, (tl[j] < z1n) ? WS_Z1 + ((4 * P + rw) * 43 + z1s + tl[j]) * 1024 : 0x7FFF0000u);
        SCHED();
      }
    }
    }
  } else {
    int vq = 2;
    const rsrc_t sw = __builtin_amdgcn_make_buffer_rsrc((void*)wqkv, 0, 512 * 3072, 0x00020000);
    const u32 vw = (8 * w + ((lane >> 2) & 7)) * 3072 + 128 * c + ((lane >> 5) * 4 + ((lane & 3) ^ ((w & 1) << 1))) * 16;
    const u32 w3k = w * 3072;
#define DMA_S(n, e)                                                                                                 \
  do {                                                                                                              \
    if ((e) < 2) DMA_X(((n) % 3) * 40960, n, (e) & 1);                                                              \
    else DMA_CC(w3k, ((n) % 3) * 40960 + 16384 + ((e) - 2) * 1024, vw, sw, "s_add_u32", (((n) + rot) & 7) * (64 * 3072), ((e) - 2) * 1024); \
  } while (0)
#define ISS_Q(u, base) do { VM(DMA_U(u, base, 0)); VM(DMA_U(u, base, 1)); mu[u] = vq; } while (0)
    int ms[8], mu[16];
    VM(DMA_S(0, 2)); VM(DMA_S(0, 3)); VM(DMA_S(0, 4)); ms[0] = vq;
    u32x4 xrow;
    VM(xrow = ld16(l16, sx, ((8 + c) * 8 + w) * 1024));
    const int mxr = vq;
#pragma unroll
    for (int e = 0; e < 5; ++e) { VM(DMA_S(1, e)); ms[1] = vq; }
    const int l32 = lane & 31, hh = lane >> 5;
    u32 xa = (32 * rw + l32) * 128 + ((hh ^ ((l32 >> 1) & 7)) * 16);
    u32 wa = 16384 + hh * 3072 + cw * 512 + ((lane >> 2) & 3) * 64 + (((((g & 1) << 1) | ((lane >> 1) & 1)) ^ (hh << 1)) * 16) + (lane & 1) * 8;
    u32 ca = 16384 + g * 3072 + min(w, 5) * 512 + ((lane >> 2) & 3) * 64 + ((((g & 1) << 1) | ((lane >> 1) & 1)) * 16) + (lane & 1) * 8;
    const f32x16 zero16 = {0, 0, 0, 0, 0, 0, 0, 0, 0, 0, 0, 0, 0, 0, 0, 0};
    f32x16 acc[3] = {zero16, zero16, zero16};
    f32x4 csa[2] = {zero4, zero4};
    u32x4 xg;
    int mxg = 0;
    WAITQ(ms[0]);
    BARRIER();
    USE_EPOCH()
    STAMP(7);
    {
      u32x4 xf[2], wf[2][4];
#pragma unroll
      for (int n = 0; n <= 8; ++n)
#pragma unroll
      for (int sq = 0; sq < 4; ++sq) {
        const int s = 4 * n + sq;
        if (s > 32) continue;
        if (s < 32 && sq == 0) {
          if (n > 0) {
            WAITQ(ms[n]);
            BARRIER();
          }
          const int d = n == 0 ? 0 : (n % 3 == 0 ? -81920 : 40960);
          xa = opq(xa + d); wa = opq(wa + d); ca = opq(ca + d);
        }
        const int q1 = (s + 1) & 1, q0 = s & 1;
#pragma unroll
        for (int m = 0; m < 4; ++m) {
          if (s > 0) {
            if (m < 3) acc[m] = mfmaL(wf[q1][m], xf[q1], acc[m]);
            else csa[q1] = mfma32(ones4, wf[q1][3], csa[q1]);
            SCHED();
          }
          if (s < 32) {
            if (m == 0) { xf[q0] = lds16(xa ^ (32 * sq)); wf[q0][0] = ldfrag(wa + sq * 6144, 256); }
            if (m == 1) { wf[q0][1] = ldfrag(wa + sq * 6144 + 1024, 256); wf[q0][2] = ldfrag(wa + sq * 6144 + 2048, 256); }
            if (m == 2) wf[q0][3] = ldfrag((ca ^ (32 * (sq & 1))) + (sq >> 1) * 12288, 256);
            if (m == 0 || m == 2) {
              const int h = 2 * sq + (m == 2);
              if (n <= 5 && h < 5) { VM(DMA_S(n + 2, h)); ms[n + 2] = vq; }
              if (n == 0 && h == 5) {
                rsrc_t sp = __builtin_amdgcn_make_buffer_rsrc((void*)wphi, 0, 16384, 0x00020000);
                VM(dma16(L_WPHI + (w & 1) * 1024, l16, sp, c * 2048 + (w & 1) * 1024));
              }
              if (n == 4 && h == 5) { VM(xg = ld16_sc1(i16 * 16, sws, WS_XG + (P * 128 + 16 * w) * 16)); mxg = vq; }
              if (n == 6) { VM(DMA_U(sq, (sq < 2 ? LQ_M : LQ_A) + (sq & 1) * 16384, h & 1)); mu[sq] = vq; }
              if (n == 7 && sq < 2) { VM(DMA_U(4 + sq, sq * 16384, h & 1)); mu[4 + sq] = vq; }
            }
          }
          SCHED();
        }
        PIN(acc[0]); PIN(acc[1]); PIN(acc[2]); PIN(csa[0]); PIN(csa[1]);
        if (s == 3) EARLY_STATS(mxr)
      }
    }
    GATHER_XS()
    sts4f(L_CS + w * 128 + i16 * 4, csa[0][0]);
    sts4f(L_CS + w * 128 + 64 + i16 * 4, csa[1][0]);
    BARRIER();
    STAMP(1);
    WAITQ(mu[3]);
    READ_U(0, LQ_M) READ_U(1, LQ_M + 16384) READ_U(2, LQ_A) READ_U(3, LQ_A + 16384)
    const float xr = lds4f(L_XS + (32 * rw + l32) * 4), xm = -lds4f(L_XS + 512 + (32 * rw + l32) * 4);
    u32x4 wp0, wp1;
    {
      const u32 a = L_WPHI + (2 * cw + (lane & 1)) * 512 + (4 * hh + ((lane >> 2) & 3)) * 32 + (2 * (g & 1) + ((lane >> 1) & 1)) * 8;
      const u32x4 f = ldfrag(a, 256);
      const bool own = (lane >> 2) & 1;
#pragma unroll
      for (int e = 0; e < 4; ++e) { wp0[e] = own ? 0u : f[e]; wp1[e] = own ? f[e] : 0u; }
    }
#define FEATURES(e_, dst, hook)                                                                                         \
  {                                                                                                                 \
    u32x4 kp[2];                                                                                                    \
    _Pragma("unroll") for (int a = 0; a < 4; ++a) {                                                                \
      const f32x4 cc = lds16f(L_CS + (2 * (e_) + cw) * 128 + a * 32 + hh * 16);                                      \
      kp[a >> 1][2 * (a & 1)] = pk2v(fma2(lo2(cc), bc2(xm), (f32x2){acc[e_][4 * a], acc[e_][4 * a + 1]}));          \
      kp[a >> 1][2 * (a & 1) + 1] = pk2v(fma2(hi2(cc), bc2(xm), (f32x2){acc[e_][4 * a + 2], acc[e_][4 * a + 3]}));  \
    }                                                                                                               \
    f32x16 pl = mfmaL(wp0, kp[0], zero16);                                                                          \
    pl = mfmaL(wp1, kp[1], pl);                                                                                     \
    hedgehog16(pl, xr, dst, hook);                                                                                  \
  }
    {
      const int s_ = 32 * rw + l32;
      const u32 key = (((s_ >> 1) & 3) ^ (((s_ >> 3) & 1) << 1)) * 16;
      {
        const u32 va = LQ_V + (s_ >> 3) * 1024 + cw * 512 + (s_ & 7) * 64 + hh * 8;
#pragma unroll
        for (int a = 0; a < 4; ++a) {
          const f32x4 cc = lds16f(L_CS + (4 + cw) * 128 + a * 32 + hh * 16);
          sts8(va + ((a * 16) ^ key), (u32x2){pk2v(fma2(lo2(cc), bc2(xm), (f32x2){acc[2][4 * a], acc[2][4 * a + 1]}) * bc2(xr)),
                                              pk2v(fma2(hi2(cc), bc2(xm), (f32x2){acc[2][4 * a + 2], acc[2][4 * a + 3]}) * bc2(xr))});
        }
      }
      WAITQ(mu[5]);
      READ_U(4, 0) READ_U(5, 16384)
      WAITL0();
      u32x4 ph[4];
      auto hk = [&](int i) __attribute__((always_inline)) {
        const int u = 6 + (i >> 1);
        SCHED();
        VM(DMA_U(u, (u < 8 ? LQ_M : (u < 10 ? LQ_A : 0)) + (u & 1) * 16384, i & 1));
        mu[u] = vq;
        SCHED();
      };
      FEATURES(1, ph, hk)
      const u32 pa = LQ_PHI + (s_ >> 3) * 2048 + (2 * cw + hh) * 512 + (s_ & 7) * 64;
#pragma unroll
      for (int a = 0; a < 4; ++a) sts16(pa + ((a * 16) ^ key), ph[a]);
    }
    BARRIER();
    STAMP(2);
    {
      const u32 kx = (((lane >> 3) & 1) ^ ((g & 1) << 1)) * 16;
      const u32 pa = LQ_PHI + g * 2048 + (w >> 1) * 512 + ((lane >> 2) & 3) * 64 + (((2 * (w & 1) + ((lane >> 1) & 1)) * 16) ^ kx) + (lane & 1) * 8;
      const u32 va = LQ_V + g * 1024 + (w >> 2) * 512 + ((lane >> 2) & 3) * 64 + (((2 * ((w >> 1) & 1) + ((lane >> 1) & 1)) * 16) ^ kx) + (lane & 1) * 8;
      u32x4 pf[4], vf[4];
#pragma unroll
      for (int k = 0; k < 4; ++k) {
        pf[k] = cat(ldstr(pa + k * 8192), ldstr((pa ^ 32) + k * 8192 + 256));
        vf[k] = cat(ldstr(va + k * 4096), ldstr((va ^ 32) + k * 4096 + 256));
      }
      f32x4 kv = zero4, kz = zero4;
#pragma unroll
      for (int k = 0; k < 4; ++k) { kv = mfma32(pf[k], vf[k], kv); kz = mfma32(pf[k], ones4, kz); }
      const u32 so = WS_KP + (c * 16 + P) * KP_SZ;
      u32x4 g0 = {__float_as_uint(kv[0]), tlo, __float_as_uint(kv[1]), tlo}, g1 = {__float_as_uint(kv[2]), tlo, __float_as_uint(kv[3]), tlo};
      u32x4 g2 = {__float_as_uint((lane & 1) ? kz[2] : kz[0]), tlo, __float_as_uint((lane & 1) ? kz[3] : kz[1]), tlo};
      PIN(g0); PIN(g1); PIN(g2);
      asm volatile("s_nop 7" ::: "memory");
      VM(st16_sc1(g0, l16, sws, so + w * 2048));
      VM(st16_sc1(g1, l16, sws, so + w * 2048 + 1024));
      VM(st16_sc1(g2, g * 32 + (lane & 1) * 16 + ((i16 >> 1) << 26), sws, so + 16384 + w * 128));
    }
    STAMP(3);
    WAITQ(mu[11]);
    READ_U(6, LQ_M) READ_U(7, LQ_M + 16384) READ_U(8, LQ_A) READ_U(9, LQ_A + 16384) READ_U(10, 0) READ_U(11, 16384)
    BARRIER();
    ISS_Q(12, LQ_M); VM(DMA_U(13, LQ_M + 16384, 0));
    u32x4 phq[4];
    {
      VWO();
      auto hk = [&](int i) __attribute__((always_inline)) {
        SCHED();
        if (i == 0) DMA_U(13, LQ_M + 16384, 1);
        else if (i < 5) DMA_U(14 + ((i - 1) >> 1), LQ_A + ((i - 1) >> 1) * 16384, (i - 1) & 1);
        else DMA_WOUT(i - 5)
        SCHED();
      };
      FEATURES(0, phq, hk)
    }
    PIN(phq[0]); PIN(phq[1]); PIN(phq[2]); PIN(phq[3]);
    STAMP(4);
    if (w >= 5) {
      const int id = 2 * P + (w == 7 ? (lane & 1) : w - 5), wq = id >> 2, gq = id & 3;
      const u32 vi0 = (4 * g) * KP_SZ + (w == 7 ? 16384 + wq * 128 + gq * 32 : wq * 2048 + (gq * 16 + i16) * 16);
      const u32 vi1 = vi0 + (w == 7 ? 16 : 1024);
      const u32 vo = (w == 7 ? (WS_KZ - WS_KF) + c * 512 + id * 16 + ((i16 >> 1) << 25) : c * 8192 + id * 256 + i16 * 16) + (g << 28);
      float sa[4];
#pragma nounroll
      for (int it = 0; it < SPIN_CAP; ++it) {
        u32x4 a[4][2];
#pragma unroll
        for (int o = 0; o < 4; ++o) {
          a[o][0] = ld16_sc1(vi0, sws, WS_KP + (c * 16 + o) * KP_SZ);
          a[o][1] = ld16_sc1(vi1, sws, WS_KP + (c * 16 + o) * KP_SZ);
        }
        WAITV(0);
        bool ok = true;
#pragma unroll
        for (int o = 0; o < 4; ++o) { PIN(a[o][0]); PIN(a[o][1]); ok = ok && tag_ok(a[o][0], tlo) && tag_ok(a[o][1], tlo); }
#pragma unroll
        for (int e = 0; e < 4; ++e)
          sa[e] = (__uint_as_float(a[0][e >> 1][2 * (e & 1)]) + __uint_as_float(a[1][e >> 1][2 * (e & 1)])) +
                  (__uint_as_float(a[2][e >> 1][2 * (e & 1)]) + __uint_as_float(a[3][e >> 1][2 * (e & 1)]));
        if (__all(ok)) break;
        __builtin_amdgcn_s_sleep(POLL_SLEEP);
      }
      red2g(sa[0], sa[1]);
      red2g(sa[2], sa[3]);
      st16_sc1((u32x4){pk2(sa[0], sa[1]), tlo, pk2(sa[2], sa[3]), tlo}, vo, sws, WS_KF);
    }
    gf = ld8_sc1((lane >> 5) * 1024, sws, WS_FA + (16 * w + c2) * 128);
    WAITV(8);
    READ_U(12, LQ_M) READ_U(13, LQ_M + 16384) READ_U(14, LQ_A) READ_U(15, LQ_A + 16384)
    BARRIER();
#pragma unroll
    for (int a = 0; a < 4; ++a) sts16(L_WO1 + w * 4096 + a * 1024 + l16, phq[a]);
    WAITV(0);
    PIN(gf);
    if (!__all(gf[0] == tlo && gf[1] == thi)) poll_flags((lane >> 5) * 1024, true, sws, WS_FA + (16 * w + c2) * 128, tlo, thi);
    STAMP(6);
    { const u32 so_ = zb0 + TILE(0, gn) * 65536; LOAD_B(0, so_) }
  }

  {
  RELANE();
  u32x4 kfg, kzg;
  const u32 kfo = WS_KF + c * 8192 + w * 1024, kzo = WS_KZ + c * 512 + w * 64;
  if (t != 0) {
    kfg = ld16_sc1(l16, sws, kfo);
    kzg = ld16_sc1(g * 16, sws, kzo);
  }
  { const u32 so_ = zb0 + TILE(1, gn) * 65536; LOAD_B(1, so_) }
  STAMP(9);
  const u32 redw = opq(L_RED + w * 4096 + lane * 16), redr = opq(L_RED + (rw ^ (2 * ux0)) * 1024 + lane * 16);
  const u32 fd = WS_FD + (p * 16 + c2) * 128;
  const u32 f3s = __builtin_amdgcn_readfirstlane(WS_FB + (P * 8 + (c2 > 0 ? c2 - 1 : 0)) * 128);
  const u32 vw3 = ((lane >> 2) & 7) * 1024 + c2 * 128 + (lane >> 5) * 64;
  const rsrc_t swo = __builtin_amdgcn_make_buffer_rsrc((void*)wo, 0, 512 * 1024, 0x00020000);
#define DMA_WO(dst, half, e) dma16((dst) + w * 4096 + (e) * 1024, (vw3 + (lane & 3) * 16) ^ (((e) & 1) * 32), swo, ((w + 8 * (half)) * 4 + (e)) * 8192)
#define FV() ((lane & 32) ? (P * 8 + ((lane >> 2) & 7)) * 128 : (WS_FD - WS_FC) + (p * 16 + (((lane >> 1) & 14) | cw)) * 128)
  u32x2 z1r[6], hold;
  u32 bs;
  f32x4 sm0;
#define EMIT_H(jt, sm_)                                                                                             \
  {                                                                                                                 \
    const f32x2 bb = bc2(blo(bs));                                                                                  \
    const u32 h0 = pk2v((f32x2){blo(z1r[jt][0]), bhi(z1r[jt][0])} * (lo2(sm_) + bb));                               \
    const u32 h1 = pk2v((f32x2){blo(z1r[jt][1]), bhi(z1r[jt][1])} * (hi2(sm_) + bb));                               \
    const u32 hb_ = WS_HF + (((4 * p + rw) * 2 + ((jt) >> 1)) * 8 + c2) * 1024;                                     \
    if ((jt) == 0 || ((jt) == 2 && gn == 6)) hold = (u32x2){h0, h1};                                                \
    else if ((jt) == 1 || ((jt) == 3 && gn == 6)) st16_sc1((u32x4){hold[0], hold[1], h0, h1}, l16, sws, hb_);       \
    else if ((jt) == 2) st16_sc1((u32x4){h0, h1, 0u, 0u}, l16, sws, hb_);                                           \
    else st16_sc1((u32x4){h0, tlo, h1, tlo}, l16, sws, WS_HL + (((4 * p + rw) * 8 + c2) * 2 + (gn - 1 - (jt))) * 1024); \
  }
#pragma unroll
  for (int j = 0; j < 6; ++j) {
    if (j < 5 || gn == 6) {
      const int u = j & 1;
      if (j == 0) WAITV(8);
      if (j == 2) WAITV(t ? 10 : 8);
      if (j == 1) { WAITV(9); WAITV_IF(cw, 1, 8); }
      if (j == 3) { WAITV(9); WAITV_IF(cw, 1, 8); }
      if (j == 4) { if (t) WAITV(4); else { WAITV(12); WAITV_IF(gn, 5, 4); } }
      if (j == 5) { WAITV(10); WAITV_IF(cw, 1, 9); }
#pragma unroll
      for (int i = 0; i < 8; ++i) PIN(bfr[u][i]);
      {
        f32x4 a_[4];
#pragma unroll
        for (int rt = 0; rt < 4; ++rt) {
          a_[rt] = zero4;
#pragma unroll
          for (int i = 0; i < 8; ++i) a_[rt] = mfma32(bfr[u][i], af[rt][i], a_[rt]);
          SCHED();
          if (rt > 0) sts16f(redw + u * 32768 + (rt - 1) * 1024, a_[rt - 1]);
          SCHED();
        }
        if (j == 0 && t != 0) {
          PIN(kfg); PIN(kzg);
          poll_gran(kfg, l16, sws, kfo, tlo);
          poll_gran(kzg, g * 16, sws, kzo, tlo);
          sts8(L_KFS + (w >> 1) * 1024 + i16 * 64 + (w & 1) * 32 + g * 8, (u32x2){kfg[0], kfg[2]});
          sts8(L_KFS + 4096 + w * 32 + g * 8, (u32x2){kzg[0], kzg[2]});
          sts8(L_KFS + 4352 + (lane & 7) * 8, (u32x2){0u, 0u});
        }
        sts16f(redw + u * 32768 + 3072, a_[3]);
      }
      u32x4 of[2];
      if (j == 1 && t != 0) {
        const int l32 = lane & 31, hh = lane >> 5;
        const bool own = ((lane >> 2) & 1) == hh;
        const u32 ka = own ? L_KFS + (2 * cw + hh) * 1024 + ((l32 & 3) + 4 * (l32 >> 3)) * 64 : L_KFS + 4352;
        const u32 zaa = own ? L_KFS + 4096 + (2 * cw + hh) * 64 : L_KFS + 4352;
        const u32 qa = L_WO1 + w * 4096 + l16;
        const f32x16 zero16 = {0, 0, 0, 0, 0, 0, 0, 0, 0, 0, 0, 0, 0, 0, 0, 0};
        f32x16 den = zero16, num = zero16;
#pragma unroll
        for (int a = 0; a < 4; ++a) {
          const u32x4 pq_ = lds16(qa + a * 1024);
          den = mfmaL(lds16(zaa + a * 16), pq_, den);
          num = mfmaL(lds16(ka + a * 16), pq_, num);
        }
        const float inv = __builtin_amdgcn_rcpf(den[0] + 1e-6f);
#pragma unroll
        for (int a = 0; a < 2; ++a) {
          of[a] = (u32x4){pk2(num[8 * a] * inv, num[8 * a + 1] * inv), pk2(num[8 * a + 2] * inv, num[8 * a + 3] * inv),
                          pk2(num[8 * a + 4] * inv, num[8 * a + 5] * inv), pk2(num[8 * a + 6] * inv, num[8 * a + 7] * inv)};
          PIN(of[a]);
        }
        STAMP(5);
      }
      BAR_IF(cw, 1, 63);
      if (w == 4) {
        if (j == 0 && t == 0) SET_FLAG(WS_FB + (P * 8 + c) * 128);
        if (j == 3) SET_FLAG(fd);
        if (j == 3 && t != 0) SET_FLAG(WS_FC + (P * 8 + c) * 128);
        if ((j == 4 && gn == 5) || j == 5) SET_FLAG(fd + 1024);
      }
      if (j == 1) {
        if (cw == 0) {
          WAITV(0);
          PIN(gf);
          if (!__all(gf[0] == tlo && gf[1] == thi)) poll_flags((lane >> 5) * 128, true, sws, f3s, tlo, thi);
          rsrc_t sb = __builtin_amdgcn_make_buffer_rsrc((void*)bsp, 0, 4096, 0x00020000);
          bs = ld2(i16 * 2, sb, (64 * p + 16 * rw) * 2);
        }
      }
      if ((j == 1 || j == 2) && cw == 0) {
#pragma unroll
        for (int v = 3 * j - 3; v < 3 * j; ++v)
          z1r[v] = ld8_sc1(l16, sws, WS_Z1 + ((2 * p + (rw >> 1)) * 43 + gs + TILE(min(v, gn - 1), gn)) * 1024 + (rw & 1) * 8);
      }
      if ((j == 4 && gn == 5) || j == 5) gf = ld8_sc1(FV(), sws, WS_FC);
      if (j == 4 && t == 0) {
        VWO();
#pragma unroll
        for (int e = 2; e < 7; ++e) DMA_WOUT(e)
      }
      const u32 ra_ = redr + ((j + 1) & 1) * 32768;
      f32x4 sm, r_[2];
#pragma unroll
      for (int i = 0; i < 8; ++i) {
        if (j >= 1 && j < (t ? 3 : 4) && (i & 1) == 0 && cw == 0) {
          if (i == 2) sm = r_[0] + r_[1];
          if (i > 2) sm += r_[0] + r_[1];
          r_[0] = lds16f(ra_ + i * 4096); r_[1] = lds16f(ra_ + (i + 1) * 4096);
        }
        SCHED();
        if (j < 3) bfr[u][i] = ld16_b(l16, sws, (zb0 + TILE(j + 2, gn) * 65536) ^ (i * 1024));
        else if (j == 3 && t == 0) ld16_b_if6(bfr[1][i], gn, l16, sws, (zb0 + TILE(5, gn) * 65536) ^ (i * 1024));
        SCHED();
      }
      if (j == 0 && cw == 0) gf = ld8_sc1((lane >> 5) * 128, sws, f3s);
      if (j == 1 && t != 0) {
        const u32 vo = ((lane >> 4) & 1) * 16384 + (lane >> 5) * 512 + i16 * 16;
        st16_sc1(of[0], vo, sws, WS_OF + ((8 * P + 2 * rw) * 16 + 2 * c + cw) * 1024);
        st16_sc1(of[1], vo, sws, WS_OF + ((8 * P + 2 * rw) * 16 + 2 * c + cw) * 1024 + 256);
      }
      if (j == 2) { PIN(bs); _Pragma("unroll") for (int v = 0; v < 3; ++v) PIN(z1r[v]); }
      if (j == 3) { _Pragma("unroll") for (int v = 3; v < 6; ++v) PIN(z1r[v]); }
      if (j >= 1 && cw == 0) {
        if (j < (t ? 3 : 4)) sm += r_[0] + r_[1];
        else {
          f32x4 q_[8];
#pragma unroll
          for (int o = 0; o < 8; ++o) q_[o] = lds16f(ra_ + o * 4096);
          SCHED();
          sm = ((q_[0] + q_[1]) + (q_[2] + q_[3])) + ((q_[4] + q_[5]) + (q_[6] + q_[7]));
        }
        if (j == 1) sm0 = sm;
        if (j == 2) EMIT_H(0, sm0)
        if (j >= 2) EMIT_H(j >= 2 ? j - 1 : 0, sm)
      }
      if (j == 3) {
#pragma unroll
        for (int e = 0; e < 4; ++e) DMA_WO(L_WO1, 0, e);
      }
      if (j == 3 && cw == 0) { if (t) WAITV(5); else { WAITV(12); WAITV_IF(gn, 5, 5); } }
      if (j == 5) WAITV(2);
      BAR_IF(cw, 0, 63);
    }
  }
  {
    STAMP(10);
    const u32 bufA = L_RED + (gn & 1) * 32768, bufB = L_RED + 32768 - (gn & 1) * 32768;
    const u32 fv = FV();
    if (cw == 0) {
      const u32 ra_ = redr + (bufB - L_RED);
      f32x4 q_[8];
#pragma unroll
      for (int o = 0; o < 8; ++o) q_[o] = lds16f(ra_ + o * 4096);
      SCHED();
      const f32x4 sm = ((q_[0] + q_[1]) + (q_[2] + q_[3])) + ((q_[4] + q_[5]) + (q_[6] + q_[7]));
      if (gn == 6) EMIT_H(5, sm) else EMIT_H(4, sm)
    }
    WAITV(2); WAITV_IF(cw, 1, 0);
    PIN(gf);
    {
      const bool n0 = (lane & 48) != 16;
      if (!__all(!n0 || (gf[0] == tlo && gf[1] == thi))) poll_flags(fv, n0, sws, WS_FC, tlo, thi);
    }
    RELANE();
    u32x4 hf[8], of[8];
    const u32 ofb = WS_OF + ((4 * p + rw) * 16 + cw) * 1024, hfb = WS_HF + ((4 * p + rw) * 16 + cw) * 1024;
#pragma unroll
    for (int e = 0; e < 4; ++e) of[e] = ld16_b(l16, sws, ofb + e * 2048);
#pragma unroll
    for (int e = 0; e < 4; ++e) hf[e] = ld16_b(l16, sws, hfb + e * 2048);
    BARRIER();
    const u32 qb = ((lane >> 2) & 3) * 64 + ((((g & 1) << 1) | ((lane >> 1) & 1)) * 16) + (lane & 1) * 8;
    f32x4 a3[4];
#pragma unroll
    for (int ct = 0; ct < 4; ++ct) a3[ct] = zero4;
    u32x4 wf[3][4], hl[4][2];
#define RDW2(dst, lo0, hi0, offl, offh) _Pragma("unroll") for (int ct = 0; ct < 4; ++ct) dst[ct] = cat(ldstr(((lo0) ^ ((ct & 1) * 32)) + (offl) + (ct >> 1) * 512), ldstr(((hi0) ^ ((ct & 1) * 32)) + (offh) + (ct >> 1) * 512))
#define MM3(src, frag) _Pragma("unroll") for (int ct = 0; ct < 4; ++ct) a3[ct] = mfma32(src[ct], frag, a3[ct])
    const u32 lb = WS_HL + ((4 * p + rw) * 8 + cw) * 2048;
    const rsrc_t sxo = __builtin_amdgcn_make_buffer_rsrc((void*)(x + (size_t)p * 65536), 0, 65536, 0x00020000);
    const u32 w0 = opq(L_WOUT + qb + (g >> 1) * 1024 + (g & 1) * 256);
    const u32 wa = w0 + cw * 4096, wb = w0 + cw * 2048;
    const u32 oq = qb + g * 1024 + cw * 4096;
    const u32 oa = opq(L_WO1 + oq), ob = opq(bufA + oq);
    u32x4 xres;
    VWO();
#pragma unroll
    for (int e = 0; e < 4; ++e) DMA_WO(bufA, 1, e);
    STAMP(11);
#define RDK(dst, k)                                                                                                 \
  if ((k) < 4) { RDW2(dst, oa, oa, (k) * 8192, (k) * 8192 + 256); }                                                 \
  else if ((k) < 8) { RDW2(dst, wa, wa, ((k) - 4) * 8192, ((k) - 4) * 8192 + 2048); }                               \
  else if ((k) < 12) { RDW2(dst, ob, ob, ((k) - 8) * 8192, ((k) - 8) * 8192 + 256); }                               \
  else { RDW2(dst, wb, w0, 32768 + ((k) - 12) * 4096, 49152 + ((k) - 12) * 2048); }
    RDK(wf[0], 0)
    RDK(wf[1], 1)
#pragma unroll
    for (int k = 0; k < 16; ++k) {
      if (k == 0) WAITV(8);
      if (k == 4) WAITV(13);
      if (k == 6) {
        WAITV(0);
        PIN(gf);
        if (!__all(gf[0] == tlo && gf[1] == thi)) poll_flags(fv, true, sws, WS_FC, tlo, thi);
#pragma unroll
        for (int e = 4; e < 8; ++e) hf[e] = ld16_b(l16, sws, hfb + e * 2048);
        BARRIER();
        STAMP(12);
      }
      if (k == 12) WAITV(9);
      if (k + 2 < 16) { RDK(wf[(k + 2) % 3], k + 2) }
      SCHED();
      if (k < 4) { PIN(of[k]); MM3(wf[k % 3], of[k]); }
      else if (k < 8) { PIN(hf[k - 4]); MM3(wf[k % 3], hf[k - 4]); }
      else if (k < 12) { PIN(of[k - 4]); MM3(wf[k % 3], of[k - 4]); }
      else { PIN(hf[k - 8]); MM3(wf[k % 3], hf[k - 8]); }
      SCHED();
      if (k < 4) {
        of[4 + k] = ld16_b(l16, sws, ofb + (4 + k) * 2048);
        DMA_WOUT_AT(bufB + w * 4096, 7 + k)
        if (k == 3) gf = ld8_sc1(fv, sws, WS_FC);
      } else if (k >= 6 && k < 10) {
        hl[k - 6][0] = ld16_sc1(l16, sws, lb + (k - 6) * 4096);
        hl[k - 6][1] = ld16_sc1(l16, sws, lb + (k - 6) * 4096 + 1024);
      } else if (k == 10) {
        xres = ld16((tid >> 3) * 1024 + (tid & 7) * 16, sxo, c2 * 128);
      }
    }
    STAMP(13);
    {
      const u32 wl_ = opq(bufB + (w0 - L_WOUT) + cw * 4096);
      u32x4 wg[4][4];
#pragma unroll
      for (int e = 0; e < 4; ++e) RDW2(wg[e], wl_, wl_, e * 8192, e * 8192 + 2048);
      WAITV(0);
      PIN(xres);
#pragma unroll
      for (int e = 0; e < 4; ++e) { PIN(hl[e][0]); PIN(hl[e][1]); }
#pragma nounroll
      for (int it = 0; it < SPIN_CAP; ++it) {
        u32 bad = 0;
#pragma unroll
        for (int e = 0; e < 8; ++e) bad |= (hl[e >> 1][e & 1][1] ^ tlo) | (hl[e >> 1][e & 1][3] ^ tlo);
        if (__all(bad == 0)) break;
        __builtin_amdgcn_s_sleep(2);
        asm volatile(
            "buffer_load_dwordx4 %0, %8, %9, %10 offen sc1\n\tbuffer_load_dwordx4 %1, %8, %9, %10 offen offset:1024 sc1\n\t"
            "buffer_load_dwordx4 %2, %8, %9, %11 offen sc1\n\tbuffer_load_dwordx4 %3, %8, %9, %11 offen offset:1024 sc1\n\t"
            "buffer_load_dwordx4 %4, %8, %9, %12 offen sc1\n\tbuffer_load_dwordx4 %5, %8, %9, %12 offen offset:1024 sc1\n\t"
            "buffer_load_dwordx4 %6, %8, %9, %13 offen sc1\n\tbuffer_load_dwordx4 %7, %8, %9, %13 offen offset:1024 sc1\n\t"
            "s_waitcnt vmcnt(0)"
            : "=&v"(hl[0][0]), "=&v"(hl[0][1]), "=&v"(hl[1][0]), "=&v"(hl[1][1]), "=&v"(hl[2][0]), "=&v"(hl[2][1]), "=&v"(hl[3][0]), "=&v"(hl[3][1])
            : "v"(l16), "s"(sws), "s"(lb), "s"(lb + 4096), "s"(lb + 8192), "s"(lb + 12288)
            : "memory");
      }
#pragma unroll
      for (int e = 0; e < 4; ++e) MM3(wg[e], ((u32x4){hl[e][0][0], hl[e][0][2], hl[e][1][0], hl[e][1][2]}));
    }
    STAMP(14);
    const u32 fin = L_WO1 - 2048;
#pragma unroll
    for (int ct = 0; ct < 4; ++ct) sts16f(fin + 2048 + cw * 16384 + (16 * rw + i16) * 256 + (((4 * ct + g) ^ i16) * 16), a3[ct]);
    BARRIER();
    {
      const int row = tid >> 3;
      const u32 a = fin + 2048 + row * 256 + (((2 * (tid & 7)) ^ (row & 15)) * 16);
      const f32x4 s0 = lds16f(a) + lds16f(a + 16384), s1 = lds16f(a ^ 16) + lds16f((a ^ 16) + 16384);
      u32x4 o = {pk2(blo(xres[0]) + s0[0], bhi(xres[0]) + s0[1]), pk2(blo(xres[1]) + s0[2], bhi(xres[1]) + s0[3]),
                 pk2(blo(xres[2]) + s1[0], bhi(xres[2]) + s1[1]), pk2(blo(xres[3]) + s1[2], bhi(xres[3]) + s1[3])};
      st16_sc1(o, row * 1024 + (tid & 7) * 16, __builtin_amdgcn_make_buffer_rsrc((void*)(out + (size_t)p * 65536), 0, 65536, 0x00020000), c2 * 128);
    }
  }
  }
  if (b == 0 && w == 0 && l16 == 0) *(u64*)(ws + WS_EP) = ((u64)thi << 32) | tlo;
  STAMP(15);
  STAMPC(31);
}

typedef u64 u64x8 __attribute__((ext_vector_type(8)));
typedef u64 u64x2 __attribute__((ext_vector_type(2)));
__global__ __launch_bounds__(512, 2) void fused_kernel(const char* a0, const char* a1, const char* a2, const char* a3, const char* a4, const char* a5,
                                                        const char* a6, const char* a7, char* a8, char* a9) {
  __shared__ __attribute__((aligned(1024))) char smem[163840];
  u64 tstart;
  asm volatile("s_memrealtime %0\n\ts_waitcnt lgkmcnt(0)" : "=s"(tstart)::"memory");
  u64x8 ka; u64x2 kb;
  asm volatile("s_load_dwordx16 %0, %2, 0x0\n\ts_load_dwordx4 %1, %2, 0x40\n\ts_waitcnt lgkmcnt(0)" : "=&s"(ka), "=&s"(kb) : "s"(__builtin_amdgcn_kernarg_segment_ptr()) : "memory");
  const char *x = (const char*)ka[0], *wqkv = (const char*)ka[1], *wphi = (const char*)ka[2], *wo = (const char*)ka[3], *win = (const char*)ka[4],
             *wsp = (const char*)ka[5], *bsp = (const char*)ka[6], *wout = (const char*)ka[7];
  char *out = (char*)kb[0], *ws = (char*)kb[1];
  asm volatile("" ::"v"((u32)(size_t)(LDSP char*)smem));
  u64 pc, ep;
  asm volatile("s_getpc_b64 %0" : "=s"(pc));
  {
    const int lane = threadIdx.x & 63, g = lane >> 4;
    const int w = __builtin_amdgcn_readfirstlane(threadIdx.x >> 6);
    const int r = blockIdx.x >> 3, P = ((blockIdx.x & 7) >> 1) * 4 + (r >> 3);
    const int rot = 2 * (((r >> 3) + (r & 3)) & 3);
    u32 acv = 0, acb = 0; (void)acv; (void)acb;
    STAMP(0);
    STAMPC(30);
    if (CODE_PF && w == 7)
      touch(L_JUNK, lane * 128, __builtin_amdgcn_make_buffer_rsrc((void*)(pc & ~255ull), 0, CODE_PF, 0x00020000), (r & 7) * 8192);
    const rsrc_t sx = __builtin_amdgcn_make_buffer_rsrc((void*)(x + (size_t)P * 131072), 0, 131072, 0x00020000);
    const u32 vx0 = (16 * w + (lane >> 3)) * 1024 + (((lane & 7) ^ g) * 16);
    const u32 vx1 = (vx0 + 8192) ^ 64;
    const u32 w2k = w * 2048;
    DMA_X(0, 0, 0);
    DMA_X(0, 0, 1);
    asm volatile("s_load_dwordx2 %0, %1, 0x0" : "=s"(ep) : "s"(ws) : "memory");
    SCHED();
  }
  if (__builtin_expect(((blockIdx.x >> 5) & 1) == 0, 1)) run<0>(x, wqkv, wphi, wo, win, wsp, bsp, wout, out, ws, ep);
  else run<1>(x, wqkv, wphi, wo, win, wsp, bsp, wout, out, ws, ep);
  for (int it = 0; it < 100000; ++it) { if (__builtin_amdgcn_s_memrealtime() - tstart >= 5000) break; __builtin_amdgcn_s_sleep(1); }
}

extern "C" void kernel_launch(void* const* d_in, const int* in_sizes, int n_in, void* d_out, int out_size, void* d_ws, size_t ws_size,
                              hipStream_t stream) {
  fused_kernel<<<256, 512, 0, stream>>>((const char*)d_in[0], (const char*)d_in[1], (const char*)d_in[2], (const char*)d_in[3],
                                        (const char*)d_in[5], (const char*)d_in[6], (const char*)d_in[7], (const char*)d_in[8], (char*)d_out,
                                        (char*)d_ws);
}
```

```cpp
#include <hip/hip_runtime.h>
#include <hip/hip_bf16.h>

typedef unsigned int u32;
typedef unsigned long long u64;
typedef short s16x4 __attribute__((ext_vector_type(4)));
typedef __bf16 bf16x2 __attribute__((ext_vector_type(2)));
typedef __bf16 bf16x8 __attribute__((ext_vector_type(8)));
typedef float f32x16 __attribute__((ext_vector_type(16)));
typedef float f32x4 __attribute__((ext_vector_type(4)));
typedef float f32x2 __attribute__((ext_vector_type(2)));
typedef u32 u32x4 __attribute__((ext_vector_type(4)));
typedef u32 u32x2 __attribute__((ext_vector_type(2)));
typedef __amdgpu_buffer_rsrc_t rsrc_t;
#define LDSP __attribute__((address_space(3)))
#define DI __device__ __forceinline__

#define STAMPS 0

constexpr u32 WS_EP = 0;
constexpr u32 WS_FA = 8192;
constexpr u32 WS_FB = WS_FA + 16384;
constexpr u32 WS_FC = WS_FB + 16384;
constexpr u32 WS_FD = WS_FC + 16384;
constexpr u32 WS_XG = WS_FD + 65536;
constexpr u32 WS_SG = WS_XG + 32768;
constexpr u32 WS_ZB = WS_SG + 524288;
constexpr u32 WS_Z1 = WS_ZB + 43 * 65536;
constexpr u32 KP_SZ = 17408;
constexpr u32 WS_KP = WS_Z1 + 64 * 43 * 1024;
constexpr u32 WS_KF = WS_KP + 128 * KP_SZ;
constexpr u32 WS_KZ = WS_KF + 65536;
constexpr u32 WS_OF = WS_KZ + 4096;
constexpr u32 WS_HF = WS_OF + 128 * 16384;
constexpr u32 WS_HL = WS_HF + 128 * 16 * 1024;
constexpr u32 WS_ST = WS_HL + 128 * 16 * 1024;
constexpr u32 WS_END = WS_ST + 256 * 8 * 32 * 8;

constexpr u32 LZ_W = 65536;
constexpr u32 LZ_U = 139264;
constexpr u32 LQ_M = 122880;
constexpr u32 LQ_A = 81920;
constexpr u32 LQ_PHI = 49152;
constexpr u32 LQ_V = 32768;
constexpr u32 L_TAB = 155648;
constexpr u32 L_XS = L_TAB;
constexpr u32 L_CS = L_TAB + 1024;
constexpr u32 L_ZST = L_TAB + 3072;
constexpr u32 L_WPHI = L_TAB + 3072;
constexpr u32 L_KFS = L_TAB + 2048;
constexpr u32 L_JUNK = L_TAB + 7168;
constexpr u32 L_WOUT = 0;
constexpr u32 L_RED = 57344;
constexpr u32 L_WO1 = 122880;

constexpr u64 Z2S = 0x26211B16110B0500ull;
constexpr u64 Z1S = 0x26211C16110C0600ull;
DI int grp_start(u64 tab, int c) { return c >= 8 ? 43 : (int)((tab >> (8 * c)) & 0xFF); }

#define SCHED() __builtin_amdgcn_sched_barrier(0)
#define TIC()
#define TOC(acc)
#define WAITV(n) do { TIC(); asm volatile("s_waitcnt vmcnt(%0)" ::"n"(n) : "memory"); TOC(acv); SCHED(); } while (0)
#define BARRIER() do { SCHED(); TIC(); asm volatile("s_waitcnt lgkmcnt(0)\n\ts_barrier" ::: "memory"); TOC(acb); SCHED(); } while (0)
#define WAITV_IF(g_, v, n) do { TIC(); asm volatile("s_cmp_lg_u32 %0, %1\n\ts_cbranch_scc1 1f\n\ts_waitcnt vmcnt(%2)\n1:" ::"s"(g_), "n"(v), "n"(n) : "scc", "memory"); TOC(acv); SCHED(); } while (0)
#define BAR_IF(g_, v, n) do { SCHED(); asm volatile("s_cmp_lg_u32 %0, %1\n\ts_cbranch_scc1 1f\n\ts_waitcnt vmcnt(%2) lgkmcnt(0)\n\ts_barrier\n1:" ::"s"(g_), "n"(v), "n"(n) : "scc", "memory"); SCHED(); } while (0)
#define WAITL0() asm volatile("s_waitcnt lgkmcnt(0)" ::: "memory")
#define PIN(x) asm volatile("" : "+v"(x))

DI void dma16(u32 lds, u32 voff, rsrc_t srd, u32 soff) {
  asm volatile("s_mov_b32 m0, %0\n\ts_nop 0\n\tbuffer_load_dwordx4 %1, %2, %3 offen lds" ::"s"(lds), "v"(voff), "s"(srd), "s"(soff) : "memory");
}
#define DMA_C(wb, CST, voff, srd, soff) asm volatile("s_add_u32 m0, %0, %1\n\ts_nop 0\n\tbuffer_load_dwordx4 %2, %3, %4 offen lds" ::"s"(wb), "n"(CST), "v"(voff), "s"(srd), "s"(soff) : "scc", "memory")
#define DMA_CC(wb, CST, voff, srd, OP, sb, SC) do { u32 so__; asm volatile("s_add_u32 m0, %1, %2\n\t" OP " %0, %5, %6\n\tbuffer_load_dwordx4 %3, %4, %0 offen lds" : "=&s"(so__) : "s"(wb), "n"(CST), "v"(voff), "s"(srd), "s"(sb), "n"(SC) : "scc", "memory"); } while (0)
DI void touch(u32 lds, u32 voff, rsrc_t srd, u32 soff) {
  asm volatile("s_mov_b32 m0, %0\n\ts_nop 0\n\tbuffer_load_dword %1, %2, %3 offen lds" ::"s"(lds), "v"(voff), "s"(srd), "s"(soff) : "memory");
}
DI u32x4 ld16_sc1(u32 voff, rsrc_t srd, u32 soff) {
  u32x4 r;
  asm volatile("buffer_load_dwordx4 %0, %1, %2, %3 offen sc1" : "=v"(r) : "v"(voff), "s"(srd), "s"(soff) : "memory");
  return r;
}
template <int OFF> DI u32x4 ld16_sc1o(u32 voff, rsrc_t srd, u32 soff) {
  u32x4 r;
  asm volatile("buffer_load_dwordx4 %0, %1, %2, %3 offen offset:%4 sc1" : "=v"(r) : "v"(voff), "s"(srd), "s"(soff), "n"(OFF) : "memory");
  return r;
}
#define BPOL " sc1"
DI u32x4 ld16_b(u32 voff, rsrc_t srd, u32 soff) {
  u32x4 r;
  asm volatile("buffer_load_dwordx4 %0, %1, %2, %3 offen" BPOL : "=v"(r) : "v"(voff), "s"(srd), "s"(soff) : "memory");
  return r;
}
template <int OFF> DI u32x4 ld16_bo(u32 voff, rsrc_t srd, u32 soff) {
  u32x4 r;
  asm volatile("buffer_load_dwordx4 %0, %1, %2, %3 offen offset:%4" BPOL : "=v"(r) : "v"(voff), "s"(srd), "s"(soff), "n"(OFF) : "memory");
  return r;
}
DI void ld16_b_if6(u32x4& r, int gn, u32 voff, rsrc_t srd, u32 soff) {
  asm volatile("s_cmp_lg_u32 %4, 6\n\ts_cbranch_scc1 1f\n\tbuffer_load_dwordx4 %0, %1, %2, %3 offen" BPOL "\n1:" : "+v"(r) : "v"(voff), "s"(srd), "s"(soff), "s"(gn) : "scc", "memory");
}
DI u32x4 ld16(u32 voff, rsrc_t srd, u32 soff) {
  u32x4 r;
  asm volatile("buffer_load_dwordx4 %0, %1, %2, %3 offen" : "=v"(r) : "v"(voff), "s"(srd), "s"(soff) : "memory");
  return r;
}
DI u32x2 ld8_sc1(u32 voff, rsrc_t srd, u32 soff) {
  u32x2 r;
  asm volatile("buffer_load_dwordx2 %0, %1, %2, %3 offen sc1" : "=v"(r) : "v"(voff), "s"(srd), "s"(soff) : "memory");
  return r;
}
DI u32 ld2(u32 voff, rsrc_t srd, u32 soff) {
  u32 r;
  asm volatile("buffer_load_ushort %0, %1, %2, %3 offen" : "=v"(r) : "v"(voff), "s"(srd), "s"(soff) : "memory");
  return r;
}
DI void st16_sc1(u32x4 v, u32 voff, rsrc_t srd, u32 soff) {
  asm volatile("buffer_store_dwordx4 %0, %1, %2, %3 offen sc1\n\ts_nop 1" ::"v"(v), "v"(voff), "s"(srd), "s"(soff) : "memory");
}
DI void st8_sc1(u32x2 v, u32 voff, rsrc_t srd, u32 soff) {
  asm volatile("buffer_store_dwordx2 %0, %1, %2, %3 offen sc1" ::"v"(v), "v"(voff), "s"(srd), "s"(soff) : "memory");
}

DI u32x4 lds16(u32 a) { return *(const LDSP u32x4*)(size_t)a; }
DI u32x2 lds8(u32 a) { return *(const volatile LDSP u32x2*)(size_t)a; }
DI float lds4f(u32 a) { return *(const LDSP float*)(size_t)a; }
DI f32x4 lds16f(u32 a) { return *(const LDSP f32x4*)(size_t)a; }
DI void sts16f(u32 a, f32x4 v) { *(LDSP f32x4*)(size_t)a = v; }
DI void sts16(u32 a, u32x4 v) { *(LDSP u32x4*)(size_t)a = v; }
DI void sts8(u32 a, u32x2 v) { *(volatile LDSP u32x2*)(size_t)a = v; }
DI void sts4f(u32 a, float v) { *(LDSP float*)(size_t)a = v; }
DI u32x2 ldstr(u32 a) {
  return __builtin_bit_cast(u32x2, __builtin_amdgcn_ds_read_tr16_b64_v4i16((LDSP s16x4*)(size_t)a)); }
DI u32x4 cat(u32x2 lo, u32x2 hi) { return (u32x4){lo[0], lo[1], hi[0], hi[1]}; }
DI u32x4 ldfrag(u32 a, u32 d) { u32x2 lo = ldstr(a), hi = ldstr(a + d); return cat(lo, hi); }

DI f32x4 mfma32(u32x4 a, u32x4 b, f32x4 c) {
  return __builtin_amdgcn_mfma_f32_16x16x32_bf16(__builtin_bit_cast(bf16x8, a), __builtin_bit_cast(bf16x8, b), c, 0, 0, 0);
}
DI f32x16 mfmaL(u32x4 a, u32x4 b, f32x16 c) {
  return __builtin_amdgcn_mfma_f32_32x32x16_bf16(__builtin_bit_cast(bf16x8, a), __builtin_bit_cast(bf16x8, b), c, 0, 0, 0);
}
DI u32 pk2(float a, float b) { bf16x2 v = {(__bf16)a, (__bf16)b}; return __builtin_bit_cast(u32, v); }
DI float blo(u32 u) { return __uint_as_float(u << 16); }
DI float bhi(u32 u) { return __uint_as_float(u & 0xffff0000u); }
DI float rbf(float a) { return blo(pk2(a, a)); }
DI float dot2(u32 a, u32 b, float c) {
  return __builtin_amdgcn_fdot2_f32_bf16(__builtin_bit_cast(bf16x2, a), __builtin_bit_cast(bf16x2, b), c, false);
}
DI f32x2 fma2(f32x2 a, f32x2 b, f32x2 c) { return __builtin_elementwise_fma(a, b, c); }
DI f32x2 bc2(float a) { return (f32x2){a, a}; }
DI f32x2 lo2(f32x4 v) { return (f32x2){v[0], v[1]}; }
DI f32x2 hi2(f32x4 v) { return (f32x2){v[2], v[3]}; }
DI u32 pk2v(f32x2 v) { return pk2(v[0], v[1]); }
DI f32x2 gelu2(f32x2 x) {
  f32x2 u = fma2(x * x, bc2(-0.10294324f), bc2(-2.3022082f)) * x;
  f32x2 e = {__builtin_amdgcn_exp2f(u[0]), __builtin_amdgcn_exp2f(u[1])};
  e = e + bc2(1.0f);
  f32x2 r = {__builtin_amdgcn_rcpf(e[0]), __builtin_amdgcn_rcpf(e[1])};
  return x * r;
}
DI void red2g(float& a, float& b) {
  auto s = __builtin_amdgcn_permlane32_swap(__float_as_uint(a), __float_as_uint(b), false, false);
  float c = __uint_as_float(s[0]) + __uint_as_float(s[1]);
  auto s2 = __builtin_amdgcn_permlane16_swap(__float_as_uint(c), __float_as_uint(c), false, false);
  float d = __uint_as_float(s2[0]) + __uint_as_float(s2[1]);
  auto s3 = __builtin_amdgcn_permlane32_swap(__float_as_uint(d), __float_as_uint(d), false, false);
  a = __uint_as_float(s3[0]);
  b = __uint_as_float(s3[1]);
}
template <int CTRL> DI float dpp(float v) {
  return __uint_as_float(__builtin_amdgcn_update_dpp(0, __float_as_uint(v), CTRL, 0xF, 0xF, true));
}
template <class F> DI void hedgehog16(const f32x16& p, float s, u32x4 (&o)[4], F hook) {
  float mx = fmaxf(p[0], p[1]), mn = fminf(p[0], p[1]);
#pragma unroll
  for (int i = 2; i < 16; i += 2) { mx = fmaxf(fmaxf(mx, p[i]), p[i + 1]); mn = fminf(fminf(mn, p[i]), p[i + 1]); }
  const float c = s * 1.44269504f;
  const float a = -mx * c, b = mn * c;
  f32x2 ep[8], en[8];
#pragma unroll
  for (int i = 0; i < 8; ++i) {
    hook(i);
    const f32x2 v = {p[2 * i], p[2 * i + 1]};
    const f32x2 u = fma2(v, bc2(c), bc2(a)), w = fma2(v, bc2(-c), bc2(b));
    ep[i] = (f32x2){__builtin_amdgcn_exp2f(u[0]), __builtin_amdgcn_exp2f(u[1])};
    en[i] = (f32x2){__builtin_amdgcn_exp2f(w[0]), __builtin_amdgcn_exp2f(w[1])};
  }
  hook(8);
  const f32x2 sp = ((ep[0] + ep[1]) + (ep[2] + ep[3])) + ((ep[4] + ep[5]) + (ep[6] + ep[7]));
  const f32x2 sn = ((en[0] + en[1]) + (en[2] + en[3])) + ((en[4] + en[5]) + (en[6] + en[7]));
  const float ip = __builtin_amdgcn_rcpf(sp[0] + sp[1]), in = __builtin_amdgcn_rcpf(sn[0] + sn[1]);
#pragma unroll
  for (int i = 0; i < 8; ++i) {
    if (i == 0 || i == 3 || i == 6) hook(9 + i / 3);
    o[i >> 2][i & 3] = pk2v(ep[i] * bc2(ip));
    o[2 + (i >> 2)][i & 3] = pk2v(en[i] * bc2(in));
  }
}
DI void ln_fin(float s, float q, float invn, float& r, float& m) {
  m = s * invn;
  float v = fmaxf(q * invn - m * m, 0.0f);
  float sd = rbf(__builtin_amdgcn_sqrtf(rbf(rbf(v) + 1e-5f)));
  r = __builtin_amdgcn_rcpf(sd);
  m = rbf(m);
}

#define POLL_SLEEP 6
constexpr int SPIN_CAP = 1 << 17;
DI void poll_flags(u32 voff, bool act, rsrc_t srd, u32 soff, u32 tlo, u32 thi) {
#pragma nounroll
  for (int it = 0; it < SPIN_CAP; ++it) {
    u32x2 v;
    asm volatile("buffer_load_dwordx2 %0, %1, %2, %3 offen sc1\n\ts_waitcnt vmcnt(0)" : "=&v"(v) : "v"(voff), "s"(srd), "s"(soff) : "memory");
    bool ok = !act || (v[0] == tlo && v[1] == thi);
    if (__all(ok)) break;
    __builtin_amdgcn_s_sleep(POLL_SLEEP);
  }
}
DI bool tag_ok(u32x4 q, u32 t) { return q[1] == t && q[3] == t; }
DI void poll_gran(u32x4& q, u32 voff, rsrc_t srd, u32 soff, u32 tlo) {
#pragma nounroll
  for (int it = 0; it < SPIN_CAP; ++it) {
    if (__all(tag_ok(q, tlo))) break;
    __builtin_amdgcn_s_sleep(POLL_SLEEP);
    asm volatile("buffer_load_dwordx4 %0, %1, %2, %3 offen sc1\n\ts_waitcnt vmcnt(0)" : "=&v"(q) : "v"(voff), "s"(srd), "s"(soff) : "memory");
  }
}
DI u32 opq(u32 v) { asm volatile("" : "+v"(v)); return v; }

#define STAMP(i)
#define STAMPC(i)

#define GQ_N 3
#define PREFETCH 0
#define CODE_PF 56000
#define OUT_SC1 1
#define PRIO 0
#define UROT 1
static_assert(WS_ZB % 8192 == 0, "");
#define TLB_PF 0
#define WSP_PF 0
#define VM(x) do { x; ++vq; } while (0)
#define WAITQ(mark) WAITV(vq - (mark))

template <int t>
DI void run(const char* __restrict__ x, const char* __restrict__ wqkv, const char* __restrict__ wphi, const char* __restrict__ wo,
            const char* __restrict__ win, const char* __restrict__ wsp, const char* __restrict__ bsp, const char* __restrict__ wout,
            char* __restrict__ out, char* ws, u64 ep) {
  const int tid = threadIdx.x, lane = tid & 63;
  const int w = __builtin_amdgcn_readfirstlane(tid >> 6);
  const int g = lane >> 4;
  const int b = blockIdx.x, xcd = b & 7, r = b >> 3;
  const int Pl = r >> 3, cl = r & 3;
  const int P = (xcd >> 1) * 4 + Pl;
  const int rot = 2 * ((Pl + cl) & 3);
  u32 acv = 0, acb = 0; (void)acv; (void)acb;
  const rsrc_t sx = __builtin_amdgcn_make_buffer_rsrc((void*)(x + (size_t)P * 131072), 0, 131072, 0x00020000);
  const u32 vx0 = (16 * w + (lane >> 3)) * 1024 + (((lane & 7) ^ g) * 16);
  const u32 vx1 = (vx0 + 8192) ^ 64;
  const u32 w2k = w * 2048;
#define DMA_X(dst, n, e) DMA_C(w2k, (dst) + (e) * 1024, (e) ? vx1 : vx0, sx, (((n) + rot) & 7) * 128)
  const int rw = w & 3, cw = w >> 2, i16 = lane & 15;
  const int c = (xcd & 1) * 4 + cl;
  const int p = 2 * P + (cl & 1);
  const int c2 = (xcd & 1) * 4 + (t ? 3 * (cl >> 1) : 1 + (cl >> 1));
  const u32 l16 = opq(lane * 16);
  const rsrc_t sa = __builtin_amdgcn_make_buffer_rsrc((void*)(wsp + (size_t)p * 262144), 0, 262144, 0x00020000);
  const rsrc_t sws = __builtin_amdgcn_make_buffer_rsrc((void*)ws, 0, WS_END, 0x00020000);
  const u32 vA0 = (lane >> 3) * 4096 + (((lane & 7) ^ g) * 16);
  const u32 vA1 = (vA0 + 32768) ^ 64;
  const int ux0 = UROT ? (cl >> 1) : 0, ux1 = UROT ? t : 0;
  const u32 uxs = w * 512 + ux1 * 256 + ux0 * 131072;
#define DMA_U(u, base, e) DMA_CC(w2k, (base) + (e) * 1024, (e) ? vA1 : vA0, sa, "s_xor_b32", uxs, (((u) >> 1) & 3) * 128 + (((u) >> 3) * 32 + ((u) & 1) * 16) * 4096)
  const f32x4 zero4 = {0, 0, 0, 0};
  u32x4 ones4 = {0x3F803F80u, 0x3F803F80u, 0x3F803F80u, 0x3F803F80u};
  u32 tlo, thi;
  u32x4 af[4][8];
  u32x4 bfr[2][8];
  u32x2 gf;
  const u32 ard = w * 2048 + i16 * 128 + (((g >> 1) ^ (i16 >> 1)) * 16) + (g & 1) * 8;
#define READ_U(u, base)                                                                                             \
  {                                                                                                                 \
    const u32 ab_ = opq(ard + (base));                                                                              \
    _Pragma("unroll") for (int kk = 0; kk < 2; ++kk)                                                               \
      af[2 * ((u) >> 3) + ((u) & 1)][((u) & 6) + kk] = cat(lds8(ab_ ^ (64 * kk)), lds8(ab_ ^ (64 * kk + 32)));      \
  }
#define USE_EPOCH() { asm volatile("" : "+s"(ep)); ep += 1; tlo = (u32)ep; thi = (u32)(ep >> 32); PIN(ones4); }
#define EARLY_STATS(mark)                                                                                           \
  {                                                                                                                 \
    WAITQ(mark); PIN(xrow);                                                                                         \
    float s_ = 0, q_ = 0;                                                                                           \
    _Pragma("unroll") for (int e = 0; e < 4; ++e) { s_ = dot2(xrow[e], ones4[0], s_); q_ = dot2(xrow[e], xrow[e], q_); } \
    s_ += dpp<0x128>(s_); q_ += dpp<0x128>(q_);                                                                     \
    s_ += dpp<0x124>(s_); q_ += dpp<0x124>(q_);                                                                     \
    s_ += dpp<0x4E>(s_); q_ += dpp<0x4E>(q_);                                                                       \
    s_ += dpp<0xB1>(s_); q_ += dpp<0xB1>(q_);                                                                       \
    red2g(s_, q_);                                                                                                  \
    float r_, m_;                                                                                                   \
    ln_fin(s_, q_, 1.0f / 512, r_, m_);                                                                             \
    VM(st16_sc1((u32x4){__float_as_uint(r_), tlo, __float_as_uint(m_), tlo}, l16 << 22, sws, WS_XG + (P * 128 + (t * 8 + c) * 8 + w) * 16)); \
  }
#define GATHER_XS()                                                                                                 \
  WAITQ(mxg); PIN(xg);                                                                                              \
  poll_gran(xg, i16 * 16, sws, WS_XG + (P * 128 + 16 * w) * 16, tlo);                                               \
  sts4f(L_XS + (16 * w + i16) * 4, __uint_as_float(xg[0]));                                                         \
  sts4f(L_XS + 512 + (16 * w + i16) * 4, __uint_as_float(xg[2]));
#define RELANE() const int lane = opq(l16) >> 4, i16 = lane & 15, g = lane >> 4, tid = w * 64 + lane; (void)tid; (void)g; (void)i16
#define SET_FLAG(off) st8_sc1((u32x2){tlo, thi}, l16 << 22, sws, (off))
  const int gs = grp_start(Z2S, c2), gn = t ? 5 : grp_start(Z2S, c2 + 1) - gs;
  const int trot = (p & 7) >= 5 ? (p & 7) - 5 : (p & 7);
#define TILE(j, n_) (((j) + trot) >= (n_) ? (j) + trot - (n_) : (j) + trot)
  const u32 zb0 = WS_ZB + (gs * 64 + 8 * w) * 1024 + ux1 * 4096;
#define DMA_WOUT(e)                                                                                                 \
  {                                                                                                                 \
    const rsrc_t so_ = __builtin_amdgcn_make_buffer_rsrc((void*)wout, 0, 688 * 1024, 0x00020000);                   \
    const int ws_ = grp_start(Z2S, w), wn_ = grp_start(Z2S, w + 1) - ws_;                                           \
    if ((e) < 6) dma16(L_WOUT + ((e) < 4 ? 2 * w + ((e) >> 1) : 16 + w) * 2048 + ((e) & 1) * 1024, vwo, so_, ((ws_ + TILE((e) >> 1, wn_)) * 2 + ((e) & 1)) * 8192); \
    else if ((e) == 6) dma16(L_WOUT + 24 * 2048 + w * 1024, vwo, so_, ((((w >> 1) == 0 ? 5 : ((w >> 1) == 1 ? 11 : ((w >> 1) == 2 ? 27 : 64))) + TILE(3, 6)) * 2 + (w & 1)) * 8192); \
  }
#define DMA_WOUT_AT(dst, e)                                                                                         \
  {                                                                                                                 \
    const rsrc_t so_ = __builtin_amdgcn_make_buffer_rsrc((void*)wout, 0, 688 * 1024, 0x00020000);                   \
    const int ws_ = grp_start(Z2S, w), wn_ = grp_start(Z2S, w + 1) - ws_;                                           \
    dma16((dst) + ((e) - 7) * 1024, vwo, so_, ((ws_ + TILE(wn_ - 1 - (((e) - 7) >> 1), wn_)) * 2 + (((e) - 7) & 1)) * 8192); \
  }
#define VWO() const u32 vwo = ((lane >> 2) & 7) * 1024 + c2 * 128 + (lane >> 5) * 64 + (((lane & 3) ^ (((lane >> 4) & 1) << 1)) * 16)
#define LOAD_B(u, so_)                                                                                              \
  bfr[u][0] = ld16_bo<0>(l16, sws, so_); bfr[u][1] = ld16_bo<1024>(l16, sws, so_);                                  \
  bfr[u][2] = ld16_bo<2048>(l16, sws, so_); bfr[u][3] = ld16_bo<3072>(l16, sws, so_);                               \
  bfr[u][4] = ld16_bo<0>(l16, sws, (so_) ^ 4096); bfr[u][5] = ld16_bo<1024>(l16, sws, (so_) ^ 4096);                \
  bfr[u][6] = ld16_bo<2048>(l16, sws, (so_) ^ 4096); bfr[u][7] = ld16_bo<3072>(l16, sws, (so_) ^ 4096);

  if constexpr (t == 0) {
    int vq = 2;
    const rsrc_t sw = __builtin_amdgcn_make_buffer_rsrc((void*)win, 0, 512 * 2752, 0x00020000);
    const int z2s = grp_start(Z2S, c), z2n = grp_start(Z2S, c + 1) - z2s;
    const int z1s = grp_start(Z1S, c), z1n = grp_start(Z1S, c + 1) - z1s;
    u32 vw[3];
    int wch[3];
#pragma unroll
    for (int e = 0; e < 3; ++e) {
      const int sg = 2 * e + (lane >> 5), hi = sg >= 3;
      wch[e] = (sg - 3 * hi) * 4 + ((lane & 3) ^ (hi << 1));
      vw[e] = wch[e] < 2 * z2n ? (16 * w + 8 * hi + ((lane >> 2) & 7)) * 2752 + 1376 + 32 * z2s + 16 * wch[e] : 0x7FFF0000u;
    }
    const u32 w3k = w * 3072;
#define DMA_W(bk, e) DMA_C(w3k, LZ_W + ((bk) % 3) * 24576 + (e) * 1024, vw[e], sw, ((2 * (bk) + rot) & 7) * (64 * 2752))
#define UBASE(u) ((u) % 5 == 0 ? LZ_U : ((u) % 5 - 1) * 16384)
#define ISS_X(n) do { VM(DMA_X(((n) & 3) * 16384, n, 0)); VM(DMA_X(((n) & 3) * 16384, n, 1)); mx[n] = vq; } while (0)
#define ISS_U(u) do { VM(DMA_U(u, UBASE(u), 0)); VM(DMA_U(u, UBASE(u), 1)); mu[u] = vq; } while (0)
#define ISS_W(bk, e0, e1) do { _Pragma("unroll") for (int e_ = e0; e_ < e1; ++e_) VM(DMA_W(bk, e_)); mw[bk] = vq; } while (0)
    int mx[8], mw[8], mu[16];
    mx[0] = vq;
    ISS_W(0, 0, 3);
    u32x4 xrow;
    VM(xrow = ld16(l16, sx, (c * 8 + w) * 1024));
    const int mxr = vq;
    ISS_X(1);
    const u32 xa0 = opq((32 * rw + i16) * 128 + ((g ^ (i16 >> 1)) * 16)), xa1 = xa0 ^ 64;
    u32 ta[3];
    int tl[3];
    {
      const u32 trb = LZ_W + g * 1536 + ((lane >> 2) & 3) * 64 + ((((g & 1) << 1) | ((lane >> 1) & 1)) * 16) + (lane & 1) * 8;
#pragma unroll
      for (int j = 0; j < 3; ++j) {
        tl[j] = 3 * cw + (j + rw) % 3;
        ta[j] = (trb + (tl[j] >> 1) * 512) ^ ((tl[j] & 1) * 32);
      }
    }
#define NEXT_BLOCK(bk) _Pragma("unroll") for (int j = 0; j < 3; ++j) ta[j] = opq(ta[j] + ((bk) == 0 ? 0 : ((bk) % 3 == 0 ? -49152 : 24576)))
    f32x4 acc[2][3], csa = zero4;
#pragma unroll
    for (int j = 0; j < 3; ++j) acc[0][j] = acc[1][j] = zero4;
    u32x4 xf[16][2], wf[2][3];
    u32x4 xg;
    int mxg = 0;
    WAITQ(mw[0]);
    BARRIER();
    USE_EPOCH()
    STAMP(7);
#define P1_MEM(n, h)                                                                                                \
  {                                                                                                                 \
    if ((n) == 0) { if ((h) == 0) ISS_X(2); if ((h) == 1) ISS_W(1, 0, 2); if ((h) == 2) ISS_W(1, 2, 3); if ((h) == 3) ISS_X(3); } \
    if ((n) == 1) { if ((h) == 0) ISS_W(2, 0, 2); if ((h) == 1) ISS_W(2, 2, 3); if ((h) == 2) VM(DMA_X(0, 4, 0)); if ((h) == 3) { VM(DMA_X(0, 4, 1)); mx[4] = vq; } } \
    if ((n) == 2 || (n) == 4) { if ((h) < 2) { VM(DMA_X((((n) + 3) & 3) * 16384, (n) + 3, (h) & 1)); mx[(n) + 3] = vq; } else ISS_W((n) / 2 + 2, (h) - 2, (h) - 1); } \
    if ((n) == 3) { if ((h) == 0) ISS_W(3, 2, 3); else if ((h) < 3) { VM(DMA_X(32768, 6, (h) - 1)); mx[6] = vq; } else ISS_U(0); } \
    if ((n) == 5) { if ((h) == 0) ISS_W(4, 2, 3); if ((h) == 1) { VM(xg = ld16_sc1(i16 * 16, sws, WS_XG + (P * 128 + 16 * w) * 16)); mxg = vq; } \
                    if ((h) >= 2) { VM(DMA_U(1, UBASE(1), (h) - 2)); mu[1] = vq; } }                                 \
    if ((n) == 6) { if ((h) < 3) ISS_W(5, h, (h) + 1); else ISS_U(2); }                                             \
    if ((n) == 7) { if ((h) < 2) { VM(DMA_U(3, UBASE(3), h)); mu[3] = vq; } }                                       \
  }
#pragma unroll
    for (int ks = 0; ks <= 16; ++ks) {
      const int n = ks >> 1, kk = ks & 1;
      if (ks > 0 && ks < 16 && kk == 0) {
        { const int mk = (n & 1) ? mx[n] : (mx[n] > mw[n >> 1] ? mx[n] : mw[n >> 1]); WAITQ(mk); }
        BARRIER();
      }
      if (ks < 16 && (ks & 3) == 0) { NEXT_BLOCK(ks >> 2); }
      if (ks == 8) {
#pragma unroll
        for (int e = 0; e < 3; ++e)
          vw[e] = wch[e] < 2 * z1n ? (16 * w + 8 * (2 * e + (lane >> 5) >= 3) + ((lane >> 2) & 7)) * 2752 + 32 * z1s + 16 * wch[e] : 0x7FFF0000u;
      }
      const int k1 = ks > 0 ? ks - 1 : 0, q1 = k1 & 1;
#pragma unroll
      for (int m = 0; m < 7; ++m) {
        if (ks > 0) {
          if (m < 6) acc[m & 1][m >> 1] = mfma32(xf[k1][m & 1], wf[q1][m >> 1], acc[m & 1][m >> 1]);
          else csa = mfma32(ones4, wf[q1][0], csa);
          SCHED();
        }
        if (ks < 16) {
          if (m == 0) { P1_MEM(n, 2 * kk) xf[ks & 15][0] = lds16((kk ? xa1 : xa0) + (n & 3) * 16384); xf[ks & 15][1] = lds16((kk ? xa1 : xa0) + (n & 3) * 16384 + 2048); }
          else if (m < 4) wf[kk][m - 1] = ldfrag(ta[m - 1] + (ks & 3) * 6144, 256);
          if (m == 4) P1_MEM(n, 2 * kk + 1)
        }
        SCHED();
      }
#pragma unroll
      for (int j = 0; j < 3; ++j) { PIN(acc[0][j]); PIN(acc[1][j]); }
      PIN(csa);
      if (ks == 2) EARLY_STATS(mxr)
    }
    GATHER_XS()
    sts4f(L_CS + tl[0] * 64 + i16 * 4, csa[0]);
    WAITQ(mu[1] > mw[4] ? mu[1] : mw[4]);
    READ_U(0, UBASE(0)) READ_U(1, UBASE(1))
    BARRIER();
    STAMP(1);
    u32x4 zp[3];
    ISS_W(6, 0, 3);
    {
      f32x2 ps[4], pq[4];
      float cs[3];
#pragma unroll
      for (int j = 0; j < 3; ++j) cs[j] = -lds4f(L_CS + tl[j] * 64 + i16 * 4);
#pragma unroll
      for (int rt = 0; rt < 2; ++rt) {
        const f32x4 rr = lds16f(L_XS + (32 * rw + 16 * rt + 4 * g) * 4), mm = lds16f(L_XS + 512 + (32 * rw + 16 * rt + 4 * g) * 4);
        f32x2 z0[3], z1[3];
#pragma unroll
        for (int j = 0; j < 3; ++j) {
          z0[j] = gelu2(fma2(lo2(mm), bc2(cs[j]), lo2(acc[rt][j])) * lo2(rr));
          z1[j] = gelu2(fma2(hi2(mm), bc2(cs[j]), hi2(acc[rt][j])) * hi2(rr));
          zp[j][2 * rt] = pk2v(z0[j]); zp[j][2 * rt + 1] = pk2v(z1[j]);
        }
        ps[2 * rt] = z0[0] + z0[1] + z0[2]; ps[2 * rt + 1] = z1[0] + z1[1] + z1[2];
        pq[2 * rt] = fma2(z0[2], z0[2], fma2(z0[1], z0[1], z0[0] * z0[0]));
        pq[2 * rt + 1] = fma2(z1[2], z1[2], fma2(z1[1], z1[1], z1[0] * z1[0]));
        SCHED();
        if (rt == 0) { ISS_U(4); ISS_U(5); }
        else ISS_U(6);
        SCHED();
      }
      const bool b3 = lane & 8, b2 = lane & 4, b1 = lane & 2;
#pragma unroll
      for (int m = 0; m < 2; ++m)
#pragma unroll
        for (int e = 0; e < 2; ++e) {
          ps[m][e] = (b3 ? ps[m + 2][e] : ps[m][e]) + dpp<0x128>(b3 ? ps[m][e] : ps[m + 2][e]);
          pq[m][e] = (b3 ? pq[m + 2][e] : pq[m][e]) + dpp<0x128>(b3 ? pq[m][e] : pq[m + 2][e]);
        }
#pragma unroll
      for (int e = 0; e < 2; ++e) {
        ps[0][e] = (b2 ? ps[1][e] : ps[0][e]) + dpp<0x141>(b2 ? ps[0][e] : ps[1][e]);
        pq[0][e] = (b2 ? pq[1][e] : pq[0][e]) + dpp<0x141>(b2 ? pq[0][e] : pq[1][e]);
      }
      float s_ = (b1 ? ps[0][1] : ps[0][0]) + dpp<0x4E>(b1 ? ps[0][0] : ps[0][1]);
      float q_ = (b1 ? pq[0][1] : pq[0][0]) + dpp<0x4E>(b1 ? pq[0][0] : pq[0][1]);
      s_ += dpp<0xB1>(s_);
      q_ += dpp<0xB1>(q_);
      const int row = 32 * rw + 16 * (i16 >> 3) + 4 * g + ((i16 >> 1) & 3);
      VM(st16_sc1((u32x4){__float_as_uint(s_), tlo, __float_as_uint(q_), tlo}, row * 16 + ((lane & 1) << 30), sws, WS_SG + ((P * 8 + c) * 2 + cw) * 2048));
      PIN(zp[0]); PIN(zp[1]); PIN(zp[2]);
    }
    STAMP(2);
    {
    RELANE();
#pragma unroll
    for (int j = 0; j < 3; ++j) acc[0][j] = acc[1][j] = zero4;
    csa = zero4;
    u32x4 q0, q1, q2, q3;
    int mq = 0, mz = 0;
    const u32 gvo = (2 * g) * 4096 + (32 * rw + 16 * cw + i16) * 16;
    const u32 gso = WS_SG + P * 32768;
#pragma unroll
    for (int ks = 0; ks <= 16; ++ks) {
      if (ks == 8) {
        WAITQ(mq); PIN(q0); PIN(q1); PIN(q2); PIN(q3);
#pragma nounroll
        for (int it = 0; it < SPIN_CAP; ++it) {
          if (__all(tag_ok(q0, tlo) && tag_ok(q1, tlo) && tag_ok(q2, tlo) && tag_ok(q3, tlo))) break;
          __builtin_amdgcn_s_sleep(POLL_SLEEP);
          asm volatile(
              "buffer_load_dwordx4 %0, %4, %5, %6 offen sc1\n\t"
              "buffer_load_dwordx4 %1, %4, %5, %6 offen offset:2048 sc1\n\t"
              "buffer_load_dwordx4 %2, %4, %5, %7 offen sc1\n\t"
              "buffer_load_dwordx4 %3, %4, %5, %7 offen offset:2048 sc1\n\t"
              "s_waitcnt vmcnt(0)"
              : "=&v"(q0), "=&v"(q1), "=&v"(q2), "=&v"(q3)
              : "v"(gvo), "s"(sws), "s"(gso), "s"(gso + 4096)
              : "memory");
        }
        float s = (__uint_as_float(q0[0]) + __uint_as_float(q1[0])) + (__uint_as_float(q2[0]) + __uint_as_float(q3[0]));
        float q = (__uint_as_float(q0[2]) + __uint_as_float(q1[2])) + (__uint_as_float(q2[2]) + __uint_as_float(q3[2]));
        red2g(s, q);
        float r2, m2;
        ln_fin(s, q, 1.0f / 688, r2, m2);
        sts4f(L_ZST + (32 * rw + 16 * cw + i16) * 4, r2);
        sts4f(L_ZST + 512 + (32 * rw + 16 * cw + i16) * 4, -m2 * r2);
        STAMP(3);
      }
      if (ks < 16 && (ks & 3) == 0) {
        if (ks > 0) { WAITQ(ks == 12 ? mz : mw[4 + (ks >> 2)]); BARRIER(); }
        if (ks == 12) { if (w == 0) SET_FLAG(WS_FA + (P * 8 + c) * 128); STAMP(5); }
        NEXT_BLOCK(4 + (ks >> 2));
      }
      if (ks == 8) {
#pragma unroll
        for (int rt = 0; rt < 2; ++rt) {
          const u32 ra = L_ZST + (32 * rw + 16 * rt + 4 * g) * 4;
          const f32x4 r4 = lds16f(ra), m4 = lds16f(ra + 512);
#pragma unroll
          for (int j = 0; j < 3; ++j) {
            const u32 u0 = zp[j][2 * rt], u1 = zp[j][2 * rt + 1];
            zp[j][2 * rt] = pk2v(fma2((f32x2){blo(u0), bhi(u0)}, lo2(r4), lo2(m4)));
            zp[j][2 * rt + 1] = pk2v(fma2((f32x2){blo(u1), bhi(u1)}, hi2(r4), hi2(m4)));
          }
        }
#pragma unroll
        for (int j = 0; j < 3; ++j)
          VM(st16_sc1(zp[j], l16, sws, (tl[j] < z2n) ? WS_ZB + ((z2s + tl[j]) * 64 + 4 * P + rw) * 1024 : 0x7FFF0000u));
        mz = vq;
        STAMP(4);
        SCHED();
      }
      if (ks >= 5 && ks <= 9) { WAITQ(mu[ks - 3]); READ_U(ks - 3, UBASE(ks - 3)) }
      if (ks >= 9 && ks <= 13) { WAITQ(mu[ks - 2]); READ_U(ks - 2, UBASE(ks - 2)) }
      if (ks >= 13) { WAITQ(mu[ks - 1]); READ_U(ks - 1, UBASE(ks - 1)) }
      const int k1 = ks > 0 ? ks - 1 : 0, q1_ = k1 & 1;
#pragma unroll
      for (int m = 0; m < 7; ++m) {
        if (ks > 0) {
          if (m < 6) acc[m & 1][m >> 1] = mfma32(wf[q1_][m >> 1], xf[k1][m & 1], acc[m & 1][m >> 1]);
          else csa = mfma32(ones4, wf[q1_][0], csa);
          SCHED();
        }
        if (ks < 16 && m >= 1 && m < 4) wf[ks & 1][m - 1] = ldfrag(ta[m - 1] + (ks & 3) * 6144, 256);
        if (m == 0 || m == 4) {
          const int h = m == 4;
          if (h == 0 && ks >= 6 && ks <= 13) asm volatile("s_waitcnt lgkmcnt(10)" ::: "memory");
          if (ks >= 6 && ks <= 10) { VM(DMA_U(ks + 1, UBASE(ks + 1), h)); mu[ks + 1] = vq; }
          if (ks >= 10 && ks <= 13) { VM(DMA_U(ks + 2, UBASE(ks + 2), h)); mu[ks + 2] = vq; }
        }
        if (m == 6) {
          if (ks >= 4 && ks <= 6) ISS_W(7, ks - 4, ks - 3);
        }
        if (m == 2) {
          if (ks == 5) { VM(q0 = ld16_sc1o<0>(gvo, sws, gso)); VM(q1 = ld16_sc1o<2048>(gvo, sws, gso)); }
          if (ks == 6) { VM(q2 = ld16_sc1o<0>(gvo, sws, gso + 4096)); VM(q3 = ld16_sc1o<2048>(gvo, sws, gso + 4096)); mq = vq; }
        }
        SCHED();
      }
#pragma unroll
      for (int j = 0; j < 3; ++j) { PIN(acc[0][j]); PIN(acc[1][j]); }
      PIN(csa);
    }
    sts4f(L_CS + 512 + tl[0] * 64 + i16 * 4, csa[0]);
    gf = ld8_sc1((lane >> 5) * 1024, sws, WS_FA + (16 * w + c2) * 128);
    BARRIER();
    VWO();
    DMA_WOUT(0) DMA_WOUT(1)
    WAITV(2);
    PIN(gf);
    if (!__all(gf[0] == tlo && gf[1] == thi)) poll_flags((lane >> 5) * 1024, true, sws, WS_FA + (16 * w + c2) * 128, tlo, thi);
    STAMP(6);
    {
      float xr[2], xm[2];
#pragma unroll
      for (int rt = 0; rt < 2; ++rt) {
        xr[rt] = lds4f(L_XS + (32 * rw + 16 * rt + i16) * 4);
        xm[rt] = -lds4f(L_XS + 512 + (32 * rw + 16 * rt + i16) * 4);
      }
      const u32 so0 = zb0 + TILE(0, gn) * 65536;
#pragma unroll
      for (int j = 0; j < 3; ++j) {
        const f32x4 cc = lds16f(L_CS + 512 + tl[j] * 64 + g * 16);
        bfr[0][3 * j] = ld16_b(l16, sws, so0 ^ (3 * j * 1024));
        bfr[0][3 * j + 1] = ld16_b(l16, sws, so0 ^ ((3 * j + 1) * 1024));
        if (j < 2) bfr[0][3 * j + 2] = ld16_b(l16, sws, so0 ^ ((3 * j + 2) * 1024));
        u32x4 zz;
#pragma unroll
        for (int rt = 0; rt < 2; ++rt) {
          zz[2 * rt] = pk2v(gelu2(fma2(lo2(cc), bc2(xm[rt]), lo2(acc[rt][j])) * bc2(xr[rt])));
          zz[2 * rt + 1] = pk2v(gelu2(fma2(hi2(cc), bc2(xm[rt]), hi2(acc[rt][j])) * bc2(xr[rt])));
        }
        st16_sc1(zz, l16, sws, (tl[j] < z1n) ? WS_Z1 + ((4 * P + rw) * 43 + z1s + tl[j]) * 1024 : 0x7FFF0000u);
        SCHED();
      }
    }
    }
  } else {
    int vq = 2;
    const rsrc_t sw = __builtin_amdgcn_make_buffer_rsrc((void*)wqkv, 0, 512 * 3072, 0x00020000);
    const u32 vw = (8 * w + ((lane >> 2) & 7)) * 3072 + 128 * c + ((lane >> 5) * 4 + ((lane & 3) ^ ((w & 1) << 1))) * 16;
    const u32 w3k = w * 3072;
#define DMA_S(n, e)                                                                                                 \
  do {                                                                                                              \
    if ((e) < 2) DMA_X(((n) % 3) * 40960, n, (e) & 1);                                                              \
    else DMA_CC(w3k, ((n) % 3) * 40960 + 16384 + ((e) - 2) * 1024, vw, sw, "s_add_u32", (((n) + rot) & 7) * (64 * 3072), ((e) - 2) * 1024); \
  } while (0)
#define ISS_Q(u, base) do { VM(DMA_U(u, base, 0)); VM(DMA_U(u, base, 1)); mu[u] = vq; } while (0)
    int ms[8], mu[16];
    VM(DMA_S(0, 2)); VM(DMA_S(0, 3)); VM(DMA_S(0, 4)); ms[0] = vq;
    u32x4 xrow;
    VM(xrow = ld16(l16, sx, ((8 + c) * 8 + w) * 1024));
    const int mxr = vq;
#pragma unroll
    for (int e = 0; e < 5; ++e) { VM(DMA_S(1, e)); ms[1] = vq; }
    const int l32 = lane & 31, hh = lane >> 5;
    u32 xa = (32 * rw + l32) * 128 + ((hh ^ ((l32 >> 1) & 7)) * 16);
    u32 wa = 16384 + hh * 3072 + cw * 512 + ((lane >> 2) & 3) * 64 + (((((g & 1) << 1) | ((lane >> 1) & 1)) ^ (hh << 1)) * 16) + (lane & 1) * 8;
    u32 ca = 16384 + g * 3072 + min(w, 5) * 512 + ((lane >> 2) & 3) * 64 + ((((g & 1) << 1) | ((lane >> 1) & 1)) * 16) + (lane & 1) * 8;
    const f32x16 zero16 = {0, 0, 0, 0, 0, 0, 0, 0, 0, 0, 0, 0, 0, 0, 0, 0};
    f32x16 acc[3] = {zero16, zero16, zero16};
    f32x4 csa[2] = {zero4, zero4};
    u32x4 xg;
    int mxg = 0;
    WAITQ(ms[0]);
    BARRIER();
    USE_EPOCH()
    STAMP(7);
    {
      u32x4 xf[2], wf[2][4];
#pragma unroll
      for (int n = 0; n <= 8; ++n)
#pragma unroll
      for (int sq = 0; sq < 4; ++sq) {
        const int s = 4 * n + sq;
        if (s > 32) continue;
        if (s < 32 && sq == 0) {
          if (n > 0) {
            WAITQ(ms[n]);
            BARRIER();
          }
          const int d = n == 0 ? 0 : (n % 3 == 0 ? -81920 : 40960);
          xa = opq(xa + d); wa = opq(wa + d); ca = opq(ca + d);
        }
        const int q1 = (s + 1) & 1, q0 = s & 1;
#pragma unroll
        for (int m = 0; m < 4; ++m) {
          if (s > 0) {
            if (m < 3) acc[m] = mfmaL(wf[q1][m], xf[q1], acc[m]);
            else csa[q1] = mfma32(ones4, wf[q1][3], csa[q1]);
            SCHED();
          }
          if (s < 32) {
            if (m == 0) { xf[q0] = lds16(xa ^ (32 * sq)); wf[q0][0] = ldfrag(wa + sq * 6144, 256); }
            if (m == 1) { wf[q0][1] = ldfrag(wa + sq * 6144 + 1024, 256); wf[q0][2] = ldfrag(wa + sq * 6144 + 2048, 256); }
            if (m == 2) wf[q0][3] = ldfrag((ca ^ (32 * (sq & 1))) + (sq >> 1) * 12288, 256);
            if (m == 0 || m == 2) {
              const int h = 2 * sq + (m == 2);
              if (n <= 5 && h < 5) { VM(DMA_S(n + 2, h)); ms[n + 2] = vq; }
              if (n == 0 && h == 5) {
                rsrc_t sp = __builtin_amdgcn_make_buffer_rsrc((void*)wphi, 0, 16384, 0x00020000);
                VM(dma16(L_WPHI + (w & 1) * 1024, l16, sp, c * 2048 + (w & 1) * 1024));
              }
              if (WSP_PF && h == 6 && (n == WSP_PF || n == WSP_PF + 2))
                VM(touch(L_JUNK, l16 * 8, sa, (((cl >> 1) * 2 + (n != WSP_PF)) * 8 + w) * 8192));
              if (n == 4 && h == 5) { VM(xg = ld16_sc1(i16 * 16, sws, WS_XG + (P * 128 + 16 * w) * 16)); mxg = vq; }
              if (n == 6) { VM(DMA_U(sq, (sq < 2 ? LQ_M : LQ_A) + (sq & 1) * 16384, h & 1)); mu[sq] = vq; }
              if (n == 7 && sq < 2) { VM(DMA_U(4 + sq, sq * 16384, h & 1)); mu[4 + sq] = vq; }
            }
          }
          SCHED();
        }
        PIN(acc[0]); PIN(acc[1]); PIN(acc[2]); PIN(csa[0]); PIN(csa[1]);
        if (s == 3) EARLY_STATS(mxr)
      }
    }
    GATHER_XS()
    sts4f(L_CS + w * 128 + i16 * 4, csa[0][0]);
    sts4f(L_CS + w * 128 + 64 + i16 * 4, csa[1][0]);
    BARRIER();
    STAMP(1);
    WAITQ(mu[3]);
    READ_U(0, LQ_M) READ_U(1, LQ_M + 16384) READ_U(2, LQ_A) READ_U(3, LQ_A + 16384)
    const float xr = lds4f(L_XS + (32 * rw + l32) * 4), xm = -lds4f(L_XS + 512 + (32 * rw + l32) * 4);
    u32x4 wp0, wp1;
    {
      const u32 a = L_WPHI + (2 * cw + (lane & 1)) * 512 + (4 * hh + ((lane >> 2) & 3)) * 32 + (2 * (g & 1) + ((lane >> 1) & 1)) * 8;
      const u32x4 f = ldfrag(a, 256);
      const bool own = (lane >> 2) & 1;
#pragma unroll
      for (int e = 0; e < 4; ++e) { wp0[e] = own ? 0u : f[e]; wp1[e] = own ? f[e] : 0u; }
    }
#define FEATURES(e_, dst, hook)                                                                                         \
  {                                                                                                                 \
    u32x4 kp[2];                                                                                                    \
    _Pragma("unroll") for (int a = 0; a < 4; ++a) {                                                                \
      const f32x4 cc = lds16f(L_CS + (2 * (e_) + cw) * 128 + a * 32 + hh * 16);                                      \
      kp[a >> 1][2 * (a & 1)] = pk2v(fma2(lo2(cc), bc2(xm), (f32x2){acc[e_][4 * a], acc[e_][4 * a + 1]}));          \
      kp[a >> 1][2 * (a & 1) + 1] = pk2v(fma2(hi2(cc), bc2(xm), (f32x2){acc[e_][4 * a + 2], acc[e_][4 * a + 3]}));  \
    }                                                                                                               \
    f32x16 pl = mfmaL(wp0, kp[0], zero16);                                                                          \
    pl = mfmaL(wp1, kp[1], pl);                                                                                     \
    hedgehog16(pl, xr, dst, hook);                                                                                  \
  }
    {
      const int s_ = 32 * rw + l32;
      const u32 key = (((s_ >> 1) & 3) ^ (((s_ >> 3) & 1) << 1)) * 16;
      {
        const u32 va = LQ_V + (s_ >> 3) * 1024 + cw * 512 + (s_ & 7) * 64 + hh * 8;
#pragma unroll
        for (int a = 0; a < 4; ++a) {
          const f32x4 cc = lds16f(L_CS + (4 + cw) * 128 + a * 32 + hh * 16);
          sts8(va + ((a * 16) ^ key), (u32x2){pk2v(fma2(lo2(cc), bc2(xm), (f32x2){acc[2][4 * a], acc[2][4 * a + 1]}) * bc2(xr)),
                                              pk2v(fma2(hi2(cc), bc2(xm), (f32x2){acc[2][4 * a + 2], acc[2][4 * a + 3]}) * bc2(xr))});
        }
      }
      WAITQ(mu[5]);
      READ_U(4, 0) READ_U(5, 16384)
      WAITL0();
      u32x4 ph[4];
      auto hk = [&](int i) __attribute__((always_inline)) {
        const int u = 6 + (i >> 1);
        SCHED();
        VM(DMA_U(u, (u < 8 ? LQ_M : (u < 10 ? LQ_A : 0)) + (u & 1) * 16384, i & 1));
        mu[u] = vq;
        SCHED();
      };
      FEATURES(1, ph, hk)
      const u32 pa = LQ_PHI + (s_ >> 3) * 2048 + (2 * cw + hh) * 512 + (s_ & 7) * 64;
#pragma unroll
      for (int a = 0; a < 4; ++a) sts16(pa + ((a * 16) ^ key), ph[a]);
    }
    BARRIER();
    STAMP(2);
    {
      const u32 kx = (((lane >> 3) & 1) ^ ((g & 1) << 1)) * 16;
      const u32 pa = LQ_PHI + g * 2048 + (w >> 1) * 512 + ((lane >> 2) & 3) * 64 + (((2 * (w & 1) + ((lane >> 1) & 1)) * 16) ^ kx) + (lane & 1) * 8;
      const u32 va = LQ_V + g * 1024 + (w >> 2) * 512 + ((lane >> 2) & 3) * 64 + (((2 * ((w >> 1) & 1) + ((lane >> 1) & 1)) * 16) ^ kx) + (lane & 1) * 8;
      u32x4 pf[4], vf[4];
#pragma unroll
      for (int k = 0; k < 4; ++k) {
        pf[k] = cat(ldstr(pa + k * 8192), ldstr((pa ^ 32) + k * 8192 + 256));
        vf[k] = cat(ldstr(va + k * 4096), ldstr((va ^ 32) + k * 4096 + 256));
      }
      f32x4 kv = zero4, kz = zero4;
#pragma unroll
      for (int k = 0; k < 4; ++k) { kv = mfma32(pf[k], vf[k], kv); kz = mfma32(pf[k], ones4, kz); }
      const u32 so = WS_KP + (c * 16 + P) * KP_SZ;
      u32x4 g0 = {__float_as_uint(kv[0]), tlo, __float_as_uint(kv[1]), tlo}, g1 = {__float_as_uint(kv[2]), tlo, __float_as_uint(kv[3]), tlo};
      u32x4 g2 = {__float_as_uint((lane & 1) ? kz[2] : kz[0]), tlo, __float_as_uint((lane & 1) ? kz[3] : kz[1]), tlo};
      PIN(g0); PIN(g1); PIN(g2);
      asm volatile("s_nop 7" ::: "memory");
      VM(st16_sc1(g0, l16, sws, so + w * 2048));
      VM(st16_sc1(g1, l16, sws, so + w * 2048 + 1024));
      VM(st16_sc1(g2, g * 32 + (lane & 1) * 16 + ((i16 >> 1) << 26), sws, so + 16384 + w * 128));
    }
    STAMP(3);
    WAITQ(mu[11]);
    READ_U(6, LQ_M) READ_U(7, LQ_M + 16384) READ_U(8, LQ_A) READ_U(9, LQ_A + 16384) READ_U(10, 0) READ_U(11, 16384)
    BARRIER();
    ISS_Q(12, LQ_M); VM(DMA_U(13, LQ_M + 16384, 0));
    u32x4 phq[4];
    {
      VWO();
      auto hk = [&](int i) __attribute__((always_inline)) {
        SCHED();
        if (i == 0) DMA_U(13, LQ_M + 16384, 1);
        else if (i < 5) DMA_U(14 + ((i - 1) >> 1), LQ_A + ((i - 1) >> 1) * 16384, (i - 1) & 1);
        else DMA_WOUT(i - 5)
        SCHED();
      };
      FEATURES(0, phq, hk)
    }
    PIN(phq[0]); PIN(phq[1]); PIN(phq[2]); PIN(phq[3]);
    STAMP(4);
    if (w >= 5) {
      const int id = 2 * P + (w == 7 ? (lane & 1) : w - 5), wq = id >> 2, gq = id & 3;
      const u32 vi0 = (4 * g) * KP_SZ + (w == 7 ? 16384 + wq * 128 + gq * 32 : wq * 2048 + (gq * 16 + i16) * 16);
      const u32 vi1 = vi0 + (w == 7 ? 16 : 1024);
      const u32 vo = (w == 7 ? (WS_KZ - WS_KF) + c * 512 + id * 16 + ((i16 >> 1) << 25) : c * 8192 + id * 256 + i16 * 16) + (g << 28);
      float sa[4];
#pragma nounroll
      for (int it = 0; it < SPIN_CAP; ++it) {
        u32x4 a[4][2];
#pragma unroll
        for (int o = 0; o < 4; ++o) {
          a[o][0] = ld16_sc1(vi0, sws, WS_KP + (c * 16 + o) * KP_SZ);
          a[o][1] = ld16_sc1(vi1, sws, WS_KP + (c * 16 + o) * KP_SZ);
        }
        WAITV(0);
        bool ok = true;
#pragma unroll
        for (int o = 0; o < 4; ++o) { PIN(a[o][0]); PIN(a[o][1]); ok = ok && tag_ok(a[o][0], tlo) && tag_ok(a[o][1], tlo); }
#pragma unroll
        for (int e = 0; e < 4; ++e)
          sa[e] = (__uint_as_float(a[0][e >> 1][2 * (e & 1)]) + __uint_as_float(a[1][e >> 1][2 * (e & 1)])) +
                  (__uint_as_float(a[2][e >> 1][2 * (e & 1)]) + __uint_as_float(a[3][e >> 1][2 * (e & 1)]));
        if (__all(ok)) break;
        __builtin_amdgcn_s_sleep(POLL_SLEEP);
      }
      red2g(sa[0], sa[1]);
      red2g(sa[2], sa[3]);
      st16_sc1((u32x4){pk2(sa[0], sa[1]), tlo, pk2(sa[2], sa[3]), tlo}, vo, sws, WS_KF);
    }
    gf = ld8_sc1((lane >> 5) * 1024, sws, WS_FA + (16 * w + c2) * 128);
    WAITV(8);
    READ_U(12, LQ_M) READ_U(13, LQ_M + 16384) READ_U(14, LQ_A) READ_U(15, LQ_A + 16384)
    BARRIER();
#pragma unroll
    for (int a = 0; a < 4; ++a) sts16(L_WO1 + w * 4096 + a * 1024 + l16, phq[a]);
    WAITV(0);
    PIN(gf);
    if (!__all(gf[0] == tlo && gf[1] == thi)) poll_flags((lane >> 5) * 1024, true, sws, WS_FA + (16 * w + c2) * 128, tlo, thi);
    STAMP(6);
    { const u32 so_ = zb0 + TILE(0, gn) * 65536; LOAD_B(0, so_) }
  }

  {
  RELANE();
  u32x4 kfg, kzg;
  const u32 kfo = WS_KF + c * 8192 + w * 1024, kzo = WS_KZ + c * 512 + w * 64;
  if (t != 0) {
    kfg = ld16_sc1(l16, sws, kfo);
    kzg = ld16_sc1(g * 16, sws, kzo);
  }
  { const u32 so_ = zb0 + TILE(1, gn) * 65536; LOAD_B(1, so_) }
  STAMP(9);
  const u32 redw = opq(L_RED + w * 4096 + lane * 16), redr = opq(L_RED + (rw ^ (2 * ux0)) * 1024 + lane * 16);
  const u32 fd = WS_FD + (p * 16 + c2) * 128;
  const u32 f3s = __builtin_amdgcn_readfirstlane(WS_FB + (P * 8 + (c2 > 0 ? c2 - 1 : 0)) * 128);
  const u32 vw3 = ((lane >> 2) & 7) * 1024 + c2 * 128 + (lane >> 5) * 64;
  const rsrc_t swo = __builtin_amdgcn_make_buffer_rsrc((void*)wo, 0, 512 * 1024, 0x00020000);
#define DMA_WO(dst, half, e) dma16((dst) + w * 4096 + (e) * 1024, (vw3 + (lane & 3) * 16) ^ (((e) & 1) * 32), swo, ((w + 8 * (half)) * 4 + (e)) * 8192)
#define FV() ((lane & 32) ? (P * 8 + ((lane >> 2) & 7)) * 128 : (WS_FD - WS_FC) + (p * 16 + (((lane >> 1) & 14) | cw)) * 128)
  u32x2 z1r[6], hold;
  u32 bs;
  f32x4 sm0;
#define EMIT_H(jt, sm_)                                                                                             \
  {                                                                                                                 \
    const f32x2 bb = bc2(blo(bs));                                                                                  \
    const u32 h0 = pk2v((f32x2){blo(z1r[jt][0]), bhi(z1r[jt][0])} * (lo2(sm_) + bb));                               \
    const u32 h1 = pk2v((f32x2){blo(z1r[jt][1]), bhi(z1r[jt][1])} * (hi2(sm_) + bb));                               \
    const u32 hb_ = WS_HF + (((4 * p + rw) * 2 + ((jt) >> 1)) * 8 + c2) * 1024;                                     \
    if ((jt) == 0 || ((jt) == 2 && gn == 6)) hold = (u32x2){h0, h1};                                                \
    else if ((jt) == 1 || ((jt) == 3 && gn == 6)) st16_sc1((u32x4){hold[0], hold[1], h0, h1}, l16, sws, hb_);       \
    else if ((jt) == 2) st16_sc1((u32x4){h0, h1, 0u, 0u}, l16, sws, hb_);                                           \
    else st16_sc1((u32x4){h0, tlo, h1, tlo}, l16, sws, WS_HL + (((4 * p + rw) * 8 + c2) * 2 + (gn - 1 - (jt))) * 1024); \
  }
#pragma unroll
  for (int j = 0; j < 6; ++j) {
    if (j < 5 || gn == 6) {
      const int u = j & 1;
      if (j == 0) WAITV(8);
      if (j == 2) WAITV(t ? 10 : 8);
      if (j == 1) { WAITV(9); WAITV_IF(cw, 1, 8); }
      if (j == 3) { WAITV(9); WAITV_IF(cw, 1, 8); }
      if (j == 4) { if (t) WAITV(4); else { WAITV(12); WAITV_IF(gn, 5, 4); } }
      if (j == 5) { WAITV(10); WAITV_IF(cw, 1, 9); }
#pragma unroll
      for (int i = 0; i < 8; ++i) PIN(bfr[u][i]);
      {
        f32x4 a_[4];
#pragma unroll
        for (int rt = 0; rt < 4; ++rt) {
          a_[rt] = zero4;
#pragma unroll
          for (int i = 0; i < 8; ++i) a_[rt] = mfma32(bfr[u][i], af[rt][i], a_[rt]);
          SCHED();
          if (rt > 0) sts16f(redw + u * 32768 + (rt - 1) * 1024, a_[rt - 1]);
          SCHED();
        }
        if (j == 0 && t != 0) {
          PIN(kfg); PIN(kzg);
          poll_gran(kfg, l16, sws, kfo, tlo);
          poll_gran(kzg, g * 16, sws, kzo, tlo);
          sts8(L_KFS + (w >> 1) * 1024 + i16 * 64 + (w & 1) * 32 + g * 8, (u32x2){kfg[0], kfg[2]});
          sts8(L_KFS + 4096 + w * 32 + g * 8, (u32x2){kzg[0], kzg[2]});
          sts8(L_KFS + 4352 + (lane & 7) * 8, (u32x2){0u, 0u});
        }
        sts16f(redw + u * 32768 + 3072, a_[3]);
      }
      u32x4 of[2];
      if (j == 1 && t != 0) {
        const int l32 = lane & 31, hh = lane >> 5;
        const bool own = ((lane >> 2) & 1) == hh;
        const u32 ka = own ? L_KFS + (2 * cw + hh) * 1024 + ((l32 & 3) + 4 * (l32 >> 3)) * 64 : L_KFS + 4352;
        const u32 zaa = own ? L_KFS + 4096 + (2 * cw + hh) * 64 : L_KFS + 4352;
        const u32 qa = L_WO1 + w * 4096 + l16;
        const f32x16 zero16 = {0, 0, 0, 0, 0, 0, 0, 0, 0, 0, 0, 0, 0, 0, 0, 0};
        f32x16 den = zero16, num = zero16;
#pragma unroll
        for (int a = 0; a < 4; ++a) {
          const u32x4 pq_ = lds16(qa + a * 1024);
          den = mfmaL(lds16(zaa + a * 16), pq_, den);
          num = mfmaL(lds16(ka + a * 16), pq_, num);
        }
        const float inv = __builtin_amdgcn_rcpf(den[0] + 1e-6f);
#pragma unroll
        for (int a = 0; a < 2; ++a) {
          of[a] = (u32x4){pk2(num[8 * a] * inv, num[8 * a + 1] * inv), pk2(num[8 * a + 2] * inv, num[8 * a + 3] * inv),
                          pk2(num[8 * a + 4] * inv, num[8 * a + 5] * inv), pk2(num[8 * a + 6] * inv, num[8 * a + 7] * inv)};
          PIN(of[a]);
        }
        STAMP(5);
      }
      BAR_IF(cw, 1, 63);
      if (w == 4) {
        if (j == 0 && t == 0) SET_FLAG(WS_FB + (P * 8 + c) * 128);
        if (j == 3) SET_FLAG(fd);
        if (j == 3 && t != 0) SET_FLAG(WS_FC + (P * 8 + c) * 128);
        if ((j == 4 && gn == 5) || j == 5) SET_FLAG(fd + 1024);
      }
      if (j == 1) {
        if (cw == 0) {
          WAITV(0);
          PIN(gf);
          if (!__all(gf[0] == tlo && gf[1] == thi)) poll_flags((lane >> 5) * 128, true, sws, f3s, tlo, thi);
          rsrc_t sb = __builtin_amdgcn_make_buffer_rsrc((void*)bsp, 0, 4096, 0x00020000);
          bs = ld2(i16 * 2, sb, (64 * p + 16 * rw) * 2);
        }
      }
      if ((j == 1 || j == 2) && cw == 0) {
#pragma unroll
        for (int v = 3 * j - 3; v < 3 * j; ++v)
          z1r[v] = ld8_sc1(l16, sws, WS_Z1 + ((2 * p + (rw >> 1)) * 43 + gs + TILE(min(v, gn - 1), gn)) * 1024 + (rw & 1) * 8);
      }
      if ((j == 4 && gn == 5) || j == 5) gf = ld8_sc1(FV(), sws, WS_FC);
      if (j == 4 && t == 0) {
        VWO();
#pragma unroll
        for (int e = 2; e < 7; ++e) DMA_WOUT(e)
      }
      const u32 ra_ = redr + ((j + 1) & 1) * 32768;
      f32x4 sm, r_[2];
#pragma unroll
      for (int i = 0; i < 8; ++i) {
        if (j >= 1 && j < (t ? 3 : 4) && (i & 1) == 0 && cw == 0) {
          if (i == 2) sm = r_[0] + r_[1];
          if (i > 2) sm += r_[0] + r_[1];
          r_[0] = lds16f(ra_ + i * 4096); r_[1] = lds16f(ra_ + (i + 1) * 4096);
        }
        SCHED();
        if (j < 3) bfr[u][i] = ld16_b(l16, sws, (zb0 + TILE(j + 2, gn) * 65536) ^ (i * 1024));
        else if (j == 3 && t == 0) ld16_b_if6(bfr[1][i], gn, l16, sws, (zb0 + TILE(5, gn) * 65536) ^ (i * 1024));
        SCHED();
      }
      if (j == 0 && cw == 0) gf = ld8_sc1((lane >> 5) * 128, sws, f3s);
      if (j == 1 && t != 0) {
        const u32 vo = ((lane >> 4) & 1) * 16384 + (lane >> 5) * 512 + i16 * 16;
        st16_sc1(of[0], vo, sws, WS_OF + ((8 * P + 2 * rw) * 16 + 2 * c + cw) * 1024);
        st16_sc1(of[1], vo, sws, WS_OF + ((8 * P + 2 * rw) * 16 + 2 * c + cw) * 1024 + 256);
      }
      if (j == 2) { PIN(bs); _Pragma("unroll") for (int v = 0; v < 3; ++v) PIN(z1r[v]); }
      if (j == 3) { _Pragma("unroll") for (int v = 3; v < 6; ++v) PIN(z1r[v]); }
      if (j >= 1 && cw == 0) {
        if (j < (t ? 3 : 4)) sm += r_[0] + r_[1];
        else {
          f32x4 q_[8];
#pragma unroll
          for (int o = 0; o < 8; ++o) q_[o] = lds16f(ra_ + o * 4096);
          SCHED();
          sm = ((q_[0] + q_[1]) + (q_[2] + q_[3])) + ((q_[4] + q_[5]) + (q_[6] + q_[7]));
        }
        if (j == 1) sm0 = sm;
        if (j == 2) EMIT_H(0, sm0)
        if (j >= 2) EMIT_H(j >= 2 ? j - 1 : 0, sm)
      }
      if (j == 3) {
#pragma unroll
        for (int e = 0; e < 4; ++e) DMA_WO(L_WO1, 0, e);
      }
      if (j == 3 && cw == 0) { if (t) WAITV(5); else { WAITV(12); WAITV_IF(gn, 5, 5); } }
      if (j == 5) WAITV(2);
      BAR_IF(cw, 0, 63);
    }
  }
  {
    STAMP(10);
    const u32 bufA = L_RED + (gn & 1) * 32768, bufB = L_RED + 32768 - (gn & 1) * 32768;
    const u32 fv = FV();
    if (cw == 0) {
      const u32 ra_ = redr + (bufB - L_RED);
      f32x4 q_[8];
#pragma unroll
      for (int o = 0; o < 8; ++o) q_[o] = lds16f(ra_ + o * 4096);
      SCHED();
      const f32x4 sm = ((q_[0] + q_[1]) + (q_[2] + q_[3])) + ((q_[4] + q_[5]) + (q_[6] + q_[7]));
      if (gn == 6) EMIT_H(5, sm) else EMIT_H(4, sm)
    }
    WAITV(2); WAITV_IF(cw, 1, 0);
    PIN(gf);
    {
      const bool n0 = (lane & 48) != 16;
      if (!__all(!n0 || (gf[0] == tlo && gf[1] == thi))) poll_flags(fv, n0, sws, WS_FC, tlo, thi);
    }
    RELANE();
    u32x4 hf[8], of[8];
    const u32 ofb = WS_OF + ((4 * p + rw) * 16 + cw) * 1024, hfb = WS_HF + ((4 * p + rw) * 16 + cw) * 1024;
#pragma unroll
    for (int e = 0; e < 4; ++e) of[e] = ld16_b(l16, sws, ofb + e * 2048);
#pragma unroll
    for (int e = 0; e < 4; ++e) hf[e] = ld16_b(l16, sws, hfb + e * 2048);
    BARRIER();
    const u32 qb = ((lane >> 2) & 3) * 64 + ((((g & 1) << 1) | ((lane >> 1) & 1)) * 16) + (lane & 1) * 8;
    f32x4 a3[4];
#pragma unroll
    for (int ct = 0; ct < 4; ++ct) a3[ct] = zero4;
    u32x4 wf[3][4], hl[4][2];
#define RDW2(dst, lo0, hi0, offl, offh) _Pragma("unroll") for (int ct = 0; ct < 4; ++ct) dst[ct] = cat(ldstr(((lo0) ^ ((ct & 1) * 32)) + (offl) + (ct >> 1) * 512), ldstr(((hi0) ^ ((ct & 1) * 32)) + (offh) + (ct >> 1) * 512))
#define MM3(src, frag) _Pragma("unroll") for (int ct = 0; ct < 4; ++ct) a3[ct] = mfma32(src[ct], frag, a3[ct])
    const u32 lb = WS_HL + ((4 * p + rw) * 8 + cw) * 2048;
    const rsrc_t sxo = __builtin_amdgcn_make_buffer_rsrc((void*)(x + (size_t)p * 65536), 0, 65536, 0x00020000);
    const u32 w0 = opq(L_WOUT + qb + (g >> 1) * 1024 + (g & 1) * 256);
    const u32 wa = w0 + cw * 4096, wb = w0 + cw * 2048;
    const u32 oq = qb + g * 1024 + cw * 4096;
    const u32 oa = opq(L_WO1 + oq), ob = opq(bufA + oq);
    u32x4 xres;
    VWO();
#pragma unroll
    for (int e = 0; e < 4; ++e) DMA_WO(bufA, 1, e);
    STAMP(11);
#define RDK(dst, k)                                                                                                 \
  if ((k) < 4) { RDW2(dst, oa, oa, (k) * 8192, (k) * 8192 + 256); }                                                 \
  else if ((k) < 8) { RDW2(dst, wa, wa, ((k) - 4) * 8192, ((k) - 4) * 8192 + 2048); }                               \
  else if ((k) < 12) { RDW2(dst, ob, ob, ((k) - 8) * 8192, ((k) - 8) * 8192 + 256); }                               \
  else { RDW2(dst, wb, w0, 32768 + ((k) - 12) * 4096, 49152 + ((k) - 12) * 2048); }
    RDK(wf[0], 0)
    RDK(wf[1], 1)
#pragma unroll
    for (int k = 0; k < 16; ++k) {
      if (k == 0) WAITV(8);
      if (k == 4) WAITV(13);
      if (k == 6) {
        WAITV(0);
        PIN(gf);
        if (!__all(gf[0] == tlo && gf[1] == thi)) poll_flags(fv, true, sws, WS_FC, tlo, thi);
#pragma unroll
        for (int e = 4; e < 8; ++e) hf[e] = ld16_b(l16, sws, hfb + e * 2048);
        BARRIER();
        STAMP(12);
      }
      if (k == 12) WAITV(9);
      if (k + 2 < 16) { RDK(wf[(k + 2) % 3], k + 2) }
      SCHED();
      if (k < 4) { PIN(of[k]); MM3(wf[k % 3], of[k]); }
      else if (k < 8) { PIN(hf[k - 4]); MM3(wf[k % 3], hf[k - 4]); }
      else if (k < 12) { PIN(of[k - 4]); MM3(wf[k % 3], of[k - 4]); }
      else { PIN(hf[k - 8]); MM3(wf[k % 3], hf[k - 8]); }
      SCHED();
      if (k < 4) {
        of[4 + k] = ld16_b(l16, sws, ofb + (4 + k) * 2048);
        DMA_WOUT_AT(bufB + w * 4096, 7 + k)
        if (k == 3) gf = ld8_sc1(fv, sws, WS_FC);
      } else if (k >= 6 && k < 10) {
        hl[k - 6][0] = ld16_sc1(l16, sws, lb + (k - 6) * 4096);
        hl[k - 6][1] = ld16_sc1(l16, sws, lb + (k - 6) * 4096 + 1024);
      } else if (k == 10) {
        xres = ld16((tid >> 3) * 1024 + (tid & 7) * 16, sxo, c2 * 128);
      }
    }
    STAMP(13);
    {
      const u32 wl_ = opq(bufB + (w0 - L_WOUT) + cw * 4096);
      u32x4 wg[4][4];
#pragma unroll
      for (int e = 0; e < 4; ++e) RDW2(wg[e], wl_, wl_, e * 8192, e * 8192 + 2048);
      WAITV(0);
      PIN(xres);
#pragma unroll
      for (int e = 0; e < 4; ++e) { PIN(hl[e][0]); PIN(hl[e][1]); }
#pragma nounroll
      for (int it = 0; it < SPIN_CAP; ++it) {
        u32 bad = 0;
#pragma unroll
        for (int e = 0; e < 8; ++e) bad |= (hl[e >> 1][e & 1][1] ^ tlo) | (hl[e >> 1][e & 1][3] ^ tlo);
        if (__all(bad == 0)) break;
        __builtin_amdgcn_s_sleep(2);
        asm volatile(
            "buffer_load_dwordx4 %0, %8, %9, %10 offen sc1\n\tbuffer_load_dwordx4 %1, %8, %9, %10 offen offset:1024 sc1\n\t"
            "buffer_load_dwordx4 %2, %8, %9, %11 offen sc1\n\tbuffer_load_dwordx4 %3, %8, %9, %11 offen offset:1024 sc1\n\t"
            "buffer_load_dwordx4 %4, %8, %9, %12 offen sc1\n\tbuffer_load_dwordx4 %5, %8, %9, %12 offen offset:1024 sc1\n\t"
            "buffer_load_dwordx4 %6, %8, %9, %13 offen sc1\n\tbuffer_load_dwordx4 %7, %8, %9, %13 offen offset:1024 sc1\n\t"
            "s_waitcnt vmcnt(0)"
            : "=&v"(hl[0][0]), "=&v"(hl[0][1]), "=&v"(hl[1][0]), "=&v"(hl[1][1]), "=&v"(hl[2][0]), "=&v"(hl[2][1]), "=&v"(hl[3][0]), "=&v"(hl[3][1])
            : "v"(l16), "s"(sws), "s"(lb), "s"(lb + 4096), "s"(lb + 8192), "s"(lb + 12288)
            : "memory");
      }
#pragma unroll
      for (int e = 0; e < 4; ++e) MM3(wg[e], ((u32x4){hl[e][0][0], hl[e][0][2], hl[e][1][0], hl[e][1][2]}));
    }
    STAMP(14);
    const u32 fin = L_WO1 - 2048;
#pragma unroll
    for (int ct = 0; ct < 4; ++ct) sts16f(fin + 2048 + cw * 16384 + (16 * rw + i16) * 256 + (((4 * ct + g) ^ i16) * 16), a3[ct]);
    BARRIER();
    {
      const int row = tid >> 3;
      const u32 a = fin + 2048 + row * 256 + (((2 * (tid & 7)) ^ (row & 15)) * 16);
      const f32x4 s0 = lds16f(a) + lds16f(a + 16384), s1 = lds16f(a ^ 16) + lds16f((a ^ 16) + 16384);
      u32x4 o = {pk2(blo(xres[0]) + s0[0], bhi(xres[0]) + s0[1]), pk2(blo(xres[1]) + s0[2], bhi(xres[1]) + s0[3]),
                 pk2(blo(xres[2]) + s1[0], bhi(xres[2]) + s1[1]), pk2(blo(xres[3]) + s1[2], bhi(xres[3]) + s1[3])};
      st16_sc1(o, row * 1024 + (tid & 7) * 16, __builtin_amdgcn_make_buffer_rsrc((void*)(out + (size_t)p * 65536), 0, 65536, 0x00020000), c2 * 128);
    }
  }
  }
  if (b == 0 && w == 0 && l16 == 0) *(u64*)(ws + WS_EP) = ((u64)thi << 32) | tlo;
  STAMP(15);
  STAMPC(31);
}

__global__ __launch_bounds__(512, 2) void fused_kernel(const char* __restrict__ x, const char* __restrict__ wqkv, const char* __restrict__ wphi,
                                                        const char* __restrict__ wo, const char* __restrict__ win, const char* __restrict__ wsp,
                                                        const char* __restrict__ bsp, const char* __restrict__ wout, char* __restrict__ out, char* ws) {
  __shared__ __attribute__((aligned(1024))) char smem[163840];
  asm volatile("" ::"v"((u32)(size_t)(LDSP char*)smem));
  asm volatile("" ::"s"(x), "s"(wqkv), "s"(win), "s"(ws), "s"(wsp));
  u64 pc, ep;
  asm volatile("s_getpc_b64 %0" : "=s"(pc));
  {
    const int lane = threadIdx.x & 63, g = lane >> 4;
    const int w = __builtin_amdgcn_readfirstlane(threadIdx.x >> 6);
    const int r = blockIdx.x >> 3, P = ((blockIdx.x & 7) >> 1) * 4 + (r >> 3);
    const int rot = 2 * (((r >> 3) + (r & 3)) & 3);
    u32 acv = 0, acb = 0; (void)acv; (void)acb;
    STAMP(0);
    STAMPC(30);
    if (CODE_PF && w == 7)
      touch(L_JUNK, lane * 128, __builtin_amdgcn_make_buffer_rsrc((void*)(pc & ~255ull), 0, CODE_PF, 0x00020000), (r & 7) * 8192);
    const rsrc_t sx = __builtin_amdgcn_make_buffer_rsrc((void*)(x + (size_t)P * 131072), 0, 131072, 0x00020000);
    const u32 vx0 = (16 * w + (lane >> 3)) * 1024 + (((lane & 7) ^ g) * 16);
    const u32 vx1 = (vx0 + 8192) ^ 64;
    const u32 w2k = w * 2048;
    DMA_X(0, 0, 0);
    DMA_X(0, 0, 1);
    asm volatile("s_load_dwordx2 %0, %1, 0x0" : "=s"(ep) : "s"(ws) : "memory");
    SCHED();
  }
  if (__builtin_expect(((blockIdx.x >> 5) & 1) == 0, 1)) run<0>(x, wqkv, wphi, wo, win, wsp, bsp, wout, out, ws, ep);
  else run<1>(x, wqkv, wphi, wo, win, wsp, bsp, wout, out, ws, ep);
}

extern "C" void kernel_launch(void* const* d_in, const int* in_sizes, int n_in, void* d_out, int out_size, void* d_ws, size_t ws_size,
                              hipStream_t stream) {
  fused_kernel<<<256, 512, 0, stream>>>((const char*)d_in[0], (const char*)d_in[1], (const char*)d_in[2], (const char*)d_in[3],
                                        (const char*)d_in[5], (const char*)d_in[6], (const char*)d_in[7], (const char*)d_in[8], (char*)d_out,
                                        (char*)d_ws);
}
```

```cpp
#define PROBE 7
#define PTYPE 2
#include <hip/hip_runtime.h>
#include <hip/hip_bf16.h>

typedef unsigned int u32;
typedef unsigned long long u64;
typedef short s16x4 __attribute__((ext_vector_type(4)));
typedef __bf16 bf16x2 __attribute__((ext_vector_type(2)));
typedef __bf16 bf16x8 __attribute__((ext_vector_type(8)));
typedef float f32x16 __attribute__((ext_vector_type(16)));
typedef float f32x4 __attribute__((ext_vector_type(4)));
typedef float f32x2 __attribute__((ext_vector_type(2)));
typedef u32 u32x4 __attribute__((ext_vector_type(4)));
typedef u32 u32x2 __attribute__((ext_vector_type(2)));
typedef __amdgpu_buffer_rsrc_t rsrc_t;
#define LDSP __attribute__((address_space(3)))
#define DI __device__ __forceinline__

#define STAMPS 0

constexpr u32 WS_EP = 0;
constexpr u32 WS_FA = 8192;
constexpr u32 WS_FB = WS_FA + 16384;
constexpr u32 WS_FC = WS_FB + 16384;
constexpr u32 WS_FD = WS_FC + 16384;
constexpr u32 WS_XG = WS_FD + 65536;
constexpr u32 WS_SG = WS_XG + 32768;
constexpr u32 WS_ZB = WS_SG + 524288;
constexpr u32 WS_Z1 = WS_ZB + 43 * 65536;
constexpr u32 KP_SZ = 17408;
constexpr u32 WS_KP = WS_Z1 + 64 * 43 * 1024;
constexpr u32 WS_KF = WS_KP + 128 * KP_SZ;
constexpr u32 WS_KZ = WS_KF + 65536;
constexpr u32 WS_OF = WS_KZ + 4096;
constexpr u32 WS_HF = WS_OF + 128 * 16384;
constexpr u32 WS_HL = WS_HF + 128 * 16 * 1024;
constexpr u32 WS_ST = WS_HL + 128 * 16 * 1024;
constexpr u32 WS_END = WS_ST + 256 * 8 * 32 * 8;

constexpr u32 LZ_W = 65536;
constexpr u32 LZ_U = 139264;
constexpr u32 LQ_M = 122880;
constexpr u32 LQ_A = 81920;
constexpr u32 LQ_PHI = 49152;
constexpr u32 LQ_V = 32768;
constexpr u32 L_TAB = 155648;
constexpr u32 L_XS = L_TAB;
constexpr u32 L_CS = L_TAB + 1024;
constexpr u32 L_ZST = L_TAB + 3072;
constexpr u32 L_WPHI = L_TAB + 3072;
constexpr u32 L_KFS = L_TAB + 2048;
constexpr u32 L_JUNK = L_TAB + 7168;
constexpr u32 L_WOUT = 0;
constexpr u32 L_RED = 57344;
constexpr u32 L_WO1 = 122880;

constexpr u64 Z2S = 0x26211B16110B0500ull;
constexpr u64 Z1S = 0x26211C16110C0600ull;
DI int grp_start(u64 tab, int c) { return c >= 8 ? 43 : (int)((tab >> (8 * c)) & 0xFF); }

#define SCHED() __builtin_amdgcn_sched_barrier(0)
#define TIC()
#define TOC(acc)
#define WAITV(n) do { TIC(); asm volatile("s_waitcnt vmcnt(%0)" ::"n"(n) : "memory"); TOC(acv); SCHED(); } while (0)
#define BARRIER() do { SCHED(); TIC(); asm volatile("s_waitcnt lgkmcnt(0)\n\ts_barrier" ::: "memory"); TOC(acb); SCHED(); } while (0)
#define WAITV_IF(g_, v, n) do { TIC(); asm volatile("s_cmp_lg_u32 %0, %1\n\ts_cbranch_scc1 1f\n\ts_waitcnt vmcnt(%2)\n1:" ::"s"(g_), "n"(v), "n"(n) : "scc", "memory"); TOC(acv); SCHED(); } while (0)
#define BAR_IF(g_, v, n) do { SCHED(); asm volatile("s_cmp_lg_u32 %0, %1\n\ts_cbranch_scc1 1f\n\ts_waitcnt vmcnt(%2) lgkmcnt(0)\n\ts_barrier\n1:" ::"s"(g_), "n"(v), "n"(n) : "scc", "memory"); SCHED(); } while (0)
#define WAITL0() asm volatile("s_waitcnt lgkmcnt(0)" ::: "memory")
#define PIN(x) asm volatile("" : "+v"(x))

DI void dma16(u32 lds, u32 voff, rsrc_t srd, u32 soff) {
  asm volatile("s_mov_b32 m0, %0\n\ts_nop 0\n\tbuffer_load_dwordx4 %1, %2, %3 offen lds" ::"s"(lds), "v"(voff), "s"(srd), "s"(soff) : "memory");
}
#define DMA_C(wb, CST, voff, srd, soff) asm volatile("s_add_u32 m0, %0, %1\n\ts_nop 0\n\tbuffer_load_dwordx4 %2, %3, %4 offen lds" ::"s"(wb), "n"(CST), "v"(voff), "s"(srd), "s"(soff) : "scc", "memory")
#define DMA_CC(wb, CST, voff, srd, OP, sb, SC) do { u32 so__; asm volatile("s_add_u32 m0, %1, %2\n\t" OP " %0, %5, %6\n\tbuffer_load_dwordx4 %3, %4, %0 offen lds" : "=&s"(so__) : "s"(wb), "n"(CST), "v"(voff), "s"(srd), "s"(sb), "n"(SC) : "scc", "memory"); } while (0)
DI void touch(u32 lds, u32 voff, rsrc_t srd, u32 soff) {
  asm volatile("s_mov_b32 m0, %0\n\ts_nop 0\n\tbuffer_load_dword %1, %2, %3 offen lds" ::"s"(lds), "v"(voff), "s"(srd), "s"(soff) : "memory");
}
DI u32x4 ld16_sc1(u32 voff, rsrc_t srd, u32 soff) {
  u32x4 r;
  asm volatile("buffer_load_dwordx4 %0, %1, %2, %3 offen sc1" : "=v"(r) : "v"(voff), "s"(srd), "s"(soff) : "memory");
  return r;
}
template <int OFF> DI u32x4 ld16_sc1o(u32 voff, rsrc_t srd, u32 soff) {
  u32x4 r;
  asm volatile("buffer_load_dwordx4 %0, %1, %2, %3 offen offset:%4 sc1" : "=v"(r) : "v"(voff), "s"(srd), "s"(soff), "n"(OFF) : "memory");
  return r;
}
#define BPOL " sc1"
DI u32x4 ld16_b(u32 voff, rsrc_t srd, u32 soff) {
  u32x4 r;
  asm volatile("buffer_load_dwordx4 %0, %1, %2, %3 offen" BPOL : "=v"(r) : "v"(voff), "s"(srd), "s"(soff) : "memory");
  return r;
}
template <int OFF> DI u32x4 ld16_bo(u32 voff, rsrc_t srd, u32 soff) {
  u32x4 r;
  asm volatile("buffer_load_dwordx4 %0, %1, %2, %3 offen offset:%4" BPOL : "=v"(r) : "v"(voff), "s"(srd), "s"(soff), "n"(OFF) : "memory");
  return r;
}
DI void ld16_b_if6(u32x4& r, int gn, u32 voff, rsrc_t srd, u32 soff) {
  asm volatile("s_cmp_lg_u32 %4, 6\n\ts_cbranch_scc1 1f\n\tbuffer_load_dwordx4 %0, %1, %2, %3 offen" BPOL "\n1:" : "+v"(r) : "v"(voff), "s"(srd), "s"(soff), "s"(gn) : "scc", "memory");
}
DI u32x4 ld16(u32 voff, rsrc_t srd, u32 soff) {
  u32x4 r;
  asm volatile("buffer_load_dwordx4 %0, %1, %2, %3 offen" : "=v"(r) : "v"(voff), "s"(srd), "s"(soff) : "memory");
  return r;
}
DI u32x2 ld8_sc1(u32 voff, rsrc_t srd, u32 soff) {
  u32x2 r;
  asm volatile("buffer_load_dwordx2 %0, %1, %2, %3 offen sc1" : "=v"(r) : "v"(voff), "s"(srd), "s"(soff) : "memory");
  return r;
}
DI u32 ld2(u32 voff, rsrc_t srd, u32 soff) {
  u32 r;
  asm volatile("buffer_load_ushort %0, %1, %2, %3 offen" : "=v"(r) : "v"(voff), "s"(srd), "s"(soff) : "memory");
  return r;
}
DI void st16_sc1(u32x4 v, u32 voff, rsrc_t srd, u32 soff) {
  asm volatile("buffer_store_dwordx4 %0, %1, %2, %3 offen sc1\n\ts_nop 1" ::"v"(v), "v"(voff), "s"(srd), "s"(soff) : "memory");
}
DI void st8_sc1(u32x2 v, u32 voff, rsrc_t srd, u32 soff) {
  asm volatile("buffer_store_dwordx2 %0, %1, %2, %3 offen sc1" ::"v"(v), "v"(voff), "s"(srd), "s"(soff) : "memory");
}

DI u32x4 lds16(u32 a) { return *(const LDSP u32x4*)(size_t)a; }
DI u32x2 lds8(u32 a) { return *(const volatile LDSP u32x2*)(size_t)a; }
DI float lds4f(u32 a) { return *(const LDSP float*)(size_t)a; }
DI f32x4 lds16f(u32 a) { return *(const LDSP f32x4*)(size_t)a; }
DI void sts16f(u32 a, f32x4 v) { *(LDSP f32x4*)(size_t)a = v; }
DI void sts16(u32 a, u32x4 v) { *(LDSP u32x4*)(size_t)a = v; }
DI void sts8(u32 a, u32x2 v) { *(volatile LDSP u32x2*)(size_t)a = v; }
DI void sts4f(u32 a, float v) { *(LDSP float*)(size_t)a = v; }
DI u32x2 ldstr(u32 a) {
  return __builtin_bit_cast(u32x2, __builtin_amdgcn_ds_read_tr16_b64_v4i16((LDSP s16x4*)(size_t)a)); }
DI u32x4 cat(u32x2 lo, u32x2 hi) { return (u32x4){lo[0], lo[1], hi[0], hi[1]}; }
DI u32x4 ldfrag(u32 a, u32 d) { u32x2 lo = ldstr(a), hi = ldstr(a + d); return cat(lo, hi); }

DI f32x4 mfma32(u32x4 a, u32x4 b, f32x4 c) {
  return __builtin_amdgcn_mfma_f32_16x16x32_bf16(__builtin_bit_cast(bf16x8, a), __builtin_bit_cast(bf16x8, b), c, 0, 0, 0);
}
DI f32x16 mfmaL(u32x4 a, u32x4 b, f32x16 c) {
  return __builtin_amdgcn_mfma_f32_32x32x16_bf16(__builtin_bit_cast(bf16x8, a), __builtin_bit_cast(bf16x8, b), c, 0, 0, 0);
}
DI u32 pk2(float a, float b) { bf16x2 v = {(__bf16)a, (__bf16)b}; return __builtin_bit_cast(u32, v); }
DI float blo(u32 u) { return __uint_as_float(u << 16); }
DI float bhi(u32 u) { return __uint_as_float(u & 0xffff0000u); }
DI float rbf(float a) { return blo(pk2(a, a)); }
DI float dot2(u32 a, u32 b, float c) {
  return __builtin_amdgcn_fdot2_f32_bf16(__builtin_bit_cast(bf16x2, a), __builtin_bit_cast(bf16x2, b), c, false);
}
DI f32x2 fma2(f32x2 a, f32x2 b, f32x2 c) { return __builtin_elementwise_fma(a, b, c); }
DI f32x2 bc2(float a) { return (f32x2){a, a}; }
DI f32x2 lo2(f32x4 v) { return (f32x2){v[0], v[1]}; }
DI f32x2 hi2(f32x4 v) { return (f32x2){v[2], v[3]}; }
DI u32 pk2v(f32x2 v) { return pk2(v[0], v[1]); }
DI f32x2 gelu2(f32x2 x) {
  f32x2 u = fma2(x * x, bc2(-0.10294324f), bc2(-2.3022082f)) * x;
  f32x2 e = {__builtin_amdgcn_exp2f(u[0]), __builtin_amdgcn_exp2f(u[1])};
  e = e + bc2(1.0f);
  f32x2 r = {__builtin_amdgcn_rcpf(e[0]), __builtin_amdgcn_rcpf(e[1])};
  return x * r;
}
DI void red2g(float& a, float& b) {
  auto s = __builtin_amdgcn_permlane32_swap(__float_as_uint(a), __float_as_uint(b), false, false);
  float c = __uint_as_float(s[0]) + __uint_as_float(s[1]);
  auto s2 = __builtin_amdgcn_permlane16_swap(__float_as_uint(c), __float_as_uint(c), false, false);
  float d = __uint_as_float(s2[0]) + __uint_as_float(s2[1]);
  auto s3 = __builtin_amdgcn_permlane32_swap(__float_as_uint(d), __float_as_uint(d), false, false);
  a = __uint_as_float(s3[0]);
  b = __uint_as_float(s3[1]);
}
template <int CTRL> DI float dpp(float v) {
  return __uint_as_float(__builtin_amdgcn_update_dpp(0, __float_as_uint(v), CTRL, 0xF, 0xF, true));
}
template <class F> DI void hedgehog16(const f32x16& p, float s, u32x4 (&o)[4], F hook) {
  float mx = fmaxf(p[0], p[1]), mn = fminf(p[0], p[1]);
#pragma unroll
  for (int i = 2; i < 16; i += 2) { mx = fmaxf(fmaxf(mx, p[i]), p[i + 1]); mn = fminf(fminf(mn, p[i]), p[i + 1]); }
  const float c = s * 1.44269504f;
  const float a = -mx * c, b = mn * c;
  f32x2 ep[8], en[8];
#pragma unroll
  for (int i = 0; i < 8; ++i) {
    hook(i);
    const f32x2 v = {p[2 * i], p[2 * i + 1]};
    const f32x2 u = fma2(v, bc2(c), bc2(a)), w = fma2(v, bc2(-c), bc2(b));
    ep[i] = (f32x2){__builtin_amdgcn_exp2f(u[0]), __builtin_amdgcn_exp2f(u[1])};
    en[i] = (f32x2){__builtin_amdgcn_exp2f(w[0]), __builtin_amdgcn_exp2f(w[1])};
  }
  hook(8);
  const f32x2 sp = ((ep[0] + ep[1]) + (ep[2] + ep[3])) + ((ep[4] + ep[5]) + (ep[6] + ep[7]));
  const f32x2 sn = ((en[0] + en[1]) + (en[2] + en[3])) + ((en[4] + en[5]) + (en[6] + en[7]));
  const float ip = __builtin_amdgcn_rcpf(sp[0] + sp[1]), in = __builtin_amdgcn_rcpf(sn[0] + sn[1]);
#pragma unroll
  for (int i = 0; i < 8; ++i) {
    if (i == 0 || i == 3 || i == 6) hook(9 + i / 3);
    o[i >> 2][i & 3] = pk2v(ep[i] * bc2(ip));
    o[2 + (i >> 2)][i & 3] = pk2v(en[i] * bc2(in));
  }
}
DI void ln_fin(float s, float q, float invn, float& r, float& m) {
  m = s * invn;
  float v = fmaxf(q * invn - m * m, 0.0f);
  float sd = rbf(__builtin_amdgcn_sqrtf(rbf(rbf(v) + 1e-5f)));
  r = __builtin_amdgcn_rcpf(sd);
  m = rbf(m);
}

#define POLL_SLEEP 6
constexpr int SPIN_CAP = 1 << 17;
DI void poll_flags(u32 voff, bool act, rsrc_t srd, u32 soff, u32 tlo, u32 thi) {
#pragma nounroll
  for (int it = 0; it < SPIN_CAP; ++it) {
    u32x2 v;
    asm volatile("buffer_load_dwordx2 %0, %1, %2, %3 offen sc1\n\ts_waitcnt vmcnt(0)" : "=&v"(v) : "v"(voff), "s"(srd), "s"(soff) : "memory");
    bool ok = !act || (v[0] == tlo && v[1] == thi);
    if (__all(ok)) break;
    __builtin_amdgcn_s_sleep(POLL_SLEEP);
  }
}
DI bool tag_ok(u32x4 q, u32 t) { return q[1] == t && q[3] == t; }
DI void poll_gran(u32x4& q, u32 voff, rsrc_t srd, u32 soff, u32 tlo) {
#pragma nounroll
  for (int it = 0; it < SPIN_CAP; ++it) {
    if (__all(tag_ok(q, tlo))) break;
    __builtin_amdgcn_s_sleep(POLL_SLEEP);
    asm volatile("buffer_load_dwordx4 %0, %1, %2, %3 offen sc1\n\ts_waitcnt vmcnt(0)" : "=&v"(q) : "v"(voff), "s"(srd), "s"(soff) : "memory");
  }
}
DI u32 opq(u32 v) { asm volatile("" : "+v"(v)); return v; }

#define STAMP(i) do { if ((i) == PROBE) asm volatile("s_memrealtime %0\n\ts_waitcnt lgkmcnt(0)" : "=s"(tprobe)::"memory"); } while (0)
#define STAMPC(i)

#define GQ_N 3
#define PREFETCH 0
#define CODE_PF 56000
#define OUT_SC1 1
#define PRIO 0
#define UROT 1
static_assert(WS_ZB % 8192 == 0, "");
#define TLB_PF 0
#define WSP_PF 0
#define VM(x) do { x; ++vq; } while (0)
#define WAITQ(mark) WAITV(vq - (mark))

template <int t>
DI u64 run(const char* __restrict__ x, const char* __restrict__ wqkv, const char* __restrict__ wphi, const char* __restrict__ wo,
            const char* __restrict__ win, const char* __restrict__ wsp, const char* __restrict__ bsp, const char* __restrict__ wout,
            char* __restrict__ out, char* ws, u64 ep) {
  u64 tprobe = 0;
  const int tid = threadIdx.x, lane = tid & 63;
  const int w = __builtin_amdgcn_readfirstlane(tid >> 6);
  const int g = lane >> 4;
  const int b = blockIdx.x, xcd = b & 7, r = b >> 3;
  const int Pl = r >> 3, cl = r & 3;
  const int P = (xcd >> 1) * 4 + Pl;
  const int rot = 2 * ((Pl + cl) & 3);
  u32 acv = 0, acb = 0; (void)acv; (void)acb;
  const rsrc_t sx = __builtin_amdgcn_make_buffer_rsrc((void*)(x + (size_t)P * 131072), 0, 131072, 0x00020000);
  const u32 vx0 = (16 * w + (lane >> 3)) * 1024 + (((lane & 7) ^ g) * 16);
  const u32 vx1 = (vx0 + 8192) ^ 64;
  const u32 w2k = w * 2048;
#define DMA_X(dst, n, e) DMA_C(w2k, (dst) + (e) * 1024, (e) ? vx1 : vx0, sx, (((n) + rot) & 7) * 128)
  const int rw = w & 3, cw = w >> 2, i16 = lane & 15;
  const int c = (xcd & 1) * 4 + cl;
  const int p = 2 * P + (cl & 1);
  const int c2 = (xcd & 1) * 4 + (t ? 3 * (cl >> 1) : 1 + (cl >> 1));
  const u32 l16 = opq(lane * 16);
  const rsrc_t sa = __builtin_amdgcn_make_buffer_rsrc((void*)(wsp + (size_t)p * 262144), 0, 262144, 0x00020000);
  const rsrc_t sws = __builtin_amdgcn_make_buffer_rsrc((void*)ws, 0, WS_END, 0x00020000);
  const u32 vA0 = (lane >> 3) * 4096 + (((lane & 7) ^ g) * 16);
  const u32 vA1 = (vA0 + 32768) ^ 64;
  const int ux0 = UROT ? (cl >> 1) : 0, ux1 = UROT ? t : 0;
  const u32 uxs = w * 512 + ux1 * 256 + ux0 * 131072;
#define DMA_U(u, base, e) DMA_CC(w2k, (base) + (e) * 1024, (e) ? vA1 : vA0, sa, "s_xor_b32", uxs, (((u) >> 1) & 3) * 128 + (((u) >> 3) * 32 + ((u) & 1) * 16) * 4096)
  const f32x4 zero4 = {0, 0, 0, 0};
  u32x4 ones4 = {0x3F803F80u, 0x3F803F80u, 0x3F803F80u, 0x3F803F80u};
  u32 tlo, thi;
  u32x4 af[4][8];
  u32x4 bfr[2][8];
  u32x2 gf;
  const u32 ard = w * 2048 + i16 * 128 + (((g >> 1) ^ (i16 >> 1)) * 16) + (g & 1) * 8;
#define READ_U(u, base)                                                                                             \
  {                                                                                                                 \
    const u32 ab_ = opq(ard + (base));                                                                              \
    _Pragma("unroll") for (int kk = 0; kk < 2; ++kk)                                                               \
      af[2 * ((u) >> 3) + ((u) & 1)][((u) & 6) + kk] = cat(lds8(ab_ ^ (64 * kk)), lds8(ab_ ^ (64 * kk + 32)));      \
  }
#define USE_EPOCH() { asm volatile("" : "+s"(ep)); ep += 1; tlo = (u32)ep; thi = (u32)(ep >> 32); PIN(ones4); }
#define EARLY_STATS(mark)                                                                                           \
  {                                                                                                                 \
    WAITQ(mark); PIN(xrow);                                                                                         \
    float s_ = 0, q_ = 0;                                                                                           \
    _Pragma("unroll") for (int e = 0; e < 4; ++e) { s_ = dot2(xrow[e], ones4[0], s_); q_ = dot2(xrow[e], xrow[e], q_); } \
    s_ += dpp<0x128>(s_); q_ += dpp<0x128>(q_);                                                                     \
    s_ += dpp<0x124>(s_); q_ += dpp<0x124>(q_);                                                                     \
    s_ += dpp<0x4E>(s_); q_ += dpp<0x4E>(q_);                                                                       \
    s_ += dpp<0xB1>(s_); q_ += dpp<0xB1>(q_);                                                                       \
    red2g(s_, q_);                                                                                                  \
    float r_, m_;                                                                                                   \
    ln_fin(s_, q_, 1.0f / 512, r_, m_);                                                                             \
    VM(st16_sc1((u32x4){__float_as_uint(r_), tlo, __float_as_uint(m_), tlo}, l16 << 22, sws, WS_XG + (P * 128 + (t * 8 + c) * 8 + w) * 16)); \
  }
#define GATHER_XS()                                                                                                 \
  WAITQ(mxg); PIN(xg);                                                                                              \
  poll_gran(xg, i16 * 16, sws, WS_XG + (P * 128 + 16 * w) * 16, tlo);                                               \
  sts4f(L_XS + (16 * w + i16) * 4, __uint_as_float(xg[0]));                                                         \
  sts4f(L_XS + 512 + (16 * w + i16) * 4, __uint_as_float(xg[2]));
#define RELANE() const int lane = opq(l16) >> 4, i16 = lane & 15, g = lane >> 4, tid = w * 64 + lane; (void)tid; (void)g; (void)i16
#define SET_FLAG(off) st8_sc1((u32x2){tlo, thi}, l16 << 22, sws, (off))
  const int gs = grp_start(Z2S, c2), gn = t ? 5 : grp_start(Z2S, c2 + 1) - gs;
  const int trot = (p & 7) >= 5 ? (p & 7) - 5 : (p & 7);
#define TILE(j, n_) (((j) + trot) >= (n_) ? (j) + trot - (n_) : (j) + trot)
  const u32 zb0 = WS_ZB + (gs * 64 + 8 * w) * 1024 + ux1 * 4096;
#define DMA_WOUT(e)                                                                                                 \
  {                                                                                                                 \
    const rsrc_t so_ = __builtin_amdgcn_make_buffer_rsrc((void*)wout, 0, 688 * 1024, 0x00020000);                   \
    const int ws_ = grp_start(Z2S, w), wn_ = grp_start(Z2S, w + 1) - ws_;                                           \
    if ((e) < 6) dma16(L_WOUT + ((e) < 4 ? 2 * w + ((e) >> 1) : 16 + w) * 2048 + ((e) & 1) * 1024, vwo, so_, ((ws_ + TILE((e) >> 1, wn_)) * 2 + ((e) & 1)) * 8192); \
    else if ((e) == 6) dma16(L_WOUT + 24 * 2048 + w * 1024, vwo, so_, ((((w >> 1) == 0 ? 5 : ((w >> 1) == 1 ? 11 : ((w >> 1) == 2 ? 27 : 64))) + TILE(3, 6)) * 2 + (w & 1)) * 8192); \
  }
#define DMA_WOUT_AT(dst, e)                                                                                         \
  {                                                                                                                 \
    const rsrc_t so_ = __builtin_amdgcn_make_buffer_rsrc((void*)wout, 0, 688 * 1024, 0x00020000);                   \
    const int ws_ = grp_start(Z2S, w), wn_ = grp_start(Z2S, w + 1) - ws_;                                           \
    dma16((dst) + ((e) - 7) * 1024, vwo, so_, ((ws_ + TILE(wn_ - 1 - (((e) - 7) >> 1), wn_)) * 2 + (((e) - 7) & 1)) * 8192); \
  }
#define VWO() const u32 vwo = ((lane >> 2) & 7) * 1024 + c2 * 128 + (lane >> 5) * 64 + (((lane & 3) ^ (((lane >> 4) & 1) << 1)) * 16)
#define LOAD_B(u, so_)                                                                                              \
  bfr[u][0] = ld16_bo<0>(l16, sws, so_); bfr[u][1] = ld16_bo<1024>(l16, sws, so_);                                  \
  bfr[u][2] = ld16_bo<2048>(l16, sws, so_); bfr[u][3] = ld16_bo<3072>(l16, sws, so_);                               \
  bfr[u][4] = ld16_bo<0>(l16, sws, (so_) ^ 4096); bfr[u][5] = ld16_bo<1024>(l16, sws, (so_) ^ 4096);                \
  bfr[u][6] = ld16_bo<2048>(l16, sws, (so_) ^ 4096); bfr[u][7] = ld16_bo<3072>(l16, sws, (so_) ^ 4096);

  if constexpr (t == 0) {
    int vq = 2;
    const rsrc_t sw = __builtin_amdgcn_make_buffer_rsrc((void*)win, 0, 512 * 2752, 0x00020000);
    const int z2s = grp_start(Z2S, c), z2n = grp_start(Z2S, c + 1) - z2s;
    const int z1s = grp_start(Z1S, c), z1n = grp_start(Z1S, c + 1) - z1s;
    u32 vw[3];
    int wch[3];
#pragma unroll
    for (int e = 0; e < 3; ++e) {
      const int sg = 2 * e + (lane >> 5), hi = sg >= 3;
      wch[e] = (sg - 3 * hi) * 4 + ((lane & 3) ^ (hi << 1));
      vw[e] = wch[e] < 2 * z2n ? (16 * w + 8 * hi + ((lane >> 2) & 7)) * 2752 + 1376 + 32 * z2s + 16 * wch[e] : 0x7FFF0000u;
    }
    const u32 w3k = w * 3072;
#define DMA_W(bk, e) DMA_C(w3k, LZ_W + ((bk) % 3) * 24576 + (e) * 1024, vw[e], sw, ((2 * (bk) + rot) & 7) * (64 * 2752))
#define UBASE(u) ((u) % 5 == 0 ? LZ_U : ((u) % 5 - 1) * 16384)
#define ISS_X(n) do { VM(DMA_X(((n) & 3) * 16384, n, 0)); VM(DMA_X(((n) & 3) * 16384, n, 1)); mx[n] = vq; } while (0)
#define ISS_U(u) do { VM(DMA_U(u, UBASE(u), 0)); VM(DMA_U(u, UBASE(u), 1)); mu[u] = vq; } while (0)
#define ISS_W(bk, e0, e1) do { _Pragma("unroll") for (int e_ = e0; e_ < e1; ++e_) VM(DMA_W(bk, e_)); mw[bk] = vq; } while (0)
    int mx[8], mw[8], mu[16];
    mx[0] = vq;
    ISS_W(0, 0, 3);
    u32x4 xrow;
    VM(xrow = ld16(l16, sx, (c * 8 + w) * 1024));
    const int mxr = vq;
    ISS_X(1);
    const u32 xa0 = opq((32 * rw + i16) * 128 + ((g ^ (i16 >> 1)) * 16)), xa1 = xa0 ^ 64;
    u32 ta[3];
    int tl[3];
    {
      const u32 trb = LZ_W + g * 1536 + ((lane >> 2) & 3) * 64 + ((((g & 1) << 1) | ((lane >> 1) & 1)) * 16) + (lane & 1) * 8;
#pragma unroll
      for (int j = 0; j < 3; ++j) {
        tl[j] = 3 * cw + (j + rw) % 3;
        ta[j] = (trb + (tl[j] >> 1) * 512) ^ ((tl[j] & 1) * 32);
      }
    }
#define NEXT_BLOCK(bk) _Pragma("unroll") for (int j = 0; j < 3; ++j) ta[j] = opq(ta[j] + ((bk) == 0 ? 0 : ((bk) % 3 == 0 ? -49152 : 24576)))
    f32x4 acc[2][3], csa = zero4;
#pragma unroll
    for (int j = 0; j < 3; ++j) acc[0][j] = acc[1][j] = zero4;
    u32x4 xf[16][2], wf[2][3];
    u32x4 xg;
    int mxg = 0;
    WAITQ(mw[0]);
    BARRIER();
    USE_EPOCH()
    STAMP(7);
#define P1_MEM(n, h)                                                                                                \
  {                                                                                                                 \
    if ((n) == 0) { if ((h) == 0) ISS_X(2); if ((h) == 1) ISS_W(1, 0, 2); if ((h) == 2) ISS_W(1, 2, 3); if ((h) == 3) ISS_X(3); } \
    if ((n) == 1) { if ((h) == 0) ISS_W(2, 0, 2); if ((h) == 1) ISS_W(2, 2, 3); if ((h) == 2) VM(DMA_X(0, 4, 0)); if ((h) == 3) { VM(DMA_X(0, 4, 1)); mx[4] = vq; } } \
    if ((n) == 2 || (n) == 4) { if ((h) < 2) { VM(DMA_X((((n) + 3) & 3) * 16384, (n) + 3, (h) & 1)); mx[(n) + 3] = vq; } else ISS_W((n) / 2 + 2, (h) - 2, (h) - 1); } \
    if ((n) == 3) { if ((h) == 0) ISS_W(3, 2, 3); else if ((h) < 3) { VM(DMA_X(32768, 6, (h) - 1)); mx[6] = vq; } else ISS_U(0); } \
    if ((n) == 5) { if ((h) == 0) ISS_W(4, 2, 3); if ((h) == 1) { VM(xg = ld16_sc1(i16 * 16, sws, WS_XG + (P * 128 + 16 * w) * 16)); mxg = vq; } \
                    if ((h) >= 2) { VM(DMA_U(1, UBASE(1), (h) - 2)); mu[1] = vq; } }                                 \
    if ((n) == 6) { if ((h) < 3) ISS_W(5, h, (h) + 1); else ISS_U(2); }                                             \
    if ((n) == 7) { if ((h) < 2) { VM(DMA_U(3, UBASE(3), h)); mu[3] = vq; } }                                       \
  }
#pragma unroll
    for (int ks = 0; ks <= 16; ++ks) {
      const int n = ks >> 1, kk = ks & 1;
      if (ks > 0 && ks < 16 && kk == 0) {
        { const int mk = (n & 1) ? mx[n] : (mx[n] > mw[n >> 1] ? mx[n] : mw[n >> 1]); WAITQ(mk); }
        BARRIER();
      }
      if (ks < 16 && (ks & 3) == 0) { NEXT_BLOCK(ks >> 2); }
      if (ks == 8) {
#pragma unroll
        for (int e = 0; e < 3; ++e)
          vw[e] = wch[e] < 2 * z1n ? (16 * w + 8 * (2 * e + (lane >> 5) >= 3) + ((lane >> 2) & 7)) * 2752 + 32 * z1s + 16 * wch[e] : 0x7FFF0000u;
      }
      const int k1 = ks > 0 ? ks - 1 : 0, q1 = k1 & 1;
#pragma unroll
      for (int m = 0; m < 7; ++m) {
        if (ks > 0) {
          if (m < 6) acc[m & 1][m >> 1] = mfma32(xf[k1][m & 1], wf[q1][m >> 1], acc[m & 1][m >> 1]);
          else csa = mfma32(ones4, wf[q1][0], csa);
          SCHED();
        }
        if (ks < 16) {
          if (m == 0) { P1_MEM(n, 2 * kk) xf[ks & 15][0] = lds16((kk ? xa1 : xa0) + (n & 3) * 16384); xf[ks & 15][1] = lds16((kk ? xa1 : xa0) + (n & 3) * 16384 + 2048); }
          else if (m < 4) wf[kk][m - 1] = ldfrag(ta[m - 1] + (ks & 3) * 6144, 256);
          if (m == 4) P1_MEM(n, 2 * kk + 1)
        }
        SCHED();
      }
#pragma unroll
      for (int j = 0; j < 3; ++j) { PIN(acc[0][j]); PIN(acc[1][j]); }
      PIN(csa);
      if (ks == 2) EARLY_STATS(mxr)
    }
    GATHER_XS()
    sts4f(L_CS + tl[0] * 64 + i16 * 4, csa[0]);
    WAITQ(mu[1] > mw[4] ? mu[1] : mw[4]);
    READ_U(0, UBASE(0)) READ_U(1, UBASE(1))
    BARRIER();
    STAMP(1);
    u32x4 zp[3];
    ISS_W(6, 0, 3);
    {
      f32x2 ps[4], pq[4];
      float cs[3];
#pragma unroll
      for (int j = 0; j < 3; ++j) cs[j] = -lds4f(L_CS + tl[j] * 64 + i16 * 4);
#pragma unroll
      for (int rt = 0; rt < 2; ++rt) {
        const f32x4 rr = lds16f(L_XS + (32 * rw + 16 * rt + 4 * g) * 4), mm = lds16f(L_XS + 512 + (32 * rw + 16 * rt + 4 * g) * 4);
        f32x2 z0[3], z1[3];
#pragma unroll
        for (int j = 0; j < 3; ++j) {
          z0[j] = gelu2(fma2(lo2(mm), bc2(cs[j]), lo2(acc[rt][j])) * lo2(rr));
          z1[j] = gelu2(fma2(hi2(mm), bc2(cs[j]), hi2(acc[rt][j])) * hi2(rr));
          zp[j][2 * rt] = pk2v(z0[j]); zp[j][2 * rt + 1] = pk2v(z1[j]);
        }
        ps[2 * rt] = z0[0] + z0[1] + z0[2]; ps[2 * rt + 1] = z1[0] + z1[1] + z1[2];
        pq[2 * rt] = fma2(z0[2], z0[2], fma2(z0[1], z0[1], z0[0] * z0[0]));
        pq[2 * rt + 1] = fma2(z1[2], z1[2], fma2(z1[1], z1[1], z1[0] * z1[0]));
        SCHED();
        if (rt == 0) { ISS_U(4); ISS_U(5); }
        else ISS_U(6);
        SCHED();
      }
      const bool b3 = lane & 8, b2 = lane & 4, b1 = lane & 2;
#pragma unroll
      for (int m = 0; m < 2; ++m)
#pragma unroll
        for (int e = 0; e < 2; ++e) {
          ps[m][e] = (b3 ? ps[m + 2][e] : ps[m][e]) + dpp<0x128>(b3 ? ps[m][e] : ps[m + 2][e]);
          pq[m][e] = (b3 ? pq[m + 2][e] : pq[m][e]) + dpp<0x128>(b3 ? pq[m][e] : pq[m + 2][e]);
        }
#pragma unroll
      for (int e = 0; e < 2; ++e) {
        ps[0][e] = (b2 ? ps[1][e] : ps[0][e]) + dpp<0x141>(b2 ? ps[0][e] : ps[1][e]);
        pq[0][e] = (b2 ? pq[1][e] : pq[0][e]) + dpp<0x141>(b2 ? pq[0][e] : pq[1][e]);
      }
      float s_ = (b1 ? ps[0][1] : ps[0][0]) + dpp<0x4E>(b1 ? ps[0][0] : ps[0][1]);
      float q_ = (b1 ? pq[0][1] : pq[0][0]) + dpp<0x4E>(b1 ? pq[0][0] : pq[0][1]);
      s_ += dpp<0xB1>(s_);
      q_ += dpp<0xB1>(q_);
      const int row = 32 * rw + 16 * (i16 >> 3) + 4 * g + ((i16 >> 1) & 3);
      VM(st16_sc1((u32x4){__float_as_uint(s_), tlo, __float_as_uint(q_), tlo}, row * 16 + ((lane & 1) << 30), sws, WS_SG + ((P * 8 + c) * 2 + cw) * 2048));
      PIN(zp[0]); PIN(zp[1]); PIN(zp[2]);
    }
    STAMP(2);
    {
    RELANE();
#pragma unroll
    for (int j = 0; j < 3; ++j) acc[0][j] = acc[1][j] = zero4;
    csa = zero4;
    u32x4 q0, q1, q2, q3;
    int mq = 0, mz = 0;
    const u32 gvo = (2 * g) * 4096 + (32 * rw + 16 * cw + i16) * 16;
    const u32 gso = WS_SG + P * 32768;
#pragma unroll
    for (int ks = 0; ks <= 16; ++ks) {
      if (ks == 8) {
        WAITQ(mq); PIN(q0); PIN(q1); PIN(q2); PIN(q3);
#pragma nounroll
        for (int it = 0; it < SPIN_CAP; ++it) {
          if (__all(tag_ok(q0, tlo) && tag_ok(q1, tlo) && tag_ok(q2, tlo) && tag_ok(q3, tlo))) break;
          __builtin_amdgcn_s_sleep(POLL_SLEEP);
          asm volatile(
              "buffer_load_dwordx4 %0, %4, %5, %6 offen sc1\n\t"
              "buffer_load_dwordx4 %1, %4, %5, %6 offen offset:2048 sc1\n\t"
              "buffer_load_dwordx4 %2, %4, %5, %7 offen sc1\n\t"
              "buffer_load_dwordx4 %3, %4, %5, %7 offen offset:2048 sc1\n\t"
              "s_waitcnt vmcnt(0)"
              : "=&v"(q0), "=&v"(q1), "=&v"(q2), "=&v"(q3)
              : "v"(gvo), "s"(sws), "s"(gso), "s"(gso + 4096)
              : "memory");
        }
        float s = (__uint_as_float(q0[0]) + __uint_as_float(q1[0])) + (__uint_as_float(q2[0]) + __uint_as_float(q3[0]));
        float q = (__uint_as_float(q0[2]) + __uint_as_float(q1[2])) + (__uint_as_float(q2[2]) + __uint_as_float(q3[2]));
        red2g(s, q);
        float r2, m2;
        ln_fin(s, q, 1.0f / 688, r2, m2);
        sts4f(L_ZST + (32 * rw + 16 * cw + i16) * 4, r2);
        sts4f(L_ZST + 512 + (32 * rw + 16 * cw + i16) * 4, -m2 * r2);
        STAMP(3);
      }
      if (ks < 16 && (ks & 3) == 0) {
        if (ks > 0) { WAITQ(ks == 12 ? mz : mw[4 + (ks >> 2)]); BARRIER(); }
        if (ks == 12) { if (w == 0) SET_FLAG(WS_FA + (P * 8 + c) * 128); STAMP(5); }
        NEXT_BLOCK(4 + (ks >> 2));
      }
      if (ks == 8) {
#pragma unroll
        for (int rt = 0; rt < 2; ++rt) {
          const u32 ra = L_ZST + (32 * rw + 16 * rt + 4 * g) * 4;
          const f32x4 r4 = lds16f(ra), m4 = lds16f(ra + 512);
#pragma unroll
          for (int j = 0; j < 3; ++j) {
            const u32 u0 = zp[j][2 * rt], u1 = zp[j][2 * rt + 1];
            zp[j][2 * rt] = pk2v(fma2((f32x2){blo(u0), bhi(u0)}, lo2(r4), lo2(m4)));
            zp[j][2 * rt + 1] = pk2v(fma2((f32x2){blo(u1), bhi(u1)}, hi2(r4), hi2(m4)));
          }
        }
#pragma unroll
        for (int j = 0; j < 3; ++j)
          VM(st16_sc1(zp[j], l16, sws, (tl[j] < z2n) ? WS_ZB + ((z2s + tl[j]) * 64 + 4 * P + rw) * 1024 : 0x7FFF0000u));
        mz = vq;
        STAMP(4);
        SCHED();
      }
      if (ks >= 5 && ks <= 9) { WAITQ(mu[ks - 3]); READ_U(ks - 3, UBASE(ks - 3)) }
      if (ks >= 9 && ks <= 13) { WAITQ(mu[ks - 2]); READ_U(ks - 2, UBASE(ks - 2)) }
      if (ks >= 13) { WAITQ(mu[ks - 1]); READ_U(ks - 1, UBASE(ks - 1)) }
      const int k1 = ks > 0 ? ks - 1 : 0, q1_ = k1 & 1;
#pragma unroll
      for (int m = 0; m < 7; ++m) {
        if (ks > 0) {
          if (m < 6) acc[m & 1][m >> 1] = mfma32(wf[q1_][m >> 1], xf[k1][m & 1], acc[m & 1][m >> 1]);
          else csa = mfma32(ones4, wf[q1_][0], csa);
          SCHED();
        }
        if (ks < 16 && m >= 1 && m < 4) wf[ks & 1][m - 1] = ldfrag(ta[m - 1] + (ks & 3) * 6144, 256);
        if (m == 0 || m == 4) {
          const int h = m == 4;
          if (h == 0 && ks >= 6 && ks <= 13) asm volatile("s_waitcnt lgkmcnt(10)" ::: "memory");
          if (ks >= 6 && ks <= 10) { VM(DMA_U(ks + 1, UBASE(ks + 1), h)); mu[ks + 1] = vq; }
          if (ks >= 10 && ks <= 13) { VM(DMA_U(ks + 2, UBASE(ks + 2), h)); mu[ks + 2] = vq; }
        }
        if (m == 6) {
          if (ks >= 4 && ks <= 6) ISS_W(7, ks - 4, ks - 3);
        }
        if (m == 2) {
          if (ks == 5) { VM(q0 = ld16_sc1o<0>(gvo, sws, gso)); VM(q1 = ld16_sc1o<2048>(gvo, sws, gso)); }
          if (ks == 6) { VM(q2 = ld16_sc1o<0>(gvo, sws, gso + 4096)); VM(q3 = ld16_sc1o<2048>(gvo, sws, gso + 4096)); mq = vq; }
        }
        SCHED();
      }
#pragma unroll
      for (int j = 0; j < 3; ++j) { PIN(acc[0][j]); PIN(acc[1][j]); }
      PIN(csa);
    }
    sts4f(L_CS + 512 + tl[0] * 64 + i16 * 4, csa[0]);
    gf = ld8_sc1((lane >> 5) * 1024, sws, WS_FA + (16 * w + c2) * 128);
    BARRIER();
    VWO();
    DMA_WOUT(0) DMA_WOUT(1)
    WAITV(2);
    PIN(gf);
    if (!__all(gf[0] == tlo && gf[1] == thi)) poll_flags((lane >> 5) * 1024, true, sws, WS_FA + (16 * w + c2) * 128, tlo, thi);
    STAMP(6);
    {
      float xr[2], xm[2];
#pragma unroll
      for (int rt = 0; rt < 2; ++rt) {
        xr[rt] = lds4f(L_XS + (32 * rw + 16 * rt + i16) * 4);
        xm[rt] = -lds4f(L_XS + 512 + (32 * rw + 16 * rt + i16) * 4);
      }
      const u32 so0 = zb0 + TILE(0, gn) * 65536;
#pragma unroll
      for (int j = 0; j < 3; ++j) {
        const f32x4 cc = lds16f(L_CS + 512 + tl[j] * 64 + g * 16);
        bfr[0][3 * j] = ld16_b(l16, sws, so0 ^ (3 * j * 1024));
        bfr[0][3 * j + 1] = ld16_b(l16, sws, so0 ^ ((3 * j + 1) * 1024));
        if (j < 2) bfr[0][3 * j + 2] = ld16_b(l16, sws, so0 ^ ((3 * j + 2) * 1024));
        u32x4 zz;
#pragma unroll
        for (int rt = 0; rt < 2; ++rt) {
          zz[2 * rt] = pk2v(gelu2(fma2(lo2(cc), bc2(xm[rt]), lo2(acc[rt][j])) * bc2(xr[rt])));
          zz[2 * rt + 1] = pk2v(gelu2(fma2(hi2(cc), bc2(xm[rt]), hi2(acc[rt][j])) * bc2(xr[rt])));
        }
        st16_sc1(zz, l16, sws, (tl[j] < z1n) ? WS_Z1 + ((4 * P + rw) * 43 + z1s + tl[j]) * 1024 : 0x7FFF0000u);
        SCHED();
      }
    }
    }
  } else {
    int vq = 2;
    const rsrc_t sw = __builtin_amdgcn_make_buffer_rsrc((void*)wqkv, 0, 512 * 3072, 0x00020000);
    const u32 vw = (8 * w + ((lane >> 2) & 7)) * 3072 + 128 * c + ((lane >> 5) * 4 + ((lane & 3) ^ ((w & 1) << 1))) * 16;
    const u32 w3k = w * 3072;
#define DMA_S(n, e)                                                                                                 \
  do {                                                                                                              \
    if ((e) < 2) DMA_X(((n) % 3) * 40960, n, (e) & 1);                                                              \
    else DMA_CC(w3k, ((n) % 3) * 40960 + 16384 + ((e) - 2) * 1024, vw, sw, "s_add_u32", (((n) + rot) & 7) * (64 * 3072), ((e) - 2) * 1024); \
  } while (0)
#define ISS_Q(u, base) do { VM(DMA_U(u, base, 0)); VM(DMA_U(u, base, 1)); mu[u] = vq; } while (0)
    int ms[8], mu[16];
    VM(DMA_S(0, 2)); VM(DMA_S(0, 3)); VM(DMA_S(0, 4)); ms[0] = vq;
    u32x4 xrow;
    VM(xrow = ld16(l16, sx, ((8 + c) * 8 + w) * 1024));
    const int mxr = vq;
#pragma unroll
    for (int e = 0; e < 5; ++e) { VM(DMA_S(1, e)); ms[1] = vq; }
    const int l32 = lane & 31, hh = lane >> 5;
    u32 xa = (32 * rw + l32) * 128 + ((hh ^ ((l32 >> 1) & 7)) * 16);
    u32 wa = 16384 + hh * 3072 + cw * 512 + ((lane >> 2) & 3) * 64 + (((((g & 1) << 1) | ((lane >> 1) & 1)) ^ (hh << 1)) * 16) + (lane & 1) * 8;
    u32 ca = 16384 + g * 3072 + min(w, 5) * 512 + ((lane >> 2) & 3) * 64 + ((((g & 1) << 1) | ((lane >> 1) & 1)) * 16) + (lane & 1) * 8;
    const f32x16 zero16 = {0, 0, 0, 0, 0, 0, 0, 0, 0, 0, 0, 0, 0, 0, 0, 0};
    f32x16 acc[3] = {zero16, zero16, zero16};
    f32x4 csa[2] = {zero4, zero4};
    u32x4 xg;
    int mxg = 0;
    WAITQ(ms[0]);
    BARRIER();
    USE_EPOCH()
    STAMP(7);
    {
      u32x4 xf[2], wf[2][4];
#pragma unroll
      for (int n = 0; n <= 8; ++n)
#pragma unroll
      for (int sq = 0; sq < 4; ++sq) {
        const int s = 4 * n + sq;
        if (s > 32) continue;
        if (s < 32 && sq == 0) {
          if (n > 0) {
            WAITQ(ms[n]);
            BARRIER();
          }
          const int d = n == 0 ? 0 : (n % 3 == 0 ? -81920 : 40960);
          xa = opq(xa + d); wa = opq(wa + d); ca = opq(ca + d);
        }
        const int q1 = (s + 1) & 1, q0 = s & 1;
#pragma unroll
        for (int m = 0; m < 4; ++m) {
          if (s > 0) {
            if (m < 3) acc[m] = mfmaL(wf[q1][m], xf[q1], acc[m]);
            else csa[q1] = mfma32(ones4, wf[q1][3], csa[q1]);
            SCHED();
          }
          if (s < 32) {
            if (m == 0) { xf[q0] = lds16(xa ^ (32 * sq)); wf[q0][0] = ldfrag(wa + sq * 6144, 256); }
            if (m == 1) { wf[q0][1] = ldfrag(wa + sq * 6144 + 1024, 256); wf[q0][2] = ldfrag(wa + sq * 6144 + 2048, 256); }
            if (m == 2) wf[q0][3] = ldfrag((ca ^ (32 * (sq & 1))) + (sq >> 1) * 12288, 256);
            if (m == 0 || m == 2) {
              const int h = 2 * sq + (m == 2);
              if (n <= 5 && h < 5) { VM(DMA_S(n + 2, h)); ms[n + 2] = vq; }
              if (n == 0 && h == 5) {
                rsrc_t sp = __builtin_amdgcn_make_buffer_rsrc((void*)wphi, 0, 16384, 0x00020000);
                VM(dma16(L_WPHI + (w & 1) * 1024, l16, sp, c * 2048 + (w & 1) * 1024));
              }
              if (WSP_PF && h == 6 && (n == WSP_PF || n == WSP_PF + 2))
                VM(touch(L_JUNK, l16 * 8, sa, (((cl >> 1) * 2 + (n != WSP_PF)) * 8 + w) * 8192));
              if (n == 4 && h == 5) { VM(xg = ld16_sc1(i16 * 16, sws, WS_XG + (P * 128 + 16 * w) * 16)); mxg = vq; }
              if (n == 6) { VM(DMA_U(sq, (sq < 2 ? LQ_M : LQ_A) + (sq & 1) * 16384, h & 1)); mu[sq] = vq; }
              if (n == 7 && sq < 2) { VM(DMA_U(4 + sq, sq * 16384, h & 1)); mu[4 + sq] = vq; }
            }
          }
          SCHED();
        }
        PIN(acc[0]); PIN(acc[1]); PIN(acc[2]); PIN(csa[0]); PIN(csa[1]);
        if (s == 3) EARLY_STATS(mxr)
      }
    }
    GATHER_XS()
    sts4f(L_CS + w * 128 + i16 * 4, csa[0][0]);
    sts4f(L_CS + w * 128 + 64 + i16 * 4, csa[1][0]);
    BARRIER();
    STAMP(1);
    WAITQ(mu[3]);
    READ_U(0, LQ_M) READ_U(1, LQ_M + 16384) READ_U(2, LQ_A) READ_U(3, LQ_A + 16384)
    const float xr = lds4f(L_XS + (32 * rw + l32) * 4), xm = -lds4f(L_XS + 512 + (32 * rw + l32) * 4);
    u32x4 wp0, wp1;
    {
      const u32 a = L_WPHI + (2 * cw + (lane & 1)) * 512 + (4 * hh + ((lane >> 2) & 3)) * 32 + (2 * (g & 1) + ((lane >> 1) & 1)) * 8;
      const u32x4 f = ldfrag(a, 256);
      const bool own = (lane >> 2) & 1;
#pragma unroll
      for (int e = 0; e < 4; ++e) { wp0[e] = own ? 0u : f[e]; wp1[e] = own ? f[e] : 0u; }
    }
#define FEATURES(e_, dst, hook)                                                                                         \
  {                                                                                                                 \
    u32x4 kp[2];                                                                                                    \
    _Pragma("unroll") for (int a = 0; a < 4; ++a) {                                                                \
      const f32x4 cc = lds16f(L_CS + (2 * (e_) + cw) * 128 + a * 32 + hh * 16);                                      \
      kp[a >> 1][2 * (a & 1)] = pk2v(fma2(lo2(cc), bc2(xm), (f32x2){acc[e_][4 * a], acc[e_][4 * a + 1]}));          \
      kp[a >> 1][2 * (a & 1) + 1] = pk2v(fma2(hi2(cc), bc2(xm), (f32x2){acc[e_][4 * a + 2], acc[e_][4 * a + 3]}));  \
    }                                                                                                               \
    f32x16 pl = mfmaL(wp0, kp[0], zero16);                                                                          \
    pl = mfmaL(wp1, kp[1], pl);                                                                                     \
    hedgehog16(pl, xr, dst, hook);                                                                                  \
  }
    {
      const int s_ = 32 * rw + l32;
      const u32 key = (((s_ >> 1) & 3) ^ (((s_ >> 3) & 1) << 1)) * 16;
      {
        const u32 va = LQ_V + (s_ >> 3) * 1024 + cw * 512 + (s_ & 7) * 64 + hh * 8;
#pragma unroll
        for (int a = 0; a < 4; ++a) {
          const f32x4 cc = lds16f(L_CS + (4 + cw) * 128 + a * 32 + hh * 16);
          sts8(va + ((a * 16) ^ key), (u32x2){pk2v(fma2(lo2(cc), bc2(xm), (f32x2){acc[2][4 * a], acc[2][4 * a + 1]}) * bc2(xr)),
                                              pk2v(fma2(hi2(cc), bc2(xm), (f32x2){acc[2][4 * a + 2], acc[2][4 * a + 3]}) * bc2(xr))});
        }
      }
      WAITQ(mu[5]);
      READ_U(4, 0) READ_U(5, 16384)
      WAITL0();
      u32x4 ph[4];
      auto hk = [&](int i) __attribute__((always_inline)) {
        const int u = 6 + (i >> 1);
        SCHED();
        VM(DMA_U(u, (u < 8 ? LQ_M : (u < 10 ? LQ_A : 0)) + (u & 1) * 16384, i & 1));
        mu[u] = vq;
        SCHED();
      };
      FEATURES(1, ph, hk)
      const u32 pa = LQ_PHI + (s_ >> 3) * 2048 + (2 * cw + hh) * 512 + (s_ & 7) * 64;
#pragma unroll
      for (int a = 0; a < 4; ++a) sts16(pa + ((a * 16) ^ key), ph[a]);
    }
    BARRIER();
    STAMP(2);
    {
      const u32 kx = (((lane >> 3) & 1) ^ ((g & 1) << 1)) * 16;
      const u32 pa = LQ_PHI + g * 2048 + (w >> 1) * 512 + ((lane >> 2) & 3) * 64 + (((2 * (w & 1) + ((lane >> 1) & 1)) * 16) ^ kx) + (lane & 1) * 8;
      const u32 va = LQ_V + g * 1024 + (w >> 2) * 512 + ((lane >> 2) & 3) * 64 + (((2 * ((w >> 1) & 1) + ((lane >> 1) & 1)) * 16) ^ kx) + (lane & 1) * 8;
      u32x4 pf[4], vf[4];
#pragma unroll
      for (int k = 0; k < 4; ++k) {
        pf[k] = cat(ldstr(pa + k * 8192), ldstr((pa ^ 32) + k * 8192 + 256));
        vf[k] = cat(ldstr(va + k * 4096), ldstr((va ^ 32) + k * 4096 + 256));
      }
      f32x4 kv = zero4, kz = zero4;
#pragma unroll
      for (int k = 0; k < 4; ++k) { kv = mfma32(pf[k], vf[k], kv); kz = mfma32(pf[k], ones4, kz); }
      const u32 so = WS_KP + (c * 16 + P) * KP_SZ;
      u32x4 g0 = {__float_as_uint(kv[0]), tlo, __float_as_uint(kv[1]), tlo}, g1 = {__float_as_uint(kv[2]), tlo, __float_as_uint(kv[3]), tlo};
      u32x4 g2 = {__float_as_uint((lane & 1) ? kz[2] : kz[0]), tlo, __float_as_uint((lane & 1) ? kz[3] : kz[1]), tlo};
      PIN(g0); PIN(g1); PIN(g2);
      asm volatile("s_nop 7" ::: "memory");
      VM(st16_sc1(g0, l16, sws, so + w * 2048));
      VM(st16_sc1(g1, l16, sws, so + w * 2048 + 1024));
      VM(st16_sc1(g2, g * 32 + (lane & 1) * 16 + ((i16 >> 1) << 26), sws, so + 16384 + w * 128));
    }
    STAMP(3);
    WAITQ(mu[11]);
    READ_U(6, LQ_M) READ_U(7, LQ_M + 16384) READ_U(8, LQ_A) READ_U(9, LQ_A + 16384) READ_U(10, 0) READ_U(11, 16384)
    BARRIER();
    ISS_Q(12, LQ_M); VM(DMA_U(13, LQ_M + 16384, 0));
    u32x4 phq[4];
    {
      VWO();
      auto hk = [&](int i) __attribute__((always_inline)) {
        SCHED();
        if (i == 0) DMA_U(13, LQ_M + 16384, 1);
        else if (i < 5) DMA_U(14 + ((i - 1) >> 1), LQ_A + ((i - 1) >> 1) * 16384, (i - 1) & 1);
        else DMA_WOUT(i - 5)
        SCHED();
      };
      FEATURES(0, phq, hk)
    }
    PIN(phq[0]); PIN(phq[1]); PIN(phq[2]); PIN(phq[3]);
    STAMP(4);
    if (w >= 5) {
      const int id = 2 * P + (w == 7 ? (lane & 1) : w - 5), wq = id >> 2, gq = id & 3;
      const u32 vi0 = (4 * g) * KP_SZ + (w == 7 ? 16384 + wq * 128 + gq * 32 : wq * 2048 + (gq * 16 + i16) * 16);
      const u32 vi1 = vi0 + (w == 7 ? 16 : 1024);
      const u32 vo = (w == 7 ? (WS_KZ - WS_KF) + c * 512 + id * 16 + ((i16 >> 1) << 25) : c * 8192 + id * 256 + i16 * 16) + (g << 28);
      float sa[4];
#pragma nounroll
      for (int it = 0; it < SPIN_CAP; ++it) {
        u32x4 a[4][2];
#pragma unroll
        for (int o = 0; o < 4; ++o) {
          a[o][0] = ld16_sc1(vi0, sws, WS_KP + (c * 16 + o) * KP_SZ);
          a[o][1] = ld16_sc1(vi1, sws, WS_KP + (c * 16 + o) * KP_SZ);
        }
        WAITV(0);
        bool ok = true;
#pragma unroll
        for (int o = 0; o < 4; ++o) { PIN(a[o][0]); PIN(a[o][1]); ok = ok && tag_ok(a[o][0], tlo) && tag_ok(a[o][1], tlo); }
#pragma unroll
        for (int e = 0; e < 4; ++e)
          sa[e] = (__uint_as_float(a[0][e >> 1][2 * (e & 1)]) + __uint_as_float(a[1][e >> 1][2 * (e & 1)])) +
                  (__uint_as_float(a[2][e >> 1][2 * (e & 1)]) + __uint_as_float(a[3][e >> 1][2 * (e & 1)]));
        if (__all(ok)) break;
        __builtin_amdgcn_s_sleep(POLL_SLEEP);
      }
      red2g(sa[0], sa[1]);
      red2g(sa[2], sa[3]);
      st16_sc1((u32x4){pk2(sa[0], sa[1]), tlo, pk2(sa[2], sa[3]), tlo}, vo, sws, WS_KF);
    }
    gf = ld8_sc1((lane >> 5) * 1024, sws, WS_FA + (16 * w + c2) * 128);
    WAITV(8);
    READ_U(12, LQ_M) READ_U(13, LQ_M + 16384) READ_U(14, LQ_A) READ_U(15, LQ_A + 16384)
    BARRIER();
#pragma unroll
    for (int a = 0; a < 4; ++a) sts16(L_WO1 + w * 4096 + a * 1024 + l16, phq[a]);
    WAITV(0);
    PIN(gf);
    if (!__all(gf[0] == tlo && gf[1] == thi)) poll_flags((lane >> 5) * 1024, true, sws, WS_FA + (16 * w + c2) * 128, tlo, thi);
    STAMP(6);
    { const u32 so_ = zb0 + TILE(0, gn) * 65536; LOAD_B(0, so_) }
  }

  {
  RELANE();
  u32x4 kfg, kzg;
  const u32 kfo = WS_KF + c * 8192 + w * 1024, kzo = WS_KZ + c * 512 + w * 64;
  if (t != 0) {
    kfg = ld16_sc1(l16, sws, kfo);
    kzg = ld16_sc1(g * 16, sws, kzo);
  }
  { const u32 so_ = zb0 + TILE(1, gn) * 65536; LOAD_B(1, so_) }
  STAMP(9);
  const u32 redw = opq(L_RED + w * 4096 + lane * 16), redr = opq(L_RED + (rw ^ (2 * ux0)) * 1024 + lane * 16);
  const u32 fd = WS_FD + (p * 16 + c2) * 128;
  const u32 f3s = __builtin_amdgcn_readfirstlane(WS_FB + (P * 8 + (c2 > 0 ? c2 - 1 : 0)) * 128);
  const u32 vw3 = ((lane >> 2) & 7) * 1024 + c2 * 128 + (lane >> 5) * 64;
  const rsrc_t swo = __builtin_amdgcn_make_buffer_rsrc((void*)wo, 0, 512 * 1024, 0x00020000);
#define DMA_WO(dst, half, e) dma16((dst) + w * 4096 + (e) * 1024, (vw3 + (lane & 3) * 16) ^ (((e) & 1) * 32), swo, ((w + 8 * (half)) * 4 + (e)) * 8192)
#define FV() ((lane & 32) ? (P * 8 + ((lane >> 2) & 7)) * 128 : (WS_FD - WS_FC) + (p * 16 + (((lane >> 1) & 14) | cw)) * 128)
  u32x2 z1r[6], hold;
  u32 bs;
  f32x4 sm0;
#define EMIT_H(jt, sm_)                                                                                             \
  {                                                                                                                 \
    const f32x2 bb = bc2(blo(bs));                                                                                  \
    const u32 h0 = pk2v((f32x2){blo(z1r[jt][0]), bhi(z1r[jt][0])} * (lo2(sm_) + bb));                               \
    const u32 h1 = pk2v((f32x2){blo(z1r[jt][1]), bhi(z1r[jt][1])} * (hi2(sm_) + bb));                               \
    const u32 hb_ = WS_HF + (((4 * p + rw) * 2 + ((jt) >> 1)) * 8 + c2) * 1024;                                     \
    if ((jt) == 0 || ((jt) == 2 && gn == 6)) hold = (u32x2){h0, h1};                                                \
    else if ((jt) == 1 || ((jt) == 3 && gn == 6)) st16_sc1((u32x4){hold[0], hold[1], h0, h1}, l16, sws, hb_);       \
    else if ((jt) == 2) st16_sc1((u32x4){h0, h1, 0u, 0u}, l16, sws, hb_);                                           \
    else st16_sc1((u32x4){h0, tlo, h1, tlo}, l16, sws, WS_HL + (((4 * p + rw) * 8 + c2) * 2 + (gn - 1 - (jt))) * 1024); \
  }
#pragma unroll
  for (int j = 0; j < 6; ++j) {
    if (j < 5 || gn == 6) {
      const int u = j & 1;
      if (j == 0) WAITV(8);
      if (j == 2) WAITV(t ? 10 : 8);
      if (j == 1) { WAITV(9); WAITV_IF(cw, 1, 8); }
      if (j == 3) { WAITV(9); WAITV_IF(cw, 1, 8); }
      if (j == 4) { if (t) WAITV(4); else { WAITV(12); WAITV_IF(gn, 5, 4); } }
      if (j == 5) { WAITV(10); WAITV_IF(cw, 1, 9); }
#pragma unroll
      for (int i = 0; i < 8; ++i) PIN(bfr[u][i]);
      {
        f32x4 a_[4];
#pragma unroll
        for (int rt = 0; rt < 4; ++rt) {
          a_[rt] = zero4;
#pragma unroll
          for (int i = 0; i < 8; ++i) a_[rt] = mfma32(bfr[u][i], af[rt][i], a_[rt]);
          SCHED();
          if (rt > 0) sts16f(redw + u * 32768 + (rt - 1) * 1024, a_[rt - 1]);
          SCHED();
        }
        if (j == 0 && t != 0) {
          PIN(kfg); PIN(kzg);
          poll_gran(kfg, l16, sws, kfo, tlo);
          poll_gran(kzg, g * 16, sws, kzo, tlo);
          sts8(L_KFS + (w >> 1) * 1024 + i16 * 64 + (w & 1) * 32 + g * 8, (u32x2){kfg[0], kfg[2]});
          sts8(L_KFS + 4096 + w * 32 + g * 8, (u32x2){kzg[0], kzg[2]});
          sts8(L_KFS + 4352 + (lane & 7) * 8, (u32x2){0u, 0u});
        }
        sts16f(redw + u * 32768 + 3072, a_[3]);
      }
      u32x4 of[2];
      if (j == 1 && t != 0) {
        const int l32 = lane & 31, hh = lane >> 5;
        const bool own = ((lane >> 2) & 1) == hh;
        const u32 ka = own ? L_KFS + (2 * cw + hh) * 1024 + ((l32 & 3) + 4 * (l32 >> 3)) * 64 : L_KFS + 4352;
        const u32 zaa = own ? L_KFS + 4096 + (2 * cw + hh) * 64 : L_KFS + 4352;
        const u32 qa = L_WO1 + w * 4096 + l16;
        const f32x16 zero16 = {0, 0, 0, 0, 0, 0, 0, 0, 0, 0, 0, 0, 0, 0, 0, 0};
        f32x16 den = zero16, num = zero16;
#pragma unroll
        for (int a = 0; a < 4; ++a) {
          const u32x4 pq_ = lds16(qa + a * 1024);
          den = mfmaL(lds16(zaa + a * 16), pq_, den);
          num = mfmaL(lds16(ka + a * 16), pq_, num);
        }
        const float inv = __builtin_amdgcn_rcpf(den[0] + 1e-6f);
#pragma unroll
        for (int a = 0; a < 2; ++a) {
          of[a] = (u32x4){pk2(num[8 * a] * inv, num[8 * a + 1] * inv), pk2(num[8 * a + 2] * inv, num[8 * a + 3] * inv),
                          pk2(num[8 * a + 4] * inv, num[8 * a + 5] * inv), pk2(num[8 * a + 6] * inv, num[8 * a + 7] * inv)};
          PIN(of[a]);
        }
        STAMP(5);
      }
      BAR_IF(cw, 1, 63);
      if (w == 4) {
        if (j == 0 && t == 0) SET_FLAG(WS_FB + (P * 8 + c) * 128);
        if (j == 3) SET_FLAG(fd);
        if (j == 3 && t != 0) SET_FLAG(WS_FC + (P * 8 + c) * 128);
        if ((j == 4 && gn == 5) || j == 5) SET_FLAG(fd + 1024);
      }
      if (j == 1) {
        if (cw == 0) {
          WAITV(0);
          PIN(gf);
          if (!__all(gf[0] == tlo && gf[1] == thi)) poll_flags((lane >> 5) * 128, true, sws, f3s, tlo, thi);
          rsrc_t sb = __builtin_amdgcn_make_buffer_rsrc((void*)bsp, 0, 4096, 0x00020000);
          bs = ld2(i16 * 2, sb, (64 * p + 16 * rw) * 2);
        }
      }
      if ((j == 1 || j == 2) && cw == 0) {
#pragma unroll
        for (int v = 3 * j - 3; v < 3 * j; ++v)
          z1r[v] = ld8_sc1(l16, sws, WS_Z1 + ((2 * p + (rw >> 1)) * 43 + gs + TILE(min(v, gn - 1), gn)) * 1024 + (rw & 1) * 8);
      }
      if ((j == 4 && gn == 5) || j == 5) gf = ld8_sc1(FV(), sws, WS_FC);
      if (j == 4 && t == 0) {
        VWO();
#pragma unroll
        for (int e = 2; e < 7; ++e) DMA_WOUT(e)
      }
      const u32 ra_ = redr + ((j + 1) & 1) * 32768;
      f32x4 sm, r_[2];
#pragma unroll
      for (int i = 0; i < 8; ++i) {
        if (j >= 1 && j < (t ? 3 : 4) && (i & 1) == 0 && cw == 0) {
          if (i == 2) sm = r_[0] + r_[1];
          if (i > 2) sm += r_[0] + r_[1];
          r_[0] = lds16f(ra_ + i * 4096); r_[1] = lds16f(ra_ + (i + 1) * 4096);
        }
        SCHED();
        if (j < 3) bfr[u][i] = ld16_b(l16, sws, (zb0 + TILE(j + 2, gn) * 65536) ^ (i * 1024));
        else if (j == 3 && t == 0) ld16_b_if6(bfr[1][i], gn, l16, sws, (zb0 + TILE(5, gn) * 65536) ^ (i * 1024));
        SCHED();
      }
      if (j == 0 && cw == 0) gf = ld8_sc1((lane >> 5) * 128, sws, f3s);
      if (j == 1 && t != 0) {
        const u32 vo = ((lane >> 4) & 1) * 16384 + (lane >> 5) * 512 + i16 * 16;
        st16_sc1(of[0], vo, sws, WS_OF + ((8 * P + 2 * rw) * 16 + 2 * c + cw) * 1024);
        st16_sc1(of[1], vo, sws, WS_OF + ((8 * P + 2 * rw) * 16 + 2 * c + cw) * 1024 + 256);
      }
      if (j == 2) { PIN(bs); _Pragma("unroll") for (int v = 0; v < 3; ++v) PIN(z1r[v]); }
      if (j == 3) { _Pragma("unroll") for (int v = 3; v < 6; ++v) PIN(z1r[v]); }
      if (j >= 1 && cw == 0) {
        if (j < (t ? 3 : 4)) sm += r_[0] + r_[1];
        else {
          f32x4 q_[8];
#pragma unroll
          for (int o = 0; o < 8; ++o) q_[o] = lds16f(ra_ + o * 4096);
          SCHED();
          sm = ((q_[0] + q_[1]) + (q_[2] + q_[3])) + ((q_[4] + q_[5]) + (q_[6] + q_[7]));
        }
        if (j == 1) sm0 = sm;
        if (j == 2) EMIT_H(0, sm0)
        if (j >= 2) EMIT_H(j >= 2 ? j - 1 : 0, sm)
      }
      if (j == 3) {
#pragma unroll
        for (int e = 0; e < 4; ++e) DMA_WO(L_WO1, 0, e);
      }
      if (j == 3 && cw == 0) { if (t) WAITV(5); else { WAITV(12); WAITV_IF(gn, 5, 5); } }
      if (j == 5) WAITV(2);
      BAR_IF(cw, 0, 63);
    }
  }
  {
    STAMP(10);
    const u32 bufA = L_RED + (gn & 1) * 32768, bufB = L_RED + 32768 - (gn & 1) * 32768;
    const u32 fv = FV();
    if (cw == 0) {
      const u32 ra_ = redr + (bufB - L_RED);
      f32x4 q_[8];
#pragma unroll
      for (int o = 0; o < 8; ++o) q_[o] = lds16f(ra_ + o * 4096);
      SCHED();
      const f32x4 sm = ((q_[0] + q_[1]) + (q_[2] + q_[3])) + ((q_[4] + q_[5]) + (q_[6] + q_[7]));
      if (gn == 6) EMIT_H(5, sm) else EMIT_H(4, sm)
    }
    WAITV(2); WAITV_IF(cw, 1, 0);
    PIN(gf);
    {
      const bool n0 = (lane & 48) != 16;
      if (!__all(!n0 || (gf[0] == tlo && gf[1] == thi))) poll_flags(fv, n0, sws, WS_FC, tlo, thi);
    }
    RELANE();
    u32x4 hf[8], of[8];
    const u32 ofb = WS_OF + ((4 * p + rw) * 16 + cw) * 1024, hfb = WS_HF + ((4 * p + rw) * 16 + cw) * 1024;
#pragma unroll
    for (int e = 0; e < 4; ++e) of[e] = ld16_b(l16, sws, ofb + e * 2048);
#pragma unroll
    for (int e = 0; e < 4; ++e) hf[e] = ld16_b(l16, sws, hfb + e * 2048);
    BARRIER();
    const u32 qb = ((lane >> 2) & 3) * 64 + ((((g & 1) << 1) | ((lane >> 1) & 1)) * 16) + (lane & 1) * 8;
    f32x4 a3[4];
#pragma unroll
    for (int ct = 0; ct < 4; ++ct) a3[ct] = zero4;
    u32x4 wf[3][4], hl[4][2];
#define RDW2(dst, lo0, hi0, offl, offh) _Pragma("unroll") for (int ct = 0; ct < 4; ++ct) dst[ct] = cat(ldstr(((lo0) ^ ((ct & 1) * 32)) + (offl) + (ct >> 1) * 512), ldstr(((hi0) ^ ((ct & 1) * 32)) + (offh) + (ct >> 1) * 512))
#define MM3(src, frag) _Pragma("unroll") for (int ct = 0; ct < 4; ++ct) a3[ct] = mfma32(src[ct], frag, a3[ct])
    const u32 lb = WS_HL + ((4 * p + rw) * 8 + cw) * 2048;
    const rsrc_t sxo = __builtin_amdgcn_make_buffer_rsrc((void*)(x + (size_t)p * 65536), 0, 65536, 0x00020000);
    const u32 w0 = opq(L_WOUT + qb + (g >> 1) * 1024 + (g & 1) * 256);
    const u32 wa = w0 + cw * 4096, wb = w0 + cw * 2048;
    const u32 oq = qb + g * 1024 + cw * 4096;
    const u32 oa = opq(L_WO1 + oq), ob = opq(bufA + oq);
    u32x4 xres;
    VWO();
#pragma unroll
    for (int e = 0; e < 4; ++e) DMA_WO(bufA, 1, e);
    STAMP(11);
#define RDK(dst, k)                                                                                                 \
  if ((k) < 4) { RDW2(dst, oa, oa, (k) * 8192, (k) * 8192 + 256); }                                                 \
  else if ((k) < 8) { RDW2(dst, wa, wa, ((k) - 4) * 8192, ((k) - 4) * 8192 + 2048); }                               \
  else if ((k) < 12) { RDW2(dst, ob, ob, ((k) - 8) * 8192, ((k) - 8) * 8192 + 256); }                               \
  else { RDW2(dst, wb, w0, 32768 + ((k) - 12) * 4096, 49152 + ((k) - 12) * 2048); }
    RDK(wf[0], 0)
    RDK(wf[1], 1)
#pragma unroll
    for (int k = 0; k < 16; ++k) {
      if (k == 0) WAITV(8);
      if (k == 4) WAITV(13);
      if (k == 6) {
        WAITV(0);
        PIN(gf);
        if (!__all(gf[0] == tlo && gf[1] == thi)) poll_flags(fv, true, sws, WS_FC, tlo, thi);
#pragma unroll
        for (int e = 4; e < 8; ++e) hf[e] = ld16_b(l16, sws, hfb + e * 2048);
        BARRIER();
        STAMP(12);
      }
      if (k == 12) WAITV(9);
      if (k + 2 < 16) { RDK(wf[(k + 2) % 3], k + 2) }
      SCHED();
      if (k < 4) { PIN(of[k]); MM3(wf[k % 3], of[k]); }
      else if (k < 8) { PIN(hf[k - 4]); MM3(wf[k % 3], hf[k - 4]); }
      else if (k < 12) { PIN(of[k - 4]); MM3(wf[k % 3], of[k - 4]); }
      else { PIN(hf[k - 8]); MM3(wf[k % 3], hf[k - 8]); }
      SCHED();
      if (k < 4) {
        of[4 + k] = ld16_b(l16, sws, ofb + (4 + k) * 2048);
        DMA_WOUT_AT(bufB + w * 4096, 7 + k)
        if (k == 3) gf = ld8_sc1(fv, sws, WS_FC);
      } else if (k >= 6 && k < 10) {
        hl[k - 6][0] = ld16_sc1(l16, sws, lb + (k - 6) * 4096);
        hl[k - 6][1] = ld16_sc1(l16, sws, lb + (k - 6) * 4096 + 1024);
      } else if (k == 10) {
        xres = ld16((tid >> 3) * 1024 + (tid & 7) * 16, sxo, c2 * 128);
      }
    }
    STAMP(13);
    {
      const u32 wl_ = opq(bufB + (w0 - L_WOUT) + cw * 4096);
      u32x4 wg[4][4];
#pragma unroll
      for (int e = 0; e < 4; ++e) RDW2(wg[e], wl_, wl_, e * 8192, e * 8192 + 2048);
      WAITV(0);
      PIN(xres);
#pragma unroll
      for (int e = 0; e < 4; ++e) { PIN(hl[e][0]); PIN(hl[e][1]); }
#pragma nounroll
      for (int it = 0; it < SPIN_CAP; ++it) {
        u32 bad = 0;
#pragma unroll
        for (int e = 0; e < 8; ++e) bad |= (hl[e >> 1][e & 1][1] ^ tlo) | (hl[e >> 1][e & 1][3] ^ tlo);
        if (__all(bad == 0)) break;
        __builtin_amdgcn_s_sleep(2);
        asm volatile(
            "buffer_load_dwordx4 %0, %8, %9, %10 offen sc1\n\tbuffer_load_dwordx4 %1, %8, %9, %10 offen offset:1024 sc1\n\t"
            "buffer_load_dwordx4 %2, %8, %9, %11 offen sc1\n\tbuffer_load_dwordx4 %3, %8, %9, %11 offen offset:1024 sc1\n\t"
            "buffer_load_dwordx4 %4, %8, %9, %12 offen sc1\n\tbuffer_load_dwordx4 %5, %8, %9, %12 offen offset:1024 sc1\n\t"
            "buffer_load_dwordx4 %6, %8, %9, %13 offen sc1\n\tbuffer_load_dwordx4 %7, %8, %9, %13 offen offset:1024 sc1\n\t"
            "s_waitcnt vmcnt(0)"
            : "=&v"(hl[0][0]), "=&v"(hl[0][1]), "=&v"(hl[1][0]), "=&v"(hl[1][1]), "=&v"(hl[2][0]), "=&v"(hl[2][1]), "=&v"(hl[3][0]), "=&v"(hl[3][1])
            : "v"(l16), "s"(sws), "s"(lb), "s"(lb + 4096), "s"(lb + 8192), "s"(lb + 12288)
            : "memory");
      }
#pragma unroll
      for (int e = 0; e < 4; ++e) MM3(wg[e], ((u32x4){hl[e][0][0], hl[e][0][2], hl[e][1][0], hl[e][1][2]}));
    }
    STAMP(14);
    const u32 fin = L_WO1 - 2048;
#pragma unroll
    for (int ct = 0; ct < 4; ++ct) sts16f(fin + 2048 + cw * 16384 + (16 * rw + i16) * 256 + (((4 * ct + g) ^ i16) * 16), a3[ct]);
    BARRIER();
    {
      const int row = tid >> 3;
      const u32 a = fin + 2048 + row * 256 + (((2 * (tid & 7)) ^ (row & 15)) * 16);
      const f32x4 s0 = lds16f(a) + lds16f(a + 16384), s1 = lds16f(a ^ 16) + lds16f((a ^ 16) + 16384);
      u32x4 o = {pk2(blo(xres[0]) + s0[0], bhi(xres[0]) + s0[1]), pk2(blo(xres[1]) + s0[2], bhi(xres[1]) + s0[3]),
                 pk2(blo(xres[2]) + s1[0], bhi(xres[2]) + s1[1]), pk2(blo(xres[3]) + s1[2], bhi(xres[3]) + s1[3])};
      st16_sc1(o, row * 1024 + (tid & 7) * 16, __builtin_amdgcn_make_buffer_rsrc((void*)(out + (size_t)p * 65536), 0, 65536, 0x00020000), c2 * 128);
    }
  }
  }
  if (b == 0 && w == 0 && l16 == 0) *(u64*)(ws + WS_EP) = ((u64)thi << 32) | tlo;
  STAMP(15);
  return tprobe;
}

__global__ __launch_bounds__(512, 2) void fused_kernel(const char* __restrict__ x, const char* __restrict__ wqkv, const char* __restrict__ wphi,
                                                        const char* __restrict__ wo, const char* __restrict__ win, const char* __restrict__ wsp,
                                                        const char* __restrict__ bsp, const char* __restrict__ wout, char* __restrict__ out, char* ws) {
  __shared__ __attribute__((aligned(1024))) char smem[163840];
  asm volatile("" ::"v"((u32)(size_t)(LDSP char*)smem));
  asm volatile("" ::"s"(x), "s"(wqkv), "s"(win), "s"(ws), "s"(wsp));
  u64 pc, ep, tprobe = 0;
  const u64 tstart = __builtin_amdgcn_s_memrealtime();
  asm volatile("s_getpc_b64 %0" : "=s"(pc));
  {
    const int lane = threadIdx.x & 63, g = lane >> 4;
    const int w = __builtin_amdgcn_readfirstlane(threadIdx.x >> 6);
    const int r = blockIdx.x >> 3, P = ((blockIdx.x & 7) >> 1) * 4 + (r >> 3);
    const int rot = 2 * (((r >> 3) + (r & 3)) & 3);
    u32 acv = 0, acb = 0; (void)acv; (void)acb;
    STAMP(0);
    STAMPC(30);
    if (CODE_PF && w == 7)
      touch(L_JUNK, lane * 128, __builtin_amdgcn_make_buffer_rsrc((void*)(pc & ~255ull), 0, CODE_PF, 0x00020000), (r & 7) * 8192);
    const rsrc_t sx = __builtin_amdgcn_make_buffer_rsrc((void*)(x + (size_t)P * 131072), 0, 131072, 0x00020000);
    const u32 vx0 = (16 * w + (lane >> 3)) * 1024 + (((lane & 7) ^ g) * 16);
    const u32 vx1 = (vx0 + 8192) ^ 64;
    const u32 w2k = w * 2048;
    DMA_X(0, 0, 0);
    DMA_X(0, 0, 1);
    asm volatile("s_load_dwordx2 %0, %1, 0x0" : "=s"(ep) : "s"(ws) : "memory");
    SCHED();
  }
  if (__builtin_expect(((blockIdx.x >> 5) & 1) == 0, 1)) tprobe = run<0>(x, wqkv, wphi, wo, win, wsp, bsp, wout, out, ws, ep);
  else tprobe = run<1>(x, wqkv, wphi, wo, win, wsp, bsp, wout, out, ws, ep);
  if ((int)((blockIdx.x >> 5) & 1) == PTYPE || PTYPE == 2) {
    const u64 lim = 4500 + (tprobe - tstart);
    for (int it = 0; it < 100000; ++it) { if (__builtin_amdgcn_s_memrealtime() - tstart >= lim) break; __builtin_amdgcn_s_sleep(1); }
  }
}

extern "C" void kernel_launch(void* const* d_in, const int* in_sizes, int n_in, void* d_out, int out_size, void* d_ws, size_t ws_size,
                              hipStream_t stream) {
  fused_kernel<<<256, 512, 0, stream>>>((const char*)d_in[0], (const char*)d_in[1], (const char*)d_in[2], (const char*)d_in[3],
                                        (const char*)d_in[5], (const char*)d_in[6], (const char*)d_in[7], (const char*)d_in[8], (char*)d_out,
                                        (char*)d_ws);
}
```

```cpp
#include <hip/hip_runtime.h>
#include <hip/hip_bf16.h>

typedef unsigned int u32;
typedef unsigned long long u64;
typedef short s16x4 __attribute__((ext_vector_type(4)));
typedef __bf16 bf16x2 __attribute__((ext_vector_type(2)));
typedef __bf16 bf16x8 __attribute__((ext_vector_type(8)));
typedef float f32x16 __attribute__((ext_vector_type(16)));
typedef float f32x4 __attribute__((ext_vector_type(4)));
typedef float f32x2 __attribute__((ext_vector_type(2)));
typedef u32 u32x4 __attribute__((ext_vector_type(4)));
typedef u32 u32x2 __attribute__((ext_vector_type(2)));
typedef __amdgpu_buffer_rsrc_t rsrc_t;
#define LDSP __attribute__((address_space(3)))
#define DI __device__ __forceinline__

constexpr u32 WS_EP = 0;
constexpr u32 WS_FA = 8192;
constexpr u32 WS_FB = WS_FA + 16384;
constexpr u32 WS_FC = WS_FB + 16384;
constexpr u32 WS_FD = WS_FC + 16384;
constexpr u32 WS_XG = WS_FD + 65536;
constexpr u32 WS_SG = WS_XG + 32768;
constexpr u32 WS_ZB = WS_SG + 524288;
constexpr u32 WS_Z1 = WS_ZB + 43 * 65536;
constexpr u32 KP_SZ = 17408;
constexpr u32 WS_KP = WS_Z1 + 64 * 43 * 1024;
constexpr u32 WS_KF = WS_KP + 128 * KP_SZ;
constexpr u32 WS_KZ = WS_KF + 65536;
constexpr u32 WS_OF = WS_KZ + 4096;
constexpr u32 WS_HF = WS_OF + 128 * 16384;
constexpr u32 WS_HL = WS_HF + 128 * 16 * 1024;
constexpr u32 WS_END = WS_HL + 128 * 16 * 1024;

constexpr u32 LZ_W = 65536;
constexpr u32 LZ_U = 139264;
constexpr u32 LQ_M = 122880;
constexpr u32 LQ_A = 81920;
constexpr u32 LQ_PHI = 49152;
constexpr u32 LQ_V = 32768;
constexpr u32 L_TAB = 155648;
constexpr u32 L_XS = L_TAB;
constexpr u32 L_CS = L_TAB + 1024;
constexpr u32 L_ZST = L_TAB + 3072;
constexpr u32 L_WPHI = L_TAB + 3072;
constexpr u32 L_KFS = L_TAB + 2048;
constexpr u32 L_JUNK = L_TAB + 7168;
constexpr u32 L_WOUT = 0;
constexpr u32 L_RED = 57344;
constexpr u32 L_WO1 = 122880;

constexpr u64 Z2S = 0x26211B16110B0500ull;
constexpr u64 Z1S = 0x26211C16110C0600ull;
DI int grp_start(u64 tab, int c) { return c >= 8 ? 43 : (int)((tab >> (8 * c)) & 0xFF); }

#define SCHED() __builtin_amdgcn_sched_barrier(0)
#define WAITV(n) do { asm volatile("s_waitcnt vmcnt(%0)" ::"n"(n) : "memory"); SCHED(); } while (0)
#define BARRIER() do { SCHED(); asm volatile("s_waitcnt lgkmcnt(0)\n\ts_barrier" ::: "memory"); SCHED(); } while (0)
#define WAITV_IF(g_, v, n) do { asm volatile("s_cmp_lg_u32 %0, %1\n\ts_cbranch_scc1 1f\n\ts_waitcnt vmcnt(%2)\n1:" ::"s"(g_), "n"(v), "n"(n) : "scc", "memory"); SCHED(); } while (0)
#define BAR_IF(g_, v, n) do { SCHED(); asm volatile("s_cmp_lg_u32 %0, %1\n\ts_cbranch_scc1 1f\n\ts_waitcnt vmcnt(%2) lgkmcnt(0)\n\ts_barrier\n1:" ::"s"(g_), "n"(v), "n"(n) : "scc", "memory"); SCHED(); } while (0)
#define WAITL0() asm volatile("s_waitcnt lgkmcnt(0)" ::: "memory")
#define PIN(x) asm volatile("" : "+v"(x))

DI void dma16(u32 lds, u32 voff, rsrc_t srd, u32 soff) {
  asm volatile("s_mov_b32 m0, %0\n\ts_nop 0\n\tbuffer_load_dwordx4 %1, %2, %3 offen lds" ::"s"(lds), "v"(voff), "s"(srd), "s"(soff) : "memory");
}
#define DMA_C(wb, CST, voff, srd, soff) asm volatile("s_add_u32 m0, %0, %1\n\ts_nop 0\n\tbuffer_load_dwordx4 %2, %3, %4 offen lds" ::"s"(wb), "n"(CST), "v"(voff), "s"(srd), "s"(soff) : "scc", "memory")
#define DMA_CC(wb, CST, voff, srd, OP, sb, SC) do { u32 so__; asm volatile("s_add_u32 m0, %1, %2\n\t" OP " %0, %5, %6\n\tbuffer_load_dwordx4 %3, %4, %0 offen lds" : "=&s"(so__) : "s"(wb), "n"(CST), "v"(voff), "s"(srd), "s"(sb), "n"(SC) : "scc", "memory"); } while (0)
DI void touch(u32 lds, u32 voff, rsrc_t srd, u32 soff) {
  asm volatile("s_mov_b32 m0, %0\n\ts_nop 0\n\tbuffer_load_dword %1, %2, %3 offen lds" ::"s"(lds), "v"(voff), "s"(srd), "s"(soff) : "memory");
}
DI u32x4 ld16_sc1(u32 voff, rsrc_t srd, u32 soff) {
  u32x4 r;
  asm volatile("buffer_load_dwordx4 %0, %1, %2, %3 offen sc1" : "=v"(r) : "v"(voff), "s"(srd), "s"(soff) : "memory");
  return r;
}
template <int OFF> DI u32x4 ld16_sc1o(u32 voff, rsrc_t srd, u32 soff) {
  u32x4 r;
  asm volatile("buffer_load_dwordx4 %0, %1, %2, %3 offen offset:%4 sc1" : "=v"(r) : "v"(voff), "s"(srd), "s"(soff), "n"(OFF) : "memory");
  return r;
}
DI u32x4 ld16_b(u32 voff, rsrc_t srd, u32 soff) {
  u32x4 r;
  asm volatile("buffer_load_dwordx4 %0, %1, %2, %3 offen sc1" : "=v"(r) : "v"(voff), "s"(srd), "s"(soff) : "memory");
  return r;
}
template <int OFF> DI u32x4 ld16_bo(u32 voff, rsrc_t srd, u32 soff) {
  u32x4 r;
  asm volatile("buffer_load_dwordx4 %0, %1, %2, %3 offen offset:%4 sc1" : "=v"(r) : "v"(voff), "s"(srd), "s"(soff), "n"(OFF) : "memory");
  return r;
}
DI void ld16_b_if6(u32x4& r, int gn, u32 voff, rsrc_t srd, u32 soff) {
  asm volatile("s_cmp_lg_u32 %4, 6\n\ts_cbranch_scc1 1f\n\tbuffer_load_dwordx4 %0, %1, %2, %3 offen sc1\n1:" : "+v"(r) : "v"(voff), "s"(srd), "s"(soff), "s"(gn) : "scc", "memory");
}
DI u32x4 ld16(u32 voff, rsrc_t srd, u32 soff) {
  u32x4 r;
  asm volatile("buffer_load_dwordx4 %0, %1, %2, %3 offen" : "=v"(r) : "v"(voff), "s"(srd), "s"(soff) : "memory");
  return r;
}
DI u32x2 ld8_sc1(u32 voff, rsrc_t srd, u32 soff) {
  u32x2 r;
  asm volatile("buffer_load_dwordx2 %0, %1, %2, %3 offen sc1" : "=v"(r) : "v"(voff), "s"(srd), "s"(soff) : "memory");
  return r;
}
DI u32 ld2(u32 voff, rsrc_t srd, u32 soff) {
  u32 r;
  asm volatile("buffer_load_ushort %0, %1, %2, %3 offen" : "=v"(r) : "v"(voff), "s"(srd), "s"(soff) : "memory");
  return r;
}
DI void st16_sc1(u32x4 v, u32 voff, rsrc_t srd, u32 soff) {
  asm volatile("buffer_store_dwordx4 %0, %1, %2, %3 offen sc1\n\ts_nop 1" ::"v"(v), "v"(voff), "s"(srd), "s"(soff) : "memory");
}
DI void st8_sc1(u32x2 v, u32 voff, rsrc_t srd, u32 soff) {
  asm volatile("buffer_store_dwordx2 %0, %1, %2, %3 offen sc1" ::"v"(v), "v"(voff), "s"(srd), "s"(soff) : "memory");
}

DI u32x4 lds16(u32 a) { return *(const LDSP u32x4*)(size_t)a; }
DI u32x2 lds8(u32 a) { return *(const volatile LDSP u32x2*)(size_t)a; }
DI float lds4f(u32 a) { return *(const LDSP float*)(size_t)a; }
DI f32x4 lds16f(u32 a) { return *(const LDSP f32x4*)(size_t)a; }
DI void sts16f(u32 a, f32x4 v) { *(LDSP f32x4*)(size_t)a = v; }
DI void sts16(u32 a, u32x4 v) { *(LDSP u32x4*)(size_t)a = v; }
DI void sts8(u32 a, u32x2 v) { *(volatile LDSP u32x2*)(size_t)a = v; }
DI void sts4f(u32 a, float v) { *(LDSP float*)(size_t)a = v; }
DI u32x2 ldstr(u32 a) {
  return __builtin_bit_cast(u32x2, __builtin_amdgcn_ds_read_tr16_b64_v4i16((LDSP s16x4*)(size_t)a)); }
DI u32x4 cat(u32x2 lo, u32x2 hi) { return (u32x4){lo[0], lo[1], hi[0], hi[1]}; }
DI u32x4 ldfrag(u32 a, u32 d) { u32x2 lo = ldstr(a), hi = ldstr(a + d); return cat(lo, hi); }

DI f32x4 mfma32(u32x4 a, u32x4 b, f32x4 c) {
  return __builtin_amdgcn_mfma_f32_16x16x32_bf16(__builtin_bit_cast(bf16x8, a), __builtin_bit_cast(bf16x8, b), c, 0, 0, 0);
}
DI f32x16 mfmaL(u32x4 a, u32x4 b, f32x16 c) {
  return __builtin_amdgcn_mfma_f32_32x32x16_bf16(__builtin_bit_cast(bf16x8, a), __builtin_bit_cast(bf16x8, b), c, 0, 0, 0);
}
DI u32 pk2(float a, float b) { bf16x2 v = {(__bf16)a, (__bf16)b}; return __builtin_bit_cast(u32, v); }
DI float blo(u32 u) { return __uint_as_float(u << 16); }
DI float bhi(u32 u) { return __uint_as_float(u & 0xffff0000u); }
DI float rbf(float a) { return blo(pk2(a, a)); }
DI float dot2(u32 a, u32 b, float c) {
  return __builtin_amdgcn_fdot2_f32_bf16(__builtin_bit_cast(bf16x2, a), __builtin_bit_cast(bf16x2, b), c, false);
}
DI f32x2 fma2(f32x2 a, f32x2 b, f32x2 c) { return __builtin_elementwise_fma(a, b, c); }
DI f32x2 bc2(float a) { return (f32x2){a, a}; }
DI f32x2 lo2(f32x4 v) { return (f32x2){v[0], v[1]}; }
DI f32x2 hi2(f32x4 v) { return (f32x2){v[2], v[3]}; }
DI u32 pk2v(f32x2 v) { return pk2(v[0], v[1]); }
DI f32x2 gelu2(f32x2 x) {
  f32x2 u = fma2(x * x, bc2(-0.10294324f), bc2(-2.3022082f)) * x;
  f32x2 e = {__builtin_amdgcn_exp2f(u[0]), __builtin_amdgcn_exp2f(u[1])};
  e = e + bc2(1.0f);
  f32x2 r = {__builtin_amdgcn_rcpf(e[0]), __builtin_amdgcn_rcpf(e[1])};
  return x * r;
}
DI void red2g(float& a, float& b) {
  auto s = __builtin_amdgcn_permlane32_swap(__float_as_uint(a), __float_as_uint(b), false, false);
  float c = __uint_as_float(s[0]) + __uint_as_float(s[1]);
  auto s2 = __builtin_amdgcn_permlane16_swap(__float_as_uint(c), __float_as_uint(c), false, false);
  float d = __uint_as_float(s2[0]) + __uint_as_float(s2[1]);
  auto s3 = __builtin_amdgcn_permlane32_swap(__float_as_uint(d), __float_as_uint(d), false, false);
  a = __uint_as_float(s3[0]);
  b = __uint_as_float(s3[1]);
}
template <int CTRL> DI float dpp(float v) {
  return __uint_as_float(__builtin_amdgcn_update_dpp(0, __float_as_uint(v), CTRL, 0xF, 0xF, true));
}
template <class F> DI void hedgehog16(const f32x16& p, float s, u32x4 (&o)[4], F hook) {
  float mx = fmaxf(p[0], p[1]), mn = fminf(p[0], p[1]);
#pragma unroll
  for (int i = 2; i < 16; i += 2) { mx = fmaxf(fmaxf(mx, p[i]), p[i + 1]); mn = fminf(fminf(mn, p[i]), p[i + 1]); }
  const float c = s * 1.44269504f;
  const float a = -mx * c, b = mn * c;
  f32x2 ep[8], en[8];
#pragma unroll
  for (int i = 0; i < 8; ++i) {
    hook(i);
    const f32x2 v = {p[2 * i], p[2 * i + 1]};
    const f32x2 u = fma2(v, bc2(c), bc2(a)), w = fma2(v, bc2(-c), bc2(b));
    ep[i] = (f32x2){__builtin_amdgcn_exp2f(u[0]), __builtin_amdgcn_exp2f(u[1])};
    en[i] = (f32x2){__builtin_amdgcn_exp2f(w[0]), __builtin_amdgcn_exp2f(w[1])};
  }
  hook(8);
  const f32x2 sp = ((ep[0] + ep[1]) + (ep[2] + ep[3])) + ((ep[4] + ep[5]) + (ep[6] + ep[7]));
  const f32x2 sn = ((en[0] + en[1]) + (en[2] + en[3])) + ((en[4] + en[5]) + (en[6] + en[7]));
  const float ip = __builtin_amdgcn_rcpf(sp[0] + sp[1]), in = __builtin_amdgcn_rcpf(sn[0] + sn[1]);
#pragma unroll
  for (int i = 0; i < 8; ++i) {
    if (i == 0 || i == 3 || i == 6) hook(9 + i / 3);
    o[i >> 2][i & 3] = pk2v(ep[i] * bc2(ip));
    o[2 + (i >> 2)][i & 3] = pk2v(en[i] * bc2(in));
  }
}
DI void ln_fin(float s, float q, float invn, float& r, float& m) {
  m = s * invn;
  float v = fmaxf(q * invn - m * m, 0.0f);
  float sd = rbf(__builtin_amdgcn_sqrtf(rbf(rbf(v) + 1e-5f)));
  r = __builtin_amdgcn_rcpf(sd);
  m = rbf(m);
}

constexpr int SPIN_CAP = 1 << 17;
DI void poll_flags(u32 voff, bool act, rsrc_t srd, u32 soff, u32 tlo, u32 thi) {
#pragma nounroll
  for (int it = 0; it < SPIN_CAP; ++it) {
    u32x2 v;
    asm volatile("buffer_load_dwordx2 %0, %1, %2, %3 offen sc1\n\ts_waitcnt vmcnt(0)" : "=&v"(v) : "v"(voff), "s"(srd), "s"(soff) : "memory");
    bool ok = !act || (v[0] == tlo && v[1] == thi);
    if (__all(ok)) break;
    __builtin_amdgcn_s_sleep(6);
  }
}
DI bool tag_ok(u32x4 q, u32 t) { return q[1] == t && q[3] == t; }
DI void poll_gran(u32x4& q, u32 voff, rsrc_t srd, u32 soff, u32 tlo) {
#pragma nounroll
  for (int it = 0; it < SPIN_CAP; ++it) {
    if (__all(tag_ok(q, tlo))) break;
    __builtin_amdgcn_s_sleep(6);
    asm volatile("buffer_load_dwordx4 %0, %1, %2, %3 offen sc1\n\ts_waitcnt vmcnt(0)" : "=&v"(q) : "v"(voff), "s"(srd), "s"(soff) : "memory");
  }
}
DI u32 opq(u32 v) { asm volatile("" : "+v"(v)); return v; }

static_assert(WS_ZB % 8192 == 0, "fragment addresses are formed by XOR");
constexpr u32 CODE_PF = 56000;

#define VM(x) do { x; ++vq; } while (0)
#define WAITQ(mark) WAITV(vq - (mark))

template <int t>
DI void run(const char* __restrict__ x, const char* __restrict__ wqkv, const char* __restrict__ wphi, const char* __restrict__ wo,
            const char* __restrict__ win, const char* __restrict__ wsp, const char* __restrict__ bsp, const char* __restrict__ wout,
            char* __restrict__ out, char* ws, u64 ep) {
  const int tid = threadIdx.x, lane = tid & 63;
  const int w = __builtin_amdgcn_readfirstlane(tid >> 6);
  const int g = lane >> 4;
  const int b = blockIdx.x, xcd = b & 7, r = b >> 3;
  const int Pl = r >> 3, cl = r & 3;
  const int P = (xcd >> 1) * 4 + Pl;
  const int rot = 2 * ((Pl + cl) & 3);
  const rsrc_t sx = __builtin_amdgcn_make_buffer_rsrc((void*)(x + (size_t)P * 131072), 0, 131072, 0x00020000);
  const u32 vx0 = (16 * w + (lane >> 3)) * 1024 + (((lane & 7) ^ g) * 16);
  const u32 vx1 = (vx0 + 8192) ^ 64;
  const u32 w2k = w * 2048;
#define DMA_X(dst, n, e) DMA_C(w2k, (dst) + (e) * 1024, (e) ? vx1 : vx0, sx, (((n) + rot) & 7) * 128)
  const int rw = w & 3, cw = w >> 2, i16 = lane & 15;
  const int c = (xcd & 1) * 4 + cl;
  const int p = 2 * P + (cl & 1);
  const int c2 = (xcd & 1) * 4 + (t ? 3 * (cl >> 1) : 1 + (cl >> 1));
  const u32 l16 = opq(lane * 16);
  const rsrc_t sa = __builtin_amdgcn_make_buffer_rsrc((void*)(wsp + (size_t)p * 262144), 0, 262144, 0x00020000);
  const rsrc_t sws = __builtin_amdgcn_make_buffer_rsrc((void*)ws, 0, WS_END, 0x00020000);
  const u32 vA0 = (lane >> 3) * 4096 + (((lane & 7) ^ g) * 16);
  const u32 vA1 = (vA0 + 32768) ^ 64;
  const int ux0 = cl >> 1, ux1 = t;
  const u32 uxs = w * 512 + ux1 * 256 + ux0 * 131072;
#define DMA_U(u, base, e) DMA_CC(w2k, (base) + (e) * 1024, (e) ? vA1 : vA0, sa, "s_xor_b32", uxs, (((u) >> 1) & 3) * 128 + (((u) >> 3) * 32 + ((u) & 1) * 16) * 4096)
  const f32x4 zero4 = {0, 0, 0, 0};
  u32x4 ones4 = {0x3F803F80u, 0x3F803F80u, 0x3F803F80u, 0x3F803F80u};
  u32 tlo, thi;
  u32x4 af[4][8];
  u32x4 bfr[2][8];
  u32x2 gf;
  const u32 ard = w * 2048 + i16 * 128 + (((g >> 1) ^ (i16 >> 1)) * 16) + (g & 1) * 8;
#define READ_U(u, base)                                                                                             \
  {                                                                                                                 \
    const u32 ab_ = opq(ard + (base));                                                                              \
    _Pragma("unroll") for (int kk = 0; kk < 2; ++kk)                                                               \
      af[2 * ((u) >> 3) + ((u) & 1)][((u) & 6) + kk] = cat(lds8(ab_ ^ (64 * kk)), lds8(ab_ ^ (64 * kk + 32)));      \
  }
#define USE_EPOCH() { asm volatile("" : "+s"(ep)); ep += 1; tlo = (u32)ep; thi = (u32)(ep >> 32); PIN(ones4); }
#define EARLY_STATS(mark)                                                                                           \
  {                                                                                                                 \
    WAITQ(mark); PIN(xrow);                                                                                         \
    float s_ = 0, q_ = 0;                                                                                           \
    _Pragma("unroll") for (int e = 0; e < 4; ++e) { s_ = dot2(xrow[e], ones4[0], s_); q_ = dot2(xrow[e], xrow[e], q_); } \
    s_ += dpp<0x128>(s_); q_ += dpp<0x128>(q_);                                                                     \
    s_ += dpp<0x124>(s_); q_ += dpp<0x124>(q_);                                                                     \
    s_ += dpp<0x4E>(s_); q_ += dpp<0x4E>(q_);                                                                       \
    s_ += dpp<0xB1>(s_); q_ += dpp<0xB1>(q_);                                                                       \
    red2g(s_, q_);                                                                                                  \
    float r_, m_;                                                                                                   \
    ln_fin(s_, q_, 1.0f / 512, r_, m_);                                                                             \
    VM(st16_sc1((u32x4){__float_as_uint(r_), tlo, __float_as_uint(m_), tlo}, l16 << 22, sws, WS_XG + (P * 128 + (t * 8 + c) * 8 + w) * 16)); \
  }
#define GATHER_XS()                                                                                                 \
  WAITQ(mxg); PIN(xg);                                                                                              \
  poll_gran(xg, i16 * 16, sws, WS_XG + (P * 128 + 16 * w) * 16, tlo);                                               \
  sts4f(L_XS + (16 * w + i16) * 4, __uint_as_float(xg[0]));                                                         \
  sts4f(L_XS + 512 + (16 * w + i16) * 4, __uint_as_float(xg[2]));
#define RELANE() const int lane = opq(l16) >> 4, i16 = lane & 15, g = lane >> 4, tid = w * 64 + lane; (void)tid; (void)g; (void)i16
#define SET_FLAG(off) st8_sc1((u32x2){tlo, thi}, l16 << 22, sws, (off))
  const int gs = grp_start(Z2S, c2), gn = t ? 5 : grp_start(Z2S, c2 + 1) - gs;
  const int trot = (p & 7) >= 5 ? (p & 7) - 5 : (p & 7);
#define TILE(j, n_) (((j) + trot) >= (n_) ? (j) + trot - (n_) : (j) + trot)
  const u32 zb0 = WS_ZB + (gs * 64 + 8 * w) * 1024 + ux1 * 4096;
#define DMA_WOUT(e)                                                                                                 \
  {                                                                                                                 \
    const rsrc_t so_ = __builtin_amdgcn_make_buffer_rsrc((void*)wout, 0, 688 * 1024, 0x00020000);                   \
    const int ws_ = grp_start(Z2S, w), wn_ = grp_start(Z2S, w + 1) - ws_;                                           \
    if ((e) < 6) dma16(L_WOUT + ((e) < 4 ? 2 * w + ((e) >> 1) : 16 + w) * 2048 + ((e) & 1) * 1024, vwo, so_, ((ws_ + TILE((e) >> 1, wn_)) * 2 + ((e) & 1)) * 8192); \
    else if ((e) == 6) dma16(L_WOUT + 24 * 2048 + w * 1024, vwo, so_, ((((w >> 1) == 0 ? 5 : ((w >> 1) == 1 ? 11 : ((w >> 1) == 2 ? 27 : 64))) + TILE(3, 6)) * 2 + (w & 1)) * 8192); \
  }
#define DMA_WOUT_AT(dst, e)                                                                                         \
  {                                                                                                                 \
    const rsrc_t so_ = __builtin_amdgcn_make_buffer_rsrc((void*)wout, 0, 688 * 1024, 0x00020000);                   \
    const int ws_ = grp_start(Z2S, w), wn_ = grp_start(Z2S, w + 1) - ws_;                                           \
    dma16((dst) + ((e) - 7) * 1024, vwo, so_, ((ws_ + TILE(wn_ - 1 - (((e) - 7) >> 1), wn_)) * 2 + (((e) - 7) & 1)) * 8192); \
  }
#define VWO() const u32 vwo = ((lane >> 2) & 7) * 1024 + c2 * 128 + (lane >> 5) * 64 + (((lane & 3) ^ (((lane >> 4) & 1) << 1)) * 16)
#define LOAD_B(u, so_)                                                                                              \
  bfr[u][0] = ld16_bo<0>(l16, sws, so_); bfr[u][1] = ld16_bo<1024>(l16, sws, so_);                                  \
  bfr[u][2] = ld16_bo<2048>(l16, sws, so_); bfr[u][3] = ld16_bo<3072>(l16, sws, so_);                               \
  bfr[u][4] = ld16_bo<0>(l16, sws, (so_) ^ 4096); bfr[u][5] = ld16_bo<1024>(l16, sws, (so_) ^ 4096);                \
  bfr[u][6] = ld16_bo<2048>(l16, sws, (so_) ^ 4096); bfr[u][7] = ld16_bo<3072>(l16, sws, (so_) ^ 4096);

  if constexpr (t == 0) {
    int vq = 2;
    const rsrc_t sw = __builtin_amdgcn_make_buffer_rsrc((void*)win, 0, 512 * 2752, 0x00020000);
    const int z2s = grp_start(Z2S, c), z2n = grp_start(Z2S, c + 1) - z2s;
    const int z1s = grp_start(Z1S, c), z1n = grp_start(Z1S, c + 1) - z1s;
    u32 vw[3];
    int wch[3];
#pragma unroll
    for (int e = 0; e < 3; ++e) {
      const int sg = 2 * e + (lane >> 5), hi = sg >= 3;
      wch[e] = (sg - 3 * hi) * 4 + ((lane & 3) ^ (hi << 1));
      vw[e] = wch[e] < 2 * z2n ? (16 * w + 8 * hi + ((lane >> 2) & 7)) * 2752 + 1376 + 32 * z2s + 16 * wch[e] : 0x7FFF0000u;
    }
    const u32 w3k = w * 3072;
#define DMA_W(bk, e) DMA_C(w3k, LZ_W + ((bk) % 3) * 24576 + (e) * 1024, vw[e], sw, ((2 * (bk) + rot) & 7) * (64 * 2752))
#define UBASE(u) ((u) % 5 == 0 ? LZ_U : ((u) % 5 - 1) * 16384)
#define ISS_X(n) do { VM(DMA_X(((n) & 3) * 16384, n, 0)); VM(DMA_X(((n) & 3) * 16384, n, 1)); mx[n] = vq; } while (0)
#define ISS_U(u) do { VM(DMA_U(u, UBASE(u), 0)); VM(DMA_U(u, UBASE(u), 1)); mu[u] = vq; } while (0)
#define ISS_W(bk, e0, e1) do { _Pragma("unroll") for (int e_ = e0; e_ < e1; ++e_) VM(DMA_W(bk, e_)); mw[bk] = vq; } while (0)
    int mx[8], mw[8], mu[16];
    mx[0] = vq;
    ISS_W(0, 0, 3);
    u32x4 xrow;
    VM(xrow = ld16(l16, sx, (c * 8 + w) * 1024));
    const int mxr = vq;
    ISS_X(1);
    const u32 xa0 = opq((32 * rw + i16) * 128 + ((g ^ (i16 >> 1)) * 16)), xa1 = xa0 ^ 64;
    u32 ta[3];
    int tl[3];
    {
      const u32 trb = LZ_W + g * 1536 + ((lane >> 2) & 3) * 64 + ((((g & 1) << 1) | ((lane >> 1) & 1)) * 16) + (lane & 1) * 8;
#pragma unroll
      for (int j = 0; j < 3; ++j) {
        tl[j] = 3 * cw + (j + rw) % 3;
        ta[j] = (trb + (tl[j] >> 1) * 512) ^ ((tl[j] & 1) * 32);
      }
    }
#define NEXT_BLOCK(bk) _Pragma("unroll") for (int j = 0; j < 3; ++j) ta[j] = opq(ta[j] + ((bk) == 0 ? 0 : ((bk) % 3 == 0 ? -49152 : 24576)))
    f32x4 acc[2][3], csa = zero4;
#pragma unroll
    for (int j = 0; j < 3; ++j) acc[0][j] = acc[1][j] = zero4;
    u32x4 xf[16][2], wf[2][3];
    u32x4 xg;
    int mxg = 0;
    WAITQ(mw[0]);
    BARRIER();
    USE_EPOCH()
#define P1_MEM(n, h)                                                                                                \
  {                                                                                                                 \
    if ((n) == 0) { if ((h) == 0) ISS_X(2); if ((h) == 1) ISS_W(1, 0, 2); if ((h) == 2) ISS_W(1, 2, 3); if ((h) == 3) ISS_X(3); } \
    if ((n) == 1) { if ((h) == 0) ISS_W(2, 0, 2); if ((h) == 1) ISS_W(2, 2, 3); if ((h) == 2) VM(DMA_X(0, 4, 0)); if ((h) == 3) { VM(DMA_X(0, 4, 1)); mx[4] = vq; } } \
    if ((n) == 2 || (n) == 4) { if ((h) < 2) { VM(DMA_X((((n) + 3) & 3) * 16384, (n) + 3, (h) & 1)); mx[(n) + 3] = vq; } else ISS_W((n) / 2 + 2, (h) - 2, (h) - 1); } \
    if ((n) == 3) { if ((h) == 0) ISS_W(3, 2, 3); else if ((h) < 3) { VM(DMA_X(32768, 6, (h) - 1)); mx[6] = vq; } else ISS_U(0); } \
    if ((n) == 5) { if ((h) == 0) ISS_W(4, 2, 3); if ((h) == 1) { VM(xg = ld16_sc1(i16 * 16, sws, WS_XG + (P * 128 + 16 * w) * 16)); mxg = vq; } \
                    if ((h) >= 2) { VM(DMA_U(1, UBASE(1), (h) - 2)); mu[1] = vq; } }                                 \
    if ((n) == 6) { if ((h) < 3) ISS_W(5, h, (h) + 1); else ISS_U(2); }                                             \
    if ((n) == 7) { if ((h) < 2) { VM(DMA_U(3, UBASE(3), h)); mu[3] = vq; } }                                       \
  }
#pragma unroll
    for (int ks = 0; ks <= 16; ++ks) {
      const int n = ks >> 1, kk = ks & 1;
      if (ks > 0 && ks < 16 && kk == 0) {
        { const int mk = (n & 1) ? mx[n] : (mx[n] > mw[n >> 1] ? mx[n] : mw[n >> 1]); WAITQ(mk); }
        BARRIER();
      }
      if (ks < 16 && (ks & 3) == 0) { NEXT_BLOCK(ks >> 2); }
      if (ks == 8) {
#pragma unroll
        for (int e = 0; e < 3; ++e)
          vw[e] = wch[e] < 2 * z1n ? (16 * w + 8 * (2 * e + (lane >> 5) >= 3) + ((lane >> 2) & 7)) * 2752 + 32 * z1s + 16 * wch[e] : 0x7FFF0000u;
      }
      const int k1 = ks > 0 ? ks - 1 : 0, q1 = k1 & 1;
#pragma unroll
      for (int m = 0; m < 7; ++m) {
        if (ks > 0) {
          if (m < 6) acc[m & 1][m >> 1] = mfma32(xf[k1][m & 1], wf[q1][m >> 1], acc[m & 1][m >> 1]);
          else csa = mfma32(ones4, wf[q1][0], csa);
          SCHED();
        }
        if (ks < 16) {
          if (m == 0) { P1_MEM(n, 2 * kk) xf[ks & 15][0] = lds16((kk ? xa1 : xa0) + (n & 3) * 16384); xf[ks & 15][1] = lds16((kk ? xa1 : xa0) + (n & 3) * 16384 + 2048); }
          else if (m < 4) wf[kk][m - 1] = ldfrag(ta[m - 1] + (ks & 3) * 6144, 256);
          if (m == 4) P1_MEM(n, 2 * kk + 1)
        }
        SCHED();
      }
#pragma unroll
      for (int j = 0; j < 3; ++j) { PIN(acc[0][j]); PIN(acc[1][j]); }
      PIN(csa);
      if (ks == 2) EARLY_STATS(mxr)
    }
    GATHER_XS()
    sts4f(L_CS + tl[0] * 64 + i16 * 4, csa[0]);
    WAITQ(mu[1] > mw[4] ? mu[1] : mw[4]);
    READ_U(0, UBASE(0)) READ_U(1, UBASE(1))
    BARRIER();
    u32x4 zp[3];
    ISS_W(6, 0, 3);
    {
      f32x2 ps[4], pq[4];
      float cs[3];
#pragma unroll
      for (int j = 0; j < 3; ++j) cs[j] = -lds4f(L_CS + tl[j] * 64 + i16 * 4);
#pragma unroll
      for (int rt = 0; rt < 2; ++rt) {
        const f32x4 rr = lds16f(L_XS + (32 * rw + 16 * rt + 4 * g) * 4), mm = lds16f(L_XS + 512 + (32 * rw + 16 * rt + 4 * g) * 4);
        f32x2 z0[3], z1[3];
#pragma unroll
        for (int j = 0; j < 3; ++j) {
          z0[j] = gelu2(fma2(lo2(mm), bc2(cs[j]), lo2(acc[rt][j])) * lo2(rr));
          z1[j] = gelu2(fma2(hi2(mm), bc2(cs[j]), hi2(acc[rt][j])) * hi2(rr));
          zp[j][2 * rt] = pk2v(z0[j]); zp[j][2 * rt + 1] = pk2v(z1[j]);
        }
        ps[2 * rt] = z0[0] + z0[1] + z0[2]; ps[2 * rt + 1] = z1[0] + z1[1] + z1[2];
        pq[2 * rt] = fma2(z0[2], z0[2], fma2(z0[1], z0[1], z0[0] * z0[0]));
        pq[2 * rt + 1] = fma2(z1[2], z1[2], fma2(z1[1], z1[1], z1[0] * z1[0]));
        SCHED();
        if (rt == 0) { ISS_U(4); ISS_U(5); }
        else ISS_U(6);
        SCHED();
      }
      const bool b3 = lane & 8, b2 = lane & 4, b1 = lane & 2;
#pragma unroll
      for (int m = 0; m < 2; ++m)
#pragma unroll
        for (int e = 0; e < 2; ++e) {
          ps[m][e] = (b3 ? ps[m + 2][e] : ps[m][e]) + dpp<0x128>(b3 ? ps[m][e] : ps[m + 2][e]);
          pq[m][e] = (b3 ? pq[m + 2][e] : pq[m][e]) + dpp<0x128>(b3 ? pq[m][e] : pq[m + 2][e]);
        }
#pragma unroll
      for (int e = 0; e < 2; ++e) {
        ps[0][e] = (b2 ? ps[1][e] : ps[0][e]) + dpp<0x141>(b2 ? ps[0][e] : ps[1][e]);
        pq[0][e] = (b2 ? pq[1][e] : pq[0][e]) + dpp<0x141>(b2 ? pq[0][e] : pq[1][e]);
      }
      float s_ = (b1 ? ps[0][1] : ps[0][0]) + dpp<0x4E>(b1 ? ps[0][0] : ps[0][1]);
      float q_ = (b1 ? pq[0][1] : pq[0][0]) + dpp<0x4E>(b1 ? pq[0][0] : pq[0][1]);
      s_ += dpp<0xB1>(s_);
      q_ += dpp<0xB1>(q_);
      const int row = 32 * rw + 16 * (i16 >> 3) + 4 * g + ((i16 >> 1) & 3);
      VM(st16_sc1((u32x4){__float_as_uint(s_), tlo, __float_as_uint(q_), tlo}, row * 16 + ((lane & 1) << 30), sws, WS_SG + ((P * 8 + c) * 2 + cw) * 2048));
      PIN(zp[0]); PIN(zp[1]); PIN(zp[2]);
    }
    {
    RELANE();
#pragma unroll
    for (int j = 0; j < 3; ++j) acc[0][j] = acc[1][j] = zero4;
    csa = zero4;
    u32x4 q0, q1, q2, q3;
    int mq = 0, mz = 0;
    const u32 gvo = (2 * g) * 4096 + (32 * rw + 16 * cw + i16) * 16;
    const u32 gso = WS_SG + P * 32768;
#pragma unroll
    for (int ks = 0; ks <= 16; ++ks) {
      if (ks == 8) {
        WAITQ(mq); PIN(q0); PIN(q1); PIN(q2); PIN(q3);
#pragma nounroll
        for (int it = 0; it < SPIN_CAP; ++it) {
          if (__all(tag_ok(q0, tlo) && tag_ok(q1, tlo) && tag_ok(q2, tlo) && tag_ok(q3, tlo))) break;
          __builtin_amdgcn_s_sleep(6);
          asm volatile(
              "buffer_load_dwordx4 %0, %4, %5, %6 offen sc1\n\t"
              "buffer_load_dwordx4 %1, %4, %5, %6 offen offset:2048 sc1\n\t"
              "buffer_load_dwordx4 %2, %4, %5, %7 offen sc1\n\t"
              "buffer_load_dwordx4 %3, %4, %5, %7 offen offset:2048 sc1\n\t"
              "s_waitcnt vmcnt(0)"
              : "=&v"(q0), "=&v"(q1), "=&v"(q2), "=&v"(q3)
              : "v"(gvo), "s"(sws), "s"(gso), "s"(gso + 4096)
              : "memory");
        }
        float s = (__uint_as_float(q0[0]) + __uint_as_float(q1[0])) + (__uint_as_float(q2[0]) + __uint_as_float(q3[0]));
        float q = (__uint_as_float(q0[2]) + __uint_as_float(q1[2])) + (__uint_as_float(q2[2]) + __uint_as_float(q3[2]));
        red2g(s, q);
        float r2, m2;
        ln_fin(s, q, 1.0f / 688, r2, m2);
        sts4f(L_ZST + (32 * rw + 16 * cw + i16) * 4, r2);
        sts4f(L_ZST + 512 + (32 * rw + 16 * cw + i16) * 4, -m2 * r2);
      }
      if (ks < 16 && (ks & 3) == 0) {
        if (ks > 0) { WAITQ(ks == 12 ? mz : mw[4 + (ks >> 2)]); BARRIER(); }
        if (ks == 12) { if (w == 0) SET_FLAG(WS_FA + (P * 8 + c) * 128); }
        NEXT_BLOCK(4 + (ks >> 2));
      }
      if (ks == 8) {
#pragma unroll
        for (int rt = 0; rt < 2; ++rt) {
          const u32 ra = L_ZST + (32 * rw + 16 * rt + 4 * g) * 4;
          const f32x4 r4 = lds16f(ra), m4 = lds16f(ra + 512);
#pragma unroll
          for (int j = 0; j < 3; ++j) {
            const u32 u0 = zp[j][2 * rt], u1 = zp[j][2 * rt + 1];
            zp[j][2 * rt] = pk2v(fma2((f32x2){blo(u0), bhi(u0)}, lo2(r4), lo2(m4)));
            zp[j][2 * rt + 1] = pk2v(fma2((f32x2){blo(u1), bhi(u1)}, hi2(r4), hi2(m4)));
          }
        }
#pragma unroll
        for (int j = 0; j < 3; ++j)
          VM(st16_sc1(zp[j], l16, sws, (tl[j] < z2n) ? WS_ZB + ((z2s + tl[j]) * 64 + 4 * P + rw) * 1024 : 0x7FFF0000u));
        mz = vq;
        SCHED();
      }
      if (ks >= 5 && ks <= 9) { WAITQ(mu[ks - 3]); READ_U(ks - 3, UBASE(ks - 3)) }
      if (ks >= 9 && ks <= 13) { WAITQ(mu[ks - 2]); READ_U(ks - 2, UBASE(ks - 2)) }
      if (ks >= 13) { WAITQ(mu[ks - 1]); READ_U(ks - 1, UBASE(ks - 1)) }
      const int k1 = ks > 0 ? ks - 1 : 0, q1_ = k1 & 1;
#pragma unroll
      for (int m = 0; m < 7; ++m) {
        if (ks > 0) {
          if (m < 6) acc[m & 1][m >> 1] = mfma32(wf[q1_][m >> 1], xf[k1][m & 1], acc[m & 1][m >> 1]);
          else csa = mfma32(ones4, wf[q1_][0], csa);
          SCHED();
        }
        if (ks < 16 && m >= 1 && m < 4) wf[ks & 1][m - 1] = ldfrag(ta[m - 1] + (ks & 3) * 6144, 256);
        if (m == 0 || m == 4) {
          const int h = m == 4;
          if (h == 0 && ks >= 6 && ks <= 13) asm volatile("s_waitcnt lgkmcnt(10)" ::: "memory");
          if (ks >= 6 && ks <= 10) { VM(DMA_U(ks + 1, UBASE(ks + 1), h)); mu[ks + 1] = vq; }
          if (ks >= 10 && ks <= 13) { VM(DMA_U(ks + 2, UBASE(ks + 2), h)); mu[ks + 2] = vq; }
        }
        if (m == 6) {
          if (ks >= 4 && ks <= 6) ISS_W(7, ks - 4, ks - 3);
        }
        if (m == 2) {
          if (ks == 5) { VM(q0 = ld16_sc1o<0>(gvo, sws, gso)); VM(q1 = ld16_sc1o<2048>(gvo, sws, gso)); }
          if (ks == 6) { VM(q2 = ld16_sc1o<0>(gvo, sws, gso + 4096)); VM(q3 = ld16_sc1o<2048>(gvo, sws, gso + 4096)); mq = vq; }
        }
        SCHED();
      }
#pragma unroll
      for (int j = 0; j < 3; ++j) { PIN(acc[0][j]); PIN(acc[1][j]); }
      PIN(csa);
    }
    sts4f(L_CS + 512 + tl[0] * 64 + i16 * 4, csa[0]);
    gf = ld8_sc1((lane >> 5) * 1024, sws, WS_FA + (16 * w + c2) * 128);
    BARRIER();
    VWO();
    DMA_WOUT(0) DMA_WOUT(1)
    WAITV(2);
    PIN(gf);
    if (!__all(gf[0] == tlo && gf[1] == thi)) poll_flags((lane >> 5) * 1024, true, sws, WS_FA + (16 * w + c2) * 128, tlo, thi);
    {
      float xr[2], xm[2];
#pragma unroll
      for (int rt = 0; rt < 2; ++rt) {
        xr[rt] = lds4f(L_XS + (32 * rw + 16 * rt + i16) * 4);
        xm[rt] = -lds4f(L_XS + 512 + (32 * rw + 16 * rt + i16) * 4);
      }
      const u32 so0 = zb0 + TILE(0, gn) * 65536;
#pragma unroll
      for (int j = 0; j < 3; ++j) {
        const f32x4 cc = lds16f(L_CS + 512 + tl[j] * 64 + g * 16);
        bfr[0][3 * j] = ld16_b(l16, sws, so0 ^ (3 * j * 1024));
        bfr[0][3 * j + 1] = ld16_b(l16, sws, so0 ^ ((3 * j + 1) * 1024));
        if (j < 2) bfr[0][3 * j + 2] = ld16_b(l16, sws, so0 ^ ((3 * j + 2) * 1024));
        u32x4 zz;
#pragma unroll
        for (int rt = 0; rt < 2; ++rt) {
          zz[2 * rt] = pk2v(gelu2(fma2(lo2(cc), bc2(xm[rt]), lo2(acc[rt][j])) * bc2(xr[rt])));
          zz[2 * rt + 1] = pk2v(gelu2(fma2(hi2(cc), bc2(xm[rt]), hi2(acc[rt][j])) * bc2(xr[rt])));
        }
        st16_sc1(zz, l16, sws, (tl[j] < z1n) ? WS_Z1 + ((4 * P + rw) * 43 + z1s + tl[j]) * 1024 : 0x7FFF0000u);
        SCHED();
      }
    }
    }
  } else {
    int vq = 2;
    const rsrc_t sw = __builtin_amdgcn_make_buffer_rsrc((void*)wqkv, 0, 512 * 3072, 0x00020000);
    const u32 vw = (8 * w + ((lane >> 2) & 7)) * 3072 + 128 * c + ((lane >> 5) * 4 + ((lane & 3) ^ ((w & 1) << 1))) * 16;
    const u32 w3k = w * 3072;
#define DMA_S(n, e)                                                                                                 \
  do {                                                                                                              \
    if ((e) < 2) DMA_X(((n) % 3) * 40960, n, (e) & 1);                                                              \
    else DMA_CC(w3k, ((n) % 3) * 40960 + 16384 + ((e) - 2) * 1024, vw, sw, "s_add_u32", (((n) + rot) & 7) * (64 * 3072), ((e) - 2) * 1024); \
  } while (0)
#define ISS_Q(u, base) do { VM(DMA_U(u, base, 0)); VM(DMA_U(u, base, 1)); mu[u] = vq; } while (0)
    int ms[8], mu[16];
    VM(DMA_S(0, 2)); VM(DMA_S(0, 3)); VM(DMA_S(0, 4)); ms[0] = vq;
    u32x4 xrow;
    VM(xrow = ld16(l16, sx, ((8 + c) * 8 + w) * 1024));
    const int mxr = vq;
#pragma unroll
    for (int e = 0; e < 5; ++e) { VM(DMA_S(1, e)); ms[1] = vq; }
    const int l32 = lane & 31, hh = lane >> 5;
    u32 xa = (32 * rw + l32) * 128 + ((hh ^ ((l32 >> 1) & 7)) * 16);
    u32 wa = 16384 + hh * 3072 + cw * 512 + ((lane >> 2) & 3) * 64 + (((((g & 1) << 1) | ((lane >> 1) & 1)) ^ (hh << 1)) * 16) + (lane & 1) * 8;
    u32 ca = 16384 + g * 3072 + min(w, 5) * 512 + ((lane >> 2) & 3) * 64 + ((((g & 1) << 1) | ((lane >> 1) & 1)) * 16) + (lane & 1) * 8;
    const f32x16 zero16 = {0, 0, 0, 0, 0, 0, 0, 0, 0, 0, 0, 0, 0, 0, 0, 0};
    f32x16 acc[3] = {zero16, zero16, zero16};
    f32x4 csa[2] = {zero4, zero4};
    u32x4 xg;
    int mxg = 0;
    WAITQ(ms[0]);
    BARRIER();
    USE_EPOCH()
    {
      u32x4 xf[2], wf[2][4];
#pragma unroll
      for (int n = 0; n <= 8; ++n)
#pragma unroll
      for (int sq = 0; sq < 4; ++sq) {
        const int s = 4 * n + sq;
        if (s > 32) continue;
        if (s < 32 && sq == 0) {
          if (n > 0) {
            WAITQ(ms[n]);
            BARRIER();
          }
          const int d = n == 0 ? 0 : (n % 3 == 0 ? -81920 : 40960);
          xa = opq(xa + d); wa = opq(wa + d); ca = opq(ca + d);
        }
        const int q1 = (s + 1) & 1, q0 = s & 1;
#pragma unroll
        for (int m = 0; m < 4; ++m) {
          if (s > 0) {
            if (m < 3) acc[m] = mfmaL(wf[q1][m], xf[q1], acc[m]);
            else csa[q1] = mfma32(ones4, wf[q1][3], csa[q1]);
            SCHED();
          }
          if (s < 32) {
            if (m == 0) { xf[q0] = lds16(xa ^ (32 * sq)); wf[q0][0] = ldfrag(wa + sq * 6144, 256); }
            if (m == 1) { wf[q0][1] = ldfrag(wa + sq * 6144 + 1024, 256); wf[q0][2] = ldfrag(wa + sq * 6144 + 2048, 256); }
            if (m == 2) wf[q0][3] = ldfrag((ca ^ (32 * (sq & 1))) + (sq >> 1) * 12288, 256);
            if (m == 0 || m == 2) {
              const int h = 2 * sq + (m == 2);
              if (n <= 5 && h < 5) { VM(DMA_S(n + 2, h)); ms[n + 2] = vq; }
              if (n == 0 && h == 5) {
                rsrc_t sp = __builtin_amdgcn_make_buffer_rsrc((void*)wphi, 0, 16384, 0x00020000);
                VM(dma16(L_WPHI + (w & 1) * 1024, l16, sp, c * 2048 + (w & 1) * 1024));
              }
              if (n == 4 && h == 5) { VM(xg = ld16_sc1(i16 * 16, sws, WS_XG + (P * 128 + 16 * w) * 16)); mxg = vq; }
              if (n == 6) { VM(DMA_U(sq, (sq < 2 ? LQ_M : LQ_A) + (sq & 1) * 16384, h & 1)); mu[sq] = vq; }
              if (n == 7 && sq < 2) { VM(DMA_U(4 + sq, sq * 16384, h & 1)); mu[4 + sq] = vq; }
            }
          }
          SCHED();
        }
        PIN(acc[0]); PIN(acc[1]); PIN(acc[2]); PIN(csa[0]); PIN(csa[1]);
        if (s == 3) EARLY_STATS(mxr)
      }
    }
    GATHER_XS()
    sts4f(L_CS + w * 128 + i16 * 4, csa[0][0]);
    sts4f(L_CS + w * 128 + 64 + i16 * 4, csa[1][0]);
    BARRIER();
    WAITQ(mu[3]);
    READ_U(0, LQ_M) READ_U(1, LQ_M + 16384) READ_U(2, LQ_A) READ_U(3, LQ_A + 16384)
    const float xr = lds4f(L_XS + (32 * rw + l32) * 4), xm = -lds4f(L_XS + 512 + (32 * rw + l32) * 4);
    u32x4 wp0, wp1;
    {
      const u32 a = L_WPHI + (2 * cw + (lane & 1)) * 512 + (4 * hh + ((lane >> 2) & 3)) * 32 + (2 * (g & 1) + ((lane >> 1) & 1)) * 8;
      const u32x4 f = ldfrag(a, 256);
      const bool own = (lane >> 2) & 1;
#pragma unroll
      for (int e = 0; e < 4; ++e) { wp0[e] = own ? 0u : f[e]; wp1[e] = own ? f[e] : 0u; }
    }
#define FEATURES(e_, dst, hook)                                                                                         \
  {                                                                                                                 \
    u32x4 kp[2];                                                                                                    \
    _Pragma("unroll") for (int a = 0; a < 4; ++a) {                                                                \
      const f32x4 cc = lds16f(L_CS + (2 * (e_) + cw) * 128 + a * 32 + hh * 16);                                      \
      kp[a >> 1][2 * (a & 1)] = pk2v(fma2(lo2(cc), bc2(xm), (f32x2){acc[e_][4 * a], acc[e_][4 * a + 1]}));          \
      kp[a >> 1][2 * (a & 1) + 1] = pk2v(fma2(hi2(cc), bc2(xm), (f32x2){acc[e_][4 * a + 2], acc[e_][4 * a + 3]}));  \
    }                                                                                                               \
    f32x16 pl = mfmaL(wp0, kp[0], zero16);                                                                          \
    pl = mfmaL(wp1, kp[1], pl);                                                                                     \
    hedgehog16(pl, xr, dst, hook);                                                                                  \
  }
    {
      const int s_ = 32 * rw + l32;
      const u32 key = (((s_ >> 1) & 3) ^ (((s_ >> 3) & 1) << 1)) * 16;
      {
        const u32 va = LQ_V + (s_ >> 3) * 1024 + cw * 512 + (s_ & 7) * 64 + hh * 8;
#pragma unroll
        for (int a = 0; a < 4; ++a) {
          const f32x4 cc = lds16f(L_CS + (4 + cw) * 128 + a * 32 + hh * 16);
          sts8(va + ((a * 16) ^ key), (u32x2){pk2v(fma2(lo2(cc), bc2(xm), (f32x2){acc[2][4 * a], acc[2][4 * a + 1]}) * bc2(xr)),
                                              pk2v(fma2(hi2(cc), bc2(xm), (f32x2){acc[2][4 * a + 2], acc[2][4 * a + 3]}) * bc2(xr))});
        }
      }
      WAITQ(mu[5]);
      READ_U(4, 0) READ_U(5, 16384)
      WAITL0();
      u32x4 ph[4];
      auto hk = [&](int i) __attribute__((always_inline)) {
        const int u = 6 + (i >> 1);
        SCHED();
        VM(DMA_U(u, (u < 8 ? LQ_M : (u < 10 ? LQ_A : 0)) + (u & 1) * 16384, i & 1));
        mu[u] = vq;
        SCHED();
      };
      FEATURES(1, ph, hk)
      const u32 pa = LQ_PHI + (s_ >> 3) * 2048 + (2 * cw + hh) * 512 + (s_ & 7) * 64;
#pragma unroll
      for (int a = 0; a < 4; ++a) sts16(pa + ((a * 16) ^ key), ph[a]);
    }
    BARRIER();
    {
      const u32 kx = (((lane >> 3) & 1) ^ ((g & 1) << 1)) * 16;
      const u32 pa = LQ_PHI + g * 2048 + (w >> 1) * 512 + ((lane >> 2) & 3) * 64 + (((2 * (w & 1) + ((lane >> 1) & 1)) * 16) ^ kx) + (lane & 1) * 8;
      const u32 va = LQ_V + g * 1024 + (w >> 2) * 512 + ((lane >> 2) & 3) * 64 + (((2 * ((w >> 1) & 1) + ((lane >> 1) & 1)) * 16) ^ kx) + (lane & 1) * 8;
      u32x4 pf[4], vf[4];
#pragma unroll
      for (int k = 0; k < 4; ++k) {
        pf[k] = cat(ldstr(pa + k * 8192), ldstr((pa ^ 32) + k * 8192 + 256));
        vf[k] = cat(ldstr(va + k * 4096), ldstr((va ^ 32) + k * 4096 + 256));
      }
      f32x4 kv = zero4, kz = zero4;
#pragma unroll
      for (int k = 0; k < 4; ++k) { kv = mfma32(pf[k], vf[k], kv); kz = mfma32(pf[k], ones4, kz); }
      const u32 so = WS_KP + (c * 16 + P) * KP_SZ;
      u32x4 g0 = {__float_as_uint(kv[0]), tlo, __float_as_uint(kv[1]), tlo}, g1 = {__float_as_uint(kv[2]), tlo, __float_as_uint(kv[3]), tlo};
      u32x4 g2 = {__float_as_uint((lane & 1) ? kz[2] : kz[0]), tlo, __float_as_uint((lane & 1) ? kz[3] : kz[1]), tlo};
      PIN(g0); PIN(g1); PIN(g2);
      asm volatile("s_nop 7" ::: "memory");
      VM(st16_sc1(g0, l16, sws, so + w * 2048));
      VM(st16_sc1(g1, l16, sws, so + w * 2048 + 1024));
      VM(st16_sc1(g2, g * 32 + (lane & 1) * 16 + ((i16 >> 1) << 26), sws, so + 16384 + w * 128));
    }
    WAITQ(mu[11]);
    READ_U(6, LQ_M) READ_U(7, LQ_M + 16384) READ_U(8, LQ_A) READ_U(9, LQ_A + 16384) READ_U(10, 0) READ_U(11, 16384)
    BARRIER();
    ISS_Q(12, LQ_M); VM(DMA_U(13, LQ_M + 16384, 0));
    u32x4 phq[4];
    {
      VWO();
      auto hk = [&](int i) __attribute__((always_inline)) {
        SCHED();
        if (i == 0) DMA_U(13, LQ_M + 16384, 1);
        else if (i < 5) DMA_U(14 + ((i - 1) >> 1), LQ_A + ((i - 1) >> 1) * 16384, (i - 1) & 1);
        else DMA_WOUT(i - 5)
        SCHED();
      };
      FEATURES(0, phq, hk)
    }
    PIN(phq[0]); PIN(phq[1]); PIN(phq[2]); PIN(phq[3]);
    if (w >= 5) {
      const int id = 2 * P + (w == 7 ? (lane & 1) : w - 5), wq = id >> 2, gq = id & 3;
      const u32 vi0 = (4 * g) * KP_SZ + (w == 7 ? 16384 + wq * 128 + gq * 32 : wq * 2048 + (gq * 16 + i16) * 16);
      const u32 vi1 = vi0 + (w == 7 ? 16 : 1024);
      const u32 vo = (w == 7 ? (WS_KZ - WS_KF) + c * 512 + id * 16 + ((i16 >> 1) << 25) : c * 8192 + id * 256 + i16 * 16) + (g << 28);
      float sa[4];
#pragma nounroll
      for (int it = 0; it < SPIN_CAP; ++it) {
        u32x4 a[4][2];
#pragma unroll
        for (int o = 0; o < 4; ++o) {
          a[o][0] = ld16_sc1(vi0, sws, WS_KP + (c * 16 + o) * KP_SZ);
          a[o][1] = ld16_sc1(vi1, sws, WS_KP + (c * 16 + o) * KP_SZ);
        }
        WAITV(0);
        bool ok = true;
#pragma unroll
        for (int o = 0; o < 4; ++o) { PIN(a[o][0]); PIN(a[o][1]); ok = ok && tag_ok(a[o][0], tlo) && tag_ok(a[o][1], tlo); }
#pragma unroll
        for (int e = 0; e < 4; ++e)
          sa[e] = (__uint_as_float(a[0][e >> 1][2 * (e & 1)]) + __uint_as_float(a[1][e >> 1][2 * (e & 1)])) +
                  (__uint_as_float(a[2][e >> 1][2 * (e & 1)]) + __uint_as_float(a[3][e >> 1][2 * (e & 1)]));
        if (__all(ok)) break;
        __builtin_amdgcn_s_sleep(6);
      }
      red2g(sa[0], sa[1]);
      red2g(sa[2], sa[3]);
      st16_sc1((u32x4){pk2(sa[0], sa[1]), tlo, pk2(sa[2], sa[3]), tlo}, vo, sws, WS_KF);
    }
    gf = ld8_sc1((lane >> 5) * 1024, sws, WS_FA + (16 * w + c2) * 128);
    WAITV(8);
    READ_U(12, LQ_M) READ_U(13, LQ_M + 16384) READ_U(14, LQ_A) READ_U(15, LQ_A + 16384)
    BARRIER();
#pragma unroll
    for (int a = 0; a < 4; ++a) sts16(L_WO1 + w * 4096 + a * 1024 + l16, phq[a]);
    WAITV(0);
    PIN(gf);
    if (!__all(gf[0] == tlo && gf[1] == thi)) poll_flags((lane >> 5) * 1024, true, sws, WS_FA + (16 * w + c2) * 128, tlo, thi);
    { const u32 so_ = zb0 + TILE(0, gn) * 65536; LOAD_B(0, so_) }
  }

  {
  RELANE();
  u32x4 kfg, kzg;
  const u32 kfo = WS_KF + c * 8192 + w * 1024, kzo = WS_KZ + c * 512 + w * 64;
  if (t != 0) {
    kfg = ld16_sc1(l16, sws, kfo);
    kzg = ld16_sc1(g * 16, sws, kzo);
  }
  { const u32 so_ = zb0 + TILE(1, gn) * 65536; LOAD_B(1, so_) }
  const u32 redw = opq(L_RED + w * 4096 + lane * 16), redr = opq(L_RED + (rw ^ (2 * ux0)) * 1024 + lane * 16);
  const u32 fd = WS_FD + (p * 16 + c2) * 128;
  const u32 f3s = __builtin_amdgcn_readfirstlane(WS_FB + (P * 8 + (c2 > 0 ? c2 - 1 : 0)) * 128);
  const u32 vw3 = ((lane >> 2) & 7) * 1024 + c2 * 128 + (lane >> 5) * 64;
  const rsrc_t swo = __builtin_amdgcn_make_buffer_rsrc((void*)wo, 0, 512 * 1024, 0x00020000);
#define DMA_WO(dst, half, e) dma16((dst) + w * 4096 + (e) * 1024, (vw3 + (lane & 3) * 16) ^ (((e) & 1) * 32), swo, ((w + 8 * (half)) * 4 + (e)) * 8192)
#define FV() ((lane & 32) ? (P * 8 + ((lane >> 2) & 7)) * 128 : (WS_FD - WS_FC) + (p * 16 + (((lane >> 1) & 14) | cw)) * 128)
  u32x2 z1r[6], hold;
  u32 bs;
  f32x4 sm0;
#define EMIT_H(jt, sm_)                                                                                             \
  {                                                                                                                 \
    const f32x2 bb = bc2(blo(bs));                                                                                  \
    const u32 h0 = pk2v((f32x2){blo(z1r[jt][0]), bhi(z1r[jt][0])} * (lo2(sm_) + bb));                               \
    const u32 h1 = pk2v((f32x2){blo(z1r[jt][1]), bhi(z1r[jt][1])} * (hi2(sm_) + bb));                               \
    const u32 hb_ = WS_HF + (((4 * p + rw) * 2 + ((jt) >> 1)) * 8 + c2) * 1024;                                     \
    if ((jt) == 0 || ((jt) == 2 && gn == 6)) hold = (u32x2){h0, h1};                                                \
    else if ((jt) == 1 || ((jt) == 3 && gn == 6)) st16_sc1((u32x4){hold[0], hold[1], h0, h1}, l16, sws, hb_);       \
    else if ((jt) == 2) st16_sc1((u32x4){h0, h1, 0u, 0u}, l16, sws, hb_);                                           \
    else st16_sc1((u32x4){h0, tlo, h1, tlo}, l16, sws, WS_HL + (((4 * p + rw) * 8 + c2) * 2 + (gn - 1 - (jt))) * 1024); \
  }
#pragma unroll
  for (int j = 0; j < 6; ++j) {
    if (j < 5 || gn == 6) {
      const int u = j & 1;
      if (j == 0) WAITV(8);
      if (j == 2) WAITV(t ? 10 : 8);
      if (j == 1) { WAITV(9); WAITV_IF(cw, 1, 8); }
      if (j == 3) { WAITV(9); WAITV_IF(cw, 1, 8); }
      if (j == 4) { if (t) WAITV(4); else { WAITV(12); WAITV_IF(gn, 5, 4); } }
      if (j == 5) { WAITV(10); WAITV_IF(cw, 1, 9); }
#pragma unroll
      for (int i = 0; i < 8; ++i) PIN(bfr[u][i]);
      {
        f32x4 a_[4];
#pragma unroll
        for (int rt = 0; rt < 4; ++rt) {
          a_[rt] = zero4;
#pragma unroll
          for (int i = 0; i < 8; ++i) a_[rt] = mfma32(bfr[u][i], af[rt][i], a_[rt]);
          SCHED();
          if (rt > 0) sts16f(redw + u * 32768 + (rt - 1) * 1024, a_[rt - 1]);
          SCHED();
        }
        if (j == 0 && t != 0) {
          PIN(kfg); PIN(kzg);
          poll_gran(kfg, l16, sws, kfo, tlo);
          poll_gran(kzg, g * 16, sws, kzo, tlo);
          sts8(L_KFS + (w >> 1) * 1024 + i16 * 64 + (w & 1) * 32 + g * 8, (u32x2){kfg[0], kfg[2]});
          sts8(L_KFS + 4096 + w * 32 + g * 8, (u32x2){kzg[0], kzg[2]});
          sts8(L_KFS + 4352 + (lane & 7) * 8, (u32x2){0u, 0u});
        }
        sts16f(redw + u * 32768 + 3072, a_[3]);
      }
      u32x4 of[2];
      if (j == 1 && t != 0) {
        const int l32 = lane & 31, hh = lane >> 5;
        const bool own = ((lane >> 2) & 1) == hh;
        const u32 ka = own ? L_KFS + (2 * cw + hh) * 1024 + ((l32 & 3) + 4 * (l32 >> 3)) * 64 : L_KFS + 4352;
        const u32 zaa = own ? L_KFS + 4096 + (2 * cw + hh) * 64 : L_KFS + 4352;
        const u32 qa = L_WO1 + w * 4096 + l16;
        const f32x16 zero16 = {0, 0, 0, 0, 0, 0, 0, 0, 0, 0, 0, 0, 0, 0, 0, 0};
        f32x16 den = zero16, num = zero16;
#pragma unroll
        for (int a = 0; a < 4; ++a) {
          const u32x4 pq_ = lds16(qa + a * 1024);
          den = mfmaL(lds16(zaa + a * 16), pq_, den);
          num = mfmaL(lds16(ka + a * 16), pq_, num);
        }
        const float inv = __builtin_amdgcn_rcpf(den[0] + 1e-6f);
#pragma unroll
        for (int a = 0; a < 2; ++a) {
          of[a] = (u32x4){pk2(num[8 * a] * inv, num[8 * a + 1] * inv), pk2(num[8 * a + 2] * inv, num[8 * a + 3] * inv),
                          pk2(num[8 * a + 4] * inv, num[8 * a + 5] * inv), pk2(num[8 * a + 6] * inv, num[8 * a + 7] * inv)};
          PIN(of[a]);
        }
      }
      BAR_IF(cw, 1, 63);
      if (w == 4) {
        if (j == 0 && t == 0) SET_FLAG(WS_FB + (P * 8 + c) * 128);
        if (j == 3) SET_FLAG(fd);
        if (j == 3 && t != 0) SET_FLAG(WS_FC + (P * 8 + c) * 128);
        if ((j == 4 && gn == 5) || j == 5) SET_FLAG(fd + 1024);
      }
      if (j == 1) {
        if (cw == 0) {
          WAITV(0);
          PIN(gf);
          if (!__all(gf[0] == tlo && gf[1] == thi)) poll_flags((lane >> 5) * 128, true, sws, f3s, tlo, thi);
          rsrc_t sb = __builtin_amdgcn_make_buffer_rsrc((void*)bsp, 0, 4096, 0x00020000);
          bs = ld2(i16 * 2, sb, (64 * p + 16 * rw) * 2);
        }
      }
      if ((j == 1 || j == 2) && cw == 0) {
#pragma unroll
        for (int v = 3 * j - 3; v < 3 * j; ++v)
          z1r[v] = ld8_sc1(l16, sws, WS_Z1 + ((2 * p + (rw >> 1)) * 43 + gs + TILE(min(v, gn - 1), gn)) * 1024 + (rw & 1) * 8);
      }
      if ((j == 4 && gn == 5) || j == 5) gf = ld8_sc1(FV(), sws, WS_FC);
      if (j == 4 && t == 0) {
        VWO();
#pragma unroll
        for (int e = 2; e < 7; ++e) DMA_WOUT(e)
      }
      const u32 ra_ = redr + ((j + 1) & 1) * 32768;
      f32x4 sm, r_[2];
#pragma unroll
      for (int i = 0; i < 8; ++i) {
        if (j >= 1 && j < (t ? 3 : 4) && (i & 1) == 0 && cw == 0) {
          if (i == 2) sm = r_[0] + r_[1];
          if (i > 2) sm += r_[0] + r_[1];
          r_[0] = lds16f(ra_ + i * 4096); r_[1] = lds16f(ra_ + (i + 1) * 4096);
        }
        SCHED();
        if (j < 3) bfr[u][i] = ld16_b(l16, sws, (zb0 + TILE(j + 2, gn) * 65536) ^ (i * 1024));
        else if (j == 3 && t == 0) ld16_b_if6(bfr[1][i], gn, l16, sws, (zb0 + TILE(5, gn) * 65536) ^ (i * 1024));
        SCHED();
      }
      if (j == 0 && cw == 0) gf = ld8_sc1((lane >> 5) * 128, sws, f3s);
      if (j == 1 && t != 0) {
        const u32 vo = ((lane >> 4) & 1) * 16384 + (lane >> 5) * 512 + i16 * 16;
        st16_sc1(of[0], vo, sws, WS_OF + ((8 * P + 2 * rw) * 16 + 2 * c + cw) * 1024);
        st16_sc1(of[1], vo, sws, WS_OF + ((8 * P + 2 * rw) * 16 + 2 * c + cw) * 1024 + 256);
      }
      if (j == 2) { PIN(bs); _Pragma("unroll") for (int v = 0; v < 3; ++v) PIN(z1r[v]); }
      if (j == 3) { _Pragma("unroll") for (int v = 3; v < 6; ++v) PIN(z1r[v]); }
      if (j >= 1 && cw == 0) {
        if (j < (t ? 3 : 4)) sm += r_[0] + r_[1];
        else {
          f32x4 q_[8];
#pragma unroll
          for (int o = 0; o < 8; ++o) q_[o] = lds16f(ra_ + o * 4096);
          SCHED();
          sm = ((q_[0] + q_[1]) + (q_[2] + q_[3])) + ((q_[4] + q_[5]) + (q_[6] + q_[7]));
        }
        if (j == 1) sm0 = sm;
        if (j == 2) EMIT_H(0, sm0)
        if (j >= 2) EMIT_H(j >= 2 ? j - 1 : 0, sm)
      }
      if (j == 3) {
#pragma unroll
        for (int e = 0; e < 4; ++e) DMA_WO(L_WO1, 0, e);
      }
      if (j == 3 && cw == 0) { if (t) WAITV(5); else { WAITV(12); WAITV_IF(gn, 5, 5); } }
      if (j == 5) WAITV(2);
      BAR_IF(cw, 0, 63);
    }
  }
  {
    const u32 bufA = L_RED + (gn & 1) * 32768, bufB = L_RED + 32768 - (gn & 1) * 32768;
    const u32 fv = FV();
    if (cw == 0) {
      const u32 ra_ = redr + (bufB - L_RED);
      f32x4 q_[8];
#pragma unroll
      for (int o = 0; o < 8; ++o) q_[o] = lds16f(ra_ + o * 4096);
      SCHED();
      const f32x4 sm = ((q_[0] + q_[1]) + (q_[2] + q_[3])) + ((q_[4] + q_[5]) + (q_[6] + q_[7]));
      if (gn == 6) EMIT_H(5, sm) else EMIT_H(4, sm)
    }
    WAITV(2); WAITV_IF(cw, 1, 0);
    PIN(gf);
    {
      const bool n0 = (lane & 48) != 16;
      if (!__all(!n0 || (gf[0] == tlo && gf[1] == thi))) poll_flags(fv, n0, sws, WS_FC, tlo, thi);
    }
    RELANE();
    u32x4 hf[8], of[8];
    const u32 ofb = WS_OF + ((4 * p + rw) * 16 + cw) * 1024, hfb = WS_HF + ((4 * p + rw) * 16 + cw) * 1024;
#pragma unroll
    for (int e = 0; e < 4; ++e) of[e] = ld16_b(l16, sws, ofb + e * 2048);
#pragma unroll
    for (int e = 0; e < 4; ++e) hf[e] = ld16_b(l16, sws, hfb + e * 2048);
    BARRIER();
    const u32 qb = ((lane >> 2) & 3) * 64 + ((((g & 1) << 1) | ((lane >> 1) & 1)) * 16) + (lane & 1) * 8;
    f32x4 a3[4];
#pragma unroll
    for (int ct = 0; ct < 4; ++ct) a3[ct] = zero4;
    u32x4 wf[3][4], hl[4][2];
#define RDW2(dst, lo0, hi0, offl, offh) _Pragma("unroll") for (int ct = 0; ct < 4; ++ct) dst[ct] = cat(ldstr(((lo0) ^ ((ct & 1) * 32)) + (offl) + (ct >> 1) * 512), ldstr(((hi0) ^ ((ct & 1) * 32)) + (offh) + (ct >> 1) * 512))
#define MM3(src, frag) _Pragma("unroll") for (int ct = 0; ct < 4; ++ct) a3[ct] = mfma32(src[ct], frag, a3[ct])
    const u32 lb = WS_HL + ((4 * p + rw) * 8 + cw) * 2048;
    const rsrc_t sxo = __builtin_amdgcn_make_buffer_rsrc((void*)(x + (size_t)p * 65536), 0, 65536, 0x00020000);
    const u32 w0 = opq(L_WOUT + qb + (g >> 1) * 1024 + (g & 1) * 256);
    const u32 wa = w0 + cw * 4096, wb = w0 + cw * 2048;
    const u32 oq = qb + g * 1024 + cw * 4096;
    const u32 oa = opq(L_WO1 + oq), ob = opq(bufA + oq);
    u32x4 xres;
    VWO();
#pragma unroll
    for (int e = 0; e < 4; ++e) DMA_WO(bufA, 1, e);
#define RDK(dst, k)                                                                                                 \
  if ((k) < 4) { RDW2(dst, oa, oa, (k) * 8192, (k) * 8192 + 256); }                                                 \
  else if ((k) < 8) { RDW2(dst, wa, wa, ((k) - 4) * 8192, ((k) - 4) * 8192 + 2048); }                               \
  else if ((k) < 12) { RDW2(dst, ob, ob, ((k) - 8) * 8192, ((k) - 8) * 8192 + 256); }                               \
  else { RDW2(dst, wb, w0, 32768 + ((k) - 12) * 4096, 49152 + ((k) - 12) * 2048); }
    RDK(wf[0], 0)
    RDK(wf[1], 1)
#pragma unroll
    for (int k = 0; k < 16; ++k) {
      if (k == 0) WAITV(8);
      if (k == 4) WAITV(13);
      if (k == 6) {
        WAITV(0);
        PIN(gf);
        if (!__all(gf[0] == tlo && gf[1] == thi)) poll_flags(fv, true, sws, WS_FC, tlo, thi);
#pragma unroll
        for (int e = 4; e < 8; ++e) hf[e] = ld16_b(l16, sws, hfb + e * 2048);
        BARRIER();
      }
      if (k == 12) WAITV(9);
      if (k + 2 < 16) { RDK(wf[(k + 2) % 3], k + 2) }
      SCHED();
      if (k < 4) { PIN(of[k]); MM3(wf[k % 3], of[k]); }
      else if (k < 8) { PIN(hf[k - 4]); MM3(wf[k % 3], hf[k - 4]); }
      else if (k < 12) { PIN(of[k - 4]); MM3(wf[k % 3], of[k - 4]); }
      else { PIN(hf[k - 8]); MM3(wf[k % 3], hf[k - 8]); }
      SCHED();
      if (k < 4) {
        of[4 + k] = ld16_b(l16, sws, ofb + (4 + k) * 2048);
        DMA_WOUT_AT(bufB + w * 4096, 7 + k)
        if (k == 3) gf = ld8_sc1(fv, sws, WS_FC);
      } else if (k >= 6 && k < 10) {
        hl[k - 6][0] = ld16_sc1(l16, sws, lb + (k - 6) * 4096);
        hl[k - 6][1] = ld16_sc1(l16, sws, lb + (k - 6) * 4096 + 1024);
      } else if (k == 10) {
        xres = ld16((tid >> 3) * 1024 + (tid & 7) * 16, sxo, c2 * 128);
      }
    }
    {
      const u32 wl_ = opq(bufB + (w0 - L_WOUT) + cw * 4096);
      u32x4 wg[4][4];
#pragma unroll
      for (int e = 0; e < 4; ++e) RDW2(wg[e], wl_, wl_, e * 8192, e * 8192 + 2048);
      WAITV(0);
      PIN(xres);
#pragma unroll
      for (int e = 0; e < 4; ++e) { PIN(hl[e][0]); PIN(hl[e][1]); }
#pragma nounroll
      for (int it = 0; it < SPIN_CAP; ++it) {
        u32 bad = 0;
#pragma unroll
        for (int e = 0; e < 8; ++e) bad |= (hl[e >> 1][e & 1][1] ^ tlo) | (hl[e >> 1][e & 1][3] ^ tlo);
        if (__all(bad == 0)) break;
        __builtin_amdgcn_s_sleep(2);
        asm volatile(
            "buffer_load_dwordx4 %0, %8, %9, %10 offen sc1\n\tbuffer_load_dwordx4 %1, %8, %9, %10 offen offset:1024 sc1\n\t"
            "buffer_load_dwordx4 %2, %8, %9, %11 offen sc1\n\tbuffer_load_dwordx4 %3, %8, %9, %11 offen offset:1024 sc1\n\t"
            "buffer_load_dwordx4 %4, %8, %9, %12 offen sc1\n\tbuffer_load_dwordx4 %5, %8, %9, %12 offen offset:1024 sc1\n\t"
            "buffer_load_dwordx4 %6, %8, %9, %13 offen sc1\n\tbuffer_load_dwordx4 %7, %8, %9, %13 offen offset:1024 sc1\n\t"
            "s_waitcnt vmcnt(0)"
            : "=&v"(hl[0][0]), "=&v"(hl[0][1]), "=&v"(hl[1][0]), "=&v"(hl[1][1]), "=&v"(hl[2][0]), "=&v"(hl[2][1]), "=&v"(hl[3][0]), "=&v"(hl[3][1])
            : "v"(l16), "s"(sws), "s"(lb), "s"(lb + 4096), "s"(lb + 8192), "s"(lb + 12288)
            : "memory");
      }
#pragma unroll
      for (int e = 0; e < 4; ++e) MM3(wg[e], ((u32x4){hl[e][0][0], hl[e][0][2], hl[e][1][0], hl[e][1][2]}));
    }
    const u32 fin = L_WO1 - 2048;
#pragma unroll
    for (int ct = 0; ct < 4; ++ct) sts16f(fin + 2048 + cw * 16384 + (16 * rw + i16) * 256 + (((4 * ct + g) ^ i16) * 16), a3[ct]);
    BARRIER();
    {
      const int row = tid >> 3;
      const u32 a = fin + 2048 + row * 256 + (((2 * (tid & 7)) ^ (row & 15)) * 16);
      const f32x4 s0 = lds16f(a) + lds16f(a + 16384), s1 = lds16f(a ^ 16) + lds16f((a ^ 16) + 16384);
      u32x4 o = {pk2(blo(xres[0]) + s0[0], bhi(xres[0]) + s0[1]), pk2(blo(xres[1]) + s0[2], bhi(xres[1]) + s0[3]),
                 pk2(blo(xres[2]) + s1[0], bhi(xres[2]) + s1[1]), pk2(blo(xres[3]) + s1[2], bhi(xres[3]) + s1[3])};
      st16_sc1(o, row * 1024 + (tid & 7) * 16, __builtin_amdgcn_make_buffer_rsrc((void*)(out + (size_t)p * 65536), 0, 65536, 0x00020000), c2 * 128);
    }
  }
  }
  if (b == 0 && w == 0 && l16 == 0) *(u64*)(ws + WS_EP) = ((u64)thi << 32) | tlo;
}

__global__ __launch_bounds__(512, 2) void fused_kernel(const char* __restrict__ x, const char* __restrict__ wqkv, const char* __restrict__ wphi,
                                                        const char* __restrict__ wo, const char* __restrict__ win, const char* __restrict__ wsp,
                                                        const char* __restrict__ bsp, const char* __restrict__ wout, char* __restrict__ out, char* ws) {
  __shared__ __attribute__((aligned(1024))) char smem[163840];
  asm volatile("" ::"v"((u32)(size_t)(LDSP char*)smem));
  asm volatile("" ::"s"(x), "s"(wqkv), "s"(win), "s"(ws), "s"(wsp));
  u64 pc, ep;
  asm volatile("s_getpc_b64 %0" : "=s"(pc));
  {
    const int lane = threadIdx.x & 63, g = lane >> 4;
    const int w = __builtin_amdgcn_readfirstlane(threadIdx.x >> 6);
    const int r = blockIdx.x >> 3, P = ((blockIdx.x & 7) >> 1) * 4 + (r >> 3);
    const int rot = 2 * (((r >> 3) + (r & 3)) & 3);
    if (w == 7)
      touch(L_JUNK, lane * 128, __builtin_amdgcn_make_buffer_rsrc((void*)(pc & ~255ull), 0, CODE_PF, 0x00020000), (r & 7) * 8192);
    const rsrc_t sx = __builtin_amdgcn_make_buffer_rsrc((void*)(x + (size_t)P * 131072), 0, 131072, 0x00020000);
    const u32 vx0 = (16 * w + (lane >> 3)) * 1024 + (((lane & 7) ^ g) * 16);
    const u32 vx1 = (vx0 + 8192) ^ 64;
    const u32 w2k = w * 2048;
    DMA_X(0, 0, 0);
    DMA_X(0, 0, 1);
    asm volatile("s_load_dwordx2 %0, %1, 0x0" : "=s"(ep) : "s"(ws) : "memory");
    SCHED();
  }
  if (__builtin_expect(((blockIdx.x >> 5) & 1) == 0, 1)) run<0>(x, wqkv, wphi, wo, win, wsp, bsp, wout, out, ws, ep);
  else run<1>(x, wqkv, wphi, wo, win, wsp, bsp, wout, out, ws, ep);
}

extern "C" void kernel_launch(void* const* d_in, const int* in_sizes, int n_in, void* d_out, int out_size, void* d_ws, size_t ws_size,
                              hipStream_t stream) {
  fused_kernel<<<256, 512, 0, stream>>>((const char*)d_in[0], (const char*)d_in[1], (const char*)d_in[2], (const char*)d_in[3],
                                        (const char*)d_in[5], (const char*)d_in[6], (const char*)d_in[7], (const char*)d_in[8], (char*)d_out,
                                        (char*)d_ws);
}
```

```cpp
#include <hip/hip_runtime.h>
#include <hip/hip_bf16.h>

typedef unsigned int u32;
typedef unsigned long long u64;
typedef short s16x4 __attribute__((ext_vector_type(4)));
typedef __bf16 bf16x2 __attribute__((ext_vector_type(2)));
typedef __bf16 bf16x8 __attribute__((ext_vector_type(8)));
typedef float f32x16 __attribute__((ext_vector_type(16)));
typedef float f32x4 __attribute__((ext_vector_type(4)));
typedef float f32x2 __attribute__((ext_vector_type(2)));
typedef u32 u32x4 __attribute__((ext_vector_type(4)));
typedef u32 u32x2 __attribute__((ext_vector_type(2)));
typedef __amdgpu_buffer_rsrc_t rsrc_t;
#define LDSP __attribute__((address_space(3)))
#define DI __device__ __forceinline__

#define STAMPS 0

constexpr u32 WS_EP = 0;
constexpr u32 WS_FA = 8192;
constexpr u32 WS_FB = WS_FA + 16384;
constexpr u32 WS_FC = WS_FB + 16384;
constexpr u32 WS_FD = WS_FC + 16384;
constexpr u32 WS_XG = WS_FD + 65536;
constexpr u32 WS_SG = WS_XG + 32768;
constexpr u32 WS_ZB = WS_SG + 524288;
constexpr u32 WS_Z1 = WS_ZB + 43 * 65536;
constexpr u32 KP_SZ = 17408;
constexpr u32 WS_KP = WS_Z1 + 64 * 43 * 1024;
constexpr u32 WS_KF = WS_KP + 128 * KP_SZ;
constexpr u32 WS_KZ = WS_KF + 65536;
constexpr u32 WS_OF = WS_KZ + 4096;
constexpr u32 WS_HF = WS_OF + 128 * 16384;
constexpr u32 WS_ST = WS_HF + 128 * 24 * 1024;
constexpr u32 WS_END = WS_ST + 256 * 8 * 32 * 8;

constexpr u32 LZ_W = 65536;
constexpr u32 LZ_U = 139264;
constexpr u32 LQ_M = 122880;
constexpr u32 LQ_A = 81920;
constexpr u32 LQ_PHI = 49152;
constexpr u32 LQ_V = 32768;
constexpr u32 L_TAB = 155648;
constexpr u32 L_XS = L_TAB;
constexpr u32 L_CS = L_TAB + 1024;
constexpr u32 L_ZST = L_TAB + 3072;
constexpr u32 L_WPHI = L_TAB + 3072;
constexpr u32 L_KFS = L_TAB + 2048;
constexpr u32 L_JUNK = L_TAB + 7168;
constexpr u32 L_WOUT = 0;
constexpr u32 L_RED = 57344;
constexpr u32 L_WO1 = 122880;

constexpr u64 Z2S = 0x26211B16110B0500ull;
constexpr u64 Z1S = 0x26211C16110C0600ull;
DI int grp_start(u64 tab, int c) { return c >= 8 ? 43 : (int)((tab >> (8 * c)) & 0xFF); }

#define SCHED() __builtin_amdgcn_sched_barrier(0)
#define TIC()
#define TOC(acc)
#define WAITV(n) do { TIC(); asm volatile("s_waitcnt vmcnt(%0)" ::"n"(n) : "memory"); TOC(acv); SCHED(); } while (0)
#define BARRIER() do { SCHED(); TIC(); asm volatile("s_waitcnt lgkmcnt(0)\n\ts_barrier" ::: "memory"); TOC(acb); SCHED(); } while (0)
#define WAITV_IF(g_, v, n) do { TIC(); asm volatile("s_cmp_lg_u32 %0, %1\n\ts_cbranch_scc1 1f\n\ts_waitcnt vmcnt(%2)\n1:" ::"s"(g_), "n"(v), "n"(n) : "scc", "memory"); TOC(acv); SCHED(); } while (0)
#define BAR_IF(g_, v, n) do { SCHED(); asm volatile("s_cmp_lg_u32 %0, %1\n\ts_cbranch_scc1 1f\n\ts_waitcnt vmcnt(%2) lgkmcnt(0)\n\ts_barrier\n1:" ::"s"(g_), "n"(v), "n"(n) : "scc", "memory"); SCHED(); } while (0)
#define WAITL0() asm volatile("s_waitcnt lgkmcnt(0)" ::: "memory")
#define PIN(x) asm volatile("" : "+v"(x))

DI void dma16(u32 lds, u32 voff, rsrc_t srd, u32 soff) {
  asm volatile("s_mov_b32 m0, %0\n\ts_nop 0\n\tbuffer_load_dwordx4 %1, %2, %3 offen lds" ::"s"(lds), "v"(voff), "s"(srd), "s"(soff) : "memory");
}
#define DMA_C(wb, CST, voff, srd, soff) asm volatile("s_add_u32 m0, %0, %1\n\ts_nop 0\n\tbuffer_load_dwordx4 %2, %3, %4 offen lds" ::"s"(wb), "n"(CST), "v"(voff), "s"(srd), "s"(soff) : "scc", "memory")
#define DMA_CC(wb, CST, voff, srd, OP, sb, SC) do { u32 so__; asm volatile("s_add_u32 m0, %1, %2\n\t" OP " %0, %5, %6\n\tbuffer_load_dwordx4 %3, %4, %0 offen lds" : "=&s"(so__) : "s"(wb), "n"(CST), "v"(voff), "s"(srd), "s"(sb), "n"(SC) : "scc", "memory"); } while (0)
DI void touch(u32 lds, u32 voff, rsrc_t srd, u32 soff) {
  asm volatile("s_mov_b32 m0, %0\n\ts_nop 0\n\tbuffer_load_dword %1, %2, %3 offen lds" ::"s"(lds), "v"(voff), "s"(srd), "s"(soff) : "memory");
}
DI u32x4 ld16_sc1(u32 voff, rsrc_t srd, u32 soff) {
  u32x4 r;
  asm volatile("buffer_load_dwordx4 %0, %1, %2, %3 offen sc1" : "=v"(r) : "v"(voff), "s"(srd), "s"(soff) : "memory");
  return r;
}
template <int OFF> DI u32x4 ld16_sc1o(u32 voff, rsrc_t srd, u32 soff) {
  u32x4 r;
  asm volatile("buffer_load_dwordx4 %0, %1, %2, %3 offen offset:%4 sc1" : "=v"(r) : "v"(voff), "s"(srd), "s"(soff), "n"(OFF) : "memory");
  return r;
}
#define BPOL " sc1"
DI u32x4 ld16_b(u32 voff, rsrc_t srd, u32 soff) {
  u32x4 r;
  asm volatile("buffer_load_dwordx4 %0, %1, %2, %3 offen" BPOL : "=v"(r) : "v"(voff), "s"(srd), "s"(soff) : "memory");
  return r;
}
template <int OFF> DI u32x4 ld16_bo(u32 voff, rsrc_t srd, u32 soff) {
  u32x4 r;
  asm volatile("buffer_load_dwordx4 %0, %1, %2, %3 offen offset:%4" BPOL : "=v"(r) : "v"(voff), "s"(srd), "s"(soff), "n"(OFF) : "memory");
  return r;
}
DI void ld16_b_if6(u32x4& r, int gn, u32 voff, rsrc_t srd, u32 soff) {
  asm volatile("s_cmp_lg_u32 %4, 6\n\ts_cbranch_scc1 1f\n\tbuffer_load_dwordx4 %0, %1, %2, %3 offen" BPOL "\n1:" : "+v"(r) : "v"(voff), "s"(srd), "s"(soff), "s"(gn) : "scc", "memory");
}
DI u32x4 ld16(u32 voff, rsrc_t srd, u32 soff) {
  u32x4 r;
  asm volatile("buffer_load_dwordx4 %0, %1, %2, %3 offen" : "=v"(r) : "v"(voff), "s"(srd), "s"(soff) : "memory");
  return r;
}
DI u32x2 ld8_sc1(u32 voff, rsrc_t srd, u32 soff) {
  u32x2 r;
  asm volatile("buffer_load_dwordx2 %0, %1, %2, %3 offen sc1" : "=v"(r) : "v"(voff), "s"(srd), "s"(soff) : "memory");
  return r;
}
DI u32 ld2(u32 voff, rsrc_t srd, u32 soff) {
  u32 r;
  asm volatile("buffer_load_ushort %0, %1, %2, %3 offen" : "=v"(r) : "v"(voff), "s"(srd), "s"(soff) : "memory");
  return r;
}
DI void st16_sc1(u32x4 v, u32 voff, rsrc_t srd, u32 soff) {
  asm volatile("buffer_store_dwordx4 %0, %1, %2, %3 offen sc1\n\ts_nop 1" ::"v"(v), "v"(voff), "s"(srd), "s"(soff) : "memory");
}
DI void st8_sc1(u32x2 v, u32 voff, rsrc_t srd, u32 soff) {
  asm volatile("buffer_store_dwordx2 %0, %1, %2, %3 offen sc1" ::"v"(v), "v"(voff), "s"(srd), "s"(soff) : "memory");
}

DI u32x4 lds16(u32 a) { return *(const LDSP u32x4*)(size_t)a; }
DI u32x2 lds8(u32 a) { return *(const volatile LDSP u32x2*)(size_t)a; }
DI float lds4f(u32 a) { return *(const LDSP float*)(size_t)a; }
DI f32x4 lds16f(u32 a) { return *(const LDSP f32x4*)(size_t)a; }
DI void sts16f(u32 a, f32x4 v) { *(LDSP f32x4*)(size_t)a = v; }
DI void sts16(u32 a, u32x4 v) { *(LDSP u32x4*)(size_t)a = v; }
DI void sts8(u32 a, u32x2 v) { *(volatile LDSP u32x2*)(size_t)a = v; }
DI void sts4f(u32 a, float v) { *(LDSP float*)(size_t)a = v; }
DI u32x2 ldstr(u32 a) {
  return __builtin_bit_cast(u32x2, __builtin_amdgcn_ds_read_tr16_b64_v4i16((LDSP s16x4*)(size_t)a)); }
DI u32x4 cat(u32x2 lo, u32x2 hi) { return (u32x4){lo[0], lo[1], hi[0], hi[1]}; }
DI u32x4 ldfrag(u32 a, u32 d) { u32x2 lo = ldstr(a), hi = ldstr(a + d); return cat(lo, hi); }

DI f32x4 mfma32(u32x4 a, u32x4 b, f32x4 c) {
  return __builtin_amdgcn_mfma_f32_16x16x32_bf16(__builtin_bit_cast(bf16x8, a), __builtin_bit_cast(bf16x8, b), c, 0, 0, 0);
}
DI f32x16 mfmaL(u32x4 a, u32x4 b, f32x16 c) {
  return __builtin_amdgcn_mfma_f32_32x32x16_bf16(__builtin_bit_cast(bf16x8, a), __builtin_bit_cast(bf16x8, b), c, 0, 0, 0);
}
DI u32 pk2(float a, float b) { bf16x2 v = {(__bf16)a, (__bf16)b}; return __builtin_bit_cast(u32, v); }
DI float blo(u32 u) { return __uint_as_float(u << 16); }
DI float bhi(u32 u) { return __uint_as_float(u & 0xffff0000u); }
DI float rbf(float a) { return blo(pk2(a, a)); }
DI float dot2(u32 a, u32 b, float c) {
  return __builtin_amdgcn_fdot2_f32_bf16(__builtin_bit_cast(bf16x2, a), __builtin_bit_cast(bf16x2, b), c, false);
}
DI f32x2 fma2(f32x2 a, f32x2 b, f32x2 c) { return __builtin_elementwise_fma(a, b, c); }
DI f32x2 bc2(float a) { return (f32x2){a, a}; }
DI f32x2 lo2(f32x4 v) { return (f32x2){v[0], v[1]}; }
DI f32x2 hi2(f32x4 v) { return (f32x2){v[2], v[3]}; }
DI u32 pk2v(f32x2 v) { return pk2(v[0], v[1]); }
DI f32x2 gelu2(f32x2 x) {
  f32x2 u = fma2(x * x, bc2(-0.10294324f), bc2(-2.3022082f)) * x;
  f32x2 e = {__builtin_amdgcn_exp2f(u[0]), __builtin_amdgcn_exp2f(u[1])};
  e = e + bc2(1.0f);
  f32x2 r = {__builtin_amdgcn_rcpf(e[0]), __builtin_amdgcn_rcpf(e[1])};
  return x * r;
}
DI void red2g(float& a, float& b) {
  auto s = __builtin_amdgcn_permlane32_swap(__float_as_uint(a), __float_as_uint(b), false, false);
  float c = __uint_as_float(s[0]) + __uint_as_float(s[1]);
  auto s2 = __builtin_amdgcn_permlane16_swap(__float_as_uint(c), __float_as_uint(c), false, false);
  float d = __uint_as_float(s2[0]) + __uint_as_float(s2[1]);
  auto s3 = __builtin_amdgcn_permlane32_swap(__float_as_uint(d), __float_as_uint(d), false, false);
  a = __uint_as_float(s3[0]);
  b = __uint_as_float(s3[1]);
}
template <int CTRL> DI float dpp(float v) {
  return __uint_as_float(__builtin_amdgcn_update_dpp(0, __float_as_uint(v), CTRL, 0xF, 0xF, true));
}
template <class F> DI void hedgehog16(const f32x16& p, float s, u32x4 (&o)[4], F hook) {
  float mx = fmaxf(p[0], p[1]), mn = fminf(p[0], p[1]);
#pragma unroll
  for (int i = 2; i < 16; i += 2) { mx = fmaxf(fmaxf(mx, p[i]), p[i + 1]); mn = fminf(fminf(mn, p[i]), p[i + 1]); }
  const float c = s * 1.44269504f;
  const float a = -mx * c, b = mn * c;
  f32x2 ep[8], en[8];
#pragma unroll
  for (int i = 0; i < 8; ++i) {
    hook(i);
    const f32x2 v = {p[2 * i], p[2 * i + 1]};
    const f32x2 u = fma2(v, bc2(c), bc2(a)), w = fma2(v, bc2(-c), bc2(b));
    ep[i] = (f32x2){__builtin_amdgcn_exp2f(u[0]), __builtin_amdgcn_exp2f(u[1])};
    en[i] = (f32x2){__builtin_amdgcn_exp2f(w[0]), __builtin_amdgcn_exp2f(w[1])};
  }
  hook(8);
  const f32x2 sp = ((ep[0] + ep[1]) + (ep[2] + ep[3])) + ((ep[4] + ep[5]) + (ep[6] + ep[7]));
  const f32x2 sn = ((en[0] + en[1]) + (en[2] + en[3])) + ((en[4] + en[5]) + (en[6] + en[7]));
  const float ip = __builtin_amdgcn_rcpf(sp[0] + sp[1]), in = __builtin_amdgcn_rcpf(sn[0] + sn[1]);
#pragma unroll
  for (int i = 0; i < 8; ++i) {
    if (i == 0 || i == 3 || i == 6) hook(9 + i / 3);
    o[i >> 2][i & 3] = pk2v(ep[i] * bc2(ip));
    o[2 + (i >> 2)][i & 3] = pk2v(en[i] * bc2(in));
  }
}
DI void ln_fin(float s, float q, float invn, float& r, float& m) {
  m = s * invn;
  float v = fmaxf(q * invn - m * m, 0.0f);
  float sd = rbf(__builtin_amdgcn_sqrtf(rbf(rbf(v) + 1e-5f)));
  r = __builtin_amdgcn_rcpf(sd);
  m = rbf(m);
}

#define POLL_SLEEP 6
constexpr int SPIN_CAP = 1 << 17;
DI void poll_flags(u32 voff, bool act, rsrc_t srd, u32 soff, u32 tlo, u32 thi) {
#pragma nounroll
  for (int it = 0; it < SPIN_CAP; ++it) {
    u32x2 v;
    asm volatile("buffer_load_dwordx2 %0, %1, %2, %3 offen sc1\n\ts_waitcnt vmcnt(0)" : "=&v"(v) : "v"(voff), "s"(srd), "s"(soff) : "memory");
    bool ok = !act || (v[0] == tlo && v[1] == thi);
    if (__all(ok)) break;
    __builtin_amdgcn_s_sleep(POLL_SLEEP);
  }
}
DI bool tag_ok(u32x4 q, u32 t) { return q[1] == t && q[3] == t; }
DI void poll_gran(u32x4& q, u32 voff, rsrc_t srd, u32 soff, u32 tlo) {
#pragma nounroll
  for (int it = 0; it < SPIN_CAP; ++it) {
    if (__all(tag_ok(q, tlo))) break;
    __builtin_amdgcn_s_sleep(POLL_SLEEP);
    asm volatile("buffer_load_dwordx4 %0, %1, %2, %3 offen sc1\n\ts_waitcnt vmcnt(0)" : "=&v"(q) : "v"(voff), "s"(srd), "s"(soff) : "memory");
  }
}
DI u32 opq(u32 v) { asm volatile("" : "+v"(v)); return v; }

#define STAMP(i)
#define STAMPC(i)

#define GQ_N 3
#define PREFETCH 0
#define CODE_PF 56000
#define OUT_SC1 1
#define PRIO 0
#define UROT 1
static_assert(WS_ZB % 8192 == 0, "");
#define TLB_PF 0
#define WSP_PF 0
#define HC_K 10
#define FC_J 2
#define VM(x) do { x; ++vq; } while (0)
#define WAITQ(mark) WAITV(vq - (mark))

template <int t>
DI void run(const char* __restrict__ x, const char* __restrict__ wqkv, const char* __restrict__ wphi, const char* __restrict__ wo,
            const char* __restrict__ win, const char* __restrict__ wsp, const char* __restrict__ bsp, const char* __restrict__ wout,
            char* __restrict__ out, char* ws, u64 ep) {
  const int tid = threadIdx.x, lane = tid & 63;
  const int w = __builtin_amdgcn_readfirstlane(tid >> 6);
  const int g = lane >> 4;
  const int b = blockIdx.x, xcd = b & 7, r = b >> 3;
  const int Pl = r >> 3, cl = r & 3;
  const int P = (xcd >> 1) * 4 + Pl;
  const int rot = 2 * ((Pl + cl) & 3);
  u32 acv = 0, acb = 0; (void)acv; (void)acb;
  const rsrc_t sx = __builtin_amdgcn_make_buffer_rsrc((void*)(x + (size_t)P * 131072), 0, 131072, 0x00020000);
  const u32 vx0 = (16 * w + (lane >> 3)) * 1024 + (((lane & 7) ^ g) * 16);
  const u32 vx1 = (vx0 + 8192) ^ 64;
  const u32 w2k = w * 2048;
#define DMA_X(dst, n, e) DMA_C(w2k, (dst) + (e) * 1024, (e) ? vx1 : vx0, sx, (((n) + rot) & 7) * 128)
  const int rw = w & 3, cw = w >> 2, i16 = lane & 15;
  const int c = (xcd & 1) * 4 + cl;
  const int p = 2 * P + (cl & 1);
  const int c2 = (xcd & 1) * 4 + (t ? 3 * (cl >> 1) : 1 + (cl >> 1));
  const u32 l16 = opq(lane * 16);
  const rsrc_t sa = __builtin_amdgcn_make_buffer_rsrc((void*)(wsp + (size_t)p * 262144), 0, 262144, 0x00020000);
  const rsrc_t sws = __builtin_amdgcn_make_buffer_rsrc((void*)ws, 0, WS_END, 0x00020000);
  const u32 vA0 = (lane >> 3) * 4096 + (((lane & 7) ^ g) * 16);
  const u32 vA1 = (vA0 + 32768) ^ 64;
  const int ux0 = UROT ? (cl >> 1) : 0, ux1 = UROT ? t : 0;
  const u32 uxs = w * 512 + ux1 * 256 + ux0 * 131072;
#define DMA_U(u, base, e) DMA_CC(w2k, (base) + (e) * 1024, (e) ? vA1 : vA0, sa, "s_xor_b32", uxs, (((u) >> 1) & 3) * 128 + (((u) >> 3) * 32 + ((u) & 1) * 16) * 4096)
  const f32x4 zero4 = {0, 0, 0, 0};
  u32x4 ones4 = {0x3F803F80u, 0x3F803F80u, 0x3F803F80u, 0x3F803F80u};
  u32 tlo, thi;
  u32x4 af[4][8];
  u32x4 bfr[2][8];
  u32x2 gf;
  const u32 ard = w * 2048 + i16 * 128 + (((g >> 1) ^ (i16 >> 1)) * 16) + (g & 1) * 8;
#define READ_U(u, base)                                                                                             \
  {                                                                                                                 \
    const u32 ab_ = opq(ard + (base));                                                                              \
    _Pragma("unroll") for (int kk = 0; kk < 2; ++kk)                                                               \
      af[2 * ((u) >> 3) + ((u) & 1)][((u) & 6) + kk] = cat(lds8(ab_ ^ (64 * kk)), lds8(ab_ ^ (64 * kk + 32)));      \
  }
#define USE_EPOCH() { asm volatile("" : "+s"(ep)); ep += 1; tlo = (u32)ep; thi = (u32)(ep >> 32); PIN(ones4); }
#define EARLY_STATS(mark)                                                                                           \
  {                                                                                                                 \
    WAITQ(mark); PIN(xrow);                                                                                         \
    float s_ = 0, q_ = 0;                                                                                           \
    _Pragma("unroll") for (int e = 0; e < 4; ++e) { s_ = dot2(xrow[e], ones4[0], s_); q_ = dot2(xrow[e], xrow[e], q_); } \
    s_ += dpp<0x128>(s_); q_ += dpp<0x128>(q_);                                                                     \
    s_ += dpp<0x124>(s_); q_ += dpp<0x124>(q_);                                                                     \
    s_ += dpp<0x4E>(s_); q_ += dpp<0x4E>(q_);                                                                       \
    s_ += dpp<0xB1>(s_); q_ += dpp<0xB1>(q_);                                                                       \
    red2g(s_, q_);                                                                                                  \
    float r_, m_;                                                                                                   \
    ln_fin(s_, q_, 1.0f / 512, r_, m_);                                                                             \
    VM(st16_sc1((u32x4){__float_as_uint(r_), tlo, __float_as_uint(m_), tlo}, l16 << 22, sws, WS_XG + (P * 128 + (t * 8 + c) * 8 + w) * 16)); \
  }
#define GATHER_XS()                                                                                                 \
  WAITQ(mxg); PIN(xg);                                                                                              \
  poll_gran(xg, i16 * 16, sws, WS_XG + (P * 128 + 16 * w) * 16, tlo);                                               \
  sts4f(L_XS + (16 * w + i16) * 4, __uint_as_float(xg[0]));                                                         \
  sts4f(L_XS + 512 + (16 * w + i16) * 4, __uint_as_float(xg[2]));
#define RELANE() const int lane = opq(l16) >> 4, i16 = lane & 15, g = lane >> 4, tid = w * 64 + lane; (void)tid; (void)g; (void)i16
#define CLEAR_H() if (cw == 0) { const u32x4 f_ = {~0u, ~0u, ~0u, ~0u}; const u32 o_ = WS_HF + ((4 * p + rw) * 24 + c2) * 1024; \
    st16_sc1(f_, l16, sws, o_); st16_sc1(f_, l16, sws, o_ + 8192); st16_sc1(f_, l16, sws, o_ + 16384); }
#define SET_FLAG(off) st8_sc1((u32x2){tlo, thi}, l16 << 22, sws, (off))
  const int gs = grp_start(Z2S, c2), gn = t ? 5 : grp_start(Z2S, c2 + 1) - gs;
  const int trot = (p & 7) >= 5 ? (p & 7) - 5 : (p & 7);
#define TILE(j, n_) (((j) + trot) >= (n_) ? (j) + trot - (n_) : (j) + trot)
  const u32 zb0 = WS_ZB + (gs * 64 + 8 * w) * 1024 + ux1 * 4096;
#define DMA_WOUT(e)                                                                                                 \
  {                                                                                                                 \
    const rsrc_t so_ = __builtin_amdgcn_make_buffer_rsrc((void*)wout, 0, 688 * 1024, 0x00020000);                   \
    const int ws_ = grp_start(Z2S, w), wn_ = grp_start(Z2S, w + 1) - ws_;                                           \
    if ((e) < 6) dma16(L_WOUT + ((e) < 4 ? 2 * w + ((e) >> 1) : 16 + w) * 2048 + ((e) & 1) * 1024, vwo, so_, ((ws_ + TILE((e) >> 1, wn_)) * 2 + ((e) & 1)) * 8192); \
    else if ((e) == 6) dma16(L_WOUT + 24 * 2048 + w * 1024, vwo, so_, ((((w >> 1) == 0 ? 5 : ((w >> 1) == 1 ? 11 : ((w >> 1) == 2 ? 27 : 64))) + TILE(3, 6)) * 2 + (w & 1)) * 8192); \
  }
#define DMA_WOUT_AT(dst, e)                                                                                         \
  {                                                                                                                 \
    const rsrc_t so_ = __builtin_amdgcn_make_buffer_rsrc((void*)wout, 0, 688 * 1024, 0x00020000);                   \
    const int ws_ = grp_start(Z2S, w), wn_ = grp_start(Z2S, w + 1) - ws_;                                           \
    dma16((dst) + ((e) - 7) * 1024, vwo, so_, ((ws_ + TILE(wn_ - 2 + (((e) - 7) >> 1), wn_)) * 2 + (((e) - 7) & 1)) * 8192); \
  }
#define VWO() const u32 vwo = ((lane >> 2) & 7) * 1024 + c2 * 128 + (lane >> 5) * 64 + (((lane & 3) ^ (((lane >> 4) & 1) << 1)) * 16)
#define LOAD_B(u, so_)                                                                                              \
  bfr[u][0] = ld16_bo<0>(l16, sws, so_); bfr[u][1] = ld16_bo<1024>(l16, sws, so_);                                  \
  bfr[u][2] = ld16_bo<2048>(l16, sws, so_); bfr[u][3] = ld16_bo<3072>(l16, sws, so_);                               \
  bfr[u][4] = ld16_bo<0>(l16, sws, (so_) ^ 4096); bfr[u][5] = ld16_bo<1024>(l16, sws, (so_) ^ 4096);                \
  bfr[u][6] = ld16_bo<2048>(l16, sws, (so_) ^ 4096); bfr[u][7] = ld16_bo<3072>(l16, sws, (so_) ^ 4096);

  if constexpr (t == 0) {
    int vq = 2;
    const rsrc_t sw = __builtin_amdgcn_make_buffer_rsrc((void*)win, 0, 512 * 2752, 0x00020000);
    const int z2s = grp_start(Z2S, c), z2n = grp_start(Z2S, c + 1) - z2s;
    const int z1s = grp_start(Z1S, c), z1n = grp_start(Z1S, c + 1) - z1s;
    u32 vw[3];
    int wch[3];
#pragma unroll
    for (int e = 0; e < 3; ++e) {
      const int sg = 2 * e + (lane >> 5), hi = sg >= 3;
      wch[e] = (sg - 3 * hi) * 4 + ((lane & 3) ^ (hi << 1));
      vw[e] = wch[e] < 2 * z2n ? (16 * w + 8 * hi + ((lane >> 2) & 7)) * 2752 + 1376 + 32 * z2s + 16 * wch[e] : 0x7FFF0000u;
    }
    const u32 w3k = w * 3072;
#define DMA_W(bk, e) DMA_C(w3k, LZ_W + ((bk) % 3) * 24576 + (e) * 1024, vw[e], sw, ((2 * (bk) + rot) & 7) * (64 * 2752))
#define UBASE(u) ((u) % 5 == 0 ? LZ_U : ((u) % 5 - 1) * 16384)
#define ISS_X(n) do { VM(DMA_X(((n) & 3) * 16384, n, 0)); VM(DMA_X(((n) & 3) * 16384, n, 1)); mx[n] = vq; } while (0)
#define ISS_U(u) do { VM(DMA_U(u, UBASE(u), 0)); VM(DMA_U(u, UBASE(u), 1)); mu[u] = vq; } while (0)
#define ISS_W(bk, e0, e1) do { _Pragma("unroll") for (int e_ = e0; e_ < e1; ++e_) VM(DMA_W(bk, e_)); mw[bk] = vq; } while (0)
    int mx[8], mw[8], mu[16];
    mx[0] = vq;
    ISS_W(0, 0, 3);
    u32x4 xrow;
    VM(xrow = ld16(l16, sx, (c * 8 + w) * 1024));
    const int mxr = vq;
    ISS_X(1);
    const u32 xa0 = opq((32 * rw + i16) * 128 + ((g ^ (i16 >> 1)) * 16)), xa1 = xa0 ^ 64;
    u32 ta[3];
    int tl[3];
    {
      const u32 trb = LZ_W + g * 1536 + ((lane >> 2) & 3) * 64 + ((((g & 1) << 1) | ((lane >> 1) & 1)) * 16) + (lane & 1) * 8;
#pragma unroll
      for (int j = 0; j < 3; ++j) {
        tl[j] = 3 * cw + (j + rw) % 3;
        ta[j] = (trb + (tl[j] >> 1) * 512) ^ ((tl[j] & 1) * 32);
      }
    }
#define NEXT_BLOCK(bk) _Pragma("unroll") for (int j = 0; j < 3; ++j) ta[j] = opq(ta[j] + ((bk) == 0 ? 0 : ((bk) % 3 == 0 ? -49152 : 24576)))
    f32x4 acc[2][3], csa = zero4;
#pragma unroll
    for (int j = 0; j < 3; ++j) acc[0][j] = acc[1][j] = zero4;
    u32x4 xf[16][2], wf[2][3];
    u32x4 xg;
    int mxg = 0;
    WAITQ(mw[0]);
    BARRIER();
    USE_EPOCH()
    STAMP(7);
#define P1_MEM(n, h)                                                                                                \
  {                                                                                                                 \
    if ((n) == 0) { if ((h) == 0) ISS_X(2); if ((h) == 1) ISS_W(1, 0, 2); if ((h) == 2) ISS_W(1, 2, 3); if ((h) == 3) ISS_X(3); } \
    if ((n) == 1) { if ((h) == 0) ISS_W(2, 0, 2); if ((h) == 1) ISS_W(2, 2, 3); if ((h) == 2) VM(DMA_X(0, 4, 0)); if ((h) == 3) { VM(DMA_X(0, 4, 1)); mx[4] = vq; } } \
    if ((n) == 2 || (n) == 4) { if ((h) < 2) { VM(DMA_X((((n) + 3) & 3) * 16384, (n) + 3, (h) & 1)); mx[(n) + 3] = vq; } else ISS_W((n) / 2 + 2, (h) - 2, (h) - 1); } \
    if ((n) == 3) { if ((h) == 0) ISS_W(3, 2, 3); else if ((h) < 3) { VM(DMA_X(32768, 6, (h) - 1)); mx[6] = vq; } else ISS_U(0); } \
    if ((n) == 5) { if ((h) == 0) ISS_W(4, 2, 3); if ((h) == 1) { VM(xg = ld16_sc1(i16 * 16, sws, WS_XG + (P * 128 + 16 * w) * 16)); mxg = vq; } \
                    if ((h) >= 2) { VM(DMA_U(1, UBASE(1), (h) - 2)); mu[1] = vq; } }                                 \
    if ((n) == 6) { if ((h) < 3) ISS_W(5, h, (h) + 1); else ISS_U(2); }                                             \
    if ((n) == 7) { if ((h) < 2) { VM(DMA_U(3, UBASE(3), h)); mu[3] = vq; } }                                       \
  }
#pragma unroll
    for (int ks = 0; ks <= 16; ++ks) {
      const int n = ks >> 1, kk = ks & 1;
      if (ks > 0 && ks < 16 && kk == 0) {
        { const int mk = (n & 1) ? mx[n] : (mx[n] > mw[n >> 1] ? mx[n] : mw[n >> 1]); WAITQ(mk); }
        BARRIER();
      }
      if (ks < 16 && (ks & 3) == 0) { NEXT_BLOCK(ks >> 2); }
      if (ks == 8) {
#pragma unroll
        for (int e = 0; e < 3; ++e)
          vw[e] = wch[e] < 2 * z1n ? (16 * w + 8 * (2 * e + (lane >> 5) >= 3) + ((lane >> 2) & 7)) * 2752 + 32 * z1s + 16 * wch[e] : 0x7FFF0000u;
      }
      const int k1 = ks > 0 ? ks - 1 : 0, q1 = k1 & 1;
#pragma unroll
      for (int m = 0; m < 7; ++m) {
        if (ks > 0) {
          if (m < 6) acc[m & 1][m >> 1] = mfma32(xf[k1][m & 1], wf[q1][m >> 1], acc[m & 1][m >> 1]);
          else csa = mfma32(ones4, wf[q1][0], csa);
          SCHED();
        }
        if (ks < 16) {
          if (m == 0) { P1_MEM(n, 2 * kk) xf[ks & 15][0] = lds16((kk ? xa1 : xa0) + (n & 3) * 16384); xf[ks & 15][1] = lds16((kk ? xa1 : xa0) + (n & 3) * 16384 + 2048); }
          else if (m < 4) wf[kk][m - 1] = ldfrag(ta[m - 1] + (ks & 3) * 6144, 256);
          if (m == 4) P1_MEM(n, 2 * kk + 1)
        }
        SCHED();
      }
#pragma unroll
      for (int j = 0; j < 3; ++j) { PIN(acc[0][j]); PIN(acc[1][j]); }
      PIN(csa);
      if (ks == 2) EARLY_STATS(mxr)
    }
    GATHER_XS()
    sts4f(L_CS + tl[0] * 64 + i16 * 4, csa[0]);
    WAITQ(mu[1] > mw[4] ? mu[1] : mw[4]);
    READ_U(0, UBASE(0)) READ_U(1, UBASE(1))
    BARRIER();
    STAMP(1);
    u32x4 zp[3];
    ISS_W(6, 0, 3);
    {
      f32x2 ps[4], pq[4];
      float cs[3];
#pragma unroll
      for (int j = 0; j < 3; ++j) cs[j] = -lds4f(L_CS + tl[j] * 64 + i16 * 4);
#pragma unroll
      for (int rt = 0; rt < 2; ++rt) {
        const f32x4 rr = lds16f(L_XS + (32 * rw + 16 * rt + 4 * g) * 4), mm = lds16f(L_XS + 512 + (32 * rw + 16 * rt + 4 * g) * 4);
        f32x2 z0[3], z1[3];
#pragma unroll
        for (int j = 0; j < 3; ++j) {
          z0[j] = gelu2(fma2(lo2(mm), bc2(cs[j]), lo2(acc[rt][j])) * lo2(rr));
          z1[j] = gelu2(fma2(hi2(mm), bc2(cs[j]), hi2(acc[rt][j])) * hi2(rr));
          zp[j][2 * rt] = pk2v(z0[j]); zp[j][2 * rt + 1] = pk2v(z1[j]);
        }
        ps[2 * rt] = z0[0] + z0[1] + z0[2]; ps[2 * rt + 1] = z1[0] + z1[1] + z1[2];
        pq[2 * rt] = fma2(z0[2], z0[2], fma2(z0[1], z0[1], z0[0] * z0[0]));
        pq[2 * rt + 1] = fma2(z1[2], z1[2], fma2(z1[1], z1[1], z1[0] * z1[0]));
        SCHED();
        if (rt == 0) { ISS_U(4); ISS_U(5); }
        else ISS_U(6);
        SCHED();
      }
      const bool b3 = lane & 8, b2 = lane & 4, b1 = lane & 2;
#pragma unroll
      for (int m = 0; m < 2; ++m)
#pragma unroll
        for (int e = 0; e < 2; ++e) {
          ps[m][e] = (b3 ? ps[m + 2][e] : ps[m][e]) + dpp<0x128>(b3 ? ps[m][e] : ps[m + 2][e]);
          pq[m][e] = (b3 ? pq[m + 2][e] : pq[m][e]) + dpp<0x128>(b3 ? pq[m][e] : pq[m + 2][e]);
        }
#pragma unroll
      for (int e = 0; e < 2; ++e) {
        ps[0][e] = (b2 ? ps[1][e] : ps[0][e]) + dpp<0x141>(b2 ? ps[0][e] : ps[1][e]);
        pq[0][e] = (b2 ? pq[1][e] : pq[0][e]) + dpp<0x141>(b2 ? pq[0][e] : pq[1][e]);
      }
      float s_ = (b1 ? ps[0][1] : ps[0][0]) + dpp<0x4E>(b1 ? ps[0][0] : ps[0][1]);
      float q_ = (b1 ? pq[0][1] : pq[0][0]) + dpp<0x4E>(b1 ? pq[0][0] : pq[0][1]);
      s_ += dpp<0xB1>(s_);
      q_ += dpp<0xB1>(q_);
      const int row = 32 * rw + 16 * (i16 >> 3) + 4 * g + ((i16 >> 1) & 3);
      VM(st16_sc1((u32x4){__float_as_uint(s_), tlo, __float_as_uint(q_), tlo}, row * 16 + ((lane & 1) << 30), sws, WS_SG + ((P * 8 + c) * 2 + cw) * 2048));
      PIN(zp[0]); PIN(zp[1]); PIN(zp[2]);
      CLEAR_H()
    }
    STAMP(2);
    {
    RELANE();
#pragma unroll
    for (int j = 0; j < 3; ++j) acc[0][j] = acc[1][j] = zero4;
    csa = zero4;
    u32x4 q0, q1, q2, q3;
    int mq = 0, mz = 0;
    const u32 gvo = (2 * g) * 4096 + (32 * rw + 16 * cw + i16) * 16;
    const u32 gso = WS_SG + P * 32768;
#pragma unroll
    for (int ks = 0; ks <= 16; ++ks) {
      if (ks == 8) {
        WAITQ(mq); PIN(q0); PIN(q1); PIN(q2); PIN(q3);
#pragma nounroll
        for (int it = 0; it < SPIN_CAP; ++it) {
          if (__all(tag_ok(q0, tlo) && tag_ok(q1, tlo) && tag_ok(q2, tlo) && tag_ok(q3, tlo))) break;
          __builtin_amdgcn_s_sleep(POLL_SLEEP);
          asm volatile(
              "buffer_load_dwordx4 %0, %4, %5, %6 offen sc1\n\t"
              "buffer_load_dwordx4 %1, %4, %5, %6 offen offset:2048 sc1\n\t"
              "buffer_load_dwordx4 %2, %4, %5, %7 offen sc1\n\t"
              "buffer_load_dwordx4 %3, %4, %5, %7 offen offset:2048 sc1\n\t"
              "s_waitcnt vmcnt(0)"
              : "=&v"(q0), "=&v"(q1), "=&v"(q2), "=&v"(q3)
              : "v"(gvo), "s"(sws), "s"(gso), "s"(gso + 4096)
              : "memory");
        }
        float s = (__uint_as_float(q0[0]) + __uint_as_float(q1[0])) + (__uint_as_float(q2[0]) + __uint_as_float(q3[0]));
        float q = (__uint_as_float(q0[2]) + __uint_as_float(q1[2])) + (__uint_as_float(q2[2]) + __uint_as_float(q3[2]));
        red2g(s, q);
        float r2, m2;
        ln_fin(s, q, 1.0f / 688, r2, m2);
        sts4f(L_ZST + (32 * rw + 16 * cw + i16) * 4, r2);
        sts4f(L_ZST + 512 + (32 * rw + 16 * cw + i16) * 4, -m2 * r2);
        STAMP(3);
      }
      if (ks < 16 && (ks & 3) == 0) {
        if (ks > 0) { WAITQ(ks == 12 ? mz : mw[4 + (ks >> 2)]); BARRIER(); }
        if (ks == 12) { if (w == 0) SET_FLAG(WS_FA + (P * 8 + c) * 128); STAMP(5); }
        NEXT_BLOCK(4 + (ks >> 2));
      }
      if (ks == 8) {
#pragma unroll
        for (int rt = 0; rt < 2; ++rt) {
          const u32 ra = L_ZST + (32 * rw + 16 * rt + 4 * g) * 4;
          const f32x4 r4 = lds16f(ra), m4 = lds16f(ra + 512);
#pragma unroll
          for (int j = 0; j < 3; ++j) {
            const u32 u0 = zp[j][2 * rt], u1 = zp[j][2 * rt + 1];
            zp[j][2 * rt] = pk2v(fma2((f32x2){blo(u0), bhi(u0)}, lo2(r4), lo2(m4)));
            zp[j][2 * rt + 1] = pk2v(fma2((f32x2){blo(u1), bhi(u1)}, hi2(r4), hi2(m4)));
          }
        }
#pragma unroll
        for (int j = 0; j < 3; ++j)
          VM(st16_sc1(zp[j], l16, sws, (tl[j] < z2n) ? WS_ZB + ((z2s + tl[j]) * 64 + 4 * P + rw) * 1024 : 0x7FFF0000u));
        mz = vq;
        STAMP(4);
        SCHED();
      }
      if (ks >= 5 && ks <= 9) { WAITQ(mu[ks - 3]); READ_U(ks - 3, UBASE(ks - 3)) }
      if (ks >= 9 && ks <= 13) { WAITQ(mu[ks - 2]); READ_U(ks - 2, UBASE(ks - 2)) }
      if (ks >= 13) { WAITQ(mu[ks - 1]); READ_U(ks - 1, UBASE(ks - 1)) }
      const int k1 = ks > 0 ? ks - 1 : 0, q1_ = k1 & 1;
#pragma unroll
      for (int m = 0; m < 7; ++m) {
        if (ks > 0) {
          if (m < 6) acc[m & 1][m >> 1] = mfma32(wf[q1_][m >> 1], xf[k1][m & 1], acc[m & 1][m >> 1]);
          else csa = mfma32(ones4, wf[q1_][0], csa);
          SCHED();
        }
        if (ks < 16 && m >= 1 && m < 4) wf[ks & 1][m - 1] = ldfrag(ta[m - 1] + (ks & 3) * 6144, 256);
        if (m == 0 || m == 4) {
          const int h = m == 4;
          if (h == 0 && ks >= 6 && ks <= 13) asm volatile("s_waitcnt lgkmcnt(10)" ::: "memory");
          if (ks >= 6 && ks <= 10) { VM(DMA_U(ks + 1, UBASE(ks + 1), h)); mu[ks + 1] = vq; }
          if (ks >= 10 && ks <= 13) { VM(DMA_U(ks + 2, UBASE(ks + 2), h)); mu[ks + 2] = vq; }
        }
        if (m == 6) {
          if (ks >= 4 && ks <= 6) ISS_W(7, ks - 4, ks - 3);
        }
        if (m == 2) {
          if (ks == 5) { VM(q0 = ld16_sc1o<0>(gvo, sws, gso)); VM(q1 = ld16_sc1o<2048>(gvo, sws, gso)); }
          if (ks == 6) { VM(q2 = ld16_sc1o<0>(gvo, sws, gso + 4096)); VM(q3 = ld16_sc1o<2048>(gvo, sws, gso + 4096)); mq = vq; }
        }
        SCHED();
      }
#pragma unroll
      for (int j = 0; j < 3; ++j) { PIN(acc[0][j]); PIN(acc[1][j]); }
      PIN(csa);
    }
    sts4f(L_CS + 512 + tl[0] * 64 + i16 * 4, csa[0]);
    gf = ld8_sc1((lane >> 5) * 1024, sws, WS_FA + (16 * w + c2) * 128);
    BARRIER();
    VWO();
    DMA_WOUT(0) DMA_WOUT(1)
    WAITV(2);
    PIN(gf);
    if (!__all(gf[0] == tlo && gf[1] == thi)) poll_flags((lane >> 5) * 1024, true, sws, WS_FA + (16 * w + c2) * 128, tlo, thi);
    STAMP(6);
    {
      float xr[2], xm[2];
#pragma unroll
      for (int rt = 0; rt < 2; ++rt) {
        xr[rt] = lds4f(L_XS + (32 * rw + 16 * rt + i16) * 4);
        xm[rt] = -lds4f(L_XS + 512 + (32 * rw + 16 * rt + i16) * 4);
      }
      const u32 so0 = zb0 + TILE(0, gn) * 65536;
#pragma unroll
      for (int j = 0; j < 3; ++j) {
        const f32x4 cc = lds16f(L_CS + 512 + tl[j] * 64 + g * 16);
        bfr[0][3 * j] = ld16_b(l16, sws, so0 ^ (3 * j * 1024));
        bfr[0][3 * j + 1] = ld16_b(l16, sws, so0 ^ ((3 * j + 1) * 1024));
        if (j < 2) bfr[0][3 * j + 2] = ld16_b(l16, sws, so0 ^ ((3 * j + 2) * 1024));
        u32x4 zz;
#pragma unroll
        for (int rt = 0; rt < 2; ++rt) {
          zz[2 * rt] = pk2v(gelu2(fma2(lo2(cc), bc2(xm[rt]), lo2(acc[rt][j])) * bc2(xr[rt])));
          zz[2 * rt + 1] = pk2v(gelu2(fma2(hi2(cc), bc2(xm[rt]), hi2(acc[rt][j])) * bc2(xr[rt])));
        }
        st16_sc1(zz, l16, sws, (tl[j] < z1n) ? WS_Z1 + ((4 * P + rw) * 43 + z1s + tl[j]) * 1024 : 0x7FFF0000u);
        SCHED();
      }
    }
    }
  } else {
    int vq = 2;
    const rsrc_t sw = __builtin_amdgcn_make_buffer_rsrc((void*)wqkv, 0, 512 * 3072, 0x00020000);
    const u32 vw = (8 * w + ((lane >> 2) & 7)) * 3072 + 128 * c + ((lane >> 5) * 4 + ((lane & 3) ^ ((w & 1) << 1))) * 16;
    const u32 w3k = w * 3072;
#define DMA_S(n, e)                                                                                                 \
  do {                                                                                                              \
    if ((e) < 2) DMA_X(((n) % 3) * 40960, n, (e) & 1);                                                              \
    else DMA_CC(w3k, ((n) % 3) * 40960 + 16384 + ((e) - 2) * 1024, vw, sw, "s_add_u32", (((n) + rot) & 7) * (64 * 3072), ((e) - 2) * 1024); \
  } while (0)
#define ISS_Q(u, base) do { VM(DMA_U(u, base, 0)); VM(DMA_U(u, base, 1)); mu[u] = vq; } while (0)
    int ms[8], mu[16];
    VM(DMA_S(0, 2)); VM(DMA_S(0, 3)); VM(DMA_S(0, 4)); ms[0] = vq;
    u32x4 xrow;
    VM(xrow = ld16(l16, sx, ((8 + c) * 8 + w) * 1024));
    const int mxr = vq;
#pragma unroll
    for (int e = 0; e < 5; ++e) { VM(DMA_S(1, e)); ms[1] = vq; }
    const int l32 = lane & 31, hh = lane >> 5;
    u32 xa = (32 * rw + l32) * 128 + ((hh ^ ((l32 >> 1) & 7)) * 16);
    u32 wa = 16384 + hh * 3072 + cw * 512 + ((lane >> 2) & 3) * 64 + (((((g & 1) << 1) | ((lane >> 1) & 1)) ^ (hh << 1)) * 16) + (lane & 1) * 8;
    u32 ca = 16384 + g * 3072 + min(w, 5) * 512 + ((lane >> 2) & 3) * 64 + ((((g & 1) << 1) | ((lane >> 1) & 1)) * 16) + (lane & 1) * 8;
    const f32x16 zero16 = {0, 0, 0, 0, 0, 0, 0, 0, 0, 0, 0, 0, 0, 0, 0, 0};
    f32x16 acc[3] = {zero16, zero16, zero16};
    f32x4 csa[2] = {zero4, zero4};
    u32x4 xg;
    int mxg = 0;
    WAITQ(ms[0]);
    BARRIER();
    USE_EPOCH()
    STAMP(7);
    {
      u32x4 xf[2], wf[2][4];
#pragma unroll
      for (int n = 0; n <= 8; ++n)
#pragma unroll
      for (int sq = 0; sq < 4; ++sq) {
        const int s = 4 * n + sq;
        if (s > 32) continue;
        if (s < 32 && sq == 0) {
          if (n > 0) {
            WAITQ(ms[n]);
            BARRIER();
          }
          const int d = n == 0 ? 0 : (n % 3 == 0 ? -81920 : 40960);
          xa = opq(xa + d); wa = opq(wa + d); ca = opq(ca + d);
        }
        const int q1 = (s + 1) & 1, q0 = s & 1;
#pragma unroll
        for (int m = 0; m < 4; ++m) {
          if (s > 0) {
            if (m < 3) acc[m] = mfmaL(wf[q1][m], xf[q1], acc[m]);
            else csa[q1] = mfma32(ones4, wf[q1][3], csa[q1]);
            SCHED();
          }
          if (s < 32) {
            if (m == 0) { xf[q0] = lds16(xa ^ (32 * sq)); wf[q0][0] = ldfrag(wa + sq * 6144, 256); }
            if (m == 1) { wf[q0][1] = ldfrag(wa + sq * 6144 + 1024, 256); wf[q0][2] = ldfrag(wa + sq * 6144 + 2048, 256); }
            if (m == 2) wf[q0][3] = ldfrag((ca ^ (32 * (sq & 1))) + (sq >> 1) * 12288, 256);
            if (m == 0 || m == 2) {
              const int h = 2 * sq + (m == 2);
              if (n <= 5 && h < 5) { VM(DMA_S(n + 2, h)); ms[n + 2] = vq; }
              if (n == 0 && h == 5) {
                rsrc_t sp = __builtin_amdgcn_make_buffer_rsrc((void*)wphi, 0, 16384, 0x00020000);
                VM(dma16(L_WPHI + (w & 1) * 1024, l16, sp, c * 2048 + (w & 1) * 1024));
              }
              if (WSP_PF && h == 6 && (n == WSP_PF || n == WSP_PF + 2))
                VM(touch(L_JUNK, l16 * 8, sa, (((cl >> 1) * 2 + (n != WSP_PF)) * 8 + w) * 8192));
              if (n == 4 && h == 5) { VM(xg = ld16_sc1(i16 * 16, sws, WS_XG + (P * 128 + 16 * w) * 16)); mxg = vq; }
              if (n == 6) { VM(DMA_U(sq, (sq < 2 ? LQ_M : LQ_A) + (sq & 1) * 16384, h & 1)); mu[sq] = vq; }
              if (n == 7 && sq < 2) { VM(DMA_U(4 + sq, sq * 16384, h & 1)); mu[4 + sq] = vq; }
            }
          }
          SCHED();
        }
        PIN(acc[0]); PIN(acc[1]); PIN(acc[2]); PIN(csa[0]); PIN(csa[1]);
        if (s == 3) EARLY_STATS(mxr)
      }
    }
    GATHER_XS()
    sts4f(L_CS + w * 128 + i16 * 4, csa[0][0]);
    sts4f(L_CS + w * 128 + 64 + i16 * 4, csa[1][0]);
    BARRIER();
    STAMP(1);
    WAITQ(mu[3]);
    READ_U(0, LQ_M) READ_U(1, LQ_M + 16384) READ_U(2, LQ_A) READ_U(3, LQ_A + 16384)
    const float xr = lds4f(L_XS + (32 * rw + l32) * 4), xm = -lds4f(L_XS + 512 + (32 * rw + l32) * 4);
    u32x4 wp0, wp1;
    {
      const u32 a = L_WPHI + (2 * cw + (lane & 1)) * 512 + (4 * hh + ((lane >> 2) & 3)) * 32 + (2 * (g & 1) + ((lane >> 1) & 1)) * 8;
      const u32x4 f = ldfrag(a, 256);
      const bool own = (lane >> 2) & 1;
#pragma unroll
      for (int e = 0; e < 4; ++e) { wp0[e] = own ? 0u : f[e]; wp1[e] = own ? f[e] : 0u; }
    }
#define FEATURES(e_, dst, hook)                                                                                         \
  {                                                                                                                 \
    u32x4 kp[2];                                                                                                    \
    _Pragma("unroll") for (int a = 0; a < 4; ++a) {                                                                \
      const f32x4 cc = lds16f(L_CS + (2 * (e_) + cw) * 128 + a * 32 + hh * 16);                                      \
      kp[a >> 1][2 * (a & 1)] = pk2v(fma2(lo2(cc), bc2(xm), (f32x2){acc[e_][4 * a], acc[e_][4 * a + 1]}));          \
      kp[a >> 1][2 * (a & 1) + 1] = pk2v(fma2(hi2(cc), bc2(xm), (f32x2){acc[e_][4 * a + 2], acc[e_][4 * a + 3]}));  \
    }                                                                                                               \
    f32x16 pl = mfmaL(wp0, kp[0], zero16);                                                                          \
    pl = mfmaL(wp1, kp[1], pl);                                                                                     \
    hedgehog16(pl, xr, dst, hook);                                                                                  \
  }
    {
      const int s_ = 32 * rw + l32;
      const u32 key = (((s_ >> 1) & 3) ^ (((s_ >> 3) & 1) << 1)) * 16;
      {
        const u32 va = LQ_V + (s_ >> 3) * 1024 + cw * 512 + (s_ & 7) * 64 + hh * 8;
#pragma unroll
        for (int a = 0; a < 4; ++a) {
          const f32x4 cc = lds16f(L_CS + (4 + cw) * 128 + a * 32 + hh * 16);
          sts8(va + ((a * 16) ^ key), (u32x2){pk2v(fma2(lo2(cc), bc2(xm), (f32x2){acc[2][4 * a], acc[2][4 * a + 1]}) * bc2(xr)),
                                              pk2v(fma2(hi2(cc), bc2(xm), (f32x2){acc[2][4 * a + 2], acc[2][4 * a + 3]}) * bc2(xr))});
        }
      }
      WAITQ(mu[5]);
      READ_U(4, 0) READ_U(5, 16384)
      WAITL0();
      u32x4 ph[4];
      auto hk = [&](int i) __attribute__((always_inline)) {
        const int u = 6 + (i >> 1);
        SCHED();
        VM(DMA_U(u, (u < 8 ? LQ_M : (u < 10 ? LQ_A : 0)) + (u & 1) * 16384, i & 1));
        mu[u] = vq;
        SCHED();
      };
      FEATURES(1, ph, hk)
      const u32 pa = LQ_PHI + (s_ >> 3) * 2048 + (2 * cw + hh) * 512 + (s_ & 7) * 64;
#pragma unroll
      for (int a = 0; a < 4; ++a) sts16(pa + ((a * 16) ^ key), ph[a]);
    }
    BARRIER();
    STAMP(2);
    {
      const u32 kx = (((lane >> 3) & 1) ^ ((g & 1) << 1)) * 16;
      const u32 pa = LQ_PHI + g * 2048 + (w >> 1) * 512 + ((lane >> 2) & 3) * 64 + (((2 * (w & 1) + ((lane >> 1) & 1)) * 16) ^ kx) + (lane & 1) * 8;
      const u32 va = LQ_V + g * 1024 + (w >> 2) * 512 + ((lane >> 2) & 3) * 64 + (((2 * ((w >> 1) & 1) + ((lane >> 1) & 1)) * 16) ^ kx) + (lane & 1) * 8;
      u32x4 pf[4], vf[4];
#pragma unroll
      for (int k = 0; k < 4; ++k) {
        pf[k] = cat(ldstr(pa + k * 8192), ldstr((pa ^ 32) + k * 8192 + 256));
        vf[k] = cat(ldstr(va + k * 4096), ldstr((va ^ 32) + k * 4096 + 256));
      }
      f32x4 kv = zero4, kz = zero4;
#pragma unroll
      for (int k = 0; k < 4; ++k) { kv = mfma32(pf[k], vf[k], kv); kz = mfma32(pf[k], ones4, kz); }
      const u32 so = WS_KP + (c * 16 + P) * KP_SZ;
      u32x4 g0 = {__float_as_uint(kv[0]), tlo, __float_as_uint(kv[1]), tlo}, g1 = {__float_as_uint(kv[2]), tlo, __float_as_uint(kv[3]), tlo};
      u32x4 g2 = {__float_as_uint((lane & 1) ? kz[2] : kz[0]), tlo, __float_as_uint((lane & 1) ? kz[3] : kz[1]), tlo};
      PIN(g0); PIN(g1); PIN(g2);
      asm volatile("s_nop 7" ::: "memory");
      VM(st16_sc1(g0, l16, sws, so + w * 2048));
      VM(st16_sc1(g1, l16, sws, so + w * 2048 + 1024));
      VM(st16_sc1(g2, g * 32 + (lane & 1) * 16 + ((i16 >> 1) << 26), sws, so + 16384 + w * 128));
      CLEAR_H()
    }
    STAMP(3);
    WAITQ(mu[11]);
    READ_U(6, LQ_M) READ_U(7, LQ_M + 16384) READ_U(8, LQ_A) READ_U(9, LQ_A + 16384) READ_U(10, 0) READ_U(11, 16384)
    BARRIER();
    ISS_Q(12, LQ_M); VM(DMA_U(13, LQ_M + 16384, 0));
    u32x4 phq[4];
    {
      VWO();
      auto hk = [&](int i) __attribute__((always_inline)) {
        SCHED();
        if (i == 0) DMA_U(13, LQ_M + 16384, 1);
        else if (i < 5) DMA_U(14 + ((i - 1) >> 1), LQ_A + ((i - 1) >> 1) * 16384, (i - 1) & 1);
        else DMA_WOUT(i - 5)
        SCHED();
      };
      FEATURES(0, phq, hk)
    }
    PIN(phq[0]); PIN(phq[1]); PIN(phq[2]); PIN(phq[3]);
    STAMP(4);
    if (w >= 5) {
      const int id = 2 * P + (w == 7 ? (lane & 1) : w - 5), wq = id >> 2, gq = id & 3;
      const u32 vi0 = (4 * g) * KP_SZ + (w == 7 ? 16384 + wq * 128 + gq * 32 : wq * 2048 + (gq * 16 + i16) * 16);
      const u32 vi1 = vi0 + (w == 7 ? 16 : 1024);
      const u32 vo = (w == 7 ? (WS_KZ - WS_KF) + c * 512 + id * 16 + ((i16 >> 1) << 25) : c * 8192 + id * 256 + i16 * 16) + (g << 28);
      float sa[4];
#pragma nounroll
      for (int it = 0; it < SPIN_CAP; ++it) {
        u32x4 a[4][2];
#pragma unroll
        for (int o = 0; o < 4; ++o) {
          a[o][0] = ld16_sc1(vi0, sws, WS_KP + (c * 16 + o) * KP_SZ);
          a[o][1] = ld16_sc1(vi1, sws, WS_KP + (c * 16 + o) * KP_SZ);
        }
        WAITV(0);
        bool ok = true;
#pragma unroll
        for (int o = 0; o < 4; ++o) { PIN(a[o][0]); PIN(a[o][1]); ok = ok && tag_ok(a[o][0], tlo) && tag_ok(a[o][1], tlo); }
#pragma unroll
        for (int e = 0; e < 4; ++e)
          sa[e] = (__uint_as_float(a[0][e >> 1][2 * (e & 1)]) + __uint_as_float(a[1][e >> 1][2 * (e & 1)])) +
                  (__uint_as_float(a[2][e >> 1][2 * (e & 1)]) + __uint_as_float(a[3][e >> 1][2 * (e & 1)]));
        if (__all(ok)) break;
        __builtin_amdgcn_s_sleep(POLL_SLEEP);
      }
      red2g(sa[0], sa[1]);
      red2g(sa[2], sa[3]);
      st16_sc1((u32x4){pk2(sa[0], sa[1]), tlo, pk2(sa[2], sa[3]), tlo}, vo, sws, WS_KF);
    }
    gf = ld8_sc1((lane >> 5) * 1024, sws, WS_FA + (16 * w + c2) * 128);
    WAITV(8);
    READ_U(12, LQ_M) READ_U(13, LQ_M + 16384) READ_U(14, LQ_A) READ_U(15, LQ_A + 16384)
    BARRIER();
#pragma unroll
    for (int a = 0; a < 4; ++a) sts16(L_WO1 + w * 4096 + a * 1024 + l16, phq[a]);
    WAITV(0);
    PIN(gf);
    if (!__all(gf[0] == tlo && gf[1] == thi)) poll_flags((lane >> 5) * 1024, true, sws, WS_FA + (16 * w + c2) * 128, tlo, thi);
    STAMP(6);
    { const u32 so_ = zb0 + TILE(0, gn) * 65536; LOAD_B(0, so_) }
  }

  {
  RELANE();
  u32x4 kfg, kzg;
  const u32 kfo = WS_KF + c * 8192 + w * 1024, kzo = WS_KZ + c * 512 + w * 64;
  if (t != 0) {
    kfg = ld16_sc1(l16, sws, kfo);
    kzg = ld16_sc1(g * 16, sws, kzo);
  }
  { const u32 so_ = zb0 + TILE(1, gn) * 65536; LOAD_B(1, so_) }
  STAMP(9);
  const u32 redw = opq(L_RED + w * 4096 + lane * 16), redr = opq(L_RED + (rw ^ (2 * ux0)) * 1024 + lane * 16);
  const u32 f3s = __builtin_amdgcn_readfirstlane(WS_FB + (P * 8 + (c2 > 0 ? c2 - 1 : 0)) * 128);
  const u32 vw3 = ((lane >> 2) & 7) * 1024 + c2 * 128 + (lane >> 5) * 64;
  const rsrc_t swo = __builtin_amdgcn_make_buffer_rsrc((void*)wo, 0, 512 * 1024, 0x00020000);
#define DMA_WO(dst, half, e) dma16((dst) + w * 4096 + (e) * 1024, (vw3 + (lane & 3) * 16) ^ (((e) & 1) * 32), swo, ((w + 8 * (half)) * 4 + (e)) * 8192)
#define FV() ((lane >> 5) * (WS_FC - WS_FB) + (P * 8 + ((lane >> 2) & 7)) * 128)
  u32x2 z1r[6], hold;
  u32 bs;
  f32x4 sm0;
#define EMIT_H(jt, sm_)                                                                                             \
  {                                                                                                                 \
    const f32x2 bb = bc2(blo(bs));                                                                                  \
    const u32 h0 = pk2v((f32x2){blo(z1r[jt][0]), bhi(z1r[jt][0])} * (lo2(sm_) + bb));                               \
    const u32 h1 = pk2v((f32x2){blo(z1r[jt][1]), bhi(z1r[jt][1])} * (hi2(sm_) + bb));                               \
    const u32 hb_ = WS_HF + ((4 * p + rw) * 24 + c2) * 1024;                                                        \
    if ((jt) == 0 || ((jt) == 2 && gn == 6) || (jt) == gn - 2) hold = (u32x2){h0, h1};                              \
    else if ((jt) == 2) st16_sc1((u32x4){h0, h1, 0u, 0u}, l16, sws, hb_ + 8192);                                    \
    else st16_sc1((u32x4){hold[0], hold[1], h0, h1}, l16, sws, hb_ + ((jt) == 1 ? 0 : ((jt) == gn - 1 ? 16384 : 8192))); \
  }
#pragma unroll
  for (int j = 0; j < 6; ++j) {
    if (j < 5 || gn == 6) {
      const int u = j & 1;
      if (j == 0) WAITV(8);
      if (j == 2) WAITV(t ? 10 : 8);
      if (j == 1) { WAITV(9); WAITV_IF(cw, 1, 8); }
      if (j == 3) { WAITV(9); WAITV_IF(cw, 1, 8); }
      if (j == 4) { if (t) WAITV(6); else { WAITV(13); WAITV_IF(cw, 1, 12); WAITV_IF(gn, 5, 6); } WAITV_IF(2 * gn + cw, 11, 4); }
      if (j == 5) { WAITV(10); WAITV_IF(cw, 1, 9); }
#pragma unroll
      for (int i = 0; i < 8; ++i) PIN(bfr[u][i]);
      {
        f32x4 a_[4];
#pragma unroll
        for (int rt = 0; rt < 4; ++rt) {
          a_[rt] = zero4;
#pragma unroll
          for (int i = 0; i < 8; ++i) a_[rt] = mfma32(bfr[u][i], af[rt][i], a_[rt]);
          SCHED();
          if (rt > 0) sts16f(redw + u * 32768 + (rt - 1) * 1024, a_[rt - 1]);
          SCHED();
        }
        if (j == 0 && t != 0) {
          PIN(kfg); PIN(kzg);
          poll_gran(kfg, l16, sws, kfo, tlo);
          poll_gran(kzg, g * 16, sws, kzo, tlo);
          sts8(L_KFS + (w >> 1) * 1024 + i16 * 64 + (w & 1) * 32 + g * 8, (u32x2){kfg[0], kfg[2]});
          sts8(L_KFS + 4096 + w * 32 + g * 8, (u32x2){kzg[0], kzg[2]});
          sts8(L_KFS + 4352 + (lane & 7) * 8, (u32x2){0u, 0u});
        }
        sts16f(redw + u * 32768 + 3072, a_[3]);
      }
      u32x4 of[2];
      if (j == 1 && t != 0) {
        const int l32 = lane & 31, hh = lane >> 5;
        const bool own = ((lane >> 2) & 1) == hh;
        const u32 ka = own ? L_KFS + (2 * cw + hh) * 1024 + ((l32 & 3) + 4 * (l32 >> 3)) * 64 : L_KFS + 4352;
        const u32 zaa = own ? L_KFS + 4096 + (2 * cw + hh) * 64 : L_KFS + 4352;
        const u32 qa = L_WO1 + w * 4096 + l16;
        const f32x16 zero16 = {0, 0, 0, 0, 0, 0, 0, 0, 0, 0, 0, 0, 0, 0, 0, 0};
        f32x16 den = zero16, num = zero16;
#pragma unroll
        for (int a = 0; a < 4; ++a) {
          const u32x4 pq_ = lds16(qa + a * 1024);
          den = mfmaL(lds16(zaa + a * 16), pq_, den);
          num = mfmaL(lds16(ka + a * 16), pq_, num);
        }
        const float inv = __builtin_amdgcn_rcpf(den[0] + 1e-6f);
#pragma unroll
        for (int a = 0; a < 2; ++a) {
          of[a] = (u32x4){pk2(num[8 * a] * inv, num[8 * a + 1] * inv), pk2(num[8 * a + 2] * inv, num[8 * a + 3] * inv),
                          pk2(num[8 * a + 4] * inv, num[8 * a + 5] * inv), pk2(num[8 * a + 6] * inv, num[8 * a + 7] * inv)};
          PIN(of[a]);
        }
        STAMP(5);
      }
      BAR_IF(cw, 1, (FC_J == 2 && j == 2 && t != 0) ? 0 : 63);
      if (w == 4) {
        if (j == 0 && t == 0) SET_FLAG(WS_FB + (P * 8 + c) * 128);
        if (j == FC_J && t != 0) SET_FLAG(WS_FC + (P * 8 + c) * 128);
      }
      if (j == 1) {
        if (cw == 0) {
          WAITV(0);
          PIN(gf);
          if (!__all(gf[0] == tlo && gf[1] == thi)) poll_flags((lane >> 5) * 128, true, sws, f3s, tlo, thi);
          rsrc_t sb = __builtin_amdgcn_make_buffer_rsrc((void*)bsp, 0, 4096, 0x00020000);
          bs = ld2(i16 * 2, sb, (64 * p + 16 * rw) * 2);
        }
      }
      if ((j == 1 || j == 2) && cw == 0) {
#pragma unroll
        for (int v = 3 * j - 3; v < 3 * j; ++v)
          z1r[v] = ld8_sc1(l16, sws, WS_Z1 + ((2 * p + (rw >> 1)) * 43 + gs + TILE(min(v, gn - 1), gn)) * 1024 + (rw & 1) * 8);
      }
      if ((j == 4 && gn == 5) || j == 5) gf = ld8_sc1(FV(), sws, WS_FB);
      if (j == 4 && t == 0) {
        VWO();
#pragma unroll
        for (int e = 2; e < 7; ++e) DMA_WOUT(e)
      }
      const u32 ra_ = redr + ((j + 1) & 1) * 32768;
      f32x4 sm, r_[2];
#pragma unroll
      for (int i = 0; i < 8; ++i) {
        if (j >= 1 && j < (t ? 3 : 4) && (i & 1) == 0 && cw == 0) {
          if (i == 2) sm = r_[0] + r_[1];
          if (i > 2) sm += r_[0] + r_[1];
          r_[0] = lds16f(ra_ + i * 4096); r_[1] = lds16f(ra_ + (i + 1) * 4096);
        }
        SCHED();
        if (j < 3) bfr[u][i] = ld16_b(l16, sws, (zb0 + TILE(j + 2, gn) * 65536) ^ (i * 1024));
        else if (j == 3 && t == 0) ld16_b_if6(bfr[1][i], gn, l16, sws, (zb0 + TILE(5, gn) * 65536) ^ (i * 1024));
        SCHED();
      }
      if (j == 0 && cw == 0) gf = ld8_sc1((lane >> 5) * 128, sws, f3s);
      if (j == 1 && t != 0) {
        const u32 vo = ((lane >> 4) & 1) * 16384 + (lane >> 5) * 512 + i16 * 16;
        st16_sc1(of[0], vo, sws, WS_OF + ((8 * P + 2 * rw) * 16 + 2 * c + cw) * 1024);
        st16_sc1(of[1], vo, sws, WS_OF + ((8 * P + 2 * rw) * 16 + 2 * c + cw) * 1024 + 256);
      }
      if (j == 2) { PIN(bs); _Pragma("unroll") for (int v = 0; v < 3; ++v) PIN(z1r[v]); }
      if (j == 3) { _Pragma("unroll") for (int v = 3; v < 6; ++v) PIN(z1r[v]); }
      if (j >= 1 && cw == 0) {
        if (j < (t ? 3 : 4)) sm += r_[0] + r_[1];
        else {
          f32x4 q_[8];
#pragma unroll
          for (int o = 0; o < 8; ++o) q_[o] = lds16f(ra_ + o * 4096);
          SCHED();
          sm = ((q_[0] + q_[1]) + (q_[2] + q_[3])) + ((q_[4] + q_[5]) + (q_[6] + q_[7]));
        }
        if (j == 1) sm0 = sm;
        if (j == 2) EMIT_H(0, sm0)
        if (j >= 2) EMIT_H(j >= 2 ? j - 1 : 0, sm)
      }
      if (j == 3) {
#pragma unroll
        for (int e = 0; e < 4; ++e) DMA_WO(L_WO1, 0, e);
      }
      if (FC_J == 2 && j == 2 && t != 0) WAITV_IF(cw, 0, 12);
      BAR_IF(cw, 0, 63);
    }
  }
  {
    STAMP(10);
    const u32 bufA = L_RED + (gn & 1) * 32768, bufB = L_RED + 32768 - (gn & 1) * 32768;
    const u32 fv = FV();
    if (cw == 0) {
      const u32 ra_ = redr + (bufB - L_RED);
      f32x4 q_[8];
#pragma unroll
      for (int o = 0; o < 8; ++o) q_[o] = lds16f(ra_ + o * 4096);
      SCHED();
      const f32x4 sm = ((q_[0] + q_[1]) + (q_[2] + q_[3])) + ((q_[4] + q_[5]) + (q_[6] + q_[7]));
      if (gn == 6) EMIT_H(5, sm) else EMIT_H(4, sm)
    }
    WAITV(1); WAITV_IF(cw, 1, 0);
    PIN(gf);
    if (!__all(gf[0] == tlo && gf[1] == thi)) poll_flags(fv, true, sws, WS_FB, tlo, thi);
    RELANE();
    u32x4 hf[12], of[8];
    const u32 ofb = WS_OF + ((4 * p + rw) * 16 + cw) * 1024, hfb = WS_HF + ((4 * p + rw) * 24 + cw) * 1024;
#pragma unroll
    for (int e = 0; e < 4; ++e) of[e] = ld16_b(l16, sws, ofb + e * 2048);
#pragma unroll
    for (int e = 0; e < 4; ++e) hf[e] = ld16_b(l16, sws, hfb + e * 2048);
    BARRIER();
    const u32 qb = ((lane >> 2) & 3) * 64 + ((((g & 1) << 1) | ((lane >> 1) & 1)) * 16) + (lane & 1) * 8;
    f32x4 a3[4];
#pragma unroll
    for (int ct = 0; ct < 4; ++ct) a3[ct] = zero4;
    u32x4 wf[3][4];
#define RDW2(dst, lo0, hi0, offl, offh) _Pragma("unroll") for (int ct = 0; ct < 4; ++ct) dst[ct] = cat(ldstr(((lo0) ^ ((ct & 1) * 32)) + (offl) + (ct >> 1) * 512), ldstr(((hi0) ^ ((ct & 1) * 32)) + (offh) + (ct >> 1) * 512))
#define MM3(src, frag) _Pragma("unroll") for (int ct = 0; ct < 4; ++ct) a3[ct] = mfma32(src[ct], frag, a3[ct])
    const rsrc_t sxo = __builtin_amdgcn_make_buffer_rsrc((void*)(x + (size_t)p * 65536), 0, 65536, 0x00020000);
    const u32 w0 = opq(L_WOUT + qb + (g >> 1) * 1024 + (g & 1) * 256);
    const u32 wa = w0 + cw * 4096, wb = w0 + cw * 2048;
    const u32 oq = qb + g * 1024 + cw * 4096;
    const u32 oa = opq(L_WO1 + oq), ob = opq(bufA + oq);
    u32x4 xres;
    VWO();
#pragma unroll
    for (int e = 0; e < 4; ++e) DMA_WO(bufA, 1, e);
    STAMP(11);
#define RDK(dst, k)                                                                                                 \
  if ((k) < 4) { RDW2(dst, oa, oa, (k) * 8192, (k) * 8192 + 256); }                                                 \
  else if ((k) < 8) { RDW2(dst, wa, wa, ((k) - 4) * 8192, ((k) - 4) * 8192 + 2048); }                               \
  else if ((k) < 12) { RDW2(dst, ob, ob, ((k) - 8) * 8192, ((k) - 8) * 8192 + 256); }                               \
  else { RDW2(dst, wb, w0, 32768 + ((k) - 12) * 4096, 49152 + ((k) - 12) * 2048); }
    RDK(wf[0], 0)
    RDK(wf[1], 1)
#define VALID4(e0)                                                                                                  \
  {                                                                                                                 \
    _Pragma("unroll") for (int e = e0; e < e0 + 4; ++e) PIN(hf[e]);                                                \
    _Pragma("nounroll") for (int it = 0; it < SPIN_CAP; ++it) {                                                    \
      u32 mx_ = 0;                                                                                                  \
      _Pragma("unroll") for (int e = e0; e < e0 + 4; ++e) mx_ = max(max(max(mx_, hf[e][0]), hf[e][1]), max(hf[e][2], hf[e][3])); \
      if (__all(mx_ != ~0u)) break;                                                                                 \
      __builtin_amdgcn_s_sleep(2);                                                                                  \
      asm volatile("buffer_load_dwordx4 %0, %4, %5, %6 offen sc1\n\tbuffer_load_dwordx4 %1, %4, %5, %6 offen offset:2048 sc1\n\t" \
                   "buffer_load_dwordx4 %2, %4, %5, %7 offen sc1\n\tbuffer_load_dwordx4 %3, %4, %5, %7 offen offset:2048 sc1\n\t" \
                   "s_waitcnt vmcnt(0)"                                                                             \
                   : "=&v"(hf[e0]), "=&v"(hf[e0 + 1]), "=&v"(hf[e0 + 2]), "=&v"(hf[e0 + 3])                         \
                   : "v"(l16), "s"(sws), "s"(hfb + (e0) * 2048), "s"(hfb + (e0) * 2048 + 4096)                       \
                   : "memory");                                                                                     \
    }                                                                                                               \
  }
#pragma unroll
    for (int k = 0; k < 16; ++k) {
      if (k == 0) WAITV(8);
      if (k == 4) { WAITV(12); VALID4(0) }
      if (k == 6) {
        WAITV(4);
        BARRIER();
        STAMP(12);
      }
      if (k == 12) { WAITV(1 + 2 * (12 - HC_K)); VALID4(4) }
      if (k + 2 < 16) { RDK(wf[(k + 2) % 3], k + 2) }
      SCHED();
      if (k < 4) { PIN(of[k]); MM3(wf[k % 3], of[k]); }
      else if (k < 8) MM3(wf[k % 3], hf[k - 4]);
      else if (k < 12) { PIN(of[k - 4]); MM3(wf[k % 3], of[k - 4]); }
      else MM3(wf[k % 3], hf[k - 8]);
      SCHED();
      if (k < 4) {
        of[4 + k] = ld16_b(l16, sws, ofb + (4 + k) * 2048);
        DMA_WOUT_AT(bufB + w * 4096, 7 + k)
      } else if (k < 6) {
        hf[2 * k - 4] = ld16_b(l16, sws, hfb + (2 * k - 4) * 2048);
        hf[2 * k - 3] = ld16_b(l16, sws, hfb + (2 * k - 3) * 2048);
      } else if (k == 6) {
        xres = ld16((tid >> 3) * 1024 + (tid & 7) * 16, sxo, c2 * 128);
      } else if (k == HC_K || k == HC_K + 1) {
        hf[8 + 2 * (k - HC_K)] = ld16_b(l16, sws, hfb + (8 + 2 * (k - HC_K)) * 2048);
        hf[9 + 2 * (k - HC_K)] = ld16_b(l16, sws, hfb + (9 + 2 * (k - HC_K)) * 2048);
      }
    }
    STAMP(13);
    {
      const u32 wl_ = opq(bufB + (w0 - L_WOUT) + cw * 4096);
      u32x4 wg[4][4];
#pragma unroll
      for (int e = 0; e < 4; ++e) RDW2(wg[e], wl_, wl_, e * 8192, e * 8192 + 2048);
      WAITV(0);
      PIN(xres);
      VALID4(8)
#pragma unroll
      for (int e = 0; e < 4; ++e) MM3(wg[e], hf[8 + e]);
    }
    STAMP(14);
    const u32 fin = L_WO1 - 2048;
#pragma unroll
    for (int ct = 0; ct < 4; ++ct) sts16f(fin + 2048 + cw * 16384 + (16 * rw + i16) * 256 + (((4 * ct + g) ^ i16) * 16), a3[ct]);
    BARRIER();
    {
      const int row = tid >> 3;
      const u32 a = fin + 2048 + row * 256 + (((2 * (tid & 7)) ^ (row & 15)) * 16);
      const f32x4 s0 = lds16f(a) + lds16f(a + 16384), s1 = lds16f(a ^ 16) + lds16f((a ^ 16) + 16384);
      u32x4 o = {pk2(blo(xres[0]) + s0[0], bhi(xres[0]) + s0[1]), pk2(blo(xres[1]) + s0[2], bhi(xres[1]) + s0[3]),
                 pk2(blo(xres[2]) + s1[0], bhi(xres[2]) + s1[1]), pk2(blo(xres[3]) + s1[2], bhi(xres[3]) + s1[3])};
      st16_sc1(o, row * 1024 + (tid & 7) * 16, __builtin_amdgcn_make_buffer_rsrc((void*)(out + (size_t)p * 65536), 0, 65536, 0x00020000), c2 * 128);
    }
  }
  }
  if (b == 0 && w == 0 && l16 == 0) *(u64*)(ws + WS_EP) = ((u64)thi << 32) | tlo;
  STAMP(15);
  STAMPC(31);
}

__global__ __launch_bounds__(512, 2) void fused_kernel(const char* __restrict__ x, const char* __restrict__ wqkv, const char* __restrict__ wphi,
                                                        const char* __restrict__ wo, const char* __restrict__ win, const char* __restrict__ wsp,
                                                        const char* __restrict__ bsp, const char* __restrict__ wout, char* __restrict__ out, char* ws) {
  __shared__ __attribute__((aligned(1024))) char smem[163840];
  asm volatile("" ::"v"((u32)(size_t)(LDSP char*)smem));
  asm volatile("" ::"s"(x), "s"(wqkv), "s"(win), "s"(ws), "s"(wsp));
  u64 pc, ep;
  asm volatile("s_getpc_b64 %0" : "=s"(pc));
  {
    const int lane = threadIdx.x & 63, g = lane >> 4;
    const int w = __builtin_amdgcn_readfirstlane(threadIdx.x >> 6);
    const int r = blockIdx.x >> 3, P = ((blockIdx.x & 7) >> 1) * 4 + (r >> 3);
    const int rot = 2 * (((r >> 3) + (r & 3)) & 3);
    u32 acv = 0, acb = 0; (void)acv; (void)acb;
    STAMP(0);
    STAMPC(30);
    if (CODE_PF && w == 7)
      touch(L_JUNK, lane * 128, __builtin_amdgcn_make_buffer_rsrc((void*)(pc & ~255ull), 0, CODE_PF, 0x00020000), (r & 7) * 8192);
    const rsrc_t sx = __builtin_amdgcn_make_buffer_rsrc((void*)(x + (size_t)P * 131072), 0, 131072, 0x00020000);
    const u32 vx0 = (16 * w + (lane >> 3)) * 1024 + (((lane & 7) ^ g) * 16);
    const u32 vx1 = (vx0 + 8192) ^ 64;
    const u32 w2k = w * 2048;
    DMA_X(0, 0, 0);
    DMA_X(0, 0, 1);
    asm volatile("s_load_dwordx2 %0, %1, 0x0" : "=s"(ep) : "s"(ws) : "memory");
    SCHED();
  }
  if (__builtin_expect(((blockIdx.x >> 5) & 1) == 0, 1)) run<0>(x, wqkv, wphi, wo, win, wsp, bsp, wout, out, ws, ep);
  else run<1>(x, wqkv, wphi, wo, win, wsp, bsp, wout, out, ws, ep);
}

extern "C" void kernel_launch(void* const* d_in, const int* in_sizes, int n_in, void* d_out, int out_size, void* d_ws, size_t ws_size,
                              hipStream_t stream) {
  fused_kernel<<<256, 512, 0, stream>>>((const char*)d_in[0], (const char*)d_in[1], (const char*)d_in[2], (const char*)d_in[3],
                                        (const char*)d_in[5], (const char*)d_in[6], (const char*)d_in[7], (const char*)d_in[8], (char*)d_out,
                                        (char*)d_ws);
}
```

```cpp
#include <hip/hip_runtime.h>
#include <hip/hip_bf16.h>

typedef unsigned int u32;
typedef unsigned long long u64;
typedef short s16x4 __attribute__((ext_vector_type(4)));
typedef __bf16 bf16x2 __attribute__((ext_vector_type(2)));
typedef __bf16 bf16x8 __attribute__((ext_vector_type(8)));
typedef float f32x16 __attribute__((ext_vector_type(16)));
typedef float f32x4 __attribute__((ext_vector_type(4)));
typedef float f32x2 __attribute__((ext_vector_type(2)));
typedef u32 u32x4 __attribute__((ext_vector_type(4)));
typedef u32 u32x2 __attribute__((ext_vector_type(2)));
typedef __amdgpu_buffer_rsrc_t rsrc_t;
#define LDSP __attribute__((address_space(3)))
#define DI __device__ __forceinline__

constexpr u32 WS_EP = 0;
constexpr u32 WS_FA = 8192;
constexpr u32 WS_FB = WS_FA + 16384;
constexpr u32 WS_FC = WS_FB + 16384;
constexpr u32 WS_XG = WS_FC + 81920;
constexpr u32 WS_SG = WS_XG + 32768;
constexpr u32 WS_ZB = WS_SG + 524288;
constexpr u32 WS_Z1 = WS_ZB + 43 * 65536;
constexpr u32 KP_SZ = 17408;
constexpr u32 WS_KP = WS_Z1 + 64 * 43 * 1024;
constexpr u32 WS_KF = WS_KP + 128 * KP_SZ;
constexpr u32 WS_KZ = WS_KF + 65536;
constexpr u32 WS_OF = WS_KZ + 4096;
constexpr u32 WS_HF = WS_OF + 128 * 16384;
constexpr u32 WS_END = WS_HF + 128 * 24 * 1024;

constexpr u32 LZ_W = 65536;
constexpr u32 LZ_U = 139264;
constexpr u32 LQ_M = 122880;
constexpr u32 LQ_A = 81920;
constexpr u32 LQ_PHI = 49152;
constexpr u32 LQ_V = 32768;
constexpr u32 L_TAB = 155648;
constexpr u32 L_XS = L_TAB;
constexpr u32 L_CS = L_TAB + 1024;
constexpr u32 L_ZST = L_TAB + 3072;
constexpr u32 L_WPHI = L_TAB + 3072;
constexpr u32 L_KFS = L_TAB + 2048;
constexpr u32 L_JUNK = L_TAB + 7168;
constexpr u32 L_WOUT = 0;
constexpr u32 L_RED = 57344;
constexpr u32 L_WO1 = 122880;

constexpr u64 Z2S = 0x26211B16110B0500ull;
constexpr u64 Z1S = 0x26211C16110C0600ull;
DI int grp_start(u64 tab, int c) { return c >= 8 ? 43 : (int)((tab >> (8 * c)) & 0xFF); }

#define SCHED() __builtin_amdgcn_sched_barrier(0)
#define WAITV(n) do { asm volatile("s_waitcnt vmcnt(%0)" ::"n"(n) : "memory"); SCHED(); } while (0)
#define BARRIER() do { SCHED(); asm volatile("s_waitcnt lgkmcnt(0)\n\ts_barrier" ::: "memory"); SCHED(); } while (0)
#define WAITV_IF(g_, v, n) do { asm volatile("s_cmp_lg_u32 %0, %1\n\ts_cbranch_scc1 1f\n\ts_waitcnt vmcnt(%2)\n1:" ::"s"(g_), "n"(v), "n"(n) : "scc", "memory"); SCHED(); } while (0)
#define BAR_IF(g_, v, n) do { SCHED(); asm volatile("s_cmp_lg_u32 %0, %1\n\ts_cbranch_scc1 1f\n\ts_waitcnt vmcnt(%2) lgkmcnt(0)\n\ts_barrier\n1:" ::"s"(g_), "n"(v), "n"(n) : "scc", "memory"); SCHED(); } while (0)
#define WAITL0() asm volatile("s_waitcnt lgkmcnt(0)" ::: "memory")
#define PIN(x) asm volatile("" : "+v"(x))

DI void dma16(u32 lds, u32 voff, rsrc_t srd, u32 soff) {
  asm volatile("s_mov_b32 m0, %0\n\ts_nop 0\n\tbuffer_load_dwordx4 %1, %2, %3 offen lds" ::"s"(lds), "v"(voff), "s"(srd), "s"(soff) : "memory");
}
#define DMA_C(wb, CST, voff, srd, soff) asm volatile("s_add_u32 m0, %0, %1\n\ts_nop 0\n\tbuffer_load_dwordx4 %2, %3, %4 offen lds" ::"s"(wb), "n"(CST), "v"(voff), "s"(srd), "s"(soff) : "scc", "memory")
#define DMA_CC(wb, CST, voff, srd, OP, sb, SC) do { u32 so__; asm volatile("s_add_u32 m0, %1, %2\n\t" OP " %0, %5, %6\n\tbuffer_load_dwordx4 %3, %4, %0 offen lds" : "=&s"(so__) : "s"(wb), "n"(CST), "v"(voff), "s"(srd), "s"(sb), "n"(SC) : "scc", "memory"); } while (0)
DI void touch(u32 lds, u32 voff, rsrc_t srd, u32 soff) {
  asm volatile("s_mov_b32 m0, %0\n\ts_nop 0\n\tbuffer_load_dword %1, %2, %3 offen lds" ::"s"(lds), "v"(voff), "s"(srd), "s"(soff) : "memory");
}
DI u32x4 ld16_sc1(u32 voff, rsrc_t srd, u32 soff) {
  u32x4 r;
  asm volatile("buffer_load_dwordx4 %0, %1, %2, %3 offen sc1" : "=v"(r) : "v"(voff), "s"(srd), "s"(soff) : "memory");
  return r;
}
template <int OFF> DI u32x4 ld16_sc1o(u32 voff, rsrc_t srd, u32 soff) {
  u32x4 r;
  asm volatile("buffer_load_dwordx4 %0, %1, %2, %3 offen offset:%4 sc1" : "=v"(r) : "v"(voff), "s"(srd), "s"(soff), "n"(OFF) : "memory");
  return r;
}
DI u32x4 ld16_b(u32 voff, rsrc_t srd, u32 soff) {
  u32x4 r;
  asm volatile("buffer_load_dwordx4 %0, %1, %2, %3 offen sc1" : "=v"(r) : "v"(voff), "s"(srd), "s"(soff) : "memory");
  return r;
}
template <int OFF> DI u32x4 ld16_bo(u32 voff, rsrc_t srd, u32 soff) {
  u32x4 r;
  asm volatile("buffer_load_dwordx4 %0, %1, %2, %3 offen offset:%4 sc1" : "=v"(r) : "v"(voff), "s"(srd), "s"(soff), "n"(OFF) : "memory");
  return r;
}
DI void ld16_b_if6(u32x4& r, int gn, u32 voff, rsrc_t srd, u32 soff) {
  asm volatile("s_cmp_lg_u32 %4, 6\n\ts_cbranch_scc1 1f\n\tbuffer_load_dwordx4 %0, %1, %2, %3 offen sc1\n1:" : "+v"(r) : "v"(voff), "s"(srd), "s"(soff), "s"(gn) : "scc", "memory");
}
DI u32x4 ld16(u32 voff, rsrc_t srd, u32 soff) {
  u32x4 r;
  asm volatile("buffer_load_dwordx4 %0, %1, %2, %3 offen" : "=v"(r) : "v"(voff), "s"(srd), "s"(soff) : "memory");
  return r;
}
DI u32x2 ld8_sc1(u32 voff, rsrc_t srd, u32 soff) {
  u32x2 r;
  asm volatile("buffer_load_dwordx2 %0, %1, %2, %3 offen sc1" : "=v"(r) : "v"(voff), "s"(srd), "s"(soff) : "memory");
  return r;
}
DI u32 ld2(u32 voff, rsrc_t srd, u32 soff) {
  u32 r;
  asm volatile("buffer_load_ushort %0, %1, %2, %3 offen" : "=v"(r) : "v"(voff), "s"(srd), "s"(soff) : "memory");
  return r;
}
DI void st16_sc1(u32x4 v, u32 voff, rsrc_t srd, u32 soff) {
  asm volatile("buffer_store_dwordx4 %0, %1, %2, %3 offen sc1\n\ts_nop 1" ::"v"(v), "v"(voff), "s"(srd), "s"(soff) : "memory");
}
DI void st8_sc1(u32x2 v, u32 voff, rsrc_t srd, u32 soff) {
  asm volatile("buffer_store_dwordx2 %0, %1, %2, %3 offen sc1" ::"v"(v), "v"(voff), "s"(srd), "s"(soff) : "memory");
}

DI u32x4 lds16(u32 a) { return *(const LDSP u32x4*)(size_t)a; }
DI u32x2 lds8(u32 a) { return *(const volatile LDSP u32x2*)(size_t)a; }
DI float lds4f(u32 a) { return *(const LDSP float*)(size_t)a; }
DI f32x4 lds16f(u32 a) { return *(const LDSP f32x4*)(size_t)a; }
DI void sts16f(u32 a, f32x4 v) { *(LDSP f32x4*)(size_t)a = v; }
DI void sts16(u32 a, u32x4 v) { *(LDSP u32x4*)(size_t)a = v; }
DI void sts8(u32 a, u32x2 v) { *(volatile LDSP u32x2*)(size_t)a = v; }
DI void sts4f(u32 a, float v) { *(LDSP float*)(size_t)a = v; }
DI u32x2 ldstr(u32 a) {
  return __builtin_bit_cast(u32x2, __builtin_amdgcn_ds_read_tr16_b64_v4i16((LDSP s16x4*)(size_t)a)); }
DI u32x4 cat(u32x2 lo, u32x2 hi) { return (u32x4){lo[0], lo[1], hi[0], hi[1]}; }
DI u32x4 ldfrag(u32 a, u32 d) { u32x2 lo = ldstr(a), hi = ldstr(a + d); return cat(lo, hi); }

DI f32x4 mfma32(u32x4 a, u32x4 b, f32x4 c) {
  return __builtin_amdgcn_mfma_f32_16x16x32_bf16(__builtin_bit_cast(bf16x8, a), __builtin_bit_cast(bf16x8, b), c, 0, 0, 0);
}
DI f32x16 mfmaL(u32x4 a, u32x4 b, f32x16 c) {
  return __builtin_amdgcn_mfma_f32_32x32x16_bf16(__builtin_bit_cast(bf16x8, a), __builtin_bit_cast(bf16x8, b), c, 0, 0, 0);
}
DI u32 pk2(float a, float b) { bf16x2 v = {(__bf16)a, (__bf16)b}; return __builtin_bit_cast(u32, v); }
DI float blo(u32 u) { return __uint_as_float(u << 16); }
DI float bhi(u32 u) { return __uint_as_float(u & 0xffff0000u); }
DI float rbf(float a) { return blo(pk2(a, a)); }
DI float dot2(u32 a, u32 b, float c) {
  return __builtin_amdgcn_fdot2_f32_bf16(__builtin_bit_cast(bf16x2, a), __builtin_bit_cast(bf16x2, b), c, false);
}
DI f32x2 fma2(f32x2 a, f32x2 b, f32x2 c) { return __builtin_elementwise_fma(a, b, c); }
DI f32x2 bc2(float a) { return (f32x2){a, a}; }
DI f32x2 lo2(f32x4 v) { return (f32x2){v[0], v[1]}; }
DI f32x2 hi2(f32x4 v) { return (f32x2){v[2], v[3]}; }
DI u32 pk2v(f32x2 v) { return pk2(v[0], v[1]); }
DI f32x2 gelu2(f32x2 x) {
  f32x2 u = fma2(x * x, bc2(-0.10294324f), bc2(-2.3022082f)) * x;
  f32x2 e = {__builtin_amdgcn_exp2f(u[0]), __builtin_amdgcn_exp2f(u[1])};
  e = e + bc2(1.0f);
  f32x2 r = {__builtin_amdgcn_rcpf(e[0]), __builtin_amdgcn_rcpf(e[1])};
  return x * r;
}
DI void red2g(float& a, float& b) {
  auto s = __builtin_amdgcn_permlane32_swap(__float_as_uint(a), __float_as_uint(b), false, false);
  float c = __uint_as_float(s[0]) + __uint_as_float(s[1]);
  auto s2 = __builtin_amdgcn_permlane16_swap(__float_as_uint(c), __float_as_uint(c), false, false);
  float d = __uint_as_float(s2[0]) + __uint_as_float(s2[1]);
  auto s3 = __builtin_amdgcn_permlane32_swap(__float_as_uint(d), __float_as_uint(d), false, false);
  a = __uint_as_float(s3[0]);
  b = __uint_as_float(s3[1]);
}
template <int CTRL> DI float dpp(float v) {
  return __uint_as_float(__builtin_amdgcn_update_dpp(0, __float_as_uint(v), CTRL, 0xF, 0xF, true));
}
template <class F> DI void hedgehog16(const f32x16& p, float s, u32x4 (&o)[4], F hook) {
  float mx = fmaxf(p[0], p[1]), mn = fminf(p[0], p[1]);
#pragma unroll
  for (int i = 2; i < 16; i += 2) { mx = fmaxf(fmaxf(mx, p[i]), p[i + 1]); mn = fminf(fminf(mn, p[i]), p[i + 1]); }
  const float c = s * 1.44269504f;
  const float a = -mx * c, b = mn * c;
  f32x2 ep[8], en[8];
#pragma unroll
  for (int i = 0; i < 8; ++i) {
    hook(i);
    const f32x2 v = {p[2 * i], p[2 * i + 1]};
    const f32x2 u = fma2(v, bc2(c), bc2(a)), w = fma2(v, bc2(-c), bc2(b));
    ep[i] = (f32x2){__builtin_amdgcn_exp2f(u[0]), __builtin_amdgcn_exp2f(u[1])};
    en[i] = (f32x2){__builtin_amdgcn_exp2f(w[0]), __builtin_amdgcn_exp2f(w[1])};
  }
  hook(8);
  const f32x2 sp = ((ep[0] + ep[1]) + (ep[2] + ep[3])) + ((ep[4] + ep[5]) + (ep[6] + ep[7]));
  const f32x2 sn = ((en[0] + en[1]) + (en[2] + en[3])) + ((en[4] + en[5]) + (en[6] + en[7]));
  const float ip = __builtin_amdgcn_rcpf(sp[0] + sp[1]), in = __builtin_amdgcn_rcpf(sn[0] + sn[1]);
#pragma unroll
  for (int i = 0; i < 8; ++i) {
    if (i == 0 || i == 3 || i == 6) hook(9 + i / 3);
    o[i >> 2][i & 3] = pk2v(ep[i] * bc2(ip));
    o[2 + (i >> 2)][i & 3] = pk2v(en[i] * bc2(in));
  }
}
DI void ln_fin(float s, float q, float invn, float& r, float& m) {
  m = s * invn;
  float v = fmaxf(q * invn - m * m, 0.0f);
  float sd = rbf(__builtin_amdgcn_sqrtf(rbf(rbf(v) + 1e-5f)));
  r = __builtin_amdgcn_rcpf(sd);
  m = rbf(m);
}

constexpr int SPIN_CAP = 1 << 17;
DI void poll_flags(u32 voff, bool act, rsrc_t srd, u32 soff, u32 tlo, u32 thi) {
#pragma nounroll
  for (int it = 0; it < SPIN_CAP; ++it) {
    u32x2 v;
    asm volatile("buffer_load_dwordx2 %0, %1, %2, %3 offen sc1\n\ts_waitcnt vmcnt(0)" : "=&v"(v) : "v"(voff), "s"(srd), "s"(soff) : "memory");
    bool ok = !act || (v[0] == tlo && v[1] == thi);
    if (__all(ok)) break;
    __builtin_amdgcn_s_sleep(6);
  }
}
DI bool tag_ok(u32x4 q, u32 t) { return q[1] == t && q[3] == t; }
DI void poll_gran(u32x4& q, u32 voff, rsrc_t srd, u32 soff, u32 tlo) {
#pragma nounroll
  for (int it = 0; it < SPIN_CAP; ++it) {
    if (__all(tag_ok(q, tlo))) break;
    __builtin_amdgcn_s_sleep(6);
    asm volatile("buffer_load_dwordx4 %0, %1, %2, %3 offen sc1\n\ts_waitcnt vmcnt(0)" : "=&v"(q) : "v"(voff), "s"(srd), "s"(soff) : "memory");
  }
}
DI u32 opq(u32 v) { asm volatile("" : "+v"(v)); return v; }

static_assert(WS_ZB % 8192 == 0, "fragment addresses are formed by XOR");
constexpr u32 CODE_PF = 56000;

#define VM(x) do { x; ++vq; } while (0)
#define WAITQ(mark) WAITV(vq - (mark))

template <int t>
DI void run(const char* __restrict__ x, const char* __restrict__ wqkv, const char* __restrict__ wphi, const char* __restrict__ wo,
            const char* __restrict__ win, const char* __restrict__ wsp, const char* __restrict__ bsp, const char* __restrict__ wout,
            char* __restrict__ out, char* ws, u64 ep) {
  const int tid = threadIdx.x, lane = tid & 63;
  const int w = __builtin_amdgcn_readfirstlane(tid >> 6);
  const int g = lane >> 4;
  const int b = blockIdx.x, xcd = b & 7, r = b >> 3;
  const int Pl = r >> 3, cl = r & 3;
  const int P = (xcd >> 1) * 4 + Pl;
  const int rot = 2 * ((Pl + cl) & 3);
  const rsrc_t sx = __builtin_amdgcn_make_buffer_rsrc((void*)(x + (size_t)P * 131072), 0, 131072, 0x00020000);
  const u32 vx0 = (16 * w + (lane >> 3)) * 1024 + (((lane & 7) ^ g) * 16);
  const u32 vx1 = (vx0 + 8192) ^ 64;
  const u32 w2k = w * 2048;
#define DMA_X(dst, n, e) DMA_C(w2k, (dst) + (e) * 1024, (e) ? vx1 : vx0, sx, (((n) + rot) & 7) * 128)
  const int rw = w & 3, cw = w >> 2, i16 = lane & 15;
  const int c = (xcd & 1) * 4 + cl;
  const int p = 2 * P + (cl & 1);
  const int c2 = (xcd & 1) * 4 + (t ? 3 * (cl >> 1) : 1 + (cl >> 1));
  const u32 l16 = opq(lane * 16);
  const rsrc_t sa = __builtin_amdgcn_make_buffer_rsrc((void*)(wsp + (size_t)p * 262144), 0, 262144, 0x00020000);
  const rsrc_t sws = __builtin_amdgcn_make_buffer_rsrc((void*)ws, 0, WS_END, 0x00020000);
  const u32 vA0 = (lane >> 3) * 4096 + (((lane & 7) ^ g) * 16);
  const u32 vA1 = (vA0 + 32768) ^ 64;
  const int ux0 = cl >> 1, ux1 = t;
  const u32 uxs = w * 512 + ux1 * 256 + ux0 * 131072;
#define DMA_U(u, base, e) DMA_CC(w2k, (base) + (e) * 1024, (e) ? vA1 : vA0, sa, "s_xor_b32", uxs, (((u) >> 1) & 3) * 128 + (((u) >> 3) * 32 + ((u) & 1) * 16) * 4096)
  const f32x4 zero4 = {0, 0, 0, 0};
  u32x4 ones4 = {0x3F803F80u, 0x3F803F80u, 0x3F803F80u, 0x3F803F80u};
  u32 tlo, thi;
  u32x4 af[4][8];
  u32x4 bfr[2][8];
  u32x2 gf;
  const u32 ard = w * 2048 + i16 * 128 + (((g >> 1) ^ (i16 >> 1)) * 16) + (g & 1) * 8;
#define READ_U(u, base)                                                                                             \
  {                                                                                                                 \
    const u32 ab_ = opq(ard + (base));                                                                              \
    _Pragma("unroll") for (int kk = 0; kk < 2; ++kk)                                                               \
      af[2 * ((u) >> 3) + ((u) & 1)][((u) & 6) + kk] = cat(lds8(ab_ ^ (64 * kk)), lds8(ab_ ^ (64 * kk + 32)));      \
  }
#define USE_EPOCH() { asm volatile("" : "+s"(ep)); ep += 1; tlo = (u32)ep; thi = (u32)(ep >> 32); PIN(ones4); }
#define EARLY_STATS(mark)                                                                                           \
  {                                                                                                                 \
    WAITQ(mark); PIN(xrow);                                                                                         \
    float s_ = 0, q_ = 0;                                                                                           \
    _Pragma("unroll") for (int e = 0; e < 4; ++e) { s_ = dot2(xrow[e], ones4[0], s_); q_ = dot2(xrow[e], xrow[e], q_); } \
    s_ += dpp<0x128>(s_); q_ += dpp<0x128>(q_);                                                                     \
    s_ += dpp<0x124>(s_); q_ += dpp<0x124>(q_);                                                                     \
    s_ += dpp<0x4E>(s_); q_ += dpp<0x4E>(q_);                                                                       \
    s_ += dpp<0xB1>(s_); q_ += dpp<0xB1>(q_);                                                                       \
    red2g(s_, q_);                                                                                                  \
    float r_, m_;                                                                                                   \
    ln_fin(s_, q_, 1.0f / 512, r_, m_);                                                                             \
    VM(st16_sc1((u32x4){__float_as_uint(r_), tlo, __float_as_uint(m_), tlo}, l16 << 22, sws, WS_XG + (P * 128 + (t * 8 + c) * 8 + w) * 16)); \
  }
#define GATHER_XS()                                                                                                 \
  WAITQ(mxg); PIN(xg);                                                                                              \
  poll_gran(xg, i16 * 16, sws, WS_XG + (P * 128 + 16 * w) * 16, tlo);                                               \
  sts4f(L_XS + (16 * w + i16) * 4, __uint_as_float(xg[0]));                                                         \
  sts4f(L_XS + 512 + (16 * w + i16) * 4, __uint_as_float(xg[2]));
#define RELANE() const int lane = opq(l16) >> 4, i16 = lane & 15, g = lane >> 4, tid = w * 64 + lane; (void)tid; (void)g; (void)i16
#define CLEAR_H() if (cw == 0) { const u32x4 f_ = {~0u, ~0u, ~0u, ~0u}; const u32 o_ = WS_HF + ((4 * p + rw) * 24 + c2) * 1024; \
    st16_sc1(f_, l16, sws, o_); st16_sc1(f_, l16, sws, o_ + 8192); st16_sc1(f_, l16, sws, o_ + 16384); }
#define SET_FLAG(off) st8_sc1((u32x2){tlo, thi}, l16 << 22, sws, (off))
  const int gs = grp_start(Z2S, c2), gn = t ? 5 : grp_start(Z2S, c2 + 1) - gs;
  const int trot = (p & 7) >= 5 ? (p & 7) - 5 : (p & 7);
#define TILE(j, n_) (((j) + trot) >= (n_) ? (j) + trot - (n_) : (j) + trot)
  const u32 zb0 = WS_ZB + (gs * 64 + 8 * w) * 1024 + ux1 * 4096;
#define DMA_WOUT(e)                                                                                                 \
  {                                                                                                                 \
    const rsrc_t so_ = __builtin_amdgcn_make_buffer_rsrc((void*)wout, 0, 688 * 1024, 0x00020000);                   \
    const int ws_ = grp_start(Z2S, w), wn_ = grp_start(Z2S, w + 1) - ws_;                                           \
    if ((e) < 6) dma16(L_WOUT + ((e) < 4 ? 2 * w + ((e) >> 1) : 16 + w) * 2048 + ((e) & 1) * 1024, vwo, so_, ((ws_ + TILE((e) >> 1, wn_)) * 2 + ((e) & 1)) * 8192); \
    else if ((e) == 6) dma16(L_WOUT + 24 * 2048 + w * 1024, vwo, so_, ((((w >> 1) == 0 ? 5 : ((w >> 1) == 1 ? 11 : ((w >> 1) == 2 ? 27 : 64))) + TILE(3, 6)) * 2 + (w & 1)) * 8192); \
  }
#define DMA_WOUT_AT(dst, e)                                                                                         \
  {                                                                                                                 \
    const rsrc_t so_ = __builtin_amdgcn_make_buffer_rsrc((void*)wout, 0, 688 * 1024, 0x00020000);                   \
    const int ws_ = grp_start(Z2S, w), wn_ = grp_start(Z2S, w + 1) - ws_;                                           \
    dma16((dst) + ((e) - 7) * 1024, vwo, so_, ((ws_ + TILE(wn_ - 2 + (((e) - 7) >> 1), wn_)) * 2 + (((e) - 7) & 1)) * 8192); \
  }
#define VWO() const u32 vwo = ((lane >> 2) & 7) * 1024 + c2 * 128 + (lane >> 5) * 64 + (((lane & 3) ^ (((lane >> 4) & 1) << 1)) * 16)
#define LOAD_B(u, so_)                                                                                              \
  bfr[u][0] = ld16_bo<0>(l16, sws, so_); bfr[u][1] = ld16_bo<1024>(l16, sws, so_);                                  \
  bfr[u][2] = ld16_bo<2048>(l16, sws, so_); bfr[u][3] = ld16_bo<3072>(l16, sws, so_);                               \
  bfr[u][4] = ld16_bo<0>(l16, sws, (so_) ^ 4096); bfr[u][5] = ld16_bo<1024>(l16, sws, (so_) ^ 4096);                \
  bfr[u][6] = ld16_bo<2048>(l16, sws, (so_) ^ 4096); bfr[u][7] = ld16_bo<3072>(l16, sws, (so_) ^ 4096);

  if constexpr (t == 0) {
    int vq = 2;
    const rsrc_t sw = __builtin_amdgcn_make_buffer_rsrc((void*)win, 0, 512 * 2752, 0x00020000);
    const int z2s = grp_start(Z2S, c), z2n = grp_start(Z2S, c + 1) - z2s;
    const int z1s = grp_start(Z1S, c), z1n = grp_start(Z1S, c + 1) - z1s;
    u32 vw[3];
    int wch[3];
#pragma unroll
    for (int e = 0; e < 3; ++e) {
      const int sg = 2 * e + (lane >> 5), hi = sg >= 3;
      wch[e] = (sg - 3 * hi) * 4 + ((lane & 3) ^ (hi << 1));
      vw[e] = wch[e] < 2 * z2n ? (16 * w + 8 * hi + ((lane >> 2) & 7)) * 2752 + 1376 + 32 * z2s + 16 * wch[e] : 0x7FFF0000u;
    }
    const u32 w3k = w * 3072;
#define DMA_W(bk, e) DMA_C(w3k, LZ_W + ((bk) % 3) * 24576 + (e) * 1024, vw[e], sw, ((2 * (bk) + rot) & 7) * (64 * 2752))
#define UBASE(u) ((u) % 5 == 0 ? LZ_U : ((u) % 5 - 1) * 16384)
#define ISS_X(n) do { VM(DMA_X(((n) & 3) * 16384, n, 0)); VM(DMA_X(((n) & 3) * 16384, n, 1)); mx[n] = vq; } while (0)
#define ISS_U(u) do { VM(DMA_U(u, UBASE(u), 0)); VM(DMA_U(u, UBASE(u), 1)); mu[u] = vq; } while (0)
#define ISS_W(bk, e0, e1) do { _Pragma("unroll") for (int e_ = e0; e_ < e1; ++e_) VM(DMA_W(bk, e_)); mw[bk] = vq; } while (0)
    int mx[8], mw[8], mu[16];
    mx[0] = vq;
    ISS_W(0, 0, 3);
    u32x4 xrow;
    VM(xrow = ld16(l16, sx, (c * 8 + w) * 1024));
    const int mxr = vq;
    ISS_X(1);
    const u32 xa0 = opq((32 * rw + i16) * 128 + ((g ^ (i16 >> 1)) * 16)), xa1 = xa0 ^ 64;
    u32 ta[3];
    int tl[3];
    {
      const u32 trb = LZ_W + g * 1536 + ((lane >> 2) & 3) * 64 + ((((g & 1) << 1) | ((lane >> 1) & 1)) * 16) + (lane & 1) * 8;
#pragma unroll
      for (int j = 0; j < 3; ++j) {
        tl[j] = 3 * cw + (j + rw) % 3;
        ta[j] = (trb + (tl[j] >> 1) * 512) ^ ((tl[j] & 1) * 32);
      }
    }
#define NEXT_BLOCK(bk) _Pragma("unroll") for (int j = 0; j < 3; ++j) ta[j] = opq(ta[j] + ((bk) == 0 ? 0 : ((bk) % 3 == 0 ? -49152 : 24576)))
    f32x4 acc[2][3], csa = zero4;
#pragma unroll
    for (int j = 0; j < 3; ++j) acc[0][j] = acc[1][j] = zero4;
    u32x4 xf[16][2], wf[2][3];
    u32x4 xg;
    int mxg = 0;
    WAITQ(mw[0]);
    BARRIER();
    USE_EPOCH()
#define P1_MEM(n, h)                                                                                                \
  {                                                                                                                 \
    if ((n) == 0) { if ((h) == 0) ISS_X(2); if ((h) == 1) ISS_W(1, 0, 2); if ((h) == 2) ISS_W(1, 2, 3); if ((h) == 3) ISS_X(3); } \
    if ((n) == 1) { if ((h) == 0) ISS_W(2, 0, 2); if ((h) == 1) ISS_W(2, 2, 3); if ((h) == 2) VM(DMA_X(0, 4, 0)); if ((h) == 3) { VM(DMA_X(0, 4, 1)); mx[4] = vq; } } \
    if ((n) == 2 || (n) == 4) { if ((h) < 2) { VM(DMA_X((((n) + 3) & 3) * 16384, (n) + 3, (h) & 1)); mx[(n) + 3] = vq; } else ISS_W((n) / 2 + 2, (h) - 2, (h) - 1); } \
    if ((n) == 3) { if ((h) == 0) ISS_W(3, 2, 3); else if ((h) < 3) { VM(DMA_X(32768, 6, (h) - 1)); mx[6] = vq; } else ISS_U(0); } \
    if ((n) == 5) { if ((h) == 0) ISS_W(4, 2, 3); if ((h) == 1) { VM(xg = ld16_sc1(i16 * 16, sws, WS_XG + (P * 128 + 16 * w) * 16)); mxg = vq; } \
                    if ((h) >= 2) { VM(DMA_U(1, UBASE(1), (h) - 2)); mu[1] = vq; } }                                 \
    if ((n) == 6) { if ((h) < 3) ISS_W(5, h, (h) + 1); else ISS_U(2); }                                             \
    if ((n) == 7) { if ((h) < 2) { VM(DMA_U(3, UBASE(3), h)); mu[3] = vq; } }                                       \
  }
#pragma unroll
    for (int ks = 0; ks <= 16; ++ks) {
      const int n = ks >> 1, kk = ks & 1;
      if (ks > 0 && ks < 16 && kk == 0) {
        { const int mk = (n & 1) ? mx[n] : (mx[n] > mw[n >> 1] ? mx[n] : mw[n >> 1]); WAITQ(mk); }
        BARRIER();
      }
      if (ks < 16 && (ks & 3) == 0) { NEXT_BLOCK(ks >> 2); }
      if (ks == 8) {
#pragma unroll
        for (int e = 0; e < 3; ++e)
          vw[e] = wch[e] < 2 * z1n ? (16 * w + 8 * (2 * e + (lane >> 5) >= 3) + ((lane >> 2) & 7)) * 2752 + 32 * z1s + 16 * wch[e] : 0x7FFF0000u;
      }
      const int k1 = ks > 0 ? ks - 1 : 0, q1 = k1 & 1;
#pragma unroll
      for (int m = 0; m < 7; ++m) {
        if (ks > 0) {
          if (m < 6) acc[m & 1][m >> 1] = mfma32(xf[k1][m & 1], wf[q1][m >> 1], acc[m & 1][m >> 1]);
          else csa = mfma32(ones4, wf[q1][0], csa);
          SCHED();
        }
        if (ks < 16) {
          if (m == 0) { P1_MEM(n, 2 * kk) xf[ks & 15][0] = lds16((kk ? xa1 : xa0) + (n & 3) * 16384); xf[ks & 15][1] = lds16((kk ? xa1 : xa0) + (n & 3) * 16384 + 2048); }
          else if (m < 4) wf[kk][m - 1] = ldfrag(ta[m - 1] + (ks & 3) * 6144, 256);
          if (m == 4) P1_MEM(n, 2 * kk + 1)
        }
        SCHED();
      }
#pragma unroll
      for (int j = 0; j < 3; ++j) { PIN(acc[0][j]); PIN(acc[1][j]); }
      PIN(csa);
      if (ks == 2) EARLY_STATS(mxr)
    }
    GATHER_XS()
    sts4f(L_CS + tl[0] * 64 + i16 * 4, csa[0]);
    WAITQ(mu[1] > mw[4] ? mu[1] : mw[4]);
    READ_U(0, UBASE(0)) READ_U(1, UBASE(1))
    BARRIER();
    u32x4 zp[3];
    ISS_W(6, 0, 3);
    {
      f32x2 ps[4], pq[4];
      float cs[3];
#pragma unroll
      for (int j = 0; j < 3; ++j) cs[j] = -lds4f(L_CS + tl[j] * 64 + i16 * 4);
#pragma unroll
      for (int rt = 0; rt < 2; ++rt) {
        const f32x4 rr = lds16f(L_XS + (32 * rw + 16 * rt + 4 * g) * 4), mm = lds16f(L_XS + 512 + (32 * rw + 16 * rt + 4 * g) * 4);
        f32x2 z0[3], z1[3];
#pragma unroll
        for (int j = 0; j < 3; ++j) {
          z0[j] = gelu2(fma2(lo2(mm), bc2(cs[j]), lo2(acc[rt][j])) * lo2(rr));
          z1[j] = gelu2(fma2(hi2(mm), bc2(cs[j]), hi2(acc[rt][j])) * hi2(rr));
          zp[j][2 * rt] = pk2v(z0[j]); zp[j][2 * rt + 1] = pk2v(z1[j]);
        }
        ps[2 * rt] = z0[0] + z0[1] + z0[2]; ps[2 * rt + 1] = z1[0] + z1[1] + z1[2];
        pq[2 * rt] = fma2(z0[2], z0[2], fma2(z0[1], z0[1], z0[0] * z0[0]));
        pq[2 * rt + 1] = fma2(z1[2], z1[2], fma2(z1[1], z1[1], z1[0] * z1[0]));
        SCHED();
        if (rt == 0) { ISS_U(4); ISS_U(5); }
        else ISS_U(6);
        SCHED();
      }
      const bool b3 = lane & 8, b2 = lane & 4, b1 = lane & 2;
#pragma unroll
      for (int m = 0; m < 2; ++m)
#pragma unroll
        for (int e = 0; e < 2; ++e) {
          ps[m][e] = (b3 ? ps[m + 2][e] : ps[m][e]) + dpp<0x128>(b3 ? ps[m][e] : ps[m + 2][e]);
          pq[m][e] = (b3 ? pq[m + 2][e] : pq[m][e]) + dpp<0x128>(b3 ? pq[m][e] : pq[m + 2][e]);
        }
#pragma unroll
      for (int e = 0; e < 2; ++e) {
        ps[0][e] = (b2 ? ps[1][e] : ps[0][e]) + dpp<0x141>(b2 ? ps[0][e] : ps[1][e]);
        pq[0][e] = (b2 ? pq[1][e] : pq[0][e]) + dpp<0x141>(b2 ? pq[0][e] : pq[1][e]);
      }
      float s_ = (b1 ? ps[0][1] : ps[0][0]) + dpp<0x4E>(b1 ? ps[0][0] : ps[0][1]);
      float q_ = (b1 ? pq[0][1] : pq[0][0]) + dpp<0x4E>(b1 ? pq[0][0] : pq[0][1]);
      s_ += dpp<0xB1>(s_);
      q_ += dpp<0xB1>(q_);
      const int row = 32 * rw + 16 * (i16 >> 3) + 4 * g + ((i16 >> 1) & 3);
      VM(st16_sc1((u32x4){__float_as_uint(s_), tlo, __float_as_uint(q_), tlo}, row * 16 + ((lane & 1) << 30), sws, WS_SG + ((P * 8 + c) * 2 + cw) * 2048));
      PIN(zp[0]); PIN(zp[1]); PIN(zp[2]);
      CLEAR_H()
    }
    {
    RELANE();
#pragma unroll
    for (int j = 0; j < 3; ++j) acc[0][j] = acc[1][j] = zero4;
    csa = zero4;
    u32x4 q0, q1, q2, q3;
    int mq = 0, mz = 0;
    const u32 gvo = (2 * g) * 4096 + (32 * rw + 16 * cw + i16) * 16;
    const u32 gso = WS_SG + P * 32768;
#pragma unroll
    for (int ks = 0; ks <= 16; ++ks) {
      if (ks == 8) {
        WAITQ(mq); PIN(q0); PIN(q1); PIN(q2); PIN(q3);
#pragma nounroll
        for (int it = 0; it < SPIN_CAP; ++it) {
          if (__all(tag_ok(q0, tlo) && tag_ok(q1, tlo) && tag_ok(q2, tlo) && tag_ok(q3, tlo))) break;
          __builtin_amdgcn_s_sleep(6);
          asm volatile(
              "buffer_load_dwordx4 %0, %4, %5, %6 offen sc1\n\t"
              "buffer_load_dwordx4 %1, %4, %5, %6 offen offset:2048 sc1\n\t"
              "buffer_load_dwordx4 %2, %4, %5, %7 offen sc1\n\t"
              "buffer_load_dwordx4 %3, %4, %5, %7 offen offset:2048 sc1\n\t"
              "s_waitcnt vmcnt(0)"
              : "=&v"(q0), "=&v"(q1), "=&v"(q2), "=&v"(q3)
              : "v"(gvo), "s"(sws), "s"(gso), "s"(gso + 4096)
              : "memory");
        }
        float s = (__uint_as_float(q0[0]) + __uint_as_float(q1[0])) + (__uint_as_float(q2[0]) + __uint_as_float(q3[0]));
        float q = (__uint_as_float(q0[2]) + __uint_as_float(q1[2])) + (__uint_as_float(q2[2]) + __uint_as_float(q3[2]));
        red2g(s, q);
        float r2, m2;
        ln_fin(s, q, 1.0f / 688, r2, m2);
        sts4f(L_ZST + (32 * rw + 16 * cw + i16) * 4, r2);
        sts4f(L_ZST + 512 + (32 * rw + 16 * cw + i16) * 4, -m2 * r2);
      }
      if (ks < 16 && (ks & 3) == 0) {
        if (ks > 0) { WAITQ(ks == 12 ? mz : mw[4 + (ks >> 2)]); BARRIER(); }
        if (ks == 12) { if (w == 0) SET_FLAG(WS_FA + (P * 8 + c) * 128); }
        NEXT_BLOCK(4 + (ks >> 2));
      }
      if (ks == 8) {
#pragma unroll
        for (int rt = 0; rt < 2; ++rt) {
          const u32 ra = L_ZST + (32 * rw + 16 * rt + 4 * g) * 4;
          const f32x4 r4 = lds16f(ra), m4 = lds16f(ra + 512);
#pragma unroll
          for (int j = 0; j < 3; ++j) {
            const u32 u0 = zp[j][2 * rt], u1 = zp[j][2 * rt + 1];
            zp[j][2 * rt] = pk2v(fma2((f32x2){blo(u0), bhi(u0)}, lo2(r4), lo2(m4)));
            zp[j][2 * rt + 1] = pk2v(fma2((f32x2){blo(u1), bhi(u1)}, hi2(r4), hi2(m4)));
          }
        }
#pragma unroll
        for (int j = 0; j < 3; ++j)
          VM(st16_sc1(zp[j], l16, sws, (tl[j] < z2n) ? WS_ZB + ((z2s + tl[j]) * 64 + 4 * P + rw) * 1024 : 0x7FFF0000u));
        mz = vq;
        SCHED();
      }
      if (ks >= 5 && ks <= 9) { WAITQ(mu[ks - 3]); READ_U(ks - 3, UBASE(ks - 3)) }
      if (ks >= 9 && ks <= 13) { WAITQ(mu[ks - 2]); READ_U(ks - 2, UBASE(ks - 2)) }
      if (ks >= 13) { WAITQ(mu[ks - 1]); READ_U(ks - 1, UBASE(ks - 1)) }
      const int k1 = ks > 0 ? ks - 1 : 0, q1_ = k1 & 1;
#pragma unroll
      for (int m = 0; m < 7; ++m) {
        if (ks > 0) {
          if (m < 6) acc[m & 1][m >> 1] = mfma32(wf[q1_][m >> 1], xf[k1][m & 1], acc[m & 1][m >> 1]);
          else csa = mfma32(ones4, wf[q1_][0], csa);
          SCHED();
        }
        if (ks < 16 && m >= 1 && m < 4) wf[ks & 1][m - 1] = ldfrag(ta[m - 1] + (ks & 3) * 6144, 256);
        if (m == 0 || m == 4) {
          const int h = m == 4;
          if (h == 0 && ks >= 6 && ks <= 13) asm volatile("s_waitcnt lgkmcnt(10)" ::: "memory");
          if (ks >= 6 && ks <= 10) { VM(DMA_U(ks + 1, UBASE(ks + 1), h)); mu[ks + 1] = vq; }
          if (ks >= 10 && ks <= 13) { VM(DMA_U(ks + 2, UBASE(ks + 2), h)); mu[ks + 2] = vq; }
        }
        if (m == 6) {
          if (ks >= 4 && ks <= 6) ISS_W(7, ks - 4, ks - 3);
        }
        if (m == 2) {
          if (ks == 5) { VM(q0 = ld16_sc1o<0>(gvo, sws, gso)); VM(q1 = ld16_sc1o<2048>(gvo, sws, gso)); }
          if (ks == 6) { VM(q2 = ld16_sc1o<0>(gvo, sws, gso + 4096)); VM(q3 = ld16_sc1o<2048>(gvo, sws, gso + 4096)); mq = vq; }
        }
        SCHED();
      }
#pragma unroll
      for (int j = 0; j < 3; ++j) { PIN(acc[0][j]); PIN(acc[1][j]); }
      PIN(csa);
    }
    sts4f(L_CS + 512 + tl[0] * 64 + i16 * 4, csa[0]);
    gf = ld8_sc1((lane >> 5) * 1024, sws, WS_FA + (16 * w + c2) * 128);
    BARRIER();
    VWO();
    DMA_WOUT(0) DMA_WOUT(1)
    WAITV(2);
    PIN(gf);
    if (!__all(gf[0] == tlo && gf[1] == thi)) poll_flags((lane >> 5) * 1024, true, sws, WS_FA + (16 * w + c2) * 128, tlo, thi);
    {
      float xr[2], xm[2];
#pragma unroll
      for (int rt = 0; rt < 2; ++rt) {
        xr[rt] = lds4f(L_XS + (32 * rw + 16 * rt + i16) * 4);
        xm[rt] = -lds4f(L_XS + 512 + (32 * rw + 16 * rt + i16) * 4);
      }
      const u32 so0 = zb0 + TILE(0, gn) * 65536;
#pragma unroll
      for (int j = 0; j < 3; ++j) {
        const f32x4 cc = lds16f(L_CS + 512 + tl[j] * 64 + g * 16);
        bfr[0][3 * j] = ld16_b(l16, sws, so0 ^ (3 * j * 1024));
        bfr[0][3 * j + 1] = ld16_b(l16, sws, so0 ^ ((3 * j + 1) * 1024));
        if (j < 2) bfr[0][3 * j + 2] = ld16_b(l16, sws, so0 ^ ((3 * j + 2) * 1024));
        u32x4 zz;
#pragma unroll
        for (int rt = 0; rt < 2; ++rt) {
          zz[2 * rt] = pk2v(gelu2(fma2(lo2(cc), bc2(xm[rt]), lo2(acc[rt][j])) * bc2(xr[rt])));
          zz[2 * rt + 1] = pk2v(gelu2(fma2(hi2(cc), bc2(xm[rt]), hi2(acc[rt][j])) * bc2(xr[rt])));
        }
        st16_sc1(zz, l16, sws, (tl[j] < z1n) ? WS_Z1 + ((4 * P + rw) * 43 + z1s + tl[j]) * 1024 : 0x7FFF0000u);
        SCHED();
      }
    }
    }
  } else {
    int vq = 2;
    const rsrc_t sw = __builtin_amdgcn_make_buffer_rsrc((void*)wqkv, 0, 512 * 3072, 0x00020000);
    const u32 vw = (8 * w + ((lane >> 2) & 7)) * 3072 + 128 * c + ((lane >> 5) * 4 + ((lane & 3) ^ ((w & 1) << 1))) * 16;
    const u32 w3k = w * 3072;
#define DMA_S(n, e)                                                                                                 \
  do {                                                                                                              \
    if ((e) < 2) DMA_X(((n) % 3) * 40960, n, (e) & 1);                                                              \
    else DMA_CC(w3k, ((n) % 3) * 40960 + 16384 + ((e) - 2) * 1024, vw, sw, "s_add_u32", (((n) + rot) & 7) * (64 * 3072), ((e) - 2) * 1024); \
  } while (0)
#define ISS_Q(u, base) do { VM(DMA_U(u, base, 0)); VM(DMA_U(u, base, 1)); mu[u] = vq; } while (0)
    int ms[8], mu[16];
    VM(DMA_S(0, 2)); VM(DMA_S(0, 3)); VM(DMA_S(0, 4)); ms[0] = vq;
    u32x4 xrow;
    VM(xrow = ld16(l16, sx, ((8 + c) * 8 + w) * 1024));
    const int mxr = vq;
#pragma unroll
    for (int e = 0; e < 5; ++e) { VM(DMA_S(1, e)); ms[1] = vq; }
    const int l32 = lane & 31, hh = lane >> 5;
    u32 xa = (32 * rw + l32) * 128 + ((hh ^ ((l32 >> 1) & 7)) * 16);
    u32 wa = 16384 + hh * 3072 + cw * 512 + ((lane >> 2) & 3) * 64 + (((((g & 1) << 1) | ((lane >> 1) & 1)) ^ (hh << 1)) * 16) + (lane & 1) * 8;
    u32 ca = 16384 + g * 3072 + min(w, 5) * 512 + ((lane >> 2) & 3) * 64 + ((((g & 1) << 1) | ((lane >> 1) & 1)) * 16) + (lane & 1) * 8;
    const f32x16 zero16 = {0, 0, 0, 0, 0, 0, 0, 0, 0, 0, 0, 0, 0, 0, 0, 0};
    f32x16 acc[3] = {zero16, zero16, zero16};
    f32x4 csa[2] = {zero4, zero4};
    u32x4 xg;
    int mxg = 0;
    WAITQ(ms[0]);
    BARRIER();
    USE_EPOCH()
    {
      u32x4 xf[2], wf[2][4];
#pragma unroll
      for (int n = 0; n <= 8; ++n)
#pragma unroll
      for (int sq = 0; sq < 4; ++sq) {
        const int s = 4 * n + sq;
        if (s > 32) continue;
        if (s < 32 && sq == 0) {
          if (n > 0) {
            WAITQ(ms[n]);
            BARRIER();
          }
          const int d = n == 0 ? 0 : (n % 3 == 0 ? -81920 : 40960);
          xa = opq(xa + d); wa = opq(wa + d); ca = opq(ca + d);
        }
        const int q1 = (s + 1) & 1, q0 = s & 1;
#pragma unroll
        for (int m = 0; m < 4; ++m) {
          if (s > 0) {
            if (m < 3) acc[m] = mfmaL(wf[q1][m], xf[q1], acc[m]);
            else csa[q1] = mfma32(ones4, wf[q1][3], csa[q1]);
            SCHED();
          }
          if (s < 32) {
            if (m == 0) { xf[q0] = lds16(xa ^ (32 * sq)); wf[q0][0] = ldfrag(wa + sq * 6144, 256); }
            if (m == 1) { wf[q0][1] = ldfrag(wa + sq * 6144 + 1024, 256); wf[q0][2] = ldfrag(wa + sq * 6144 + 2048, 256); }
            if (m == 2) wf[q0][3] = ldfrag((ca ^ (32 * (sq & 1))) + (sq >> 1) * 12288, 256);
            if (m == 0 || m == 2) {
              const int h = 2 * sq + (m == 2);
              if (n <= 5 && h < 5) { VM(DMA_S(n + 2, h)); ms[n + 2] = vq; }
              if (n == 0 && h == 5) {
                rsrc_t sp = __builtin_amdgcn_make_buffer_rsrc((void*)wphi, 0, 16384, 0x00020000);
                VM(dma16(L_WPHI + (w & 1) * 1024, l16, sp, c * 2048 + (w & 1) * 1024));
              }
              if (n == 4 && h == 5) { VM(xg = ld16_sc1(i16 * 16, sws, WS_XG + (P * 128 + 16 * w) * 16)); mxg = vq; }
              if (n == 6) { VM(DMA_U(sq, (sq < 2 ? LQ_M : LQ_A) + (sq & 1) * 16384, h & 1)); mu[sq] = vq; }
              if (n == 7 && sq < 2) { VM(DMA_U(4 + sq, sq * 16384, h & 1)); mu[4 + sq] = vq; }
            }
          }
          SCHED();
        }
        PIN(acc[0]); PIN(acc[1]); PIN(acc[2]); PIN(csa[0]); PIN(csa[1]);
        if (s == 3) EARLY_STATS(mxr)
      }
    }
    GATHER_XS()
    sts4f(L_CS + w * 128 + i16 * 4, csa[0][0]);
    sts4f(L_CS + w * 128 + 64 + i16 * 4, csa[1][0]);
    BARRIER();
    WAITQ(mu[3]);
    READ_U(0, LQ_M) READ_U(1, LQ_M + 16384) READ_U(2, LQ_A) READ_U(3, LQ_A + 16384)
    const float xr = lds4f(L_XS + (32 * rw + l32) * 4), xm = -lds4f(L_XS + 512 + (32 * rw + l32) * 4);
    u32x4 wp0, wp1;
    {
      const u32 a = L_WPHI + (2 * cw + (lane & 1)) * 512 + (4 * hh + ((lane >> 2) & 3)) * 32 + (2 * (g & 1) + ((lane >> 1) & 1)) * 8;
      const u32x4 f = ldfrag(a, 256);
      const bool own = (lane >> 2) & 1;
#pragma unroll
      for (int e = 0; e < 4; ++e) { wp0[e] = own ? 0u : f[e]; wp1[e] = own ? f[e] : 0u; }
    }
#define FEATURES(e_, dst, hook)                                                                                         \
  {                                                                                                                 \
    u32x4 kp[2];                                                                                                    \
    _Pragma("unroll") for (int a = 0; a < 4; ++a) {                                                                \
      const f32x4 cc = lds16f(L_CS + (2 * (e_) + cw) * 128 + a * 32 + hh * 16);                                      \
      kp[a >> 1][2 * (a & 1)] = pk2v(fma2(lo2(cc), bc2(xm), (f32x2){acc[e_][4 * a], acc[e_][4 * a + 1]}));          \
      kp[a >> 1][2 * (a & 1) + 1] = pk2v(fma2(hi2(cc), bc2(xm), (f32x2){acc[e_][4 * a + 2], acc[e_][4 * a + 3]}));  \
    }                                                                                                               \
    f32x16 pl = mfmaL(wp0, kp[0], zero16);                                                                          \
    pl = mfmaL(wp1, kp[1], pl);                                                                                     \
    hedgehog16(pl, xr, dst, hook);                                                                                  \
  }
    {
      const int s_ = 32 * rw + l32;
      const u32 key = (((s_ >> 1) & 3) ^ (((s_ >> 3) & 1) << 1)) * 16;
      {
        const u32 va = LQ_V + (s_ >> 3) * 1024 + cw * 512 + (s_ & 7) * 64 + hh * 8;
#pragma unroll
        for (int a = 0; a < 4; ++a) {
          const f32x4 cc = lds16f(L_CS + (4 + cw) * 128 + a * 32 + hh * 16);
          sts8(va + ((a * 16) ^ key), (u32x2){pk2v(fma2(lo2(cc), bc2(xm), (f32x2){acc[2][4 * a], acc[2][4 * a + 1]}) * bc2(xr)),
                                              pk2v(fma2(hi2(cc), bc2(xm), (f32x2){acc[2][4 * a + 2], acc[2][4 * a + 3]}) * bc2(xr))});
        }
      }
      WAITQ(mu[5]);
      READ_U(4, 0) READ_U(5, 16384)
      WAITL0();
      u32x4 ph[4];
      auto hk = [&](int i) __attribute__((always_inline)) {
        const int u = 6 + (i >> 1);
        SCHED();
        VM(DMA_U(u, (u < 8 ? LQ_M : (u < 10 ? LQ_A : 0)) + (u & 1) * 16384, i & 1));
        mu[u] = vq;
        SCHED();
      };
      FEATURES(1, ph, hk)
      const u32 pa = LQ_PHI + (s_ >> 3) * 2048 + (2 * cw + hh) * 512 + (s_ & 7) * 64;
#pragma unroll
      for (int a = 0; a < 4; ++a) sts16(pa + ((a * 16) ^ key), ph[a]);
    }
    BARRIER();
    {
      const u32 kx = (((lane >> 3) & 1) ^ ((g & 1) << 1)) * 16;
      const u32 pa = LQ_PHI + g * 2048 + (w >> 1) * 512 + ((lane >> 2) & 3) * 64 + (((2 * (w & 1) + ((lane >> 1) & 1)) * 16) ^ kx) + (lane & 1) * 8;
      const u32 va = LQ_V + g * 1024 + (w >> 2) * 512 + ((lane >> 2) & 3) * 64 + (((2 * ((w >> 1) & 1) + ((lane >> 1) & 1)) * 16) ^ kx) + (lane & 1) * 8;
      u32x4 pf[4], vf[4];
#pragma unroll
      for (int k = 0; k < 4; ++k) {
        pf[k] = cat(ldstr(pa + k * 8192), ldstr((pa ^ 32) + k * 8192 + 256));
        vf[k] = cat(ldstr(va + k * 4096), ldstr((va ^ 32) + k * 4096 + 256));
      }
      f32x4 kv = zero4, kz = zero4;
#pragma unroll
      for (int k = 0; k < 4; ++k) { kv = mfma32(pf[k], vf[k], kv); kz = mfma32(pf[k], ones4, kz); }
      const u32 so = WS_KP + (c * 16 + P) * KP_SZ;
      u32x4 g0 = {__float_as_uint(kv[0]), tlo, __float_as_uint(kv[1]), tlo}, g1 = {__float_as_uint(kv[2]), tlo, __float_as_uint(kv[3]), tlo};
      u32x4 g2 = {__float_as_uint((lane & 1) ? kz[2] : kz[0]), tlo, __float_as_uint((lane & 1) ? kz[3] : kz[1]), tlo};
      PIN(g0); PIN(g1); PIN(g2);
      asm volatile("s_nop 7" ::: "memory");
      VM(st16_sc1(g0, l16, sws, so + w * 2048));
      VM(st16_sc1(g1, l16, sws, so + w * 2048 + 1024));
      VM(st16_sc1(g2, g * 32 + (lane & 1) * 16 + ((i16 >> 1) << 26), sws, so + 16384 + w * 128));
      CLEAR_H()
    }
    WAITQ(mu[11]);
    READ_U(6, LQ_M) READ_U(7, LQ_M + 16384) READ_U(8, LQ_A) READ_U(9, LQ_A + 16384) READ_U(10, 0) READ_U(11, 16384)
    BARRIER();
    ISS_Q(12, LQ_M); VM(DMA_U(13, LQ_M + 16384, 0));
    u32x4 phq[4];
    {
      VWO();
      auto hk = [&](int i) __attribute__((always_inline)) {
        SCHED();
        if (i == 0) DMA_U(13, LQ_M + 16384, 1);
        else if (i < 5) DMA_U(14 + ((i - 1) >> 1), LQ_A + ((i - 1) >> 1) * 16384, (i - 1) & 1);
        else DMA_WOUT(i - 5)
        SCHED();
      };
      FEATURES(0, phq, hk)
    }
    PIN(phq[0]); PIN(phq[1]); PIN(phq[2]); PIN(phq[3]);
    if (w >= 5) {
      const int id = 2 * P + (w == 7 ? (lane & 1) : w - 5), wq = id >> 2, gq = id & 3;
      const u32 vi0 = (4 * g) * KP_SZ + (w == 7 ? 16384 + wq * 128 + gq * 32 : wq * 2048 + (gq * 16 + i16) * 16);
      const u32 vi1 = vi0 + (w == 7 ? 16 : 1024);
      const u32 vo = (w == 7 ? (WS_KZ - WS_KF) + c * 512 + id * 16 + ((i16 >> 1) << 25) : c * 8192 + id * 256 + i16 * 16) + (g << 28);
      float sa[4];
#pragma nounroll
      for (int it = 0; it < SPIN_CAP; ++it) {
        u32x4 a[4][2];
#pragma unroll
        for (int o = 0; o < 4; ++o) {
          a[o][0] = ld16_sc1(vi0, sws, WS_KP + (c * 16 + o) * KP_SZ);
          a[o][1] = ld16_sc1(vi1, sws, WS_KP + (c * 16 + o) * KP_SZ);
        }
        WAITV(0);
        bool ok = true;
#pragma unroll
        for (int o = 0; o < 4; ++o) { PIN(a[o][0]); PIN(a[o][1]); ok = ok && tag_ok(a[o][0], tlo) && tag_ok(a[o][1], tlo); }
#pragma unroll
        for (int e = 0; e < 4; ++e)
          sa[e] = (__uint_as_float(a[0][e >> 1][2 * (e & 1)]) + __uint_as_float(a[1][e >> 1][2 * (e & 1)])) +
                  (__uint_as_float(a[2][e >> 1][2 * (e & 1)]) + __uint_as_float(a[3][e >> 1][2 * (e & 1)]));
        if (__all(ok)) break;
        __builtin_amdgcn_s_sleep(6);
      }
      red2g(sa[0], sa[1]);
      red2g(sa[2], sa[3]);
      st16_sc1((u32x4){pk2(sa[0], sa[1]), tlo, pk2(sa[2], sa[3]), tlo}, vo, sws, WS_KF);
    }
    gf = ld8_sc1((lane >> 5) * 1024, sws, WS_FA + (16 * w + c2) * 128);
    WAITV(8);
    READ_U(12, LQ_M) READ_U(13, LQ_M + 16384) READ_U(14, LQ_A) READ_U(15, LQ_A + 16384)
    BARRIER();
#pragma unroll
    for (int a = 0; a < 4; ++a) sts16(L_WO1 + w * 4096 + a * 1024 + l16, phq[a]);
    WAITV(0);
    PIN(gf);
    if (!__all(gf[0] == tlo && gf[1] == thi)) poll_flags((lane >> 5) * 1024, true, sws, WS_FA + (16 * w + c2) * 128, tlo, thi);
    { const u32 so_ = zb0 + TILE(0, gn) * 65536; LOAD_B(0, so_) }
  }

  {
  RELANE();
  u32x4 kfg, kzg;
  const u32 kfo = WS_KF + c * 8192 + w * 1024, kzo = WS_KZ + c * 512 + w * 64;
  if (t != 0) {
    kfg = ld16_sc1(l16, sws, kfo);
    kzg = ld16_sc1(g * 16, sws, kzo);
  }
  { const u32 so_ = zb0 + TILE(1, gn) * 65536; LOAD_B(1, so_) }
  const u32 redw = opq(L_RED + w * 4096 + lane * 16), redr = opq(L_RED + (rw ^ (2 * ux0)) * 1024 + lane * 16);
  const u32 f3s = __builtin_amdgcn_readfirstlane(WS_FB + (P * 8 + (c2 > 0 ? c2 - 1 : 0)) * 128);
  const u32 vw3 = ((lane >> 2) & 7) * 1024 + c2 * 128 + (lane >> 5) * 64;
  const rsrc_t swo = __builtin_amdgcn_make_buffer_rsrc((void*)wo, 0, 512 * 1024, 0x00020000);
#define DMA_WO(dst, half, e) dma16((dst) + w * 4096 + (e) * 1024, (vw3 + (lane & 3) * 16) ^ (((e) & 1) * 32), swo, ((w + 8 * (half)) * 4 + (e)) * 8192)
#define FV() ((lane >> 5) * (WS_FC - WS_FB) + (P * 8 + ((lane >> 2) & 7)) * 128)
  u32x2 z1r[6], hold;
  u32 bs;
  f32x4 sm0;
#define EMIT_H(jt, sm_)                                                                                             \
  {                                                                                                                 \
    const f32x2 bb = bc2(blo(bs));                                                                                  \
                                                                 \
    const u32 h0 = min(pk2v((f32x2){blo(z1r[jt][0]), bhi(z1r[jt][0])} * (lo2(sm_) + bb)), 0xFFFFFFFEu);             \
    const u32 h1 = min(pk2v((f32x2){blo(z1r[jt][1]), bhi(z1r[jt][1])} * (hi2(sm_) + bb)), 0xFFFFFFFEu);             \
    const u32 hb_ = WS_HF + ((4 * p + rw) * 24 + c2) * 1024;                                                        \
    if ((jt) == 0 || ((jt) == 2 && gn == 6) || (jt) == gn - 2) hold = (u32x2){h0, h1};                              \
    else if ((jt) == 2) st16_sc1((u32x4){h0, h1, 0u, 0u}, l16, sws, hb_ + 8192);                                    \
    else st16_sc1((u32x4){hold[0], hold[1], h0, h1}, l16, sws, hb_ + ((jt) == 1 ? 0 : ((jt) == gn - 1 ? 16384 : 8192))); \
  }
#pragma unroll
  for (int j = 0; j < 6; ++j) {
    if (j < 5 || gn == 6) {
      const int u = j & 1;
      if (j == 0) WAITV(8);
      if (j == 2) WAITV(t ? 10 : 8);
      if (j == 1) { WAITV(9); WAITV_IF(cw, 1, 8); }
      if (j == 3) { WAITV(9); WAITV_IF(cw, 1, 8); }
      if (j == 4) { if (t) WAITV(6); else { WAITV(13); WAITV_IF(cw, 1, 12); WAITV_IF(gn, 5, 6); } WAITV_IF(2 * gn + cw, 11, 4); }
      if (j == 5) { WAITV(10); WAITV_IF(cw, 1, 9); }
#pragma unroll
      for (int i = 0; i < 8; ++i) PIN(bfr[u][i]);
      {
        f32x4 a_[4];
#pragma unroll
        for (int rt = 0; rt < 4; ++rt) {
          a_[rt] = zero4;
#pragma unroll
          for (int i = 0; i < 8; ++i) a_[rt] = mfma32(bfr[u][i], af[rt][i], a_[rt]);
          SCHED();
          if (rt > 0) sts16f(redw + u * 32768 + (rt - 1) * 1024, a_[rt - 1]);
          SCHED();
        }
        if (j == 0 && t != 0) {
          PIN(kfg); PIN(kzg);
          poll_gran(kfg, l16, sws, kfo, tlo);
          poll_gran(kzg, g * 16, sws, kzo, tlo);
          sts8(L_KFS + (w >> 1) * 1024 + i16 * 64 + (w & 1) * 32 + g * 8, (u32x2){kfg[0], kfg[2]});
          sts8(L_KFS + 4096 + w * 32 + g * 8, (u32x2){kzg[0], kzg[2]});
          sts8(L_KFS + 4352 + (lane & 7) * 8, (u32x2){0u, 0u});
        }
        sts16f(redw + u * 32768 + 3072, a_[3]);
      }
      u32x4 of[2];
      if (j == 1 && t != 0) {
        const int l32 = lane & 31, hh = lane >> 5;
        const bool own = ((lane >> 2) & 1) == hh;
        const u32 ka = own ? L_KFS + (2 * cw + hh) * 1024 + ((l32 & 3) + 4 * (l32 >> 3)) * 64 : L_KFS + 4352;
        const u32 zaa = own ? L_KFS + 4096 + (2 * cw + hh) * 64 : L_KFS + 4352;
        const u32 qa = L_WO1 + w * 4096 + l16;
        const f32x16 zero16 = {0, 0, 0, 0, 0, 0, 0, 0, 0, 0, 0, 0, 0, 0, 0, 0};
        f32x16 den = zero16, num = zero16;
#pragma unroll
        for (int a = 0; a < 4; ++a) {
          const u32x4 pq_ = lds16(qa + a * 1024);
          den = mfmaL(lds16(zaa + a * 16), pq_, den);
          num = mfmaL(lds16(ka + a * 16), pq_, num);
        }
        const float inv = __builtin_amdgcn_rcpf(den[0] + 1e-6f);
#pragma unroll
        for (int a = 0; a < 2; ++a) {
          of[a] = (u32x4){pk2(num[8 * a] * inv, num[8 * a + 1] * inv), pk2(num[8 * a + 2] * inv, num[8 * a + 3] * inv),
                          pk2(num[8 * a + 4] * inv, num[8 * a + 5] * inv), pk2(num[8 * a + 6] * inv, num[8 * a + 7] * inv)};
          PIN(of[a]);
        }
      }
      BAR_IF(cw, 1, (j == 2 && t != 0) ? 0 : 63);
      if (w == 4) {
        if (j == 0 && t == 0) SET_FLAG(WS_FB + (P * 8 + c) * 128);
        if (j == 2 && t != 0) SET_FLAG(WS_FC + (P * 8 + c) * 128);
      }
      if (j == 1) {
        if (cw == 0) {
          WAITV(0);
          PIN(gf);
          if (!__all(gf[0] == tlo && gf[1] == thi)) poll_flags((lane >> 5) * 128, true, sws, f3s, tlo, thi);
          rsrc_t sb = __builtin_amdgcn_make_buffer_rsrc((void*)bsp, 0, 4096, 0x00020000);
          bs = ld2(i16 * 2, sb, (64 * p + 16 * rw) * 2);
        }
      }
      if ((j == 1 || j == 2) && cw == 0) {
#pragma unroll
        for (int v = 3 * j - 3; v < 3 * j && v < 6 - t; ++v)
          z1r[v] = ld8_sc1(l16, sws, WS_Z1 + ((2 * p + (rw >> 1)) * 43 + gs + TILE(min(v, gn - 1), gn)) * 1024 + (rw & 1) * 8);
      }
      if ((j == 4 && gn == 5) || j == 5) gf = ld8_sc1(FV(), sws, WS_FB);
      if (j == 4 && t == 0) {
        VWO();
#pragma unroll
        for (int e = 2; e < 7; ++e) DMA_WOUT(e)
      }
      const u32 ra_ = redr + ((j + 1) & 1) * 32768;
      f32x4 sm, r_[2];
#pragma unroll
      for (int i = 0; i < 8; ++i) {
        if (j >= 1 && j < (t ? 3 : 4) && (i & 1) == 0 && cw == 0) {
          if (i == 2) sm = r_[0] + r_[1];
          if (i > 2) sm += r_[0] + r_[1];
          r_[0] = lds16f(ra_ + i * 4096); r_[1] = lds16f(ra_ + (i + 1) * 4096);
        }
        SCHED();
        if (j < 3) bfr[u][i] = ld16_b(l16, sws, (zb0 + TILE(j + 2, gn) * 65536) ^ (i * 1024));
        else if (j == 3 && t == 0) ld16_b_if6(bfr[1][i], gn, l16, sws, (zb0 + TILE(5, gn) * 65536) ^ (i * 1024));
        SCHED();
      }
      if (j == 0 && cw == 0) gf = ld8_sc1((lane >> 5) * 128, sws, f3s);
      if (j == 1 && t != 0) {
        const u32 vo = ((lane >> 4) & 1) * 16384 + (lane >> 5) * 512 + i16 * 16;
        st16_sc1(of[0], vo, sws, WS_OF + ((8 * P + 2 * rw) * 16 + 2 * c + cw) * 1024);
        st16_sc1(of[1], vo, sws, WS_OF + ((8 * P + 2 * rw) * 16 + 2 * c + cw) * 1024 + 256);
      }
      if (j == 2) { PIN(bs); _Pragma("unroll") for (int v = 0; v < 3; ++v) PIN(z1r[v]); }
      if (j == 3) { _Pragma("unroll") for (int v = 3; v < 6 - t; ++v) PIN(z1r[v]); }
      if (j >= 1 && cw == 0) {
        if (j < (t ? 3 : 4)) sm += r_[0] + r_[1];
        else {
          f32x4 q_[8];
#pragma unroll
          for (int o = 0; o < 8; ++o) q_[o] = lds16f(ra_ + o * 4096);
          SCHED();
          sm = ((q_[0] + q_[1]) + (q_[2] + q_[3])) + ((q_[4] + q_[5]) + (q_[6] + q_[7]));
        }
        if (j == 1) sm0 = sm;
        if (j == 2) EMIT_H(0, sm0)
        if (j >= 2) EMIT_H(j >= 2 ? j - 1 : 0, sm)
      }
      if (j == 3) {
#pragma unroll
        for (int e = 0; e < 4; ++e) DMA_WO(L_WO1, 0, e);
      }
      if (j == 2 && t != 0) WAITV_IF(cw, 0, 11);
      BAR_IF(cw, 0, 63);
    }
  }
  {
    const u32 bufA = L_RED + (gn & 1) * 32768, bufB = L_RED + 32768 - (gn & 1) * 32768;
    const u32 fv = FV();
    if (cw == 0) {
      const u32 ra_ = redr + (bufB - L_RED);
      f32x4 q_[8];
#pragma unroll
      for (int o = 0; o < 8; ++o) q_[o] = lds16f(ra_ + o * 4096);
      SCHED();
      const f32x4 sm = ((q_[0] + q_[1]) + (q_[2] + q_[3])) + ((q_[4] + q_[5]) + (q_[6] + q_[7]));
      if (gn == 6) EMIT_H(5, sm) else EMIT_H(4, sm)
    }
    WAITV(1); WAITV_IF(cw, 1, 0);
    PIN(gf);
    if (!__all(gf[0] == tlo && gf[1] == thi)) poll_flags(fv, true, sws, WS_FB, tlo, thi);
    RELANE();
    u32x4 hf[12], of[8];
    const u32 ofb = WS_OF + ((4 * p + rw) * 16 + cw) * 1024, hfb = WS_HF + ((4 * p + rw) * 24 + cw) * 1024;
#pragma unroll
    for (int e = 0; e < 4; ++e) of[e] = ld16_b(l16, sws, ofb + e * 2048);
#pragma unroll
    for (int e = 0; e < 4; ++e) hf[e] = ld16_b(l16, sws, hfb + e * 2048);
    BARRIER();
    const u32 qb = ((lane >> 2) & 3) * 64 + ((((g & 1) << 1) | ((lane >> 1) & 1)) * 16) + (lane & 1) * 8;
    f32x4 a3[4];
#pragma unroll
    for (int ct = 0; ct < 4; ++ct) a3[ct] = zero4;
    u32x4 wf[3][4];
#define RDW2(dst, lo0, hi0, offl, offh) _Pragma("unroll") for (int ct = 0; ct < 4; ++ct) dst[ct] = cat(ldstr(((lo0) ^ ((ct & 1) * 32)) + (offl) + (ct >> 1) * 512), ldstr(((hi0) ^ ((ct & 1) * 32)) + (offh) + (ct >> 1) * 512))
#define MM3(src, frag) _Pragma("unroll") for (int ct = 0; ct < 4; ++ct) a3[ct] = mfma32(src[ct], frag, a3[ct])
    const rsrc_t sxo = __builtin_amdgcn_make_buffer_rsrc((void*)(x + (size_t)p * 65536), 0, 65536, 0x00020000);
    const u32 w0 = opq(L_WOUT + qb + (g >> 1) * 1024 + (g & 1) * 256);
    const u32 wa = w0 + cw * 4096, wb = w0 + cw * 2048;
    const u32 oq = qb + g * 1024 + cw * 4096;
    const u32 oa = opq(L_WO1 + oq), ob = opq(bufA + oq);
    u32x4 xres;
    VWO();
#pragma unroll
    for (int e = 0; e < 4; ++e) DMA_WO(bufA, 1, e);
#define RDK(dst, k)                                                                                                 \
  if ((k) < 4) { RDW2(dst, oa, oa, (k) * 8192, (k) * 8192 + 256); }                                                 \
  else if ((k) < 8) { RDW2(dst, wa, wa, ((k) - 4) * 8192, ((k) - 4) * 8192 + 2048); }                               \
  else if ((k) < 12) { RDW2(dst, ob, ob, ((k) - 8) * 8192, ((k) - 8) * 8192 + 256); }                               \
  else { RDW2(dst, wb, w0, 32768 + ((k) - 12) * 4096, 49152 + ((k) - 12) * 2048); }
    RDK(wf[0], 0)
    RDK(wf[1], 1)
#define VALID4(e0)                                                                                                  \
  {                                                                                                                 \
    _Pragma("unroll") for (int e = e0; e < e0 + 4; ++e) PIN(hf[e]);                                                \
    _Pragma("nounroll") for (int it = 0; it < SPIN_CAP; ++it) {                                                    \
      u32 mx_ = 0;                                                                                                  \
      _Pragma("unroll") for (int e = e0; e < e0 + 4; ++e) mx_ = max(max(max(mx_, hf[e][0]), hf[e][1]), max(hf[e][2], hf[e][3])); \
      if (__all(mx_ != ~0u)) break;                                                                                 \
      __builtin_amdgcn_s_sleep(2);                                                                                  \
      asm volatile("buffer_load_dwordx4 %0, %4, %5, %6 offen sc1\n\tbuffer_load_dwordx4 %1, %4, %5, %6 offen offset:2048 sc1\n\t" \
                   "buffer_load_dwordx4 %2, %4, %5, %7 offen sc1\n\tbuffer_load_dwordx4 %3, %4, %5, %7 offen offset:2048 sc1\n\t" \
                   "s_waitcnt vmcnt(0)"                                                                             \
                   : "=&v"(hf[e0]), "=&v"(hf[e0 + 1]), "=&v"(hf[e0 + 2]), "=&v"(hf[e0 + 3])                         \
                   : "v"(l16), "s"(sws), "s"(hfb + (e0) * 2048), "s"(hfb + (e0) * 2048 + 4096)                       \
                   : "memory");                                                                                     \
    }                                                                                                               \
  }
#pragma unroll
    for (int k = 0; k < 16; ++k) {
      if (k == 0) WAITV(8);
      if (k == 4) { WAITV(12); VALID4(0) }
      if (k == 6) {
        WAITV(4);
        BARRIER();
      }
      if (k == 12) { WAITV(5); VALID4(4) }
      if (k + 2 < 16) { RDK(wf[(k + 2) % 3], k + 2) }
      SCHED();
      if (k < 4) { PIN(of[k]); MM3(wf[k % 3], of[k]); }
      else if (k < 8) MM3(wf[k % 3], hf[k - 4]);
      else if (k < 12) { PIN(of[k - 4]); MM3(wf[k % 3], of[k - 4]); }
      else MM3(wf[k % 3], hf[k - 8]);
      SCHED();
      if (k < 4) {
        of[4 + k] = ld16_b(l16, sws, ofb + (4 + k) * 2048);
        DMA_WOUT_AT(bufB + w * 4096, 7 + k)
      } else if (k < 6) {
        hf[2 * k - 4] = ld16_b(l16, sws, hfb + (2 * k - 4) * 2048);
        hf[2 * k - 3] = ld16_b(l16, sws, hfb + (2 * k - 3) * 2048);
      } else if (k == 6) {
        xres = ld16((tid >> 3) * 1024 + (tid & 7) * 16, sxo, c2 * 128);
      } else if (k == 10 || k == 11) {
        hf[8 + 2 * (k - 10)] = ld16_b(l16, sws, hfb + (8 + 2 * (k - 10)) * 2048);
        hf[9 + 2 * (k - 10)] = ld16_b(l16, sws, hfb + (9 + 2 * (k - 10)) * 2048);
      }
    }
    {
      const u32 wl_ = opq(bufB + (w0 - L_WOUT) + cw * 4096);
      u32x4 wg[4][4];
#pragma unroll
      for (int e = 0; e < 4; ++e) RDW2(wg[e], wl_, wl_, e * 8192, e * 8192 + 2048);
      WAITV(0);
      PIN(xres);
      VALID4(8)
#pragma unroll
      for (int e = 0; e < 4; ++e) MM3(wg[e], hf[8 + e]);
    }
    const u32 fin = L_WO1 - 2048;
#pragma unroll
    for (int ct = 0; ct < 4; ++ct) sts16f(fin + 2048 + cw * 16384 + (16 * rw + i16) * 256 + (((4 * ct + g) ^ i16) * 16), a3[ct]);
    BARRIER();
    {
      const int row = tid >> 3;
      const u32 a = fin + 2048 + row * 256 + (((2 * (tid & 7)) ^ (row & 15)) * 16);
      const f32x4 s0 = lds16f(a) + lds16f(a + 16384), s1 = lds16f(a ^ 16) + lds16f((a ^ 16) + 16384);
      u32x4 o = {pk2(blo(xres[0]) + s0[0], bhi(xres[0]) + s0[1]), pk2(blo(xres[1]) + s0[2], bhi(xres[1]) + s0[3]),
                 pk2(blo(xres[2]) + s1[0], bhi(xres[2]) + s1[1]), pk2(blo(xres[3]) + s1[2], bhi(xres[3]) + s1[3])};
      st16_sc1(o, row * 1024 + (tid & 7) * 16, __builtin_amdgcn_make_buffer_rsrc((void*)(out + (size_t)p * 65536), 0, 65536, 0x00020000), c2 * 128);
    }
  }
  }
  if (b == 0 && w == 0 && l16 == 0) *(u64*)(ws + WS_EP) = ((u64)thi << 32) | tlo;
}

__global__ __launch_bounds__(512, 2) void fused_kernel(const char* __restrict__ x, const char* __restrict__ wqkv, const char* __restrict__ wphi,
                                                        const char* __restrict__ wo, const char* __restrict__ win, const char* __restrict__ wsp,
                                                        const char* __restrict__ bsp, const char* __restrict__ wout, char* __restrict__ out, char* ws) {
  __shared__ __attribute__((aligned(1024))) char smem[163840];
  asm volatile("" ::"v"((u32)(size_t)(LDSP char*)smem));
  asm volatile("" ::"s"(x), "s"(wqkv), "s"(win), "s"(ws), "s"(wsp));
  u64 pc, ep;
  asm volatile("s_getpc_b64 %0" : "=s"(pc));
  {
    const int lane = threadIdx.x & 63, g = lane >> 4;
    const int w = __builtin_amdgcn_readfirstlane(threadIdx.x >> 6);
    const int r = blockIdx.x >> 3, P = ((blockIdx.x & 7) >> 1) * 4 + (r >> 3);
    const int rot = 2 * (((r >> 3) + (r & 3)) & 3);
    if (w == 7)
      touch(L_JUNK, lane * 128, __builtin_amdgcn_make_buffer_rsrc((void*)(pc & ~255ull), 0, CODE_PF, 0x00020000), (r & 7) * 8192);
    const rsrc_t sx = __builtin_amdgcn_make_buffer_rsrc((void*)(x + (size_t)P * 131072), 0, 131072, 0x00020000);
    const u32 vx0 = (16 * w + (lane >> 3)) * 1024 + (((lane & 7) ^ g) * 16);
    const u32 vx1 = (vx0 + 8192) ^ 64;
    const u32 w2k = w * 2048;
    DMA_X(0, 0, 0);
    DMA_X(0, 0, 1);
    asm volatile("s_load_dwordx2 %0, %1, 0x0" : "=s"(ep) : "s"(ws) : "memory");
    SCHED();
  }
  if (__builtin_expect(((blockIdx.x >> 5) & 1) == 0, 1)) run<0>(x, wqkv, wphi, wo, win, wsp, bsp, wout, out, ws, ep);
  else run<1>(x, wqkv, wphi, wo, win, wsp, bsp, wout, out, ws, ep);
}

extern "C" void kernel_launch(void* const* d_in, const int* in_sizes, int n_in, void* d_out, int out_size, void* d_ws, size_t ws_size,
                              hipStream_t stream) {
  fused_kernel<<<256, 512, 0, stream>>>((const char*)d_in[0], (const char*)d_in[1], (const char*)d_in[2], (const char*)d_in[3],
                                        (const char*)d_in[5], (const char*)d_in[6], (const char*)d_in[7], (const char*)d_in[8], (char*)d_out,
                                        (char*)d_ws);
}
```

```cpp
#include <hip/hip_runtime.h>
#include <hip/hip_bf16.h>

typedef unsigned int u32;
typedef unsigned long long u64;
typedef short s16x4 __attribute__((ext_vector_type(4)));
typedef __bf16 bf16x2 __attribute__((ext_vector_type(2)));
typedef __bf16 bf16x8 __attribute__((ext_vector_type(8)));
typedef float f32x16 __attribute__((ext_vector_type(16)));
typedef float f32x4 __attribute__((ext_vector_type(4)));
typedef float f32x2 __attribute__((ext_vector_type(2)));
typedef u32 u32x4 __attribute__((ext_vector_type(4)));
typedef u32 u32x2 __attribute__((ext_vector_type(2)));
typedef __amdgpu_buffer_rsrc_t rsrc_t;
#define LDSP __attribute__((address_space(3)))
#define DI __device__ __forceinline__

constexpr u32 WS_EP = 0;
constexpr u32 WS_FA = 8192;
constexpr u32 WS_FB = WS_FA + 16384;
constexpr u32 WS_FC = WS_FB + 16384;
constexpr u32 WS_XG = WS_FC + 81920;
constexpr u32 WS_SG = WS_XG + 32768;
constexpr u32 WS_ZB = WS_SG + 524288;
constexpr u32 WS_Z1 = WS_ZB + 43 * 65536;
constexpr u32 KP_SZ = 17408;
constexpr u32 WS_KP = WS_Z1 + 64 * 43 * 1024;
constexpr u32 WS_KF = WS_KP + 128 * KP_SZ;
constexpr u32 WS_KZ = WS_KF + 65536;
constexpr u32 WS_OF = WS_KZ + 4096;
constexpr u32 WS_HF = WS_OF + 128 * 16384;
constexpr u32 WS_END = WS_HF + 128 * 24 * 1024;

constexpr u32 LZ_W = 65536;
constexpr u32 LZ_U = 139264;
constexpr u32 LQ_M = 122880;
constexpr u32 LQ_A = 81920;
constexpr u32 LQ_PHI = 49152;
constexpr u32 LQ_V = 32768;
constexpr u32 L_TAB = 155648;
constexpr u32 L_XS = L_TAB;
constexpr u32 L_CS = L_TAB + 1024;
constexpr u32 L_ZST = L_TAB + 3072;
constexpr u32 L_WPHI = L_TAB + 3072;
constexpr u32 L_KFS = L_TAB + 2048;
constexpr u32 L_JUNK = L_TAB + 7168;
constexpr u32 L_WOUT = 0;
constexpr u32 L_RED = 57344;
constexpr u32 L_WO1 = 122880;

constexpr u64 Z2S = 0x26211B16110B0500ull;
constexpr u64 Z1S = 0x26211C16110C0600ull;
DI int grp_start(u64 tab, int c) { return c >= 8 ? 43 : (int)((tab >> (8 * c)) & 0xFF); }

#define SCHED() __builtin_amdgcn_sched_barrier(0)
#define WAITV(n) do { asm volatile("s_waitcnt vmcnt(%0)" ::"n"(n) : "memory"); SCHED(); } while (0)
#define BARRIER() do { SCHED(); asm volatile("s_waitcnt lgkmcnt(0)\n\ts_barrier" ::: "memory"); SCHED(); } while (0)
#define WAITV_IF(g_, v, n) do { asm volatile("s_cmp_lg_u32 %0, %1\n\ts_cbranch_scc1 1f\n\ts_waitcnt vmcnt(%2)\n1:" ::"s"(g_), "n"(v), "n"(n) : "scc", "memory"); SCHED(); } while (0)
#define BAR_IF(g_, v, n) do { SCHED(); asm volatile("s_cmp_lg_u32 %0, %1\n\ts_cbranch_scc1 1f\n\ts_waitcnt vmcnt(%2) lgkmcnt(0)\n\ts_barrier\n1:" ::"s"(g_), "n"(v), "n"(n) : "scc", "memory"); SCHED(); } while (0)
#define WAITL0() asm volatile("s_waitcnt lgkmcnt(0)" ::: "memory")
#define PIN(x) asm volatile("" : "+v"(x))

DI void dma16(u32 lds, u32 voff, rsrc_t srd, u32 soff) {
  asm volatile("s_mov_b32 m0, %0\n\ts_nop 0\n\tbuffer_load_dwordx4 %1, %2, %3 offen lds" ::"s"(lds), "v"(voff), "s"(srd), "s"(soff) : "memory");
}
#define DMA_C(wb, CST, voff, srd, soff) asm volatile("s_add_u32 m0, %0, %1\n\ts_nop 0\n\tbuffer_load_dwordx4 %2, %3, %4 offen lds" ::"s"(wb), "n"(CST), "v"(voff), "s"(srd), "s"(soff) : "scc", "memory")
#define DMA_CC(wb, CST, voff, srd, OP, sb, SC) do { u32 so__; asm volatile("s_add_u32 m0, %1, %2\n\t" OP " %0, %5, %6\n\tbuffer_load_dwordx4 %3, %4, %0 offen lds" : "=&s"(so__) : "s"(wb), "n"(CST), "v"(voff), "s"(srd), "s"(sb), "n"(SC) : "scc", "memory"); } while (0)
DI void touch(u32 lds, u32 voff, rsrc_t srd, u32 soff) {
  asm volatile("s_mov_b32 m0, %0\n\ts_nop 0\n\tbuffer_load_dword %1, %2, %3 offen lds" ::"s"(lds), "v"(voff), "s"(srd), "s"(soff) : "memory");
}
DI u32x4 ld16_sc1(u32 voff, rsrc_t srd, u32 soff) {
  u32x4 r;
  asm volatile("buffer_load_dwordx4 %0, %1, %2, %3 offen sc1" : "=v"(r) : "v"(voff), "s"(srd), "s"(soff) : "memory");
  return r;
}
template <int OFF> DI u32x4 ld16_sc1o(u32 voff, rsrc_t srd, u32 soff) {
  u32x4 r;
  asm volatile("buffer_load_dwordx4 %0, %1, %2, %3 offen offset:%4 sc1" : "=v"(r) : "v"(voff), "s"(srd), "s"(soff), "n"(OFF) : "memory");
  return r;
}
DI u32x4 ld16_b(u32 voff, rsrc_t srd, u32 soff) {
  u32x4 r;
  asm volatile("buffer_load_dwordx4 %0, %1, %2, %3 offen sc1" : "=v"(r) : "v"(voff), "s"(srd), "s"(soff) : "memory");
  return r;
}
template <int OFF> DI u32x4 ld16_bo(u32 voff, rsrc_t srd, u32 soff) {
  u32x4 r;
  asm volatile("buffer_load_dwordx4 %0, %1, %2, %3 offen offset:%4 sc1" : "=v"(r) : "v"(voff), "s"(srd), "s"(soff), "n"(OFF) : "memory");
  return r;
}
DI void ld16_b_if6(u32x4& r, int gn, u32 voff, rsrc_t srd, u32 soff) {
  asm volatile("s_cmp_lg_u32 %4, 6\n\ts_cbranch_scc1 1f\n\tbuffer_load_dwordx4 %0, %1, %2, %3 offen sc1\n1:" : "+v"(r) : "v"(voff), "s"(srd), "s"(soff), "s"(gn) : "scc", "memory");
}
DI u32x4 ld16(u32 voff, rsrc_t srd, u32 soff) {
  u32x4 r;
  asm volatile("buffer_load_dwordx4 %0, %1, %2, %3 offen" : "=v"(r) : "v"(voff), "s"(srd), "s"(soff) : "memory");
  return r;
}
DI u32x2 ld8_sc1(u32 voff, rsrc_t srd, u32 soff) {
  u32x2 r;
  asm volatile("buffer_load_dwordx2 %0, %1, %2, %3 offen sc1" : "=v"(r) : "v"(voff), "s"(srd), "s"(soff) : "memory");
  return r;
}
DI u32 ld2(u32 voff, rsrc_t srd, u32 soff) {
  u32 r;
  asm volatile("buffer_load_ushort %0, %1, %2, %3 offen" : "=v"(r) : "v"(voff), "s"(srd), "s"(soff) : "memory");
  return r;
}
DI void st16_sc1(u32x4 v, u32 voff, rsrc_t srd, u32 soff) {
  asm volatile("buffer_store_dwordx4 %0, %1, %2, %3 offen sc1\n\ts_nop 1" ::"v"(v), "v"(voff), "s"(srd), "s"(soff) : "memory");
}
DI void st8_sc1(u32x2 v, u32 voff, rsrc_t srd, u32 soff) {
  asm volatile("buffer_store_dwordx2 %0, %1, %2, %3 offen sc1" ::"v"(v), "v"(voff), "s"(srd), "s"(soff) : "memory");
}

DI u32x4 lds16(u32 a) { return *(const LDSP u32x4*)(size_t)a; }
DI u32x2 lds8(u32 a) { return *(const volatile LDSP u32x2*)(size_t)a; }
DI float lds4f(u32 a) { return *(const LDSP float*)(size_t)a; }
DI f32x4 lds16f(u32 a) { return *(const LDSP f32x4*)(size_t)a; }
DI void sts16f(u32 a, f32x4 v) { *(LDSP f32x4*)(size_t)a = v; }
DI void sts16(u32 a, u32x4 v) { *(LDSP u32x4*)(size_t)a = v; }
DI void sts8(u32 a, u32x2 v) { *(volatile LDSP u32x2*)(size_t)a = v; }
DI void sts4f(u32 a, float v) { *(LDSP float*)(size_t)a = v; }
DI u32x2 ldstr(u32 a) {
  return __builtin_bit_cast(u32x2, __builtin_amdgcn_ds_read_tr16_b64_v4i16((LDSP s16x4*)(size_t)a)); }
DI u32x4 cat(u32x2 lo, u32x2 hi) { return (u32x4){lo[0], lo[1], hi[0], hi[1]}; }
DI u32x4 ldfrag(u32 a, u32 d) { u32x2 lo = ldstr(a), hi = ldstr(a + d); return cat(lo, hi); }

DI f32x4 mfma32(u32x4 a, u32x4 b, f32x4 c) {
  return __builtin_amdgcn_mfma_f32_16x16x32_bf16(__builtin_bit_cast(bf16x8, a), __builtin_bit_cast(bf16x8, b), c, 0, 0, 0);
}
DI f32x16 mfmaL(u32x4 a, u32x4 b, f32x16 c) {
  return __builtin_amdgcn_mfma_f32_32x32x16_bf16(__builtin_bit_cast(bf16x8, a), __builtin_bit_cast(bf16x8, b), c, 0, 0, 0);
}
DI u32 pk2(float a, float b) { bf16x2 v = {(__bf16)a, (__bf16)b}; return __builtin_bit_cast(u32, v); }
DI float blo(u32 u) { return __uint_as_float(u << 16); }
DI float bhi(u32 u) { return __uint_as_float(u & 0xffff0000u); }
DI float rbf(float a) { return blo(pk2(a, a)); }
DI float dot2(u32 a, u32 b, float c) {
  return __builtin_amdgcn_fdot2_f32_bf16(__builtin_bit_cast(bf16x2, a), __builtin_bit_cast(bf16x2, b), c, false);
}
DI f32x2 fma2(f32x2 a, f32x2 b, f32x2 c) { return __builtin_elementwise_fma(a, b, c); }
DI f32x2 bc2(float a) { return (f32x2){a, a}; }
DI f32x2 lo2(f32x4 v) { return (f32x2){v[0], v[1]}; }
DI f32x2 hi2(f32x4 v) { return (f32x2){v[2], v[3]}; }
DI u32 pk2v(f32x2 v) { return pk2(v[0], v[1]); }
DI f32x2 gelu2(f32x2 x) {
  f32x2 u = fma2(x * x, bc2(-0.10294324f), bc2(-2.3022082f)) * x;
  f32x2 e = {__builtin_amdgcn_exp2f(u[0]), __builtin_amdgcn_exp2f(u[1])};
  e = e + bc2(1.0f);
  f32x2 r = {__builtin_amdgcn_rcpf(e[0]), __builtin_amdgcn_rcpf(e[1])};
  return x * r;
}
DI void red2g(float& a, float& b) {
  auto s = __builtin_amdgcn_permlane32_swap(__float_as_uint(a), __float_as_uint(b), false, false);
  float c = __uint_as_float(s[0]) + __uint_as_float(s[1]);
  auto s2 = __builtin_amdgcn_permlane16_swap(__float_as_uint(c), __float_as_uint(c), false, false);
  float d = __uint_as_float(s2[0]) + __uint_as_float(s2[1]);
  auto s3 = __builtin_amdgcn_permlane32_swap(__float_as_uint(d), __float_as_uint(d), false, false);
  a = __uint_as_float(s3[0]);
  b = __uint_as_float(s3[1]);
}
template <int CTRL> DI float dpp(float v) {
  return __uint_as_float(__builtin_amdgcn_update_dpp(0, __float_as_uint(v), CTRL, 0xF, 0xF, true));
}
template <class F> DI void hedgehog16(const f32x16& p, float s, u32x4 (&o)[4], F hook) {
  float mx = fmaxf(p[0], p[1]), mn = fminf(p[0], p[1]);
#pragma unroll
  for (int i = 2; i < 16; i += 2) { mx = fmaxf(fmaxf(mx, p[i]), p[i + 1]); mn = fminf(fminf(mn, p[i]), p[i + 1]); }
  const float c = s * 1.44269504f;
  const float a = -mx * c, b = mn * c;
  f32x2 ep[8], en[8];
#pragma unroll
  for (int i = 0; i < 8; ++i) {
    hook(i);
    const f32x2 v = {p[2 * i], p[2 * i + 1]};
    const f32x2 u = fma2(v, bc2(c), bc2(a)), w = fma2(v, bc2(-c), bc2(b));
    ep[i] = (f32x2){__builtin_amdgcn_exp2f(u[0]), __builtin_amdgcn_exp2f(u[1])};
    en[i] = (f32x2){__builtin_amdgcn_exp2f(w[0]), __builtin_amdgcn_exp2f(w[1])};
  }
  hook(8);
  const f32x2 sp = ((ep[0] + ep[1]) + (ep[2] + ep[3])) + ((ep[4] + ep[5]) + (ep[6] + ep[7]));
  const f32x2 sn = ((en[0] + en[1]) + (en[2] + en[3])) + ((en[4] + en[5]) + (en[6] + en[7]));
  const float ip = __builtin_amdgcn_rcpf(sp[0] + sp[1]), in = __builtin_amdgcn_rcpf(sn[0] + sn[1]);
#pragma unroll
  for (int i = 0; i < 8; ++i) {
    if (i == 0 || i == 3 || i == 6) hook(9 + i / 3);
    o[i >> 2][i & 3] = pk2v(ep[i] * bc2(ip));
    o[2 + (i >> 2)][i & 3] = pk2v(en[i] * bc2(in));
  }
}
DI void ln_fin(float s, float q, float invn, float& r, float& m) {
  m = s * invn;
  float v = fmaxf(q * invn - m * m, 0.0f);
  float sd = rbf(__builtin_amdgcn_sqrtf(rbf(rbf(v) + 1e-5f)));
  r = __builtin_amdgcn_rcpf(sd);
  m = rbf(m);
}

constexpr int SPIN_CAP = 1 << 17;
DI void poll_flags(u32 voff, bool act, rsrc_t srd, u32 soff, u32 tlo, u32 thi) {
#pragma nounroll
  for (int it = 0; it < SPIN_CAP; ++it) {
    u32x2 v;
    asm volatile("buffer_load_dwordx2 %0, %1, %2, %3 offen sc1\n\ts_waitcnt vmcnt(0)" : "=&v"(v) : "v"(voff), "s"(srd), "s"(soff) : "memory");
    bool ok = !act || (v[0] == tlo && v[1] == thi);
    if (__all(ok)) break;
    __builtin_amdgcn_s_sleep(6);
  }
}
DI bool tag_ok(u32x4 q, u32 t) { return q[1] == t && q[3] == t; }
DI void poll_gran(u32x4& q, u32 voff, rsrc_t srd, u32 soff, u32 tlo) {
#pragma nounroll
  for (int it = 0; it < SPIN_CAP; ++it) {
    if (__all(tag_ok(q, tlo))) break;
    __builtin_amdgcn_s_sleep(6);
    asm volatile("buffer_load_dwordx4 %0, %1, %2, %3 offen sc1\n\ts_waitcnt vmcnt(0)" : "=&v"(q) : "v"(voff), "s"(srd), "s"(soff) : "memory");
  }
}
DI u32 opq(u32 v) { asm volatile("" : "+v"(v)); return v; }

static_assert(WS_ZB % 8192 == 0, "fragment addresses are formed by XOR");
constexpr u32 CODE_PF = 45056;

#define VM(x) do { x; ++vq; } while (0)
#define WAITQ(mark) WAITV(vq - (mark))

template <int t>
DI void run(const char* __restrict__ x, const char* __restrict__ wqkv, const char* __restrict__ wphi, const char* __restrict__ wo,
            const char* __restrict__ win, const char* __restrict__ wsp, const char* __restrict__ bsp, const char* __restrict__ wout,
            char* __restrict__ out, char* ws, u64 ep) {
  const int tid = threadIdx.x, lane = tid & 63;
  const int w = __builtin_amdgcn_readfirstlane(tid >> 6);
  const int g = lane >> 4;
  const int b = blockIdx.x, xcd = b & 7, r = b >> 3;
  const int Pl = r >> 3, cl = r & 3;
  const int P = (xcd >> 1) * 4 + Pl;
  const int rot = 2 * ((Pl + cl) & 3);
  const rsrc_t sx = __builtin_amdgcn_make_buffer_rsrc((void*)(x + (size_t)P * 131072), 0, 131072, 0x00020000);
#define XROW(t_) ((t_) ? 16 * w + (lane >> 3) : 32 * (w >> 1) + 4 * (w & 1) + 8 * (lane >> 5) + ((lane >> 3) & 3))
  const u32 vx0 = XROW(t) * 1024 + (((lane & 7) ^ g) * 16);
  const u32 vx1 = (vx0 + (t ? 8192 : 16384)) ^ 64;
  const u32 w2k = w * 2048;
#define DMA_X(dst, n, e) DMA_C(w2k, (dst) + (e) * 1024, (e) ? vx1 : vx0, sx, (((n) + rot) & 7) * 128)
  const int rw = w & 3, cw = w >> 2, i16 = lane & 15;
  const int c = (xcd & 1) * 4 + cl;
  const int p = 2 * P + (cl & 1);
  const int c2 = (xcd & 1) * 4 + (t ? 3 * (cl >> 1) : 1 + (cl >> 1));
  const u32 l16 = opq(lane * 16);
  const rsrc_t sa = __builtin_amdgcn_make_buffer_rsrc((void*)(wsp + (size_t)p * 262144), 0, 262144, 0x00020000);
  const rsrc_t sws = __builtin_amdgcn_make_buffer_rsrc((void*)ws, 0, WS_END, 0x00020000);
  const u32 vA0 = (lane >> 3) * 4096 + (((lane & 7) ^ g) * 16);
  const u32 vA1 = (vA0 + 32768) ^ 64;
  const int ux0 = cl >> 1, ux1 = t;
  const u32 uxs = w * 512 + ux1 * 256 + ux0 * 131072;
#define DMA_U(u, base, e) DMA_CC(w2k, (base) + (e) * 1024, (e) ? vA1 : vA0, sa, "s_xor_b32", uxs, (((u) >> 1) & 3) * 128 + (((u) >> 3) * 32 + ((u) & 1) * 16) * 4096)
  const f32x4 zero4 = {0, 0, 0, 0};
  u32x4 ones4 = {0x3F803F80u, 0x3F803F80u, 0x3F803F80u, 0x3F803F80u};
  u32 tlo, thi;
  u32x4 af[4][8];
  u32x4 bfr[2][8];
  u32x2 gf;
  const u32 ard = w * 2048 + i16 * 128 + ((g ^ (i16 >> 1)) * 16);
#define READ_U(u, base)                                                                                             \
  {                                                                                                                 \
    const u32 ab_ = opq(ard + (base));                                                                              \
    _Pragma("unroll") for (int kk = 0; kk < 2; ++kk)                                                               \
      af[2 * ((u) >> 3) + ((u) & 1)][((u) & 6) + kk] = lds16(ab_ ^ (64 * kk));                                     \
  }
#define USE_EPOCH() { asm volatile("" : "+s"(ep)); ep += 1; tlo = (u32)ep; thi = (u32)(ep >> 32); PIN(ones4); }
#define EARLY_STATS(mark)                                                                                           \
  {                                                                                                                 \
    WAITQ(mark); PIN(xrow);                                                                                         \
    float s_ = 0, q_ = 0;                                                                                           \
    _Pragma("unroll") for (int e = 0; e < 4; ++e) { s_ = dot2(xrow[e], ones4[0], s_); q_ = dot2(xrow[e], xrow[e], q_); } \
    s_ += dpp<0x128>(s_); q_ += dpp<0x128>(q_);                                                                     \
    s_ += dpp<0x124>(s_); q_ += dpp<0x124>(q_);                                                                     \
    s_ += dpp<0x4E>(s_); q_ += dpp<0x4E>(q_);                                                                       \
    s_ += dpp<0xB1>(s_); q_ += dpp<0xB1>(q_);                                                                       \
    red2g(s_, q_);                                                                                                  \
    float r_, m_;                                                                                                   \
    ln_fin(s_, q_, 1.0f / 512, r_, m_);                                                                             \
    VM(st16_sc1((u32x4){__float_as_uint(r_), tlo, __float_as_uint(m_), tlo}, l16 << 22, sws, WS_XG + (P * 128 + (t * 8 + c) * 8 + w) * 16)); \
  }
#define GATHER_XS()                                                                                                 \
  WAITQ(mxg); PIN(xg);                                                                                              \
  poll_gran(xg, i16 * 16, sws, WS_XG + (P * 128 + 16 * w) * 16, tlo);                                               \
  sts4f(L_XS + (16 * w + i16) * 4, __uint_as_float(xg[0]));                                                         \
  sts4f(L_XS + 512 + (16 * w + i16) * 4, __uint_as_float(xg[2]));
#define RELANE() const int lane = opq(l16) >> 4, i16 = lane & 15, g = lane >> 4, tid = w * 64 + lane; (void)tid; (void)g; (void)i16
#define CLEAR_H() if (cw == 0) { const u32x4 f_ = {~0u, ~0u, ~0u, ~0u}; const u32 o_ = WS_HF + ((4 * p + rw) * 24 + c2) * 1024; \
    st16_sc1(f_, l16, sws, o_); st16_sc1(f_, l16, sws, o_ + 8192); st16_sc1(f_, l16, sws, o_ + 16384); }
#define SET_FLAG(off) st8_sc1((u32x2){tlo, thi}, l16 << 22, sws, (off))
  const int gs = grp_start(Z2S, c2), gn = t ? 5 : grp_start(Z2S, c2 + 1) - gs;
  const int trot = (p & 7) >= 5 ? (p & 7) - 5 : (p & 7);
#define TILE(j, n_) (((j) + trot) >= (n_) ? (j) + trot - (n_) : (j) + trot)
  const u32 zb0 = WS_ZB + (gs * 64 + 8 * w) * 1024 + ux1 * 4096;
#define DMA_WOUT(e)                                                                                                 \
  {                                                                                                                 \
    const rsrc_t so_ = __builtin_amdgcn_make_buffer_rsrc((void*)wout, 0, 688 * 1024, 0x00020000);                   \
    const int ws_ = grp_start(Z2S, w), wn_ = grp_start(Z2S, w + 1) - ws_;                                           \
    if ((e) < 6) dma16(L_WOUT + ((e) < 4 ? 2 * w + ((e) >> 1) : 16 + w) * 2048 + ((e) & 1) * 1024, vwo, so_, ((ws_ + TILE((e) >> 1, wn_)) * 2 + ((e) & 1)) * 8192); \
    else if ((e) == 6) dma16(L_WOUT + 24 * 2048 + w * 1024, vwo, so_, ((((w >> 1) == 0 ? 5 : ((w >> 1) == 1 ? 11 : ((w >> 1) == 2 ? 27 : 64))) + TILE(3, 6)) * 2 + (w & 1)) * 8192); \
  }
#define DMA_WOUT_AT(dst, e)                                                                                         \
  {                                                                                                                 \
    const rsrc_t so_ = __builtin_amdgcn_make_buffer_rsrc((void*)wout, 0, 688 * 1024, 0x00020000);                   \
    const int ws_ = grp_start(Z2S, w), wn_ = grp_start(Z2S, w + 1) - ws_;                                           \
    dma16((dst) + ((e) - 7) * 1024, vwo, so_, ((ws_ + TILE(wn_ - 2 + (((e) - 7) >> 1), wn_)) * 2 + (((e) - 7) & 1)) * 8192); \
  }
#define VWO() const u32 vwo = ((lane >> 2) & 7) * 1024 + c2 * 128 + (lane >> 5) * 64 + (((lane & 3) ^ (((lane >> 4) & 1) << 1)) * 16)
#define LOAD_B(u, so_)                                                                                              \
  bfr[u][0] = ld16_bo<0>(l16, sws, so_); bfr[u][1] = ld16_bo<1024>(l16, sws, so_);                                  \
  bfr[u][2] = ld16_bo<2048>(l16, sws, so_); bfr[u][3] = ld16_bo<3072>(l16, sws, so_);                               \
  bfr[u][4] = ld16_bo<0>(l16, sws, (so_) ^ 4096); bfr[u][5] = ld16_bo<1024>(l16, sws, (so_) ^ 4096);                \
  bfr[u][6] = ld16_bo<2048>(l16, sws, (so_) ^ 4096); bfr[u][7] = ld16_bo<3072>(l16, sws, (so_) ^ 4096);

  if constexpr (t == 0) {
    int vq = 2;
    const rsrc_t sw = __builtin_amdgcn_make_buffer_rsrc((void*)win, 0, 512 * 2752, 0x00020000);
    const int z2s = grp_start(Z2S, c), z2n = grp_start(Z2S, c + 1) - z2s;
    const int z1s = grp_start(Z1S, c), z1n = grp_start(Z1S, c + 1) - z1s;
    u32 vw[3];
    int wch[3];
#pragma unroll
    for (int e = 0; e < 3; ++e) {
      const int sg = 2 * e + (lane >> 5), hi = sg >= 3;
      wch[e] = (sg - 3 * hi) * 4 + ((lane & 3) ^ (hi << 1));
      vw[e] = wch[e] < 2 * z2n ? (16 * w + 8 * hi + ((lane >> 2) & 7)) * 2752 + 1376 + 32 * z2s + 16 * wch[e] : 0x7FFF0000u;
    }
    const u32 w3k = w * 3072;
#define DMA_W(bk, e) DMA_C(w3k, LZ_W + ((bk) % 3) * 24576 + (e) * 1024, vw[e], sw, ((2 * (bk) + rot) & 7) * (64 * 2752))
#define UBASE(u) ((u) % 5 == 0 ? LZ_U : ((u) % 5 - 1) * 16384)
#define ISS_X(n) do { VM(DMA_X(((n) & 3) * 16384, n, 0)); VM(DMA_X(((n) & 3) * 16384, n, 1)); mx[n] = vq; } while (0)
#define ISS_U(u) do { VM(DMA_U(u, UBASE(u), 0)); VM(DMA_U(u, UBASE(u), 1)); mu[u] = vq; } while (0)
#define ISS_W(bk, e0, e1) do { _Pragma("unroll") for (int e_ = e0; e_ < e1; ++e_) VM(DMA_W(bk, e_)); mw[bk] = vq; } while (0)
    int mx[8], mw[8], mu[16];
    mx[0] = vq;
    ISS_W(0, 0, 3);
    u32x4 xrow;
    VM(xrow = ld16(l16, sx, (c * 8 + w) * 1024));
    const int mxr = vq;
    ISS_X(1);
    const u32 xa0 = opq((32 * rw + i16) * 128 + ((g ^ (i16 >> 1)) * 16)), xa1 = xa0 ^ 64;
    u32 ta[3];
    int tl[3];
    {
      const u32 trb = LZ_W + g * 1536 + ((lane >> 2) & 3) * 64 + ((((g & 1) << 1) | ((lane >> 1) & 1)) * 16) + (lane & 1) * 8;
#pragma unroll
      for (int j = 0; j < 3; ++j) {
        tl[j] = 3 * cw + (j + rw) % 3;
        ta[j] = (trb + (tl[j] >> 1) * 512) ^ ((tl[j] & 1) * 32);
      }
    }
#define NEXT_BLOCK(bk) _Pragma("unroll") for (int j = 0; j < 3; ++j) ta[j] = opq(ta[j] + ((bk) == 0 ? 0 : ((bk) % 3 == 0 ? -49152 : 24576)))
    f32x4 acc[2][3], csa = zero4;
#pragma unroll
    for (int j = 0; j < 3; ++j) acc[0][j] = acc[1][j] = zero4;
    u32x4 xf[16][2], wf[2][3];
    u32x4 xg;
    int mxg = 0;
    WAITQ(mw[0]);
    BARRIER();
    USE_EPOCH()
#define P1_MEM(n, h)                                                                                                \
  {                                                                                                                 \
    if ((n) == 0) { if ((h) == 0) ISS_X(2); if ((h) == 1) ISS_W(1, 0, 2); if ((h) == 2) ISS_W(1, 2, 3); if ((h) == 3) ISS_X(3); } \
    if ((n) == 1) { if ((h) == 0) ISS_W(2, 0, 2); if ((h) == 1) ISS_W(2, 2, 3); if ((h) == 2) VM(DMA_X(0, 4, 0)); if ((h) == 3) { VM(DMA_X(0, 4, 1)); mx[4] = vq; } } \
    if ((n) == 2 || (n) == 4) { if ((h) < 2) { VM(DMA_X((((n) + 3) & 3) * 16384, (n) + 3, (h) & 1)); mx[(n) + 3] = vq; } else ISS_W((n) / 2 + 2, (h) - 2, (h) - 1); } \
    if ((n) == 3) { if ((h) == 0) ISS_W(3, 2, 3); else if ((h) < 3) { VM(DMA_X(32768, 6, (h) - 1)); mx[6] = vq; } else ISS_U(0); } \
    if ((n) == 5) { if ((h) == 0) ISS_W(4, 2, 3); if ((h) == 1) { VM(xg = ld16_sc1(i16 * 16, sws, WS_XG + (P * 128 + 16 * w) * 16)); mxg = vq; } \
                    if ((h) >= 2) { VM(DMA_U(1, UBASE(1), (h) - 2)); mu[1] = vq; } }                                 \
    if ((n) == 6) { if ((h) < 3) ISS_W(5, h, (h) + 1); else ISS_U(2); }                                             \
    if ((n) == 7) { if ((h) < 2) { VM(DMA_U(3, UBASE(3), h)); mu[3] = vq; } }                                       \
  }
#pragma unroll
    for (int ks = 0; ks <= 16; ++ks) {
      const int n = ks >> 1, kk = ks & 1;
      if (ks > 0 && ks < 16 && kk == 0) {
        { const int mk = (n & 1) ? mx[n] : (mx[n] > mw[n >> 1] ? mx[n] : mw[n >> 1]); WAITQ(mk); }
        BARRIER();
      }
      if (ks < 16 && (ks & 3) == 0) { NEXT_BLOCK(ks >> 2); }
      if (ks == 8) {
#pragma unroll
        for (int e = 0; e < 3; ++e)
          vw[e] = wch[e] < 2 * z1n ? (16 * w + 8 * (2 * e + (lane >> 5) >= 3) + ((lane >> 2) & 7)) * 2752 + 32 * z1s + 16 * wch[e] : 0x7FFF0000u;
      }
      const int k1 = ks > 0 ? ks - 1 : 0, q1 = k1 & 1;
#pragma unroll
      for (int m = 0; m < 7; ++m) {
        if (ks > 0) {
          if (m < 6) acc[m & 1][m >> 1] = mfma32(xf[k1][m & 1], wf[q1][m >> 1], acc[m & 1][m >> 1]);
          else csa = mfma32(ones4, wf[q1][0], csa);
          SCHED();
        }
        if (ks < 16) {
          if (m == 0) { P1_MEM(n, 2 * kk) xf[ks & 15][0] = lds16((kk ? xa1 : xa0) + (n & 3) * 16384); xf[ks & 15][1] = lds16((kk ? xa1 : xa0) + (n & 3) * 16384 + 2048); }
          else if (m < 4) wf[kk][m - 1] = ldfrag(ta[m - 1] + (ks & 3) * 6144, 256);
          if (m == 4) P1_MEM(n, 2 * kk + 1)
        }
        SCHED();
      }
#pragma unroll
      for (int j = 0; j < 3; ++j) { PIN(acc[0][j]); PIN(acc[1][j]); }
      PIN(csa);
      if (ks == 2) EARLY_STATS(mxr)
    }
    GATHER_XS()
    sts4f(L_CS + tl[0] * 64 + i16 * 4, csa[0]);
    WAITQ(mu[1] > mw[4] ? mu[1] : mw[4]);
    READ_U(0, UBASE(0)) READ_U(1, UBASE(1))
    BARRIER();
    u32x4 zp[3];
    ISS_W(6, 0, 3);
    {
      f32x2 ps[4], pq[4];
      float cs[3];
#pragma unroll
      for (int j = 0; j < 3; ++j) cs[j] = -lds4f(L_CS + tl[j] * 64 + i16 * 4);
#pragma unroll
      for (int rt = 0; rt < 2; ++rt) {
        const f32x4 rr = lds16f(L_XS + (32 * rw + 8 * g + 4 * rt) * 4), mm = lds16f(L_XS + 512 + (32 * rw + 8 * g + 4 * rt) * 4);
        f32x2 z0[3], z1[3];
#pragma unroll
        for (int j = 0; j < 3; ++j) {
          z0[j] = gelu2(fma2(lo2(mm), bc2(cs[j]), lo2(acc[rt][j])) * lo2(rr));
          z1[j] = gelu2(fma2(hi2(mm), bc2(cs[j]), hi2(acc[rt][j])) * hi2(rr));
          zp[j][2 * rt] = pk2v(z0[j]); zp[j][2 * rt + 1] = pk2v(z1[j]);
        }
        ps[2 * rt] = z0[0] + z0[1] + z0[2]; ps[2 * rt + 1] = z1[0] + z1[1] + z1[2];
        pq[2 * rt] = fma2(z0[2], z0[2], fma2(z0[1], z0[1], z0[0] * z0[0]));
        pq[2 * rt + 1] = fma2(z1[2], z1[2], fma2(z1[1], z1[1], z1[0] * z1[0]));
        SCHED();
        if (rt == 0) { ISS_U(4); ISS_U(5); }
        else ISS_U(6);
        SCHED();
      }
      const bool b3 = lane & 8, b2 = lane & 4, b1 = lane & 2;
#pragma unroll
      for (int m = 0; m < 2; ++m)
#pragma unroll
        for (int e = 0; e < 2; ++e) {
          ps[m][e] = (b3 ? ps[m + 2][e] : ps[m][e]) + dpp<0x128>(b3 ? ps[m][e] : ps[m + 2][e]);
          pq[m][e] = (b3 ? pq[m + 2][e] : pq[m][e]) + dpp<0x128>(b3 ? pq[m][e] : pq[m + 2][e]);
        }
#pragma unroll
      for (int e = 0; e < 2; ++e) {
        ps[0][e] = (b2 ? ps[1][e] : ps[0][e]) + dpp<0x141>(b2 ? ps[0][e] : ps[1][e]);
        pq[0][e] = (b2 ? pq[1][e] : pq[0][e]) + dpp<0x141>(b2 ? pq[0][e] : pq[1][e]);
      }
      float s_ = (b1 ? ps[0][1] : ps[0][0]) + dpp<0x4E>(b1 ? ps[0][0] : ps[0][1]);
      float q_ = (b1 ? pq[0][1] : pq[0][0]) + dpp<0x4E>(b1 ? pq[0][0] : pq[0][1]);
      s_ += dpp<0xB1>(s_);
      q_ += dpp<0xB1>(q_);
      const int row = 32 * rw + 8 * g + (i16 >> 1);
      VM(st16_sc1((u32x4){__float_as_uint(s_), tlo, __float_as_uint(q_), tlo}, row * 16 + ((lane & 1) << 30), sws, WS_SG + ((P * 8 + c) * 2 + cw) * 2048));
      PIN(zp[0]); PIN(zp[1]); PIN(zp[2]);
      CLEAR_H()
    }
    {
    RELANE();
#pragma unroll
    for (int j = 0; j < 3; ++j) acc[0][j] = acc[1][j] = zero4;
    csa = zero4;
    u32x4 q0, q1, q2, q3;
    int mq = 0, mz = 0;
    const u32 gvo = (2 * g) * 4096 + (32 * rw + 16 * cw + i16) * 16;
    const u32 gso = WS_SG + P * 32768;
#pragma unroll
    for (int ks = 0; ks <= 16; ++ks) {
      if (ks == 8) {
        WAITQ(mq); PIN(q0); PIN(q1); PIN(q2); PIN(q3);
#pragma nounroll
        for (int it = 0; it < SPIN_CAP; ++it) {
          if (__all(tag_ok(q0, tlo) && tag_ok(q1, tlo) && tag_ok(q2, tlo) && tag_ok(q3, tlo))) break;
          __builtin_amdgcn_s_sleep(6);
          asm volatile(
              "buffer_load_dwordx4 %0, %4, %5, %6 offen sc1\n\t"
              "buffer_load_dwordx4 %1, %4, %5, %6 offen offset:2048 sc1\n\t"
              "buffer_load_dwordx4 %2, %4, %5, %7 offen sc1\n\t"
              "buffer_load_dwordx4 %3, %4, %5, %7 offen offset:2048 sc1\n\t"
              "s_waitcnt vmcnt(0)"
              : "=&v"(q0), "=&v"(q1), "=&v"(q2), "=&v"(q3)
              : "v"(gvo), "s"(sws), "s"(gso), "s"(gso + 4096)
              : "memory");
        }
        float s = (__uint_as_float(q0[0]) + __uint_as_float(q1[0])) + (__uint_as_float(q2[0]) + __uint_as_float(q3[0]));
        float q = (__uint_as_float(q0[2]) + __uint_as_float(q1[2])) + (__uint_as_float(q2[2]) + __uint_as_float(q3[2]));
        red2g(s, q);
        float r2, m2;
        ln_fin(s, q, 1.0f / 688, r2, m2);
        sts4f(L_ZST + (32 * rw + 16 * cw + i16) * 4, r2);
        sts4f(L_ZST + 512 + (32 * rw + 16 * cw + i16) * 4, -m2 * r2);
      }
      if (ks < 16 && (ks & 3) == 0) {
        if (ks > 0) { WAITQ(ks == 12 ? mz : mw[4 + (ks >> 2)]); BARRIER(); }
        if (ks == 12) { if (w == 0) SET_FLAG(WS_FA + (P * 8 + c) * 128); }
        NEXT_BLOCK(4 + (ks >> 2));
      }
      if (ks == 8) {
#pragma unroll
        for (int rt = 0; rt < 2; ++rt) {
          const u32 ra = L_ZST + (32 * rw + 8 * g + 4 * rt) * 4;
          const f32x4 r4 = lds16f(ra), m4 = lds16f(ra + 512);
#pragma unroll
          for (int j = 0; j < 3; ++j) {
            const u32 u0 = zp[j][2 * rt], u1 = zp[j][2 * rt + 1];
            zp[j][2 * rt] = pk2v(fma2((f32x2){blo(u0), bhi(u0)}, lo2(r4), lo2(m4)));
            zp[j][2 * rt + 1] = pk2v(fma2((f32x2){blo(u1), bhi(u1)}, hi2(r4), hi2(m4)));
          }
        }
#pragma unroll
        for (int j = 0; j < 3; ++j)
          VM(st16_sc1(zp[j], l16, sws, (tl[j] < z2n) ? WS_ZB + ((z2s + tl[j]) * 64 + 4 * P + rw) * 1024 : 0x7FFF0000u));
        mz = vq;
        SCHED();
      }
      if (ks >= 5 && ks <= 9) { WAITQ(mu[ks - 3]); READ_U(ks - 3, UBASE(ks - 3)) }
      if (ks >= 9 && ks <= 13) { WAITQ(mu[ks - 2]); READ_U(ks - 2, UBASE(ks - 2)) }
      if (ks >= 13) { WAITQ(mu[ks - 1]); READ_U(ks - 1, UBASE(ks - 1)) }
      const int k1 = ks > 0 ? ks - 1 : 0, q1_ = k1 & 1;
#pragma unroll
      for (int m = 0; m < 7; ++m) {
        if (ks > 0) {
          if (m < 6) acc[m & 1][m >> 1] = mfma32(wf[q1_][m >> 1], xf[k1][m & 1], acc[m & 1][m >> 1]);
          else csa = mfma32(ones4, wf[q1_][0], csa);
          SCHED();
        }
        if (ks < 16 && m >= 1 && m < 4) wf[ks & 1][m - 1] = ldfrag(ta[m - 1] + (ks & 3) * 6144, 256);
        if (m == 0 || m == 4) {
          const int h = m == 4;
          if (h == 0 && ks >= 6 && ks <= 13) asm volatile("s_waitcnt lgkmcnt(8)" ::: "memory");
          if (ks >= 6 && ks <= 10) { VM(DMA_U(ks + 1, UBASE(ks + 1), h)); mu[ks + 1] = vq; }
          if (ks >= 10 && ks <= 13) { VM(DMA_U(ks + 2, UBASE(ks + 2), h)); mu[ks + 2] = vq; }
        }
        if (m == 6) {
          if (ks >= 4 && ks <= 6) ISS_W(7, ks - 4, ks - 3);
        }
        if (m == 2) {
          if (ks == 5) { VM(q0 = ld16_sc1o<0>(gvo, sws, gso)); VM(q1 = ld16_sc1o<2048>(gvo, sws, gso)); }
          if (ks == 6) { VM(q2 = ld16_sc1o<0>(gvo, sws, gso + 4096)); VM(q3 = ld16_sc1o<2048>(gvo, sws, gso + 4096)); mq = vq; }
        }
        SCHED();
      }
#pragma unroll
      for (int j = 0; j < 3; ++j) { PIN(acc[0][j]); PIN(acc[1][j]); }
      PIN(csa);
    }
    sts4f(L_CS + 512 + tl[0] * 64 + i16 * 4, csa[0]);
    gf = ld8_sc1((lane >> 5) * 1024, sws, WS_FA + (16 * w + c2) * 128);
    BARRIER();
    VWO();
    DMA_WOUT(0) DMA_WOUT(1)
    WAITV(2);
    PIN(gf);
    if (!__all(gf[0] == tlo && gf[1] == thi)) poll_flags((lane >> 5) * 1024, true, sws, WS_FA + (16 * w + c2) * 128, tlo, thi);
    {
      float xr[2], xm[2];
#pragma unroll
      for (int rt = 0; rt < 2; ++rt) {
        xr[rt] = lds4f(L_XS + (32 * rw + 8 * (i16 >> 2) + 4 * rt + (i16 & 3)) * 4);
        xm[rt] = -lds4f(L_XS + 512 + (32 * rw + 8 * (i16 >> 2) + 4 * rt + (i16 & 3)) * 4);
      }
      const u32 so0 = zb0 + TILE(0, gn) * 65536;
#pragma unroll
      for (int j = 0; j < 3; ++j) {
        const f32x4 cc = lds16f(L_CS + 512 + tl[j] * 64 + g * 16);
        bfr[0][3 * j] = ld16_b(l16, sws, so0 ^ (3 * j * 1024));
        bfr[0][3 * j + 1] = ld16_b(l16, sws, so0 ^ ((3 * j + 1) * 1024));
        if (j < 2) bfr[0][3 * j + 2] = ld16_b(l16, sws, so0 ^ ((3 * j + 2) * 1024));
        u32x4 zz;
#pragma unroll
        for (int rt = 0; rt < 2; ++rt) {
          zz[2 * rt] = pk2v(gelu2(fma2(lo2(cc), bc2(xm[rt]), lo2(acc[rt][j])) * bc2(xr[rt])));
          zz[2 * rt + 1] = pk2v(gelu2(fma2(hi2(cc), bc2(xm[rt]), hi2(acc[rt][j])) * bc2(xr[rt])));
        }
        st16_sc1(zz, l16, sws, (tl[j] < z1n) ? WS_Z1 + ((4 * P + rw) * 43 + z1s + tl[j]) * 1024 : 0x7FFF0000u);
        SCHED();
      }
    }
    }
  } else {
    int vq = 2;
    const rsrc_t sw = __builtin_amdgcn_make_buffer_rsrc((void*)wqkv, 0, 512 * 3072, 0x00020000);
    const u32 vw = (8 * w + ((lane >> 2) & 7)) * 3072 + 128 * c + ((lane >> 5) * 4 + ((lane & 3) ^ ((w & 1) << 1))) * 16;
    const u32 w3k = w * 3072;
#define DMA_S(n, e)                                                                                                 \
  do {                                                                                                              \
    if ((e) < 2) DMA_X(((n) % 3) * 40960, n, (e) & 1);                                                              \
    else DMA_CC(w3k, ((n) % 3) * 40960 + 16384 + ((e) - 2) * 1024, vw, sw, "s_add_u32", (((n) + rot) & 7) * (64 * 3072), ((e) - 2) * 1024); \
  } while (0)
#define ISS_Q(u, base) do { VM(DMA_U(u, base, 0)); VM(DMA_U(u, base, 1)); mu[u] = vq; } while (0)
    int ms[8], mu[16];
    VM(DMA_S(0, 2)); VM(DMA_S(0, 3)); VM(DMA_S(0, 4)); ms[0] = vq;
    u32x4 xrow;
    VM(xrow = ld16(l16, sx, ((8 + c) * 8 + w) * 1024));
    const int mxr = vq;
#pragma unroll
    for (int e = 0; e < 5; ++e) { VM(DMA_S(1, e)); ms[1] = vq; }
    const int l32 = lane & 31, hh = lane >> 5;
    u32 xa = (32 * rw + l32) * 128 + ((hh ^ ((l32 >> 1) & 7)) * 16);
    u32 wa = 16384 + hh * 3072 + cw * 512 + ((lane >> 2) & 3) * 64 + (((((g & 1) << 1) | ((lane >> 1) & 1)) ^ (hh << 1)) * 16) + (lane & 1) * 8;
    u32 ca = 16384 + g * 3072 + min(w, 5) * 512 + ((lane >> 2) & 3) * 64 + ((((g & 1) << 1) | ((lane >> 1) & 1)) * 16) + (lane & 1) * 8;
    const f32x16 zero16 = {0, 0, 0, 0, 0, 0, 0, 0, 0, 0, 0, 0, 0, 0, 0, 0};
    f32x16 acc[3] = {zero16, zero16, zero16};
    f32x4 csa[2] = {zero4, zero4};
    u32x4 xg;
    int mxg = 0;
    WAITQ(ms[0]);
    BARRIER();
    USE_EPOCH()
    {
      u32x4 xf[2], wf[2][4];
#pragma unroll
      for (int n = 0; n <= 8; ++n)
#pragma unroll
      for (int sq = 0; sq < 4; ++sq) {
        const int s = 4 * n + sq;
        if (s > 32) continue;
        if (s < 32 && sq == 0) {
          if (n > 0) {
            WAITQ(ms[n]);
            BARRIER();
          }
          const int d = n == 0 ? 0 : (n % 3 == 0 ? -81920 : 40960);
          xa = opq(xa + d); wa = opq(wa + d); ca = opq(ca + d);
        }
        const int q1 = (s + 1) & 1, q0 = s & 1;
#pragma unroll
        for (int m = 0; m < 4; ++m) {
          if (s > 0) {
            if (m < 3) acc[m] = mfmaL(wf[q1][m], xf[q1], acc[m]);
            else csa[q1] = mfma32(ones4, wf[q1][3], csa[q1]);
            SCHED();
          }
          if (s < 32) {
            if (m == 0) { xf[q0] = lds16(xa ^ (32 * sq)); wf[q0][0] = ldfrag(wa + sq * 6144, 256); }
            if (m == 1) { wf[q0][1] = ldfrag(wa + sq * 6144 + 1024, 256); wf[q0][2] = ldfrag(wa + sq * 6144 + 2048, 256); }
            if (m == 2) wf[q0][3] = ldfrag((ca ^ (32 * (sq & 1))) + (sq >> 1) * 12288, 256);
            if (m == 0 || m == 2) {
              const int h = 2 * sq + (m == 2);
              if (n <= 5 && h < 5) { VM(DMA_S(n + 2, h)); ms[n + 2] = vq; }
              if (n == 0 && h == 5) {
                rsrc_t sp = __builtin_amdgcn_make_buffer_rsrc((void*)wphi, 0, 16384, 0x00020000);
                if (w < 2) dma16(L_WPHI + w * 1024, l16, sp, c * 2048 + w * 1024);
              }
              if (n == 4 && h == 5) { VM(xg = ld16_sc1(i16 * 16, sws, WS_XG + (P * 128 + 16 * w) * 16)); mxg = vq; }
              if (n == 6) { VM(DMA_U(sq, (sq < 2 ? LQ_M : LQ_A) + (sq & 1) * 16384, h & 1)); mu[sq] = vq; }
              if (n == 7 && sq < 2) { VM(DMA_U(4 + sq, sq * 16384, h & 1)); mu[4 + sq] = vq; }
            }
          }
          SCHED();
        }
        PIN(acc[0]); PIN(acc[1]); PIN(acc[2]); PIN(csa[0]); PIN(csa[1]);
        if (s == 3) EARLY_STATS(mxr)
      }
    }
    GATHER_XS()
    sts4f(L_CS + w * 128 + i16 * 4, csa[0][0]);
    sts4f(L_CS + w * 128 + 64 + i16 * 4, csa[1][0]);
    BARRIER();
    WAITQ(mu[3]);
    READ_U(0, LQ_M) READ_U(1, LQ_M + 16384) READ_U(2, LQ_A) READ_U(3, LQ_A + 16384)
    const float xr = lds4f(L_XS + (32 * rw + l32) * 4), xm = -lds4f(L_XS + 512 + (32 * rw + l32) * 4);
    u32x4 wp0, wp1;
    {
      const u32 a = L_WPHI + (2 * cw + (lane & 1)) * 512 + (4 * hh + ((lane >> 2) & 3)) * 32 + (2 * (g & 1) + ((lane >> 1) & 1)) * 8;
      const u32x4 f = ldfrag(a, 256);
      const bool own = (lane >> 2) & 1;
#pragma unroll
      for (int e = 0; e < 4; ++e) { wp0[e] = own ? 0u : f[e]; wp1[e] = own ? f[e] : 0u; }
    }
#define FEATURES(e_, dst, hook)                                                                                         \
  {                                                                                                                 \
    u32x4 kp[2];                                                                                                    \
    _Pragma("unroll") for (int a = 0; a < 4; ++a) {                                                                \
      const f32x4 cc = lds16f(L_CS + (2 * (e_) + cw) * 128 + a * 32 + hh * 16);                                      \
      kp[a >> 1][2 * (a & 1)] = pk2v(fma2(lo2(cc), bc2(xm), (f32x2){acc[e_][4 * a], acc[e_][4 * a + 1]}));          \
      kp[a >> 1][2 * (a & 1) + 1] = pk2v(fma2(hi2(cc), bc2(xm), (f32x2){acc[e_][4 * a + 2], acc[e_][4 * a + 3]}));  \
    }                                                                                                               \
    f32x16 pl = mfmaL(wp0, kp[0], zero16);                                                                          \
    pl = mfmaL(wp1, kp[1], pl);                                                                                     \
    hedgehog16(pl, xr, dst, hook);                                                                                  \
  }
    {
      const int s_ = 32 * rw + l32;
      const u32 key = (((s_ >> 1) & 3) ^ (((s_ >> 3) & 1) << 1)) * 16;
      {
        const u32 va = LQ_V + (s_ >> 3) * 1024 + cw * 512 + (s_ & 7) * 64 + hh * 8;
#pragma unroll
        for (int a = 0; a < 4; ++a) {
          const f32x4 cc = lds16f(L_CS + (4 + cw) * 128 + a * 32 + hh * 16);
          sts8(va + ((a * 16) ^ key), (u32x2){pk2v(fma2(lo2(cc), bc2(xm), (f32x2){acc[2][4 * a], acc[2][4 * a + 1]}) * bc2(xr)),
                                              pk2v(fma2(hi2(cc), bc2(xm), (f32x2){acc[2][4 * a + 2], acc[2][4 * a + 3]}) * bc2(xr))});
        }
      }
      WAITQ(mu[5]);
      READ_U(4, 0) READ_U(5, 16384)
      WAITL0();
      u32x4 ph[4];
      auto hk = [&](int i) __attribute__((always_inline)) {
        const int u = 6 + (i >> 1);
        SCHED();
        VM(DMA_U(u, (u < 8 ? LQ_M : (u < 10 ? LQ_A : 0)) + (u & 1) * 16384, i & 1));
        mu[u] = vq;
        SCHED();
      };
      FEATURES(1, ph, hk)
      const u32 pa = LQ_PHI + (s_ >> 3) * 2048 + (2 * cw + hh) * 512 + (s_ & 7) * 64;
#pragma unroll
      for (int a = 0; a < 4; ++a) sts16(pa + ((a * 16) ^ key), ph[a]);
    }
    BARRIER();
    {
      const u32 kx = (((lane >> 3) & 1) ^ ((g & 1) << 1)) * 16;
      const u32 pa = LQ_PHI + g * 2048 + (w >> 1) * 512 + ((lane >> 2) & 3) * 64 + (((2 * (w & 1) + ((lane >> 1) & 1)) * 16) ^ kx) + (lane & 1) * 8;
      const u32 va = LQ_V + g * 1024 + (w >> 2) * 512 + ((lane >> 2) & 3) * 64 + (((2 * ((w >> 1) & 1) + ((lane >> 1) & 1)) * 16) ^ kx) + (lane & 1) * 8;
      u32x4 pf[4], vf[4];
#pragma unroll
      for (int k = 0; k < 4; ++k) {
        pf[k] = cat(ldstr(pa + k * 8192), ldstr((pa ^ 32) + k * 8192 + 256));
        vf[k] = cat(ldstr(va + k * 4096), ldstr((va ^ 32) + k * 4096 + 256));
      }
      f32x4 kv = zero4, kz = zero4;
#pragma unroll
      for (int k = 0; k < 4; ++k) { kv = mfma32(pf[k], vf[k], kv); kz = mfma32(pf[k], ones4, kz); }
      const u32 so = WS_KP + (c * 16 + P) * KP_SZ;
      u32x4 g0 = {__float_as_uint(kv[0]), tlo, __float_as_uint(kv[1]), tlo}, g1 = {__float_as_uint(kv[2]), tlo, __float_as_uint(kv[3]), tlo};
      u32x4 g2 = {__float_as_uint((lane & 1) ? kz[2] : kz[0]), tlo, __float_as_uint((lane & 1) ? kz[3] : kz[1]), tlo};
      PIN(g0); PIN(g1); PIN(g2);
      asm volatile("s_nop 7" ::: "memory");
      VM(st16_sc1(g0, l16, sws, so + w * 2048));
      VM(st16_sc1(g1, l16, sws, so + w * 2048 + 1024));
      VM(st16_sc1(g2, g * 32 + (lane & 1) * 16 + ((i16 >> 1) << 26), sws, so + 16384 + w * 128));
      CLEAR_H()
    }
    WAITQ(mu[11]);
    READ_U(6, LQ_M) READ_U(7, LQ_M + 16384) READ_U(8, LQ_A) READ_U(9, LQ_A + 16384) READ_U(10, 0) READ_U(11, 16384)
    BARRIER();
    ISS_Q(12, LQ_M); VM(DMA_U(13, LQ_M + 16384, 0));
    u32x4 phq[4];
    {
      VWO();
      auto hk = [&](int i) __attribute__((always_inline)) {
        SCHED();
        if (i == 0) DMA_U(13, LQ_M + 16384, 1);
        else if (i < 5) DMA_U(14 + ((i - 1) >> 1), LQ_A + ((i - 1) >> 1) * 16384, (i - 1) & 1);
        else DMA_WOUT(i - 5)
        SCHED();
      };
      FEATURES(0, phq, hk)
    }
    PIN(phq[0]); PIN(phq[1]); PIN(phq[2]); PIN(phq[3]);
    if (w >= 5) {
      const int id = 2 * P + (w == 7 ? (lane & 1) : w - 5), wq = id >> 2, gq = id & 3;
      const u32 vi0 = (4 * g) * KP_SZ + (w == 7 ? 16384 + wq * 128 + gq * 32 : wq * 2048 + (gq * 16 + i16) * 16);
      const u32 vi1 = vi0 + (w == 7 ? 16 : 1024);
      const u32 vo = (w == 7 ? (WS_KZ - WS_KF) + c * 512 + id * 16 + ((i16 >> 1) << 25) : c * 8192 + id * 256 + i16 * 16) + (g << 28);
      float sa[4];
#pragma nounroll
      for (int it = 0; it < SPIN_CAP; ++it) {
        u32x4 a[4][2];
#pragma unroll
        for (int o = 0; o < 4; ++o) {
          a[o][0] = ld16_sc1(vi0, sws, WS_KP + (c * 16 + o) * KP_SZ);
          a[o][1] = ld16_sc1(vi1, sws, WS_KP + (c * 16 + o) * KP_SZ);
        }
        WAITV(0);
        bool ok = true;
#pragma unroll
        for (int o = 0; o < 4; ++o) { PIN(a[o][0]); PIN(a[o][1]); ok = ok && tag_ok(a[o][0], tlo) && tag_ok(a[o][1], tlo); }
#pragma unroll
        for (int e = 0; e < 4; ++e)
          sa[e] = (__uint_as_float(a[0][e >> 1][2 * (e & 1)]) + __uint_as_float(a[1][e >> 1][2 * (e & 1)])) +
                  (__uint_as_float(a[2][e >> 1][2 * (e & 1)]) + __uint_as_float(a[3][e >> 1][2 * (e & 1)]));
        if (__all(ok)) break;
        __builtin_amdgcn_s_sleep(6);
      }
      red2g(sa[0], sa[1]);
      red2g(sa[2], sa[3]);
      st16_sc1((u32x4){pk2(sa[0], sa[1]), tlo, pk2(sa[2], sa[3]), tlo}, vo, sws, WS_KF);
    }
    gf = ld8_sc1((lane >> 5) * 1024, sws, WS_FA + (16 * w + c2) * 128);
    WAITV(8);
    READ_U(12, LQ_M) READ_U(13, LQ_M + 16384) READ_U(14, LQ_A) READ_U(15, LQ_A + 16384)
    BARRIER();
#pragma unroll
    for (int a = 0; a < 4; ++a) sts16(L_WO1 + w * 4096 + a * 1024 + l16, phq[a]);
    WAITV(0);
    PIN(gf);
    if (!__all(gf[0] == tlo && gf[1] == thi)) poll_flags((lane >> 5) * 1024, true, sws, WS_FA + (16 * w + c2) * 128, tlo, thi);
    { const u32 so_ = zb0 + TILE(0, gn) * 65536; LOAD_B(0, so_) }
  }

  {
  RELANE();
  u32x4 kfg, kzg;
  const u32 kfo = WS_KF + c * 8192 + w * 1024, kzo = WS_KZ + c * 512 + w * 64;
  if (t != 0) {
    kfg = ld16_sc1(l16, sws, kfo);
    kzg = ld16_sc1(g * 16, sws, kzo);
  }
  { const u32 so_ = zb0 + TILE(1, gn) * 65536; LOAD_B(1, so_) }
  const u32 redw = opq(L_RED + w * 4096 + lane * 16), redr = opq(L_RED + (rw ^ (2 * ux0)) * 1024 + lane * 16);
  const u32 f3s = __builtin_amdgcn_readfirstlane(WS_FB + (P * 8 + (c2 > 0 ? c2 - 1 : 0)) * 128);
  const u32 vw3 = ((lane >> 2) & 7) * 1024 + c2 * 128 + (lane >> 5) * 64;
  const rsrc_t swo = __builtin_amdgcn_make_buffer_rsrc((void*)wo, 0, 512 * 1024, 0x00020000);
#define DMA_WO(dst, half, e) dma16((dst) + w * 4096 + (e) * 1024, (vw3 + (lane & 3) * 16) ^ (((e) & 1) * 32), swo, ((w + 8 * (half)) * 4 + (e)) * 8192)
#define FV() ((lane >> 5) * (WS_FC - WS_FB) + (P * 8 + ((lane >> 2) & 7)) * 128)
  u32x2 z1r[6], hold;
  u32 bs;
  f32x4 sm0;
#define EMIT_H(jt, sm_)                                                                                             \
  {                                                                                                                 \
    const f32x2 bb = bc2(blo(bs));                                                                                  \
                                                                 \
    const u32 h0 = min(pk2v((f32x2){blo(z1r[jt][0]), bhi(z1r[jt][0])} * (lo2(sm_) + bb)), 0xFFFFFFFEu);             \
    const u32 h1 = min(pk2v((f32x2){blo(z1r[jt][1]), bhi(z1r[jt][1])} * (hi2(sm_) + bb)), 0xFFFFFFFEu);             \
    const u32 hb_ = WS_HF + ((4 * p + rw) * 24 + c2) * 1024;                                                        \
    if ((jt) == 0 || ((jt) == 2 && gn == 6) || (jt) == gn - 2) hold = (u32x2){h0, h1};                              \
    else if ((jt) == 2) st16_sc1((u32x4){h0, h1, 0u, 0u}, l16, sws, hb_ + 8192);                                    \
    else st16_sc1((u32x4){hold[0], hold[1], h0, h1}, l16, sws, hb_ + ((jt) == 1 ? 0 : ((jt) == gn - 1 ? 16384 : 8192))); \
  }
#pragma unroll
  for (int j = 0; j < 6; ++j) {
    if (j < 5 || gn == 6) {
      const int u = j & 1;
      if (j == 0) WAITV(8);
      if (j == 2) WAITV(t ? 10 : 8);
      if (j == 1) { WAITV(9); WAITV_IF(cw, 1, 8); }
      if (j == 3) { WAITV(9); WAITV_IF(cw, 1, 8); }
      if (j == 4) { if (t) WAITV(6); else { WAITV(13); WAITV_IF(cw, 1, 12); WAITV_IF(gn, 5, 6); } WAITV_IF(2 * gn + cw, 11, 4); }
      if (j == 5) { WAITV(10); WAITV_IF(cw, 1, 9); }
#pragma unroll
      for (int i = 0; i < 8; ++i) PIN(bfr[u][i]);
      {
        f32x4 a_[4];
#pragma unroll
        for (int rt = 0; rt < 4; ++rt) {
          a_[rt] = zero4;
#pragma unroll
          for (int i = 0; i < 8; ++i) a_[rt] = mfma32(bfr[u][i], af[rt][i], a_[rt]);
          SCHED();
          if (rt > 0) sts16f(redw + u * 32768 + (rt - 1) * 1024, a_[rt - 1]);
          SCHED();
        }
        if (j == 0 && t != 0) {
          PIN(kfg); PIN(kzg);
          poll_gran(kfg, l16, sws, kfo, tlo);
          poll_gran(kzg, g * 16, sws, kzo, tlo);
          sts8(L_KFS + (w >> 1) * 1024 + i16 * 64 + (w & 1) * 32 + g * 8, (u32x2){kfg[0], kfg[2]});
          sts8(L_KFS + 4096 + w * 32 + g * 8, (u32x2){kzg[0], kzg[2]});
          sts8(L_KFS + 4352 + (lane & 7) * 8, (u32x2){0u, 0u});
        }
        sts16f(redw + u * 32768 + 3072, a_[3]);
      }
      u32x4 of[2];
      if (j == 1 && t != 0) {
        const int l32 = lane & 31, hh = lane >> 5;
        const bool own = ((lane >> 2) & 1) == hh;
        const u32 ka = own ? L_KFS + (2 * cw + hh) * 1024 + ((l32 & 3) + 4 * (l32 >> 3)) * 64 : L_KFS + 4352;
        const u32 zaa = own ? L_KFS + 4096 + (2 * cw + hh) * 64 : L_KFS + 4352;
        const u32 qa = L_WO1 + w * 4096 + l16;
        const f32x16 zero16 = {0, 0, 0, 0, 0, 0, 0, 0, 0, 0, 0, 0, 0, 0, 0, 0};
        f32x16 den = zero16, num = zero16;
#pragma unroll
        for (int a = 0; a < 4; ++a) {
          const u32x4 pq_ = lds16(qa + a * 1024);
          den = mfmaL(lds16(zaa + a * 16), pq_, den);
          num = mfmaL(lds16(ka + a * 16), pq_, num);
        }
        const float inv = __builtin_amdgcn_rcpf(den[0] + 1e-6f);
#pragma unroll
        for (int a = 0; a < 2; ++a) {
          of[a] = (u32x4){pk2(num[8 * a] * inv, num[8 * a + 1] * inv), pk2(num[8 * a + 2] * inv, num[8 * a + 3] * inv),
                          pk2(num[8 * a + 4] * inv, num[8 * a + 5] * inv), pk2(num[8 * a + 6] * inv, num[8 * a + 7] * inv)};
          PIN(of[a]);
        }
      }
      BAR_IF(cw, 1, (j == 2 && t != 0) ? 0 : 63);
      if (w == 4) {
        if (j == 0 && t == 0) SET_FLAG(WS_FB + (P * 8 + c) * 128);
        if (j == 2 && t != 0) SET_FLAG(WS_FC + (P * 8 + c) * 128);
      }
      if (j == 1) {
        if (cw == 0) {
          WAITV(0);
          PIN(gf);
          if (!__all(gf[0] == tlo && gf[1] == thi)) poll_flags((lane >> 5) * 128, true, sws, f3s, tlo, thi);
          rsrc_t sb = __builtin_amdgcn_make_buffer_rsrc((void*)bsp, 0, 4096, 0x00020000);
          bs = ld2(i16 * 2, sb, (64 * p + 16 * rw) * 2);
        }
      }
      if ((j == 1 || j == 2) && cw == 0) {
#pragma unroll
        for (int v = 3 * j - 3; v < 3 * j && v < 6 - t; ++v)
          z1r[v] = ld8_sc1(g * 256 + (i16 >> 3) * 64 + (i16 & 3) * 16 + ((i16 >> 2) & 1) * 8, sws,
                           WS_Z1 + ((2 * p + (rw >> 1)) * 43 + gs + TILE(min(v, gn - 1), gn)) * 1024 + (rw & 1) * 128);
      }
      if ((j == 4 && gn == 5) || j == 5) gf = ld8_sc1(FV(), sws, WS_FB);
      if (j == 4 && t == 0) {
        VWO();
#pragma unroll
        for (int e = 2; e < 7; ++e) DMA_WOUT(e)
      }
      const u32 ra_ = redr + ((j + 1) & 1) * 32768;
      f32x4 sm, r_[2];
#pragma unroll
      for (int i = 0; i < 8; ++i) {
        if (j >= 1 && j < (t ? 3 : 4) && (i & 1) == 0 && cw == 0) {
          if (i == 2) sm = r_[0] + r_[1];
          if (i > 2) sm += r_[0] + r_[1];
          r_[0] = lds16f(ra_ + i * 4096); r_[1] = lds16f(ra_ + (i + 1) * 4096);
        }
        SCHED();
        if (j < 3) bfr[u][i] = ld16_b(l16, sws, (zb0 + TILE(j + 2, gn) * 65536) ^ (i * 1024));
        else if (j == 3 && t == 0) ld16_b_if6(bfr[1][i], gn, l16, sws, (zb0 + TILE(5, gn) * 65536) ^ (i * 1024));
        SCHED();
      }
      if (j == 0 && cw == 0) gf = ld8_sc1((lane >> 5) * 128, sws, f3s);
      if (j == 1 && t != 0) {
        const u32 vo = ((lane >> 4) & 1) * 16384 + (lane >> 5) * 512 + i16 * 16;
        st16_sc1(of[0], vo, sws, WS_OF + ((8 * P + 2 * rw) * 16 + 2 * c + cw) * 1024);
        st16_sc1(of[1], vo, sws, WS_OF + ((8 * P + 2 * rw) * 16 + 2 * c + cw) * 1024 + 256);
      }
      if (j == 2) { PIN(bs); _Pragma("unroll") for (int v = 0; v < 3; ++v) PIN(z1r[v]); }
      if (j == 3) { _Pragma("unroll") for (int v = 3; v < 6 - t; ++v) PIN(z1r[v]); }
      if (j >= 1 && cw == 0) {
        if (j < (t ? 3 : 4)) sm += r_[0] + r_[1];
        else {
          f32x4 q_[8];
#pragma unroll
          for (int o = 0; o < 8; ++o) q_[o] = lds16f(ra_ + o * 4096);
          SCHED();
          sm = ((q_[0] + q_[1]) + (q_[2] + q_[3])) + ((q_[4] + q_[5]) + (q_[6] + q_[7]));
        }
        if (j == 1) sm0 = sm;
        if (j == 2) EMIT_H(0, sm0)
        if (j >= 2) EMIT_H(j >= 2 ? j - 1 : 0, sm)
      }
      if (j == 3) {
#pragma unroll
        for (int e = 0; e < 4; ++e) DMA_WO(L_WO1, 0, e);
      }
      if (j == 2 && t != 0) WAITV_IF(cw, 0, 11);
      BAR_IF(cw, 0, 63);
    }
  }
  {
    const u32 bufA = L_RED + (gn & 1) * 32768, bufB = L_RED + 32768 - (gn & 1) * 32768;
    const u32 fv = FV();
    if (cw == 0) {
      const u32 ra_ = redr + (bufB - L_RED);
      f32x4 q_[8];
#pragma unroll
      for (int o = 0; o < 8; ++o) q_[o] = lds16f(ra_ + o * 4096);
      SCHED();
      const f32x4 sm = ((q_[0] + q_[1]) + (q_[2] + q_[3])) + ((q_[4] + q_[5]) + (q_[6] + q_[7]));
      if (gn == 6) EMIT_H(5, sm) else EMIT_H(4, sm)
    }
    WAITV(1); WAITV_IF(cw, 1, 0);
    PIN(gf);
    if (!__all(gf[0] == tlo && gf[1] == thi)) poll_flags(fv, true, sws, WS_FB, tlo, thi);
    RELANE();
    u32x4 hf[12], of[8];
    const u32 ofb = WS_OF + ((4 * p + rw) * 16 + cw) * 1024, hfb = WS_HF + ((4 * p + rw) * 24 + cw) * 1024;
#pragma unroll
    for (int e = 0; e < 4; ++e) of[e] = ld16_b(l16, sws, ofb + e * 2048);
#pragma unroll
    for (int e = 0; e < 4; ++e) hf[e] = ld16_b(l16, sws, hfb + e * 2048);
    BARRIER();
    const u32 qb = ((lane >> 2) & 3) * 64 + ((((g & 1) << 1) | ((lane >> 1) & 1)) * 16) + (lane & 1) * 8;
    f32x4 a3[4];
#pragma unroll
    for (int ct = 0; ct < 4; ++ct) a3[ct] = zero4;
    u32x4 wf[3][4];
#define RDW2(dst, lo0, hi0, offl, offh) _Pragma("unroll") for (int ct = 0; ct < 4; ++ct) dst[ct] = cat(ldstr(((lo0) ^ ((ct & 1) * 32)) + (offl) + (ct >> 1) * 512), ldstr(((hi0) ^ ((ct & 1) * 32)) + (offh) + (ct >> 1) * 512))
#define MM3(src, frag) _Pragma("unroll") for (int ct = 0; ct < 4; ++ct) a3[ct] = mfma32(src[ct], frag, a3[ct])
    const rsrc_t sxo = __builtin_amdgcn_make_buffer_rsrc((void*)(x + (size_t)p * 65536), 0, 65536, 0x00020000);
    const u32 w0 = opq(L_WOUT + qb + (g >> 1) * 1024 + (g & 1) * 256);
    const u32 wa = w0 + cw * 4096, wb = w0 + cw * 2048;
    const u32 oq = qb + g * 1024 + cw * 4096;
    const u32 oa = opq(L_WO1 + oq), ob = opq(bufA + oq);
    u32x4 xres;
    VWO();
#pragma unroll
    for (int e = 0; e < 4; ++e) DMA_WO(bufA, 1, e);
#define RDK(dst, k)                                                                                                 \
  if ((k) < 4) { RDW2(dst, oa, oa, (k) * 8192, (k) * 8192 + 256); }                                                 \
  else if ((k) < 8) { RDW2(dst, wa, wa, ((k) - 4) * 8192, ((k) - 4) * 8192 + 2048); }                               \
  else if ((k) < 12) { RDW2(dst, ob, ob, ((k) - 8) * 8192, ((k) - 8) * 8192 + 256); }                               \
  else { RDW2(dst, wb, w0, 32768 + ((k) - 12) * 4096, 49152 + ((k) - 12) * 2048); }
    RDK(wf[0], 0)
    RDK(wf[1], 1)
#define VALID4(e0)                                                                                                  \
  {                                                                                                                 \
    _Pragma("unroll") for (int e = e0; e < e0 + 4; ++e) PIN(hf[e]);                                                \
    _Pragma("nounroll") for (int it = 0; it < SPIN_CAP; ++it) {                                                    \
      u32 mx_ = 0;                                                                                                  \
      _Pragma("unroll") for (int e = e0; e < e0 + 4; ++e) mx_ = max(max(max(mx_, hf[e][0]), hf[e][1]), max(hf[e][2], hf[e][3])); \
      if (__all(mx_ != ~0u)) break;                                                                                 \
      __builtin_amdgcn_s_sleep(2);                                                                                  \
      asm volatile("buffer_load_dwordx4 %0, %4, %5, %6 offen sc1\n\tbuffer_load_dwordx4 %1, %4, %5, %6 offen offset:2048 sc1\n\t" \
                   "buffer_load_dwordx4 %2, %4, %5, %7 offen sc1\n\tbuffer_load_dwordx4 %3, %4, %5, %7 offen offset:2048 sc1\n\t" \
                   "s_waitcnt vmcnt(0)"                                                                             \
                   : "=&v"(hf[e0]), "=&v"(hf[e0 + 1]), "=&v"(hf[e0 + 2]), "=&v"(hf[e0 + 3])                         \
                   : "v"(l16), "s"(sws), "s"(hfb + (e0) * 2048), "s"(hfb + (e0) * 2048 + 4096)                       \
                   : "memory");                                                                                     \
    }                                                                                                               \
  }
#pragma unroll
    for (int k = 0; k < 16; ++k) {
      if (k == 0) WAITV(8);
      if (k == 4) { WAITV(12); VALID4(0) }
      if (k == 6) {
        WAITV(4);
        BARRIER();
      }
      if (k == 12) { WAITV(5); VALID4(4) }
      if (k + 2 < 16) { RDK(wf[(k + 2) % 3], k + 2) }
      SCHED();
      if (k < 4) { PIN(of[k]); MM3(wf[k % 3], of[k]); }
      else if (k < 8) MM3(wf[k % 3], hf[k - 4]);
      else if (k < 12) { PIN(of[k - 4]); MM3(wf[k % 3], of[k - 4]); }
      else MM3(wf[k % 3], hf[k - 8]);
      SCHED();
      if (k < 4) {
        of[4 + k] = ld16_b(l16, sws, ofb + (4 + k) * 2048);
        DMA_WOUT_AT(bufB + w * 4096, 7 + k)
      } else if (k < 6) {
        hf[2 * k - 4] = ld16_b(l16, sws, hfb + (2 * k - 4) * 2048);
        hf[2 * k - 3] = ld16_b(l16, sws, hfb + (2 * k - 3) * 2048);
      } else if (k == 6) {
        xres = ld16((tid >> 3) * 1024 + (tid & 7) * 16, sxo, c2 * 128);
      } else if (k == 10 || k == 11) {
        hf[8 + 2 * (k - 10)] = ld16_b(l16, sws, hfb + (8 + 2 * (k - 10)) * 2048);
        hf[9 + 2 * (k - 10)] = ld16_b(l16, sws, hfb + (9 + 2 * (k - 10)) * 2048);
      }
    }
    {
      const u32 wl_ = opq(bufB + (w0 - L_WOUT) + cw * 4096);
      u32x4 wg[4][4];
#pragma unroll
      for (int e = 0; e < 4; ++e) RDW2(wg[e], wl_, wl_, e * 8192, e * 8192 + 2048);
      WAITV(0);
      PIN(xres);
      VALID4(8)
#pragma unroll
      for (int e = 0; e < 4; ++e) MM3(wg[e], hf[8 + e]);
    }
    const u32 fin = L_WO1 - 2048;
#pragma unroll
    for (int ct = 0; ct < 4; ++ct) sts16f(fin + 2048 + cw * 16384 + (16 * rw + i16) * 256 + (((4 * ct + g) ^ i16) * 16), a3[ct]);
    BARRIER();
    {
      const int row = tid >> 3;
      const u32 a = fin + 2048 + row * 256 + (((2 * (tid & 7)) ^ (row & 15)) * 16);
      const f32x4 s0 = lds16f(a) + lds16f(a + 16384), s1 = lds16f(a ^ 16) + lds16f((a ^ 16) + 16384);
      u32x4 o = {pk2(blo(xres[0]) + s0[0], bhi(xres[0]) + s0[1]), pk2(blo(xres[1]) + s0[2], bhi(xres[1]) + s0[3]),
                 pk2(blo(xres[2]) + s1[0], bhi(xres[2]) + s1[1]), pk2(blo(xres[3]) + s1[2], bhi(xres[3]) + s1[3])};
      st16_sc1(o, row * 1024 + (tid & 7) * 16, __builtin_amdgcn_make_buffer_rsrc((void*)(out + (size_t)p * 65536), 0, 65536, 0x00020000), c2 * 128);
    }
  }
  }
  if (b == 0 && w == 0 && l16 == 0) *(u64*)(ws + WS_EP) = ((u64)thi << 32) | tlo;
}

__global__ __launch_bounds__(512, 2) void fused_kernel(const char* __restrict__ x, const char* __restrict__ wqkv, const char* __restrict__ wphi,
                                                        const char* __restrict__ wo, const char* __restrict__ win, const char* __restrict__ wsp,
                                                        const char* __restrict__ bsp, const char* __restrict__ wout, char* __restrict__ out, char* ws) {
  __shared__ __attribute__((aligned(1024))) char smem[163840];
  asm volatile("" ::"v"((u32)(size_t)(LDSP char*)smem));
  asm volatile("" ::"s"(x), "s"(wqkv), "s"(win), "s"(ws), "s"(wsp));
  u64 pc, ep;
  asm volatile("s_getpc_b64 %0" : "=s"(pc));
  {
    const int lane = threadIdx.x & 63, g = lane >> 4;
    const int w = __builtin_amdgcn_readfirstlane(threadIdx.x >> 6);
    const int r = blockIdx.x >> 3, P = ((blockIdx.x & 7) >> 1) * 4 + (r >> 3);
    const int rot = 2 * (((r >> 3) + (r & 3)) & 3);
    if (w == 7)
      touch(L_JUNK, lane * 128, __builtin_amdgcn_make_buffer_rsrc((void*)(pc & ~255ull), 0, CODE_PF, 0x00020000), (r & 7) * 8192);
    const rsrc_t sx = __builtin_amdgcn_make_buffer_rsrc((void*)(x + (size_t)P * 131072), 0, 131072, 0x00020000);
    const int tq = (r >> 2) & 1;
    const u32 vx0 = XROW(tq) * 1024 + (((lane & 7) ^ g) * 16);
    const u32 vx1 = (vx0 + (tq ? 8192 : 16384)) ^ 64;
    const u32 w2k = w * 2048;
    DMA_X(0, 0, 0);
    DMA_X(0, 0, 1);
    asm volatile("s_load_dwordx2 %0, %1, 0x0" : "=s"(ep) : "s"(ws) : "memory");
    SCHED();
  }
  if (__builtin_expect(((blockIdx.x >> 5) & 1) == 0, 1)) run<0>(x, wqkv, wphi, wo, win, wsp, bsp, wout, out, ws, ep);
  else run<1>(x, wqkv, wphi, wo, win, wsp, bsp, wout, out, ws, ep);
}

extern "C" void kernel_launch(void* const* d_in, const int* in_sizes, int n_in, void* d_out, int out_size, void* d_ws, size_t ws_size,
                              hipStream_t stream) {
  fused_kernel<<<256, 512, 0, stream>>>((const char*)d_in[0], (const char*)d_in[1], (const char*)d_in[2], (const char*)d_in[3],
                                        (const char*)d_in[5], (const char*)d_in[6], (const char*)d_in[7], (const char*)d_in[8], (char*)d_out,
                                        (char*)d_ws);
}
```

```cpp
#include <hip/hip_runtime.h>
#include <hip/hip_bf16.h>

typedef unsigned int u32;
typedef unsigned long long u64;
typedef short s16x4 __attribute__((ext_vector_type(4)));
typedef __bf16 bf16x2 __attribute__((ext_vector_type(2)));
typedef __bf16 bf16x8 __attribute__((ext_vector_type(8)));
typedef float f32x16 __attribute__((ext_vector_type(16)));
typedef float f32x4 __attribute__((ext_vector_type(4)));
typedef float f32x2 __attribute__((ext_vector_type(2)));
typedef u32 u32x4 __attribute__((ext_vector_type(4)));
typedef u32 u32x2 __attribute__((ext_vector_type(2)));
typedef __amdgpu_buffer_rsrc_t rsrc_t;
#define LDSP __attribute__((address_space(3)))
#define DI __device__ __forceinline__

constexpr u32 WS_EP = 0;
constexpr u32 WS_FA = 8192;
constexpr u32 WS_FB = WS_FA + 16384;
constexpr u32 WS_FC = WS_FB + 16384;
constexpr u32 WS_XG = WS_FC + 81920;
constexpr u32 WS_SG = WS_XG + 32768;
constexpr u32 WS_ZB = WS_SG + 524288;
constexpr u32 WS_Z1 = WS_ZB + 43 * 65536;
constexpr u32 KP_SZ = 17408;
constexpr u32 WS_KP = WS_Z1 + 64 * 43 * 1024;
constexpr u32 WS_KF = WS_KP + 128 * KP_SZ;
constexpr u32 WS_KZ = WS_KF + 65536;
constexpr u32 WS_OF = WS_KZ + 4096;
constexpr u32 WS_HF = WS_OF + 128 * 16384;
constexpr u32 WS_END = WS_HF + 128 * 24 * 1024;

constexpr u32 LZ_W = 65536;
constexpr u32 LZ_U = 139264;
constexpr u32 LQ_M = 122880;
constexpr u32 LQ_A = 81920;
constexpr u32 LQ_PHI = 49152;
constexpr u32 LQ_V = 32768;
constexpr u32 L_TAB = 155648;
constexpr u32 L_XS = L_TAB;
constexpr u32 L_CS = L_TAB + 1024;
constexpr u32 L_ZST = L_TAB + 3072;
constexpr u32 L_WPHI = L_TAB + 3072;
constexpr u32 L_KFS = L_TAB + 2048;
constexpr u32 L_JUNK = L_TAB + 7168;
constexpr u32 L_WOUT = 0;
constexpr u32 L_RED = 57344;
constexpr u32 L_WO1 = 122880;

constexpr u64 Z2S = 0x26211B16110B0500ull;
constexpr u64 Z1S = 0x26211C16110C0600ull;
DI int grp_start(u64 tab, int c) { return c >= 8 ? 43 : (int)((tab >> (8 * c)) & 0xFF); }

#define SCHED() __builtin_amdgcn_sched_barrier(0)
#define WAITV(n) do { asm volatile("s_waitcnt vmcnt(%0)" ::"n"(n) : "memory"); SCHED(); } while (0)
#define BARRIER() do { SCHED(); asm volatile("s_waitcnt lgkmcnt(0)\n\ts_barrier" ::: "memory"); SCHED(); } while (0)
#define WAITV_IF(g_, v, n) do { asm volatile("s_cmp_lg_u32 %0, %1\n\ts_cbranch_scc1 1f\n\ts_waitcnt vmcnt(%2)\n1:" ::"s"(g_), "n"(v), "n"(n) : "scc", "memory"); SCHED(); } while (0)
#define BAR_IF(g_, v, n) do { SCHED(); asm volatile("s_cmp_lg_u32 %0, %1\n\ts_cbranch_scc1 1f\n\ts_waitcnt vmcnt(%2) lgkmcnt(0)\n\ts_barrier\n1:" ::"s"(g_), "n"(v), "n"(n) : "scc", "memory"); SCHED(); } while (0)
#define WAITL0() asm volatile("s_waitcnt lgkmcnt(0)" ::: "memory")
#define PIN(x) asm volatile("" : "+v"(x))

DI void dma16(u32 lds, u32 voff, rsrc_t srd, u32 soff) {
  asm volatile("s_mov_b32 m0, %0\n\ts_nop 0\n\tbuffer_load_dwordx4 %1, %2, %3 offen lds" ::"s"(lds), "v"(voff), "s"(srd), "s"(soff) : "memory");
}
#define DMA_C(wb, CST, voff, srd, soff) asm volatile("s_add_u32 m0, %0, %1\n\ts_nop 0\n\tbuffer_load_dwordx4 %2, %3, %4 offen lds" ::"s"(wb), "n"(CST), "v"(voff), "s"(srd), "s"(soff) : "scc", "memory")
#define DMA_CC(wb, CST, voff, srd, OP, sb, SC) do { u32 so__; asm volatile("s_add_u32 m0, %1, %2\n\t" OP " %0, %5, %6\n\tbuffer_load_dwordx4 %3, %4, %0 offen lds" : "=&s"(so__) : "s"(wb), "n"(CST), "v"(voff), "s"(srd), "s"(sb), "n"(SC) : "scc", "memory"); } while (0)
DI void touch(u32 lds, u32 voff, rsrc_t srd, u32 soff) {
  asm volatile("s_mov_b32 m0, %0\n\ts_nop 0\n\tbuffer_load_dword %1, %2, %3 offen lds" ::"s"(lds), "v"(voff), "s"(srd), "s"(soff) : "memory");
}
DI u32x4 ld16_sc1(u32 voff, rsrc_t srd, u32 soff) {
  u32x4 r;
  asm volatile("buffer_load_dwordx4 %0, %1, %2, %3 offen sc1" : "=v"(r) : "v"(voff), "s"(srd), "s"(soff) : "memory");
  return r;
}
template <int OFF> DI u32x4 ld16_sc1o(u32 voff, rsrc_t srd, u32 soff) {
  u32x4 r;
  asm volatile("buffer_load_dwordx4 %0, %1, %2, %3 offen offset:%4 sc1" : "=v"(r) : "v"(voff), "s"(srd), "s"(soff), "n"(OFF) : "memory");
  return r;
}
DI u32x4 ld16_b(u32 voff, rsrc_t srd, u32 soff) {
  u32x4 r;
  asm volatile("buffer_load_dwordx4 %0, %1, %2, %3 offen sc1" : "=v"(r) : "v"(voff), "s"(srd), "s"(soff) : "memory");
  return r;
}
template <int OFF> DI u32x4 ld16_bo(u32 voff, rsrc_t srd, u32 soff) {
  u32x4 r;
  asm volatile("buffer_load_dwordx4 %0, %1, %2, %3 offen offset:%4 sc1" : "=v"(r) : "v"(voff), "s"(srd), "s"(soff), "n"(OFF) : "memory");
  return r;
}
DI void ld16_b_if6(u32x4& r, int gn, u32 voff, rsrc_t srd, u32 soff) {
  asm volatile("s_cmp_lg_u32 %4, 6\n\ts_cbranch_scc1 1f\n\tbuffer_load_dwordx4 %0, %1, %2, %3 offen sc1\n1:" : "+v"(r) : "v"(voff), "s"(srd), "s"(soff), "s"(gn) : "scc", "memory");
}
DI u32x4 ld16(u32 voff, rsrc_t srd, u32 soff) {
  u32x4 r;
  asm volatile("buffer_load_dwordx4 %0, %1, %2, %3 offen" : "=v"(r) : "v"(voff), "s"(srd), "s"(soff) : "memory");
  return r;
}
DI u32x2 ld8_sc1(u32 voff, rsrc_t srd, u32 soff) {
  u32x2 r;
  asm volatile("buffer_load_dwordx2 %0, %1, %2, %3 offen sc1" : "=v"(r) : "v"(voff), "s"(srd), "s"(soff) : "memory");
  return r;
}
DI u32 ld2(u32 voff, rsrc_t srd, u32 soff) {
  u32 r;
  asm volatile("buffer_load_ushort %0, %1, %2, %3 offen" : "=v"(r) : "v"(voff), "s"(srd), "s"(soff) : "memory");
  return r;
}
DI void st16_sc1(u32x4 v, u32 voff, rsrc_t srd, u32 soff) {
  asm volatile("buffer_store_dwordx4 %0, %1, %2, %3 offen sc1\n\ts_nop 1" ::"v"(v), "v"(voff), "s"(srd), "s"(soff) : "memory");
}
DI void st8_sc1(u32x2 v, u32 voff, rsrc_t srd, u32 soff) {
  asm volatile("buffer_store_dwordx2 %0, %1, %2, %3 offen sc1" ::"v"(v), "v"(voff), "s"(srd), "s"(soff) : "memory");
}

DI u32x4 lds16(u32 a) { return *(const LDSP u32x4*)(size_t)a; }
DI u32x2 lds8(u32 a) { return *(const volatile LDSP u32x2*)(size_t)a; }
DI float lds4f(u32 a) { return *(const LDSP float*)(size_t)a; }
DI f32x4 lds16f(u32 a) { return *(const LDSP f32x4*)(size_t)a; }
DI void sts16f(u32 a, f32x4 v) { *(LDSP f32x4*)(size_t)a = v; }
DI void sts16(u32 a, u32x4 v) { *(LDSP u32x4*)(size_t)a = v; }
DI void sts8(u32 a, u32x2 v) { *(volatile LDSP u32x2*)(size_t)a = v; }
DI void sts4f(u32 a, float v) { *(LDSP float*)(size_t)a = v; }
DI u32x2 ldstr(u32 a) {
  return __builtin_bit_cast(u32x2, __builtin_amdgcn_ds_read_tr16_b64_v4i16((LDSP s16x4*)(size_t)a)); }
DI u32x4 cat(u32x2 lo, u32x2 hi) { return (u32x4){lo[0], lo[1], hi[0], hi[1]}; }
DI u32x4 ldfrag(u32 a, u32 d) { u32x2 lo = ldstr(a), hi = ldstr(a + d); return cat(lo, hi); }

DI f32x4 mfma32(u32x4 a, u32x4 b, f32x4 c) {
  return __builtin_amdgcn_mfma_f32_16x16x32_bf16(__builtin_bit_cast(bf16x8, a), __builtin_bit_cast(bf16x8, b), c, 0, 0, 0);
}
DI f32x16 mfmaL(u32x4 a, u32x4 b, f32x16 c) {
  return __builtin_amdgcn_mfma_f32_32x32x16_bf16(__builtin_bit_cast(bf16x8, a), __builtin_bit_cast(bf16x8, b), c, 0, 0, 0);
}
DI u32 pk2(float a, float b) { bf16x2 v = {(__bf16)a, (__bf16)b}; return __builtin_bit_cast(u32, v); }
DI float blo(u32 u) { return __uint_as_float(u << 16); }
DI float bhi(u32 u) { return __uint_as_float(u & 0xffff0000u); }
DI float rbf(float a) { return blo(pk2(a, a)); }
DI float dot2(u32 a, u32 b, float c) {
  return __builtin_amdgcn_fdot2_f32_bf16(__builtin_bit_cast(bf16x2, a), __builtin_bit_cast(bf16x2, b), c, false);
}
DI f32x2 fma2(f32x2 a, f32x2 b, f32x2 c) { return __builtin_elementwise_fma(a, b, c); }
DI f32x2 bc2(float a) { return (f32x2){a, a}; }
DI f32x2 lo2(f32x4 v) { return (f32x2){v[0], v[1]}; }
DI f32x2 hi2(f32x4 v) { return (f32x2){v[2], v[3]}; }
DI u32 pk2v(f32x2 v) { return pk2(v[0], v[1]); }
DI f32x2 gelu2(f32x2 x) {
  f32x2 u = fma2(x * x, bc2(-0.10294324f), bc2(-2.3022082f)) * x;
  f32x2 e = {__builtin_amdgcn_exp2f(u[0]), __builtin_amdgcn_exp2f(u[1])};
  e = e + bc2(1.0f);
  f32x2 r = {__builtin_amdgcn_rcpf(e[0]), __builtin_amdgcn_rcpf(e[1])};
  return x * r;
}
DI void red2g(float& a, float& b) {
  auto s = __builtin_amdgcn_permlane32_swap(__float_as_uint(a), __float_as_uint(b), false, false);
  float c = __uint_as_float(s[0]) + __uint_as_float(s[1]);
  auto s2 = __builtin_amdgcn_permlane16_swap(__float_as_uint(c), __float_as_uint(c), false, false);
  float d = __uint_as_float(s2[0]) + __uint_as_float(s2[1]);
  auto s3 = __builtin_amdgcn_permlane32_swap(__float_as_uint(d), __float_as_uint(d), false, false);
  a = __uint_as_float(s3[0]);
  b = __uint_as_float(s3[1]);
}
template <int CTRL> DI float dpp(float v) {
  return __uint_as_float(__builtin_amdgcn_update_dpp(0, __float_as_uint(v), CTRL, 0xF, 0xF, true));
}
template <class F> DI void hedgehog16(const f32x16& p, float s, u32x4 (&o)[4], F hook) {
  float mx = fmaxf(p[0], p[1]), mn = fminf(p[0], p[1]);
#pragma unroll
  for (int i = 2; i < 16; i += 2) { mx = fmaxf(fmaxf(mx, p[i]), p[i + 1]); mn = fminf(fminf(mn, p[i]), p[i + 1]); }
  const float c = s * 1.44269504f;
  const float a = -mx * c, b = mn * c;
  f32x2 ep[8], en[8];
#pragma unroll
  for (int i = 0; i < 8; ++i) {
    hook(i);
    const f32x2 v = {p[2 * i], p[2 * i + 1]};
    const f32x2 u = fma2(v, bc2(c), bc2(a)), w = fma2(v, bc2(-c), bc2(b));
    ep[i] = (f32x2){__builtin_amdgcn_exp2f(u[0]), __builtin_amdgcn_exp2f(u[1])};
    en[i] = (f32x2){__builtin_amdgcn_exp2f(w[0]), __builtin_amdgcn_exp2f(w[1])};
  }
  hook(8);
  const f32x2 sp = ((ep[0] + ep[1]) + (ep[2] + ep[3])) + ((ep[4] + ep[5]) + (ep[6] + ep[7]));
  const f32x2 sn = ((en[0] + en[1]) + (en[2] + en[3])) + ((en[4] + en[5]) + (en[6] + en[7]));
  const float ip = __builtin_amdgcn_rcpf(sp[0] + sp[1]), in = __builtin_amdgcn_rcpf(sn[0] + sn[1]);
#pragma unroll
  for (int i = 0; i < 8; ++i) {
    if (i == 0 || i == 3 || i == 6) hook(9 + i / 3);
    o[i >> 2][i & 3] = pk2v(ep[i] * bc2(ip));
    o[2 + (i >> 2)][i & 3] = pk2v(en[i] * bc2(in));
  }
}
DI void ln_fin(float s, float q, float invn, float& r, float& m) {
  m = s * invn;
  float v = fmaxf(q * invn - m * m, 0.0f);
  float sd = rbf(__builtin_amdgcn_sqrtf(rbf(rbf(v) + 1e-5f)));
  r = __builtin_amdgcn_rcpf(sd);
  m = rbf(m);
}

constexpr int SPIN_CAP = 1 << 17;
DI void poll_flags(u32 voff, bool act, rsrc_t srd, u32 soff, u32 tlo, u32 thi) {
#pragma nounroll
  for (int it = 0; it < SPIN_CAP; ++it) {
    u32x2 v;
    asm volatile("buffer_load_dwordx2 %0, %1, %2, %3 offen sc1\n\ts_waitcnt vmcnt(0)" : "=&v"(v) : "v"(voff), "s"(srd), "s"(soff) : "memory");
    bool ok = !act || (v[0] == tlo && v[1] == thi);
    if (__all(ok)) break;
    __builtin_amdgcn_s_sleep(6);
  }
}
DI bool tag_ok(u32x4 q, u32 t) { return q[1] == t && q[3] == t; }
DI void poll_gran(u32x4& q, u32 voff, rsrc_t srd, u32 soff, u32 tlo) {
#pragma nounroll
  for (int it = 0; it < SPIN_CAP; ++it) {
    if (__all(tag_ok(q, tlo))) break;
    __builtin_amdgcn_s_sleep(6);
    asm volatile("buffer_load_dwordx4 %0, %1, %2, %3 offen sc1\n\ts_waitcnt vmcnt(0)" : "=&v"(q) : "v"(voff), "s"(srd), "s"(soff) : "memory");
  }
}
DI u32 opq(u32 v) { asm volatile("" : "+v"(v)); return v; }

static_assert(WS_ZB % 8192 == 0, "fragment addresses are formed by XOR");
constexpr u32 CODE_PF = 45056;

#define VM(x) do { x; ++vq; } while (0)
#define WAITQ(mark) WAITV(vq - (mark))

template <int t>
DI void run(const char* __restrict__ x, const char* __restrict__ wqkv, const char* __restrict__ wphi, const char* __restrict__ wo,
            const char* __restrict__ win, const char* __restrict__ wsp, const char* __restrict__ bsp, const char* __restrict__ wout,
            char* __restrict__ out, char* ws, u64 ep) {
  const int tid = threadIdx.x, lane = tid & 63;
  const int w = __builtin_amdgcn_readfirstlane(tid >> 6);
  const int g = lane >> 4;
  const int b = blockIdx.x, xcd = b & 7, r = b >> 3;
  const int Pl = r >> 3, cl = r & 3;
  const int P = (xcd >> 1) * 4 + Pl;
  const int rot = 2 * ((Pl + cl) & 3);
  const rsrc_t sx = __builtin_amdgcn_make_buffer_rsrc((void*)(x + (size_t)P * 131072), 0, 131072, 0x00020000);
#define XROW(t_) ((t_) ? 16 * w + (lane >> 3) : 32 * (w >> 1) + 4 * (w & 1) + 8 * (lane >> 5) + ((lane >> 3) & 3))
  const u32 vx0 = XROW(t) * 1024 + (((lane & 7) ^ g) * 16);
  const u32 vx1 = (vx0 + (t ? 8192 : 16384)) ^ 64;
  const u32 w2k = w * 2048;
#define DMA_X(dst, n, e) DMA_C(w2k, (dst) + (e) * 1024, (e) ? vx1 : vx0, sx, (((n) + rot) & 7) * 128)
  const int rw = w & 3, cw = w >> 2, i16 = lane & 15;
  const int c = (xcd & 1) * 4 + cl;
  const int p = 2 * P + (cl & 1);
  const int c2 = (xcd & 1) * 4 + (t ? 3 * (cl >> 1) : 1 + (cl >> 1));
  const u32 l16 = opq(lane * 16);
  const rsrc_t sa = __builtin_amdgcn_make_buffer_rsrc((void*)(wsp + (size_t)p * 262144), 0, 262144, 0x00020000);
  const rsrc_t sws = __builtin_amdgcn_make_buffer_rsrc((void*)ws, 0, WS_END, 0x00020000);
  const u32 vA0 = (lane >> 3) * 4096 + (((lane & 7) ^ g) * 16);
  const u32 vA1 = (vA0 + 32768) ^ 64;
  const int ux0 = cl >> 1, ux1 = t;
  const u32 uxs = w * 512 + ux1 * 256 + ux0 * 131072;
#define DMA_U(u, base, e) DMA_CC(w2k, (base) + (e) * 1024, (e) ? vA1 : vA0, sa, "s_xor_b32", uxs, (((u) >> 1) & 3) * 128 + (((u) >> 3) * 32 + ((u) & 1) * 16) * 4096)
  const f32x4 zero4 = {0, 0, 0, 0};
  u32x4 ones4 = {0x3F803F80u, 0x3F803F80u, 0x3F803F80u, 0x3F803F80u};
  u32 tlo, thi;
  u32x4 af[4][8];
  u32x4 bfr[2][8];
  u32x2 gf;
  const u32 ard = w * 2048 + i16 * 128 + ((g ^ (i16 >> 1)) * 16);
#define READ_U(u, base)                                                                                             \
  {                                                                                                                 \
    const u32 ab_ = opq(ard + (base));                                                                              \
    _Pragma("unroll") for (int kk = 0; kk < 2; ++kk)                                                               \
      af[2 * ((u) >> 3) + ((u) & 1)][((u) & 6) + kk] = lds16(ab_ ^ (64 * kk));                                     \
  }
#define USE_EPOCH() { asm volatile("" : "+s"(ep)); ep += 1; tlo = (u32)ep; thi = (u32)(ep >> 32); PIN(ones4); }
#define EARLY_STATS(mark)                                                                                           \
  {                                                                                                                 \
    WAITQ(mark); PIN(xrow);                                                                                         \
    float s_ = 0, q_ = 0;                                                                                           \
    _Pragma("unroll") for (int e = 0; e < 4; ++e) { s_ = dot2(xrow[e], ones4[0], s_); q_ = dot2(xrow[e], xrow[e], q_); } \
    s_ += dpp<0x128>(s_); q_ += dpp<0x128>(q_);                                                                     \
    s_ += dpp<0x124>(s_); q_ += dpp<0x124>(q_);                                                                     \
    s_ += dpp<0x4E>(s_); q_ += dpp<0x4E>(q_);                                                                       \
    s_ += dpp<0xB1>(s_); q_ += dpp<0xB1>(q_);                                                                       \
    red2g(s_, q_);                                                                                                  \
    float r_, m_;                                                                                                   \
    ln_fin(s_, q_, 1.0f / 512, r_, m_);                                                                             \
    VM(st16_sc1((u32x4){__float_as_uint(r_), tlo, __float_as_uint(m_), tlo}, l16 << 22, sws, WS_XG + (P * 128 + (t * 8 + c) * 8 + w) * 16)); \
  }
#define GATHER_XS()                                                                                                 \
  WAITQ(mxg); PIN(xg);                                                                                              \
  poll_gran(xg, i16 * 16, sws, WS_XG + (P * 128 + 16 * w) * 16, tlo);                                               \
  sts4f(L_XS + (16 * w + i16) * 4, __uint_as_float(xg[0]));                                                         \
  sts4f(L_XS + 512 + (16 * w + i16) * 4, __uint_as_float(xg[2]));
#define RELANE() const int lane = opq(l16) >> 4, i16 = lane & 15, g = lane >> 4, tid = w * 64 + lane; (void)tid; (void)g; (void)i16
#define CLEAR_H() if (cw == 0) { const u32x4 f_ = {~0u, ~0u, ~0u, ~0u}; const u32 o_ = WS_HF + ((4 * p + rw) * 24 + c2) * 1024; \
    st16_sc1(f_, l16, sws, o_); st16_sc1(f_, l16, sws, o_ + 8192); st16_sc1(f_, l16, sws, o_ + 16384); }
#define SET_FLAG(off) st8_sc1((u32x2){tlo, thi}, l16 << 22, sws, (off))
  const int gs = grp_start(Z2S, c2), gn = t ? 5 : grp_start(Z2S, c2 + 1) - gs;
  const int trot = (p & 7) >= 5 ? (p & 7) - 5 : (p & 7);
#define TILE(j, n_) (((j) + trot) >= (n_) ? (j) + trot - (n_) : (j) + trot)
  const u32 zb0 = WS_ZB + (gs * 64 + 8 * w) * 1024 + ux1 * 4096;
#define DMA_WOUT(e)                                                                                                 \
  {                                                                                                                 \
    const rsrc_t so_ = __builtin_amdgcn_make_buffer_rsrc((void*)wout, 0, 688 * 1024, 0x00020000);                   \
    const int ws_ = grp_start(Z2S, w), wn_ = grp_start(Z2S, w + 1) - ws_;                                           \
    if ((e) < 6) dma16(L_WOUT + ((e) < 4 ? 2 * w + ((e) >> 1) : 16 + w) * 2048 + ((e) & 1) * 1024, vwo, so_, ((ws_ + TILE((e) >> 1, wn_)) * 2 + ((e) & 1)) * 8192); \
    else if ((e) == 6) dma16(L_WOUT + 24 * 2048 + w * 1024, vwo, so_, ((((w >> 1) == 0 ? 5 : ((w >> 1) == 1 ? 11 : ((w >> 1) == 2 ? 27 : 64))) + TILE(3, 6)) * 2 + (w & 1)) * 8192); \
  }
#define DMA_WOUT_AT(dst, e)                                                                                         \
  {                                                                                                                 \
    const rsrc_t so_ = __builtin_amdgcn_make_buffer_rsrc((void*)wout, 0, 688 * 1024, 0x00020000);                   \
    const int ws_ = grp_start(Z2S, w), wn_ = grp_start(Z2S, w + 1) - ws_;                                           \
    dma16((dst) + ((e) - 7) * 1024, vwo, so_, ((ws_ + TILE(wn_ - 2 + (((e) - 7) >> 1), wn_)) * 2 + (((e) - 7) & 1)) * 8192); \
  }
#define VWO() const u32 vwo = ((lane >> 2) & 7) * 1024 + c2 * 128 + (lane >> 5) * 64 + (((lane & 3) ^ (((lane >> 4) & 1) << 1)) * 16)
#define LOAD_B(u, so_)                                                                                              \
  bfr[u][0] = ld16_bo<0>(l16, sws, so_); bfr[u][1] = ld16_bo<1024>(l16, sws, so_);                                  \
  bfr[u][2] = ld16_bo<2048>(l16, sws, so_); bfr[u][3] = ld16_bo<3072>(l16, sws, so_);                               \
  bfr[u][4] = ld16_bo<0>(l16, sws, (so_) ^ 4096); bfr[u][5] = ld16_bo<1024>(l16, sws, (so_) ^ 4096);                \
  bfr[u][6] = ld16_bo<2048>(l16, sws, (so_) ^ 4096); bfr[u][7] = ld16_bo<3072>(l16, sws, (so_) ^ 4096);

  if constexpr (t == 0) {
    int vq = 2;
    const rsrc_t sw = __builtin_amdgcn_make_buffer_rsrc((void*)win, 0, 512 * 2752, 0x00020000);
    const int z2s = grp_start(Z2S, c), z2n = grp_start(Z2S, c + 1) - z2s;
    const int z1s = grp_start(Z1S, c), z1n = grp_start(Z1S, c + 1) - z1s;
    u32 vw[3];
    int wch[3];
#pragma unroll
    for (int e = 0; e < 3; ++e) {
      const int sg = 2 * e + (lane >> 5), hi = sg >= 3;
      wch[e] = (sg - 3 * hi) * 4 + ((lane & 3) ^ (hi << 1));
      vw[e] = wch[e] < 2 * z2n ? (16 * w + 8 * hi + ((lane >> 2) & 7)) * 2752 + 1376 + 32 * z2s + 16 * wch[e] : 0x7FFF0000u;
    }
    const u32 w3k = w * 3072;
#define DMA_W(bk, e) DMA_C(w3k, LZ_W + ((bk) % 3) * 24576 + (e) * 1024, vw[e], sw, ((2 * (bk) + rot) & 7) * (64 * 2752))
#define UBASE(u) ((u) % 5 == 0 ? LZ_U : ((u) % 5 - 1) * 16384)
#define ISS_X(n) do { VM(DMA_X(((n) & 3) * 16384, n, 0)); VM(DMA_X(((n) & 3) * 16384, n, 1)); mx[n] = vq; } while (0)
#define ISS_U(u) do { VM(DMA_U(u, UBASE(u), 0)); VM(DMA_U(u, UBASE(u), 1)); mu[u] = vq; } while (0)
#define ISS_W(bk, e0, e1) do { _Pragma("unroll") for (int e_ = e0; e_ < e1; ++e_) VM(DMA_W(bk, e_)); mw[bk] = vq; } while (0)
    int mx[8], mw[8], mu[16];
    mx[0] = vq;
    ISS_W(0, 0, 3);
    u32x4 xrow;
    VM(xrow = ld16(l16, sx, (c * 8 + w) * 1024));
    const int mxr = vq;
    ISS_X(1);
    const u32 xa0 = opq((32 * rw + i16) * 128 + ((g ^ (i16 >> 1)) * 16)), xa1 = xa0 ^ 64;
    u32 ta[3];
    int tl[3];
    {
      const u32 trb = LZ_W + g * 1536 + ((lane >> 2) & 3) * 64 + ((((g & 1) << 1) | ((lane >> 1) & 1)) * 16) + (lane & 1) * 8;
#pragma unroll
      for (int j = 0; j < 3; ++j) {
        tl[j] = 3 * cw + (j + rw) % 3;
        ta[j] = (trb + (tl[j] >> 1) * 512) ^ ((tl[j] & 1) * 32);
      }
    }
#define NEXT_BLOCK(bk) _Pragma("unroll") for (int j = 0; j < 3; ++j) ta[j] = opq(ta[j] + ((bk) == 0 ? 0 : ((bk) % 3 == 0 ? -49152 : 24576)))
    f32x4 acc[2][3], csa = zero4;
#pragma unroll
    for (int j = 0; j < 3; ++j) acc[0][j] = acc[1][j] = zero4;
    u32x4 xf[16][2], wf[2][3];
    u32x4 xg;
    int mxg = 0;
    WAITQ(mw[0]);
    BARRIER();
    USE_EPOCH()
#define P1_MEM(n, h)                                                                                                \
  {                                                                                                                 \
    if ((n) == 0) { if ((h) == 0) ISS_X(2); if ((h) == 1) ISS_W(1, 0, 2); if ((h) == 2) ISS_W(1, 2, 3); if ((h) == 3) ISS_X(3); } \
    if ((n) == 1) { if ((h) == 0) ISS_W(2, 0, 2); if ((h) == 1) ISS_W(2, 2, 3); if ((h) == 2) VM(DMA_X(0, 4, 0)); if ((h) == 3) { VM(DMA_X(0, 4, 1)); mx[4] = vq; } } \
    if ((n) == 2 || (n) == 4) { if ((h) < 2) { VM(DMA_X((((n) + 3) & 3) * 16384, (n) + 3, (h) & 1)); mx[(n) + 3] = vq; } else ISS_W((n) / 2 + 2, (h) - 2, (h) - 1); } \
    if ((n) == 3) { if ((h) == 0) ISS_W(3, 2, 3); else if ((h) < 3) { VM(DMA_X(32768, 6, (h) - 1)); mx[6] = vq; } else ISS_U(0); } \
    if ((n) == 5) { if ((h) == 0) ISS_W(4, 2, 3); if ((h) == 1) { VM(xg = ld16_sc1(i16 * 16, sws, WS_XG + (P * 128 + 16 * w) * 16)); mxg = vq; } \
                    if ((h) >= 2) { VM(DMA_U(1, UBASE(1), (h) - 2)); mu[1] = vq; } }                                 \
    if ((n) == 6) { if ((h) < 3) ISS_W(5, h, (h) + 1); else ISS_U(2); }                                             \
    if ((n) == 7) { if ((h) < 2) { VM(DMA_U(3, UBASE(3), h)); mu[3] = vq; } }                                       \
  }
#pragma unroll
    for (int ks = 0; ks <= 16; ++ks) {
      const int n = ks >> 1, kk = ks & 1;
      if (ks > 0 && ks < 16 && kk == 0) {
        { const int mk = (n & 1) ? mx[n] : (mx[n] > mw[n >> 1] ? mx[n] : mw[n >> 1]); WAITQ(mk); }
        BARRIER();
      }
      if (ks < 16 && (ks & 3) == 0) { NEXT_BLOCK(ks >> 2); }
      if (ks == 8) {
#pragma unroll
        for (int e = 0; e < 3; ++e)
          vw[e] = wch[e] < 2 * z1n ? (16 * w + 8 * (2 * e + (lane >> 5) >= 3) + ((lane >> 2) & 7)) * 2752 + 32 * z1s + 16 * wch[e] : 0x7FFF0000u;
      }
      const int k1 = ks > 0 ? ks - 1 : 0, q1 = k1 & 1;
#pragma unroll
      for (int m = 0; m < 7; ++m) {
        if (ks > 0) {
          if (m < 6) acc[m & 1][m >> 1] = mfma32(xf[k1][m & 1], wf[q1][m >> 1], acc[m & 1][m >> 1]);
          else csa = mfma32(ones4, wf[q1][0], csa);
          SCHED();
        }
        if (ks < 16) {
          if (m == 0) { P1_MEM(n, 2 * kk) xf[ks & 15][0] = lds16((kk ? xa1 : xa0) + (n & 3) * 16384); xf[ks & 15][1] = lds16((kk ? xa1 : xa0) + (n & 3) * 16384 + 2048); }
          else if (m < 4) wf[kk][m - 1] = ldfrag(ta[m - 1] + (ks & 3) * 6144, 256);
          if (m == 4) P1_MEM(n, 2 * kk + 1)
        }
        SCHED();
      }
#pragma unroll
      for (int j = 0; j < 3; ++j) { PIN(acc[0][j]); PIN(acc[1][j]); }
      PIN(csa);
      if (ks == 2) EARLY_STATS(mxr)
    }
    GATHER_XS()
    sts4f(L_CS + tl[0] * 64 + i16 * 4, csa[0]);
    WAITQ(mu[1] > mw[4] ? mu[1] : mw[4]);
    READ_U(0, UBASE(0)) READ_U(1, UBASE(1))
    BARRIER();
    u32x4 zp[3];
    ISS_W(6, 0, 3);
    {
      f32x2 ps[4], pq[4];
      float cs[3];
#pragma unroll
      for (int j = 0; j < 3; ++j) cs[j] = -lds4f(L_CS + tl[j] * 64 + i16 * 4);
#pragma unroll
      for (int rt = 0; rt < 2; ++rt) {
        const f32x4 rr = lds16f(L_XS + (32 * rw + 8 * g + 4 * rt) * 4), mm = lds16f(L_XS + 512 + (32 * rw + 8 * g + 4 * rt) * 4);
        f32x2 z0[3], z1[3];
#pragma unroll
        for (int j = 0; j < 3; ++j) {
          z0[j] = gelu2(fma2(lo2(mm), bc2(cs[j]), lo2(acc[rt][j])) * lo2(rr));
          z1[j] = gelu2(fma2(hi2(mm), bc2(cs[j]), hi2(acc[rt][j])) * hi2(rr));
          zp[j][2 * rt] = pk2v(z0[j]); zp[j][2 * rt + 1] = pk2v(z1[j]);
        }
        ps[2 * rt] = z0[0] + z0[1] + z0[2]; ps[2 * rt + 1] = z1[0] + z1[1] + z1[2];
        pq[2 * rt] = fma2(z0[2], z0[2], fma2(z0[1], z0[1], z0[0] * z0[0]));
        pq[2 * rt + 1] = fma2(z1[2], z1[2], fma2(z1[1], z1[1], z1[0] * z1[0]));
        SCHED();
        if (rt == 0) { ISS_U(4); ISS_U(5); }
        else ISS_U(6);
        SCHED();
      }
      const bool b3 = lane & 8, b2 = lane & 4, b1 = lane & 2;
#pragma unroll
      for (int m = 0; m < 2; ++m)
#pragma unroll
        for (int e = 0; e < 2; ++e) {
          ps[m][e] = (b3 ? ps[m + 2][e] : ps[m][e]) + dpp<0x128>(b3 ? ps[m][e] : ps[m + 2][e]);
          pq[m][e] = (b3 ? pq[m + 2][e] : pq[m][e]) + dpp<0x128>(b3 ? pq[m][e] : pq[m + 2][e]);
        }
#pragma unroll
      for (int e = 0; e < 2; ++e) {
        ps[0][e] = (b2 ? ps[1][e] : ps[0][e]) + dpp<0x141>(b2 ? ps[0][e] : ps[1][e]);
        pq[0][e] = (b2 ? pq[1][e] : pq[0][e]) + dpp<0x141>(b2 ? pq[0][e] : pq[1][e]);
      }
      float s_ = (b1 ? ps[0][1] : ps[0][0]) + dpp<0x4E>(b1 ? ps[0][0] : ps[0][1]);
      float q_ = (b1 ? pq[0][1] : pq[0][0]) + dpp<0x4E>(b1 ? pq[0][0] : pq[0][1]);
      s_ += dpp<0xB1>(s_);
      q_ += dpp<0xB1>(q_);
      const int row = 32 * rw + 8 * g + (i16 >> 1);
      VM(st16_sc1((u32x4){__float_as_uint(s_), tlo, __float_as_uint(q_), tlo}, row * 16 + ((lane & 1) << 30), sws, WS_SG + ((P * 8 + c) * 2 + cw) * 2048));
      PIN(zp[0]); PIN(zp[1]); PIN(zp[2]);
      CLEAR_H()
    }
    {
    RELANE();
#pragma unroll
    for (int j = 0; j < 3; ++j) acc[0][j] = acc[1][j] = zero4;
    csa = zero4;
    u32x4 q0, q1, q2, q3;
    int mq = 0;
    const u32 gvo = (2 * g) * 4096 + (32 * rw + 16 * cw + i16) * 16;
    const u32 gso = WS_SG + P * 32768;
#pragma unroll
    for (int ks = 0; ks <= 16; ++ks) {
      if (ks == 8) {
        WAITQ(mq); PIN(q0); PIN(q1); PIN(q2); PIN(q3);
#pragma nounroll
        for (int it = 0; it < SPIN_CAP; ++it) {
          if (__all(tag_ok(q0, tlo) && tag_ok(q1, tlo) && tag_ok(q2, tlo) && tag_ok(q3, tlo))) break;
          __builtin_amdgcn_s_sleep(6);
          asm volatile(
              "buffer_load_dwordx4 %0, %4, %5, %6 offen sc1\n\t"
              "buffer_load_dwordx4 %1, %4, %5, %6 offen offset:2048 sc1\n\t"
              "buffer_load_dwordx4 %2, %4, %5, %7 offen sc1\n\t"
              "buffer_load_dwordx4 %3, %4, %5, %7 offen offset:2048 sc1\n\t"
              "s_waitcnt vmcnt(0)"
              : "=&v"(q0), "=&v"(q1), "=&v"(q2), "=&v"(q3)
              : "v"(gvo), "s"(sws), "s"(gso), "s"(gso + 4096)
              : "memory");
        }
        float s = (__uint_as_float(q0[0]) + __uint_as_float(q1[0])) + (__uint_as_float(q2[0]) + __uint_as_float(q3[0]));
        float q = (__uint_as_float(q0[2]) + __uint_as_float(q1[2])) + (__uint_as_float(q2[2]) + __uint_as_float(q3[2]));
        red2g(s, q);
        float r2, m2;
        ln_fin(s, q, 1.0f / 688, r2, m2);
        sts4f(L_ZST + (32 * rw + 16 * cw + i16) * 4, r2);
        sts4f(L_ZST + 512 + (32 * rw + 16 * cw + i16) * 4, -m2 * r2);
      }
      if (ks < 16 && (ks & 3) == 0) {
        if (ks > 0) { WAITQ(ks == 12 ? vq : mw[4 + (ks >> 2)]); BARRIER(); }
        if (ks == 12) { if (w == 0) SET_FLAG(WS_FA + (P * 8 + c) * 128); }
        NEXT_BLOCK(4 + (ks >> 2));
      }
      if (ks == 8) {
#pragma unroll
        for (int rt = 0; rt < 2; ++rt) {
          const u32 ra = L_ZST + (32 * rw + 8 * g + 4 * rt) * 4;
          const f32x4 r4 = lds16f(ra), m4 = lds16f(ra + 512);
#pragma unroll
          for (int j = 0; j < 3; ++j) {
            const u32 u0 = zp[j][2 * rt], u1 = zp[j][2 * rt + 1];
            zp[j][2 * rt] = pk2v(fma2((f32x2){blo(u0), bhi(u0)}, lo2(r4), lo2(m4)));
            zp[j][2 * rt + 1] = pk2v(fma2((f32x2){blo(u1), bhi(u1)}, hi2(r4), hi2(m4)));
          }
        }
#pragma unroll
        for (int j = 0; j < 3; ++j)
          VM(st16_sc1(zp[j], l16, sws, (tl[j] < z2n) ? WS_ZB + ((z2s + tl[j]) * 64 + 4 * P + rw) * 1024 : 0x7FFF0000u));
        SCHED();
      }
      if (ks >= 5 && ks <= 9) { WAITQ(mu[ks - 3]); READ_U(ks - 3, UBASE(ks - 3)) }
      if (ks >= 9 && ks <= 13) { WAITQ(mu[ks - 2]); READ_U(ks - 2, UBASE(ks - 2)) }
      if (ks >= 13) { WAITQ(mu[ks - 1]); READ_U(ks - 1, UBASE(ks - 1)) }
      const int k1 = ks > 0 ? ks - 1 : 0, q1_ = k1 & 1;
#pragma unroll
      for (int m = 0; m < 7; ++m) {
        if (ks > 0) {
          if (m < 6) acc[m & 1][m >> 1] = mfma32(wf[q1_][m >> 1], xf[k1][m & 1], acc[m & 1][m >> 1]);
          else csa = mfma32(ones4, wf[q1_][0], csa);
          SCHED();
        }
        if (ks < 16 && m >= 1 && m < 4) wf[ks & 1][m - 1] = ldfrag(ta[m - 1] + (ks & 3) * 6144, 256);
        if (m == 0 || m == 4) {
          const int h = m == 4;
          if (h == 0 && ks >= 6 && ks <= 13) asm volatile("s_waitcnt lgkmcnt(8)" ::: "memory");
          if (ks >= 6 && ks <= 10) { VM(DMA_U(ks + 1, UBASE(ks + 1), h)); mu[ks + 1] = vq; }
          if (ks >= 10 && ks <= 13) { VM(DMA_U(ks + 2, UBASE(ks + 2), h)); mu[ks + 2] = vq; }
        }
        if (m == 6) {
          if (ks >= 4 && ks <= 6) ISS_W(7, ks - 4, ks - 3);
        }
        if (m == 2) {
          if (ks == 5) { VM(q0 = ld16_sc1o<0>(gvo, sws, gso)); VM(q1 = ld16_sc1o<2048>(gvo, sws, gso)); }
          if (ks == 6) { VM(q2 = ld16_sc1o<0>(gvo, sws, gso + 4096)); VM(q3 = ld16_sc1o<2048>(gvo, sws, gso + 4096)); mq = vq; }
        }
        SCHED();
      }
#pragma unroll
      for (int j = 0; j < 3; ++j) { PIN(acc[0][j]); PIN(acc[1][j]); }
      PIN(csa);
    }
    sts4f(L_CS + 512 + tl[0] * 64 + i16 * 4, csa[0]);
    gf = ld8_sc1((lane >> 5) * 1024, sws, WS_FA + (16 * w + c2) * 128);
    BARRIER();
    VWO();
    DMA_WOUT(0) DMA_WOUT(1)
    WAITV(2);
    PIN(gf);
    if (!__all(gf[0] == tlo && gf[1] == thi)) poll_flags((lane >> 5) * 1024, true, sws, WS_FA + (16 * w + c2) * 128, tlo, thi);
    {
      float xr[2], xm[2];
#pragma unroll
      for (int rt = 0; rt < 2; ++rt) {
        xr[rt] = lds4f(L_XS + (32 * rw + 8 * (i16 >> 2) + 4 * rt + (i16 & 3)) * 4);
        xm[rt] = -lds4f(L_XS + 512 + (32 * rw + 8 * (i16 >> 2) + 4 * rt + (i16 & 3)) * 4);
      }
      const u32 so0 = zb0 + TILE(0, gn) * 65536;
#pragma unroll
      for (int j = 0; j < 3; ++j) {
        const f32x4 cc = lds16f(L_CS + 512 + tl[j] * 64 + g * 16);
        bfr[0][3 * j] = ld16_b(l16, sws, so0 ^ (3 * j * 1024));
        bfr[0][3 * j + 1] = ld16_b(l16, sws, so0 ^ ((3 * j + 1) * 1024));
        if (j < 2) bfr[0][3 * j + 2] = ld16_b(l16, sws, so0 ^ ((3 * j + 2) * 1024));
        u32x4 zz;
#pragma unroll
        for (int rt = 0; rt < 2; ++rt) {
          zz[2 * rt] = pk2v(gelu2(fma2(lo2(cc), bc2(xm[rt]), lo2(acc[rt][j])) * bc2(xr[rt])));
          zz[2 * rt + 1] = pk2v(gelu2(fma2(hi2(cc), bc2(xm[rt]), hi2(acc[rt][j])) * bc2(xr[rt])));
        }
        st16_sc1(zz, l16, sws, (tl[j] < z1n) ? WS_Z1 + ((4 * P + rw) * 43 + z1s + tl[j]) * 1024 : 0x7FFF0000u);
        SCHED();
      }
    }
    }
  } else {
    int vq = 2;
    const rsrc_t sw = __builtin_amdgcn_make_buffer_rsrc((void*)wqkv, 0, 512 * 3072, 0x00020000);
    const u32 vw = (8 * w + ((lane >> 2) & 7)) * 3072 + 128 * c + ((lane >> 5) * 4 + ((lane & 3) ^ ((w & 1) << 1))) * 16;
    const u32 w3k = w * 3072;
#define DMA_S(n, e)                                                                                                 \
  do {                                                                                                              \
    if ((e) < 2) DMA_X(((n) % 3) * 40960, n, (e) & 1);                                                              \
    else DMA_CC(w3k, ((n) % 3) * 40960 + 16384 + ((e) - 2) * 1024, vw, sw, "s_add_u32", (((n) + rot) & 7) * (64 * 3072), ((e) - 2) * 1024); \
  } while (0)
#define ISS_Q(u, base) do { VM(DMA_U(u, base, 0)); VM(DMA_U(u, base, 1)); mu[u] = vq; } while (0)
    int ms[8], mu[16];
    VM(DMA_S(0, 2)); VM(DMA_S(0, 3)); VM(DMA_S(0, 4)); ms[0] = vq;
    u32x4 xrow;
    VM(xrow = ld16(l16, sx, ((8 + c) * 8 + w) * 1024));
    const int mxr = vq;
#pragma unroll
    for (int e = 0; e < 5; ++e) { VM(DMA_S(1, e)); ms[1] = vq; }
    const int l32 = lane & 31, hh = lane >> 5;
    u32 xa = (32 * rw + l32) * 128 + ((hh ^ ((l32 >> 1) & 7)) * 16);
    u32 wa = 16384 + hh * 3072 + cw * 512 + ((lane >> 2) & 3) * 64 + (((((g & 1) << 1) | ((lane >> 1) & 1)) ^ (hh << 1)) * 16) + (lane & 1) * 8;
    u32 ca = 16384 + g * 3072 + min(w, 5) * 512 + ((lane >> 2) & 3) * 64 + ((((g & 1) << 1) | ((lane >> 1) & 1)) * 16) + (lane & 1) * 8;
    const f32x16 zero16 = {0, 0, 0, 0, 0, 0, 0, 0, 0, 0, 0, 0, 0, 0, 0, 0};
    f32x16 acc[3] = {zero16, zero16, zero16};
    f32x4 csa[2] = {zero4, zero4};
    u32x4 xg;
    int mxg = 0;
    WAITQ(ms[0]);
    BARRIER();
    USE_EPOCH()
    {
      u32x4 xf[2], wf[2][4];
#pragma unroll
      for (int n = 0; n <= 8; ++n)
#pragma unroll
      for (int sq = 0; sq < 4; ++sq) {
        const int s = 4 * n + sq;
        if (s > 32) continue;
        if (s < 32 && sq == 0) {
          if (n > 0) {
            WAITQ(ms[n]);
            BARRIER();
          }
          const int d = n == 0 ? 0 : (n % 3 == 0 ? -81920 : 40960);
          xa = opq(xa + d); wa = opq(wa + d); ca = opq(ca + d);
        }
        const int q1 = (s + 1) & 1, q0 = s & 1;
#pragma unroll
        for (int m = 0; m < 4; ++m) {
          if (s > 0) {
            if (m < 3) acc[m] = mfmaL(wf[q1][m], xf[q1], acc[m]);
            else csa[q1] = mfma32(ones4, wf[q1][3], csa[q1]);
            SCHED();
          }
          if (s < 32) {
            if (m == 0) { xf[q0] = lds16(xa ^ (32 * sq)); wf[q0][0] = ldfrag(wa + sq * 6144, 256); }
            if (m == 1) { wf[q0][1] = ldfrag(wa + sq * 6144 + 1024, 256); wf[q0][2] = ldfrag(wa + sq * 6144 + 2048, 256); }
            if (m == 2) wf[q0][3] = ldfrag((ca ^ (32 * (sq & 1))) + (sq >> 1) * 12288, 256);
            if (m == 0 || m == 2) {
              const int h = 2 * sq + (m == 2);
              if (n <= 5 && h < 5) { VM(DMA_S(n + 2, h)); ms[n + 2] = vq; }
              if (n == 0 && h == 5) {
                rsrc_t sp = __builtin_amdgcn_make_buffer_rsrc((void*)wphi, 0, 16384, 0x00020000);
                if (w < 2) dma16(L_WPHI + w * 1024, l16, sp, c * 2048 + w * 1024);
              }
              if (n == 4 && h == 5) { VM(xg = ld16_sc1(i16 * 16, sws, WS_XG + (P * 128 + 16 * w) * 16)); mxg = vq; }
              if (n == 6) { VM(DMA_U(sq, (sq < 2 ? LQ_M : LQ_A) + (sq & 1) * 16384, h & 1)); mu[sq] = vq; }
              if (n == 7 && sq < 2) { VM(DMA_U(4 + sq, sq * 16384, h & 1)); mu[4 + sq] = vq; }
            }
          }
          SCHED();
        }
        PIN(acc[0]); PIN(acc[1]); PIN(acc[2]); PIN(csa[0]); PIN(csa[1]);
        if (s == 3) EARLY_STATS(mxr)
      }
    }
    GATHER_XS()
    sts4f(L_CS + w * 128 + i16 * 4, csa[0][0]);
    sts4f(L_CS + w * 128 + 64 + i16 * 4, csa[1][0]);
    BARRIER();
    WAITQ(mu[3]);
    READ_U(0, LQ_M) READ_U(1, LQ_M + 16384) READ_U(2, LQ_A) READ_U(3, LQ_A + 16384)
    const float xr = lds4f(L_XS + (32 * rw + l32) * 4), xm = -lds4f(L_XS + 512 + (32 * rw + l32) * 4);
    u32x4 wp0, wp1;
    {
      const u32 a = L_WPHI + (2 * cw + (lane & 1)) * 512 + (4 * hh + ((lane >> 2) & 3)) * 32 + (2 * (g & 1) + ((lane >> 1) & 1)) * 8;
      const u32x4 f = ldfrag(a, 256);
      const bool own = (lane >> 2) & 1;
#pragma unroll
      for (int e = 0; e < 4; ++e) { wp0[e] = own ? 0u : f[e]; wp1[e] = own ? f[e] : 0u; }
    }
#define FEATURES(e_, dst, hook)                                                                                         \
  {                                                                                                                 \
    u32x4 kp[2];                                                                                                    \
    _Pragma("unroll") for (int a = 0; a < 4; ++a) {                                                                \
      const f32x4 cc = lds16f(L_CS + (2 * (e_) + cw) * 128 + a * 32 + hh * 16);                                      \
      kp[a >> 1][2 * (a & 1)] = pk2v(fma2(lo2(cc), bc2(xm), (f32x2){acc[e_][4 * a], acc[e_][4 * a + 1]}));          \
      kp[a >> 1][2 * (a & 1) + 1] = pk2v(fma2(hi2(cc), bc2(xm), (f32x2){acc[e_][4 * a + 2], acc[e_][4 * a + 3]}));  \
    }                                                                                                               \
    f32x16 pl = mfmaL(wp0, kp[0], zero16);                                                                          \
    pl = mfmaL(wp1, kp[1], pl);                                                                                     \
    hedgehog16(pl, xr, dst, hook);                                                                                  \
  }
    {
      const int s_ = 32 * rw + l32;
      const u32 key = (((s_ >> 1) & 3) ^ (((s_ >> 3) & 1) << 1)) * 16;
      {
        const u32 va = LQ_V + (s_ >> 3) * 1024 + cw * 512 + (s_ & 7) * 64 + hh * 8;
#pragma unroll
        for (int a = 0; a < 4; ++a) {
          const f32x4 cc = lds16f(L_CS + (4 + cw) * 128 + a * 32 + hh * 16);
          sts8(va + ((a * 16) ^ key), (u32x2){pk2v(fma2(lo2(cc), bc2(xm), (f32x2){acc[2][4 * a], acc[2][4 * a + 1]}) * bc2(xr)),
                                              pk2v(fma2(hi2(cc), bc2(xm), (f32x2){acc[2][4 * a + 2], acc[2][4 * a + 3]}) * bc2(xr))});
        }
      }
      WAITQ(mu[5]);
      READ_U(4, 0) READ_U(5, 16384)
      WAITL0();
      u32x4 ph[4];
      auto hk = [&](int i) __attribute__((always_inline)) {
        const int u = 6 + (i >> 1);
        SCHED();
        VM(DMA_U(u, (u < 8 ? LQ_M : (u < 10 ? LQ_A : 0)) + (u & 1) * 16384, i & 1));
        mu[u] = vq;
        SCHED();
      };
      FEATURES(1, ph, hk)
      const u32 pa = LQ_PHI + (s_ >> 3) * 2048 + (2 * cw + hh) * 512 + (s_ & 7) * 64;
#pragma unroll
      for (int a = 0; a < 4; ++a) sts16(pa + ((a * 16) ^ key), ph[a]);
    }
    BARRIER();
    {
      const u32 kx = (((lane >> 3) & 1) ^ ((g & 1) << 1)) * 16;
      const u32 pa = LQ_PHI + g * 2048 + (w >> 1) * 512 + ((lane >> 2) & 3) * 64 + (((2 * (w & 1) + ((lane >> 1) & 1)) * 16) ^ kx) + (lane & 1) * 8;
      const u32 va = LQ_V + g * 1024 + (w >> 2) * 512 + ((lane >> 2) & 3) * 64 + (((2 * ((w >> 1) & 1) + ((lane >> 1) & 1)) * 16) ^ kx) + (lane & 1) * 8;
      u32x4 pf[4], vf[4];
#pragma unroll
      for (int k = 0; k < 4; ++k) {
        pf[k] = cat(ldstr(pa + k * 8192), ldstr((pa ^ 32) + k * 8192 + 256));
        vf[k] = cat(ldstr(va + k * 4096), ldstr((va ^ 32) + k * 4096 + 256));
      }
      f32x4 kv = zero4, kz = zero4;
#pragma unroll
      for (int k = 0; k < 4; ++k) { kv = mfma32(pf[k], vf[k], kv); kz = mfma32(pf[k], ones4, kz); }
      const u32 so = WS_KP + (c * 16 + P) * KP_SZ;
      u32x4 g0 = {__float_as_uint(kv[0]), tlo, __float_as_uint(kv[1]), tlo}, g1 = {__float_as_uint(kv[2]), tlo, __float_as_uint(kv[3]), tlo};
      u32x4 g2 = {__float_as_uint((lane & 1) ? kz[2] : kz[0]), tlo, __float_as_uint((lane & 1) ? kz[3] : kz[1]), tlo};
      PIN(g0); PIN(g1); PIN(g2);
      asm volatile("s_nop 7" ::: "memory");
      VM(st16_sc1(g0, l16, sws, so + w * 2048));
      VM(st16_sc1(g1, l16, sws, so + w * 2048 + 1024));
      VM(st16_sc1(g2, g * 32 + (lane & 1) * 16 + ((i16 >> 1) << 26), sws, so + 16384 + w * 128));
      CLEAR_H()
    }
    WAITQ(mu[11]);
    READ_U(6, LQ_M) READ_U(7, LQ_M + 16384) READ_U(8, LQ_A) READ_U(9, LQ_A + 16384) READ_U(10, 0) READ_U(11, 16384)
    BARRIER();
    ISS_Q(12, LQ_M); VM(DMA_U(13, LQ_M + 16384, 0));
    u32x4 phq[4];
    {
      VWO();
      auto hk = [&](int i) __attribute__((always_inline)) {
        SCHED();
        if (i == 0) DMA_U(13, LQ_M + 16384, 1);
        else if (i < 5) DMA_U(14 + ((i - 1) >> 1), LQ_A + ((i - 1) >> 1) * 16384, (i - 1) & 1);
        else DMA_WOUT(i - 5)
        SCHED();
      };
      FEATURES(0, phq, hk)
    }
    PIN(phq[0]); PIN(phq[1]); PIN(phq[2]); PIN(phq[3]);
    if (w >= 5) {
      const int id = 2 * P + (w == 7 ? (lane & 1) : w - 5), wq = id >> 2, gq = id & 3;
      const u32 vi0 = (4 * g) * KP_SZ + (w == 7 ? 16384 + wq * 128 + gq * 32 : wq * 2048 + (gq * 16 + i16) * 16);
      const u32 vi1 = vi0 + (w == 7 ? 16 : 1024);
      const u32 vo = (w == 7 ? (WS_KZ - WS_KF) + c * 512 + id * 16 + ((i16 >> 1) << 25) : c * 8192 + id * 256 + i16 * 16) + (g << 28);
      float sa[4];
#pragma nounroll
      for (int it = 0; it < SPIN_CAP; ++it) {
        u32x4 a[4][2];
#pragma unroll
        for (int o = 0; o < 4; ++o) {
          a[o][0] = ld16_sc1(vi0, sws, WS_KP + (c * 16 + o) * KP_SZ);
          a[o][1] = ld16_sc1(vi1, sws, WS_KP + (c * 16 + o) * KP_SZ);
        }
        WAITV(0);
        bool ok = true;
#pragma unroll
        for (int o = 0; o < 4; ++o) { PIN(a[o][0]); PIN(a[o][1]); ok = ok && tag_ok(a[o][0], tlo) && tag_ok(a[o][1], tlo); }
#pragma unroll
        for (int e = 0; e < 4; ++e)
          sa[e] = (__uint_as_float(a[0][e >> 1][2 * (e & 1)]) + __uint_as_float(a[1][e >> 1][2 * (e & 1)])) +
                  (__uint_as_float(a[2][e >> 1][2 * (e & 1)]) + __uint_as_float(a[3][e >> 1][2 * (e & 1)]));
        if (__all(ok)) break;
        __builtin_amdgcn_s_sleep(6);
      }
      red2g(sa[0], sa[1]);
      red2g(sa[2], sa[3]);
      st16_sc1((u32x4){pk2(sa[0], sa[1]), tlo, pk2(sa[2], sa[3]), tlo}, vo, sws, WS_KF);
    }
    gf = ld8_sc1((lane >> 5) * 1024, sws, WS_FA + (16 * w + c2) * 128);
    WAITV(8);
    READ_U(12, LQ_M) READ_U(13, LQ_M + 16384) READ_U(14, LQ_A) READ_U(15, LQ_A + 16384)
    BARRIER();
#pragma unroll
    for (int a = 0; a < 4; ++a) sts16(L_WO1 + w * 4096 + a * 1024 + l16, phq[a]);
    WAITV(0);
    PIN(gf);
    if (!__all(gf[0] == tlo && gf[1] == thi)) poll_flags((lane >> 5) * 1024, true, sws, WS_FA + (16 * w + c2) * 128, tlo, thi);
    { const u32 so_ = zb0 + TILE(0, gn) * 65536; LOAD_B(0, so_) }
  }

  {
  RELANE();
  u32x4 kfg, kzg;
  const u32 kfo = WS_KF + c * 8192 + w * 1024, kzo = WS_KZ + c * 512 + w * 64;
  if (t != 0) {
    kfg = ld16_sc1(l16, sws, kfo);
    kzg = ld16_sc1(g * 16, sws, kzo);
  }
  { const u32 so_ = zb0 + TILE(1, gn) * 65536; LOAD_B(1, so_) }
  const u32 redw = opq(L_RED + w * 4096 + lane * 16), redr = opq(L_RED + (rw ^ (2 * ux0)) * 1024 + lane * 16);
  const u32 f3s = __builtin_amdgcn_readfirstlane(WS_FB + (P * 8 + (c2 > 0 ? c2 - 1 : 0)) * 128);
  const u32 vw3 = ((lane >> 2) & 7) * 1024 + c2 * 128 + (lane >> 5) * 64;
  const rsrc_t swo = __builtin_amdgcn_make_buffer_rsrc((void*)wo, 0, 512 * 1024, 0x00020000);
#define DMA_WO(dst, half, e) dma16((dst) + w * 4096 + (e) * 1024, (vw3 + (lane & 3) * 16) ^ (((e) & 1) * 32), swo, ((w + 8 * (half)) * 4 + (e)) * 8192)
#define FV() ((lane >> 5) * (WS_FC - WS_FB) + (P * 8 + ((lane >> 2) & 7)) * 128)
  u32x2 z1r[6], hold;
  u32 bs;
  f32x4 sm0;
#define EMIT_H(jt, sm_)                                                                                             \
  {                                                                                                                 \
    const f32x2 bb = bc2(blo(bs));                                                                                  \
                                                                 \
    const u32 h0 = min(pk2v((f32x2){blo(z1r[jt][0]), bhi(z1r[jt][0])} * (lo2(sm_) + bb)), 0xFFFFFFFEu);             \
    const u32 h1 = min(pk2v((f32x2){blo(z1r[jt][1]), bhi(z1r[jt][1])} * (hi2(sm_) + bb)), 0xFFFFFFFEu);             \
    const u32 hb_ = WS_HF + ((4 * p + rw) * 24 + c2) * 1024;                                                        \
    if ((jt) == 0 || ((jt) == 2 && gn == 6) || (jt) == gn - 2) hold = (u32x2){h0, h1};                              \
    else if ((jt) == 2) st16_sc1((u32x4){h0, h1, 0u, 0u}, l16, sws, hb_ + 8192);                                    \
    else st16_sc1((u32x4){hold[0], hold[1], h0, h1}, l16, sws, hb_ + ((jt) == 1 ? 0 : ((jt) == gn - 1 ? 16384 : 8192))); \
  }
#pragma unroll
  for (int j = 0; j < 6; ++j) {
    if (j < 5 || gn == 6) {
      const int u = j & 1;
      if (j == 0) WAITV(8);
      if (j == 2) WAITV(t ? 10 : 8);
      if (j == 1) { WAITV(9); WAITV_IF(cw, 1, 8); }
      if (j == 3) { WAITV(9); WAITV_IF(cw, 1, 8); }
      if (j == 4) { if (t) WAITV(6); else { WAITV(13); WAITV_IF(cw, 1, 12); WAITV_IF(gn, 5, 6); } WAITV_IF(2 * gn + cw, 11, 4); }
      if (j == 5) { WAITV(10); WAITV_IF(cw, 1, 9); }
#pragma unroll
      for (int i = 0; i < 8; ++i) PIN(bfr[u][i]);
      {
        f32x4 a_[4];
#pragma unroll
        for (int rt = 0; rt < 4; ++rt) {
          a_[rt] = zero4;
#pragma unroll
          for (int i = 0; i < 8; ++i) a_[rt] = mfma32(bfr[u][i], af[rt][i], a_[rt]);
          SCHED();
          if (rt > 0) sts16f(redw + u * 32768 + (rt - 1) * 1024, a_[rt - 1]);
          SCHED();
        }
        if (j == 0 && t != 0) {
          PIN(kfg); PIN(kzg);
          poll_gran(kfg, l16, sws, kfo, tlo);
          poll_gran(kzg, g * 16, sws, kzo, tlo);
          sts8(L_KFS + (w >> 1) * 1024 + i16 * 64 + (w & 1) * 32 + g * 8, (u32x2){kfg[0], kfg[2]});
          sts8(L_KFS + 4096 + w * 32 + g * 8, (u32x2){kzg[0], kzg[2]});
          sts8(L_KFS + 4352 + (lane & 7) * 8, (u32x2){0u, 0u});
        }
        sts16f(redw + u * 32768 + 3072, a_[3]);
      }
      u32x4 of[2];
      if (j == 1 && t != 0) {
        const int l32 = lane & 31, hh = lane >> 5;
        const bool own = ((lane >> 2) & 1) == hh;
        const u32 ka = own ? L_KFS + (2 * cw + hh) * 1024 + ((l32 & 3) + 4 * (l32 >> 3)) * 64 : L_KFS + 4352;
        const u32 zaa = own ? L_KFS + 4096 + (2 * cw + hh) * 64 : L_KFS + 4352;
        const u32 qa = L_WO1 + w * 4096 + l16;
        const f32x16 zero16 = {0, 0, 0, 0, 0, 0, 0, 0, 0, 0, 0, 0, 0, 0, 0, 0};
        f32x16 den = zero16, num = zero16;
#pragma unroll
        for (int a = 0; a < 4; ++a) {
          const u32x4 pq_ = lds16(qa + a * 1024);
          den = mfmaL(lds16(zaa + a * 16), pq_, den);
          num = mfmaL(lds16(ka + a * 16), pq_, num);
        }
        const float inv = __builtin_amdgcn_rcpf(den[0] + 1e-6f);
#pragma unroll
        for (int a = 0; a < 2; ++a) {
          of[a] = (u32x4){pk2(num[8 * a] * inv, num[8 * a + 1] * inv), pk2(num[8 * a + 2] * inv, num[8 * a + 3] * inv),
                          pk2(num[8 * a + 4] * inv, num[8 * a + 5] * inv), pk2(num[8 * a + 6] * inv, num[8 * a + 7] * inv)};
          PIN(of[a]);
        }
      }
      if (j == (t ? 2 : 0)) WAITV(0);
      BAR_IF(cw, 1, 63);
      if (w == 4) {
        if (j == 0 && t == 0) SET_FLAG(WS_FB + (P * 8 + c) * 128);
        if (j == 2 && t != 0) SET_FLAG(WS_FC + (P * 8 + c) * 128);
      }
      if (j == 1) {
        if (cw == 0) {
          WAITV(0);
          PIN(gf);
          if (!__all(gf[0] == tlo && gf[1] == thi)) poll_flags((lane >> 5) * 128, true, sws, f3s, tlo, thi);
          rsrc_t sb = __builtin_amdgcn_make_buffer_rsrc((void*)bsp, 0, 4096, 0x00020000);
          bs = ld2(i16 * 2, sb, (64 * p + 16 * rw) * 2);
        }
      }
      if ((j == 1 || j == 2) && cw == 0) {
#pragma unroll
        for (int v = 3 * j - 3; v < 3 * j && v < 6 - t; ++v)
          z1r[v] = ld8_sc1(g * 256 + (i16 >> 3) * 64 + (i16 & 3) * 16 + ((i16 >> 2) & 1) * 8, sws,
                           WS_Z1 + ((2 * p + (rw >> 1)) * 43 + gs + TILE(min(v, gn - 1), gn)) * 1024 + (rw & 1) * 128);
      }
      if ((j == 4 && gn == 5) || j == 5) gf = ld8_sc1(FV(), sws, WS_FB);
      if (j == 4 && t == 0) {
        VWO();
#pragma unroll
        for (int e = 2; e < 7; ++e) DMA_WOUT(e)
      }
      const u32 ra_ = redr + ((j + 1) & 1) * 32768;
      f32x4 sm, r_[2];
#pragma unroll
      for (int i = 0; i < 8; ++i) {
        if (j >= 1 && j < (t ? 3 : 4) && (i & 1) == 0 && cw == 0) {
          if (i == 2) sm = r_[0] + r_[1];
          if (i > 2) sm += r_[0] + r_[1];
          r_[0] = lds16f(ra_ + i * 4096); r_[1] = lds16f(ra_ + (i + 1) * 4096);
        }
        SCHED();
        if (j < 3) bfr[u][i] = ld16_b(l16, sws, (zb0 + TILE(j + 2, gn) * 65536) ^ (i * 1024));
        else if (j == 3 && t == 0) ld16_b_if6(bfr[1][i], gn, l16, sws, (zb0 + TILE(5, gn) * 65536) ^ (i * 1024));
        SCHED();
      }
      if (j == 0 && cw == 0) gf = ld8_sc1((lane >> 5) * 128, sws, f3s);
      if (j == 1 && t != 0) {
        const u32 vo = ((lane >> 4) & 1) * 16384 + (lane >> 5) * 512 + i16 * 16;
        st16_sc1(of[0], vo, sws, WS_OF + ((8 * P + 2 * rw) * 16 + 2 * c + cw) * 1024);
        st16_sc1(of[1], vo, sws, WS_OF + ((8 * P + 2 * rw) * 16 + 2 * c + cw) * 1024 + 256);
      }
      if (j == 2) { PIN(bs); _Pragma("unroll") for (int v = 0; v < 3; ++v) PIN(z1r[v]); }
      if (j == 3) { _Pragma("unroll") for (int v = 3; v < 6 - t; ++v) PIN(z1r[v]); }
      if (j >= 1 && cw == 0) {
        if (j < (t ? 3 : 4)) sm += r_[0] + r_[1];
        else {
          f32x4 q_[8];
#pragma unroll
          for (int o = 0; o < 8; ++o) q_[o] = lds16f(ra_ + o * 4096);
          SCHED();
          sm = ((q_[0] + q_[1]) + (q_[2] + q_[3])) + ((q_[4] + q_[5]) + (q_[6] + q_[7]));
        }
        if (j == 1) sm0 = sm;
        if (j == 2) EMIT_H(0, sm0)
        if (j >= 2) EMIT_H(j >= 2 ? j - 1 : 0, sm)
      }
      if (j == 3) {
#pragma unroll
        for (int e = 0; e < 4; ++e) DMA_WO(L_WO1, 0, e);
      }
      BAR_IF(cw, 0, 63);
    }
  }
  {
    const u32 bufA = L_RED + (gn & 1) * 32768, bufB = L_RED + 32768 - (gn & 1) * 32768;
    const u32 fv = FV();
    if (cw == 0) {
      const u32 ra_ = redr + (bufB - L_RED);
      f32x4 q_[8];
#pragma unroll
      for (int o = 0; o < 8; ++o) q_[o] = lds16f(ra_ + o * 4096);
      SCHED();
      const f32x4 sm = ((q_[0] + q_[1]) + (q_[2] + q_[3])) + ((q_[4] + q_[5]) + (q_[6] + q_[7]));
      if (gn == 6) EMIT_H(5, sm) else EMIT_H(4, sm)
    }
    WAITV(1); WAITV_IF(cw, 1, 0);
    PIN(gf);
    if (!__all(gf[0] == tlo && gf[1] == thi)) poll_flags(fv, true, sws, WS_FB, tlo, thi);
    RELANE();
    u32x4 hf[12], of[8];
    const u32 ofb = WS_OF + ((4 * p + rw) * 16 + cw) * 1024, hfb = WS_HF + ((4 * p + rw) * 24 + cw) * 1024;
#pragma unroll
    for (int e = 0; e < 4; ++e) of[e] = ld16_b(l16, sws, ofb + e * 2048);
#pragma unroll
    for (int e = 0; e < 4; ++e) hf[e] = ld16_b(l16, sws, hfb + e * 2048);
    BARRIER();
    const u32 qb = ((lane >> 2) & 3) * 64 + ((((g & 1) << 1) | ((lane >> 1) & 1)) * 16) + (lane & 1) * 8;
    f32x4 a3[4];
#pragma unroll
    for (int ct = 0; ct < 4; ++ct) a3[ct] = zero4;
    u32x4 wf[3][4];
#define RDW2(dst, lo0, hi0, offl, offh) _Pragma("unroll") for (int ct = 0; ct < 4; ++ct) dst[ct] = cat(ldstr(((lo0) ^ ((ct & 1) * 32)) + (offl) + (ct >> 1) * 512), ldstr(((hi0) ^ ((ct & 1) * 32)) + (offh) + (ct >> 1) * 512))
#define MM3(src, frag) _Pragma("unroll") for (int ct = 0; ct < 4; ++ct) a3[ct] = mfma32(src[ct], frag, a3[ct])
    const rsrc_t sxo = __builtin_amdgcn_make_buffer_rsrc((void*)(x + (size_t)p * 65536), 0, 65536, 0x00020000);
    const u32 w0 = opq(L_WOUT + qb + (g >> 1) * 1024 + (g & 1) * 256);
    const u32 wa = w0 + cw * 4096, wb = w0 + cw * 2048;
    const u32 oq = qb + g * 1024 + cw * 4096;
    const u32 oa = opq(L_WO1 + oq), ob = opq(bufA + oq);
    u32x4 xres;
    VWO();
#pragma unroll
    for (int e = 0; e < 4; ++e) DMA_WO(bufA, 1, e);
#define RDK(dst, k)                                                                                                 \
  if ((k) < 4) { RDW2(dst, oa, oa, (k) * 8192, (k) * 8192 + 256); }                                                 \
  else if ((k) < 8) { RDW2(dst, wa, wa, ((k) - 4) * 8192, ((k) - 4) * 8192 + 2048); }                               \
  else if ((k) < 12) { RDW2(dst, ob, ob, ((k) - 8) * 8192, ((k) - 8) * 8192 + 256); }                               \
  else { RDW2(dst, wb, w0, 32768 + ((k) - 12) * 4096, 49152 + ((k) - 12) * 2048); }
    RDK(wf[0], 0)
    RDK(wf[1], 1)
#define VALID4(e0)                                                                                                  \
  {                                                                                                                 \
    _Pragma("unroll") for (int e = e0; e < e0 + 4; ++e) PIN(hf[e]);                                                \
    _Pragma("nounroll") for (int it = 0; it < SPIN_CAP; ++it) {                                                    \
      u32 mx_ = 0;                                                                                                  \
      _Pragma("unroll") for (int e = e0; e < e0 + 4; ++e) mx_ = max(max(max(mx_, hf[e][0]), hf[e][1]), max(hf[e][2], hf[e][3])); \
      if (__all(mx_ != ~0u)) break;                                                                                 \
      __builtin_amdgcn_s_sleep(2);                                                                                  \
      asm volatile("buffer_load_dwordx4 %0, %4, %5, %6 offen sc1\n\tbuffer_load_dwordx4 %1, %4, %5, %6 offen offset:2048 sc1\n\t" \
                   "buffer_load_dwordx4 %2, %4, %5, %7 offen sc1\n\tbuffer_load_dwordx4 %3, %4, %5, %7 offen offset:2048 sc1\n\t" \
                   "s_waitcnt vmcnt(0)"                                                                             \
                   : "=&v"(hf[e0]), "=&v"(hf[e0 + 1]), "=&v"(hf[e0 + 2]), "=&v"(hf[e0 + 3])                         \
                   : "v"(l16), "s"(sws), "s"(hfb + (e0) * 2048), "s"(hfb + (e0) * 2048 + 4096)                       \
                   : "memory");                                                                                     \
    }                                                                                                               \
  }
#pragma unroll
    for (int k = 0; k < 16; ++k) {
      if (k == 0) WAITV(8);
      if (k == 4) { WAITV(12); VALID4(0) }
      if (k == 6) {
        WAITV(4);
        BARRIER();
      }
      if (k == 12) { WAITV(5); VALID4(4) }
      if (k + 2 < 16) { RDK(wf[(k + 2) % 3], k + 2) }
      SCHED();
      if (k < 4) { PIN(of[k]); MM3(wf[k % 3], of[k]); }
      else if (k < 8) MM3(wf[k % 3], hf[k - 4]);
      else if (k < 12) { PIN(of[k - 4]); MM3(wf[k % 3], of[k - 4]); }
      else MM3(wf[k % 3], hf[k - 8]);
      SCHED();
      if (k < 4) {
        of[4 + k] = ld16_b(l16, sws, ofb + (4 + k) * 2048);
        DMA_WOUT_AT(bufB + w * 4096, 7 + k)
      } else if (k < 6) {
        hf[2 * k - 4] = ld16_b(l16, sws, hfb + (2 * k - 4) * 2048);
        hf[2 * k - 3] = ld16_b(l16, sws, hfb + (2 * k - 3) * 2048);
      } else if (k == 6) {
        xres = ld16((tid >> 3) * 1024 + (tid & 7) * 16, sxo, c2 * 128);
      } else if (k == 10 || k == 11) {
        hf[8 + 2 * (k - 10)] = ld16_b(l16, sws, hfb + (8 + 2 * (k - 10)) * 2048);
        hf[9 + 2 * (k - 10)] = ld16_b(l16, sws, hfb + (9 + 2 * (k - 10)) * 2048);
      }
    }
    {
      const u32 wl_ = opq(bufB + (w0 - L_WOUT) + cw * 4096);
      u32x4 wg[4][4];
#pragma unroll
      for (int e = 0; e < 4; ++e) RDW2(wg[e], wl_, wl_, e * 8192, e * 8192 + 2048);
      WAITV(0);
      PIN(xres);
      VALID4(8)
#pragma unroll
      for (int e = 0; e < 4; ++e) MM3(wg[e], hf[8 + e]);
    }
    const u32 fin = L_WO1 - 2048;
#pragma unroll
    for (int ct = 0; ct < 4; ++ct) sts16f(fin + 2048 + cw * 16384 + (16 * rw + i16) * 256 + (((4 * ct + g) ^ i16) * 16), a3[ct]);
    BARRIER();
    {
      const int row = tid >> 3;
      const u32 a = fin + 2048 + row * 256 + (((2 * (tid & 7)) ^ (row & 15)) * 16);
      const f32x4 s0 = lds16f(a) + lds16f(a + 16384), s1 = lds16f(a ^ 16) + lds16f((a ^ 16) + 16384);
      u32x4 o = {pk2(blo(xres[0]) + s0[0], bhi(xres[0]) + s0[1]), pk2(blo(xres[1]) + s0[2], bhi(xres[1]) + s0[3]),
                 pk2(blo(xres[2]) + s1[0], bhi(xres[2]) + s1[1]), pk2(blo(xres[3]) + s1[2], bhi(xres[3]) + s1[3])};
      st16_sc1(o, row * 1024 + (tid & 7) * 16, __builtin_amdgcn_make_buffer_rsrc((void*)(out + (size_t)p * 65536), 0, 65536, 0x00020000), c2 * 128);
    }
  }
  }
  if (b == 0 && w == 0 && l16 == 0) *(u64*)(ws + WS_EP) = ((u64)thi << 32) | tlo;
}

__global__ __launch_bounds__(512, 2) void fused_kernel(const char* __restrict__ x, const char* __restrict__ wqkv, const char* __restrict__ wphi,
                                                        const char* __restrict__ wo, const char* __restrict__ win, const char* __restrict__ wsp,
                                                        const char* __restrict__ bsp, const char* __restrict__ wout, char* __restrict__ out, char* ws) {
  __shared__ __attribute__((aligned(1024))) char smem[163840];
  asm volatile("" ::"v"((u32)(size_t)(LDSP char*)smem));
  asm volatile("" ::"s"(x), "s"(wqkv), "s"(win), "s"(ws), "s"(wsp));
  u64 pc, ep;
  asm volatile("s_getpc_b64 %0" : "=s"(pc));
  {
    const int lane = threadIdx.x & 63, g = lane >> 4;
    const int w = __builtin_amdgcn_readfirstlane(threadIdx.x >> 6);
    const int r = blockIdx.x >> 3, P = ((blockIdx.x & 7) >> 1) * 4 + (r >> 3);
    const int rot = 2 * (((r >> 3) + (r & 3)) & 3);
    if (w == 7)
      touch(L_JUNK, lane * 128, __builtin_amdgcn_make_buffer_rsrc((void*)(pc & ~255ull), 0, CODE_PF, 0x00020000), (r & 7) * 8192);
    const rsrc_t sx = __builtin_amdgcn_make_buffer_rsrc((void*)(x + (size_t)P * 131072), 0, 131072, 0x00020000);
    const int tq = (r >> 2) & 1;
    const u32 vx0 = XROW(tq) * 1024 + (((lane & 7) ^ g) * 16);
    const u32 vx1 = (vx0 + (tq ? 8192 : 16384)) ^ 64;
    const u32 w2k = w * 2048;
    DMA_X(0, 0, 0);
    DMA_X(0, 0, 1);
    asm volatile("s_load_dwordx2 %0, %1, 0x0" : "=s"(ep) : "s"(ws) : "memory");
    SCHED();
  }
  if (__builtin_expect(((blockIdx.x >> 5) & 1) == 0, 1)) run<0>(x, wqkv, wphi, wo, win, wsp, bsp, wout, out, ws, ep);
  else run<1>(x, wqkv, wphi, wo, win, wsp, bsp, wout, out, ws, ep);
}

extern "C" void kernel_launch(void* const* d_in, const int* in_sizes, int n_in, void* d_out, int out_size, void* d_ws, size_t ws_size,
                              hipStream_t stream) {
  fused_kernel<<<256, 512, 0, stream>>>((const char*)d_in[0], (const char*)d_in[1], (const char*)d_in[2], (const char*)d_in[3],
                                        (const char*)d_in[5], (const char*)d_in[6], (const char*)d_in[7], (const char*)d_in[8], (char*)d_out,
                                        (char*)d_ws);
}
```
